```python
import torch

def _softmax(x, axis=-1):
    return torch.softmax(x, dim=axis)

def _gelu(x):
    return torch.nn.functional.gelu(x, approximate='tanh')

def _silu(x):
    return torch.nn.functional.silu(x)

def _sigmoid(x):
    return torch.sigmoid(x)

def _layernorm(x, axis=-1, eps=1e-05):
    m = x.mean(dim=axis, keepdim=True)
    v = x.var(dim=axis, keepdim=True, unbiased=False)
    return (x - m) / torch.sqrt(v + eps)

def _scatter_add(tgt, idx, src, axis=0):
    return tgt.clone().index_add_(axis, idx, src)

def _segment_sum(src, seg, n):
    out = torch.zeros((n,) + src.shape[1:], dtype=src.dtype)
    return out.index_add_(0, seg, src)

def _pad(x, pw):
    flat = tuple((v for pair in reversed(pw) for v in pair))
    return torch.nn.functional.pad(x, flat)
kernel_name = 'standalone__kv_cache_quant_pre_rope_vs_post_rope__sampling'

def setup_inputs():

    _w = lambda a: (lambda t: t.to(torch.bfloat16) if t.is_floating_point() else t)(torch.as_tensor(a))
    B, Sq, Skv = (8, 1, 2048)
    H, Dh0 = (32, 192)
    G = -1 if -1 > 0 else Dh0
    G = max(2, G // 2 * 2)
    Dh = max(G, Dh0 // G * G)
    Skv = max(Skv, Sq)
    ng = Dh // G
    bits = 4 if 4 > 1 else 4
    bits = max(2, min(8, bits))
    qmax = (1 << bits) - 1
    half = Dh // 2
    inv = 1.0 / 10000.0 ** (torch.arange(half) / max(1, half))
    ang_k = torch.arange(Skv)[:, None] * inv[None, :]
    ang_q = torch.arange(Skv - Sq, Skv)[:, None] * inv[None, :]
    kc = torch.cat([torch.cos(ang_k), torch.cos(ang_k)], dim=-1)
    ks = torch.cat([torch.sin(ang_k), torch.sin(ang_k)], dim=-1)
    qc = torch.cat([torch.cos(ang_q), torch.cos(ang_q)], dim=-1)
    qs = torch.cat([torch.sin(ang_q), torch.sin(ang_q)], dim=-1)
    qk_i = torch.randint(0, qmax + 1, (B, H, Skv, Dh)).to(torch.int32)
    qv_i = torch.randint(0, qmax + 1, (B, H, Skv, Dh)).to(torch.int32)
    ksc = torch.abs((lambda *s: torch.randn(*s, dtype=torch.bfloat16) * 0.2)(B, H, Skv, ng)) * 0.1 + 0.01
    kzp = torch.full((B, H, Skv, ng), qmax / 2.0)
    vsc = torch.abs((lambda *s: torch.randn(*s, dtype=torch.bfloat16) * 0.2)(B, H, Skv, ng)) * 0.1 + 0.01
    vzp = torch.full((B, H, Skv, ng), qmax / 2.0)
    return {'q': _w((lambda *s: torch.randn(*s, dtype=torch.bfloat16) * 6.4)(B, H, Sq, Dh) * 0.5), 'qk': _w(qk_i), 'k_scales': _w(ksc), 'k_zeros': _w(kzp), 'qv': _w(qv_i), 'v_scales': _w(vsc), 'v_zeros': _w(vzp), 'q_cos': _w(qc), 'q_sin': _w(qs), 'k_cos': _w(kc), 'k_sin': _w(ks)}

def reference(q, qk, k_scales, k_zeros, qv, v_scales, v_zeros, q_cos, q_sin, k_cos, k_sin):
    B, H, Sq, Dh = q.shape
    ng = k_scales.shape[-1]
    G = Dh // ng
    half = Dh // 2
    ks = (lambda x, n, axis: torch.repeat_interleave(x, n, dim=axis))(k_scales, G, axis=-1)
    kz = (lambda x, n, axis: torch.repeat_interleave(x, n, dim=axis))(k_zeros, G, axis=-1)
    k = ((lambda x, dt: x.to(dt))(qk, q.dtype) - kz) * ks
    vs = (lambda x, n, axis: torch.repeat_interleave(x, n, dim=axis))(v_scales, G, axis=-1)
    vz = (lambda x, n, axis: torch.repeat_interleave(x, n, dim=axis))(v_zeros, G, axis=-1)
    v = ((lambda x, dt: x.to(dt))(qv, q.dtype) - vz) * vs
    k1 = k[..., :half].clone()
    k2 = k[..., half:].clone()
    krh = torch.concatenate([-k2, k1], axis=-1)
    kr = k * k_cos[None, None, :, :] + krh * k_sin[None, None, :, :]
    q1 = q[..., :half].clone()
    q2 = q[..., half:].clone()
    qrh = torch.concatenate([-q2, q1], axis=-1)
    qr = q * q_cos[None, None, :, :] + qrh * q_sin[None, None, :, :]
    logits = torch.einsum('bhqd,bhkd->bhqk', qr, kr) * float(Dh) ** (-0.5)
    attn = _softmax(logits, axis=-1)
    out = torch.einsum('bhqk,bhkd->bhqd', attn, v)
    return out
```

```cpp
#define PFL 240
#include <hip/hip_runtime.h>
#include <hip/hip_bf16.h>
#include <stdint.h>

#define DH 192
#define HALF 96
#define SKV 2048
#define NH 256
#define NSW 8
#define NWAVES (NSW + 4)
#define NTHREADS (64 * NWAVES)
#define BROWS 128
#define NSPLIT (SKV / BROWS)
#define GSZ 16
#define NU (NH / GSZ)
#define NPC (2 * NU * 32)
#define N1 (NH - GSZ)
#define NIN 4
#define NOUT 4
#define ENTW 132
#define SC1 16
#define LIKELY 1
#define SLD(P) (*(P))
#define LSTRIDE 4352
#define PUBP 2
#define PHALF 9
#define PHI 0x0235712457024671ull
#define KAP 0x0202020220202020ull
#define PHIV PHI
#define KAPV KAP
#define XM 0
#define PRIO 1
#define PRIO0 "s_setprio 0\n"
#define PRIO1 "\n\ts_setprio 2"
#define GTH 10
#define GS1 2

#define WS_EPOCH 0
#define WS_DEC 8192
#define WS_REF 16384
#define WS_LOG 49152
#define WS_PART 1048576
#define WS_FIN (WS_PART + NH * NSPLIT * 512)

typedef unsigned int v2u __attribute__((ext_vector_type(2)));
typedef unsigned int v3u __attribute__((ext_vector_type(3)));
typedef unsigned int v4u __attribute__((ext_vector_type(4)));
typedef int v4i __attribute__((ext_vector_type(4)));
typedef float v2f __attribute__((ext_vector_type(2)));
typedef float v4f __attribute__((ext_vector_type(4)));
typedef unsigned long long u64;

__device__ __forceinline__ float bflo(unsigned u) { return __uint_as_float(u << 16); }
__device__ __forceinline__ float bfhi(unsigned u) { return __uint_as_float(u & 0xffff0000u); }
__device__ __forceinline__ float bfr(float x) {
    __bf16 b = (__bf16)x;
    return __uint_as_float(((unsigned)__builtin_bit_cast(unsigned short, b)) << 16);
}

#define SLEEP(n) SLEEP_(n)
#define SLEEP_(n) asm volatile("s_sleep " #n "\n\tv_add_co_u32 %0, vcc, 1, %0\n\ts_cbranch_vccz 1f\n\ts_endpgm\n1:" : "+v"(spins) :: "vcc", "memory");
#define SPIN0 0xf0000000u
#define CBAR() asm volatile("" ::: "memory")

__device__ __forceinline__ void wave_sync() {
    __builtin_amdgcn_fence(__ATOMIC_RELEASE, "wavefront");
    __builtin_amdgcn_wave_barrier();
    __builtin_amdgcn_fence(__ATOMIC_ACQUIRE, "wavefront");
}
template <int CTRL>
__device__ __forceinline__ float dpp_add(float x) {
    int y = __builtin_amdgcn_update_dpp(0, __float_as_int(x), CTRL, 0xf, 0xf, true);
    return x + __int_as_float(y);
}
template <int CTRL>
__device__ __forceinline__ float dpp_max(float x) {
    int y = __builtin_amdgcn_update_dpp(__float_as_int(x), __float_as_int(x), CTRL, 0xf, 0xf, false);
    return fmaxf(x, __int_as_float(y));
}
__device__ __forceinline__ float row_sum(float x) { x = dpp_add<0x128>(x); x = dpp_add<0x124>(x); x = dpp_add<0x122>(x); return dpp_add<0x121>(x); }
__device__ __forceinline__ float row_max(float m) { m = dpp_max<0x128>(m); m = dpp_max<0x124>(m); m = dpp_max<0x122>(m); return dpp_max<0x121>(m); }
__device__ __forceinline__ float rdlane(float x, int l) { return __int_as_float(__builtin_amdgcn_readlane(__float_as_int(x), l)); }
__device__ __forceinline__ float wave_max(float m) { m = row_max(m); return fmaxf(fmaxf(rdlane(m, 0), rdlane(m, 16)), fmaxf(rdlane(m, 32), rdlane(m, 48))); }
__device__ __forceinline__ float wave_maxv(float m) {
    m = row_max(m);
    const auto a = __builtin_amdgcn_permlane16_swap(__float_as_uint(m), __float_as_uint(m), false, false);
    m = fmaxf(__uint_as_float(a[0]), __uint_as_float(a[1]));
    const auto b = __builtin_amdgcn_permlane32_swap(__float_as_uint(m), __float_as_uint(m), false, false);
    return fmaxf(__uint_as_float(b[0]), __uint_as_float(b[1]));
}
__device__ __forceinline__ float wave_sum(float x) { x = row_sum(x); return (rdlane(x, 0) + rdlane(x, 16)) + (rdlane(x, 32) + rdlane(x, 48)); }
__device__ __forceinline__ unsigned lds_ld(const unsigned* p) { return __builtin_amdgcn_readfirstlane(__hip_atomic_load(p, __ATOMIC_RELAXED, __HIP_MEMORY_SCOPE_WORKGROUP)); }
__device__ __forceinline__ unsigned lds_addr(const void* p) { return (unsigned)(size_t)(const __attribute__((address_space(3))) void*)p; }
#define DMA16(SRC, LDS) asm volatile("s_mov_b32 %0, m0\n\ts_mov_b32 m0, %2\n\ts_nop 0\n\tglobal_load_lds_dwordx4 %1, off\n\ts_mov_b32 m0, %0" : "=&s"(m0keep) : "v"(SRC), "s"(LDS) : "memory")
__device__ __forceinline__ void lds_st(unsigned* p, unsigned v) { __hip_atomic_store(p, v, __ATOMIC_RELAXED, __HIP_MEMORY_SCOPE_WORKGROUP); }
#define NOW() ((unsigned)__builtin_amdgcn_s_memrealtime())

struct __attribute__((aligned(16))) Smem {
    unsigned cs[32][768];
    double acc[NOUT][DH];
    float ent[NU][ENTW];
    unsigned q[NIN][HALF];
    float2 kz[NIN][BROWS];
    float2 vz[NIN][BROWS];
    unsigned rdy[NIN], cnt[NOUT];
    v2u tu[2 * NU + 1];
    unsigned grab, merged, tabs;
    unsigned tag_rdy, tag_lo, tag_hi, cdone;
    int stat[32];
};
#define STAMP(i) {}
#define STAMPV(i, v) {}
#define STAMP_DUMP()
#define STAMP_UNIT(N)

#define TU(I) (*(const volatile v2u __attribute__((address_space(3)))*)(size_t)lds_addr(&sm.tu[I]))
#define LDS_ADD(P, V) { u64 ex_; asm volatile("s_mov_b64 %0, exec\n\ts_mov_b64 exec, 1\n\tds_add_u32 %1, %2\n\ts_mov_b64 exec, %0" : "=&s"(ex_) : "v"(lds_addr(P)), "v"(V) : "memory"); }
#define LDS_OR(P, V) { u64 ex_; asm volatile("s_mov_b64 %0, exec\n\ts_mov_b64 exec, 1\n\tds_or_b32 %1, %2\n\ts_mov_b64 exec, %0" : "=&s"(ex_) : "v"(lds_addr(P)), "v"(V) : "memory"); }

__global__ __launch_bounds__(NTHREADS, 3) void attn_kernel(
    const unsigned short* __restrict__ q, const int* __restrict__ qk,
    const unsigned short* __restrict__ k_scales, const unsigned short* __restrict__ k_zeros,
    const int* __restrict__ qv,
    const unsigned short* __restrict__ v_scales, const unsigned short* __restrict__ v_zeros,
    const unsigned short* __restrict__ q_cos, const unsigned short* __restrict__ q_sin,
    const unsigned short* __restrict__ k_cos, const unsigned short* __restrict__ k_sin,
    __hip_bfloat16* __restrict__ out, unsigned char* __restrict__ ws)
{
    __shared__ Smem sm;
    const int tid = threadIdx.x;
    unsigned spins = SPIN0;
    if ((tid >> 6) == 1) {
        const char* pc0; asm volatile("s_getpc_b64 %0" : "=s"(pc0));
        unsigned ln = (blockIdx.x >> 3) * PFL / 32 + (tid & 63);
        ln = ln >= PFL ? ln - PFL : ln;
        asm volatile("global_load_dword a6, %0, %1" :: "v"(ln * 128u), "s"(pc0) : "a6", "memory");
    }
    asm volatile("" :: "s"(q), "s"(qk), "s"(k_scales), "s"(k_zeros), "s"(qv), "s"(v_scales), "s"(v_zeros), "s"(q_cos), "s"(q_sin), "s"(k_cos), "s"(k_sin), "s"(out), "s"(ws));
    const int hw = __builtin_amdgcn_readfirstlane(tid >> 6);
    const int wave = hw >= 4 ? hw - 4 : hw == 0 ? NSW + 2 : hw == 1 ? NSW : hw == 2 ? NSW + 1 : NSW + 3;
    const int lane = tid & 63;
    STAMP(0)
    const unsigned ts = NOW();
    const int PK = (1266), PV = (1262), TOF = (110), P0 = (1270);
    const int split = (blockIdx.x & 3) * 4 + ((blockIdx.x >> 3) & 3);
    const int rank = (blockIdx.x >> 5) * 2 + ((blockIdx.x >> 2) & 1);
    const int brow0 = split * 16;
#define ROWOF(RG) (((RG) >> 2) * 256 + ((RG) & 3) * 4)

    if (LIKELY ? __builtin_expect(wave < NSW, 1) : wave < NSW) {
        const unsigned rot = 4 * ((unsigned)(PHI >> (60 - 4 * rank)) & 7u), kap = (unsigned)(KAP >> (60 - 4 * rank)) & 3u;
        const unsigned rotv = 4 * ((unsigned)(PHIV >> (60 - 4 * rank)) & 7u), kapv = (unsigned)(KAPV >> (60 - 4 * rank)) & 3u;
#define RGOF(G) ((G) >= NPC / 2 ? (((G) + rotv) & 31u) ^ kapv : (((G) + rot) & 31u) ^ kap)
        const char* cbase[3];
#pragma unroll
        for (int j = 0; j < 3; j++) { const int x = j * 64 + lane; cbase[j] = (x < 96 ? (const char*)k_cos + x * 16 : (const char*)k_sin + (x - 96) * 16) + brow0 * (DH * 2); }
        unsigned m0keep;
#define CS_DMA(RG) { _Pragma("unroll") for (int j = 0; j < 3; j++) DMA16(cbase[j] + ROWOF(RG) * (DH * 2), lds_addr(&sm.cs[0][0]) + (RG) * 3072 + j * 1024); }
#define REPACK(RG) { \
        unsigned* cw = &sm.cs[RG][0]; \
        const unsigned* l = cw + rsel * (DH / 2) + qoff; \
        unsigned r[2][6]; \
        _Pragma("unroll") \
        for (int j = 0; j < 2; j++) { \
            unsigned e[4][2]; \
            _Pragma("unroll") \
            for (int k = 0; k < 4; k++) { \
                const unsigned* a = l + j * DH + (k & 1) * 48 + (k >> 1) * 384;       \
                unsigned x0 = a[0], x1 = a[1]; \
                e[k][0] = __builtin_amdgcn_alignbit(x1, x0, sh); \
                e[k][1] = x1 >> sh; \
            } \
            r[j][0] = (e[0][0] & 0xffffu) | (e[3][0] << 16); \
            r[j][1] = (e[0][0] >> 16) | (e[3][0] & 0xffff0000u); \
            r[j][2] = (e[0][1] & 0xffffu) | (e[3][1] << 16); \
            r[j][3] = (e[1][0] & 0xffffu) | (e[2][0] << 16); \
            r[j][4] = (e[1][0] >> 16) | (e[2][0] & 0xffff0000u); \
            r[j][5] = (e[1][1] & 0xffffu) | (e[2][1] << 16); \
        } \
        wave_sync(); \
        _Pragma("unroll") \
        for (int j = 0; j < 2; j++) { *(v4u*)&cw[j * 256 + lane * 4] = (v4u){r[j][0], r[j][1], r[j][2], r[j][3]}; *(v2u*)&cw[512 + j * 128 + lane * 2] = (v2u){r[j][4], r[j][5]}; } \
        wave_sync(); \
        LDS_OR(&sm.tabs, 1u << (RG)) }
        const int sub = lane & 31;
        const int rsel = lane >> 5;
        const unsigned sh = (sub & 1) * 16;
        const int qoff = 3 * (sub >> 1) + (sub & 1);
        const char* kbase = (const char*)qk + (size_t)(rank * SKV + brow0) * (DH * 4);
        const char* vbase = (const char*)qv + (size_t)(rank * SKV + brow0) * (DH * 4);
        const unsigned koff = (rsel * DH + 3 * sub) * 4, voff = 3 * lane * 4;
#define AR(S) (16 + (S) * 8)
        asm volatile("" ::: "a16", "a17", "a18", "a19", "a20", "a21", "a22", "a23", "a24", "a25", "a26", "a27", "a28", "a29", "a30", "a31",
                            "a32", "a33", "a34", "a35", "a36", "a37", "a38", "a39", "a40", "a41", "a42", "a43", "a44", "a45", "a46", "a47");
        asm volatile("" ::: "a48", "a49", "a50", "a51", "a52", "a53", "a54", "a55", "a56", "a57", "a58", "a59", "a60", "a61", "a62", "a63");
        __builtin_amdgcn_s_waitcnt(0x0f70);
        int ta[3], tb[3];
#define GLD(A, OFF, BASE, IMM) asm volatile("global_load_dwordx3 a[%c0:%c1], %2, %3 offset:%4 nt" :: "i"(A), "i"((A) + 2), "v"(OFF), "s"(BASE), "i"(IMM))
#define ISSUE(S, J, BASE, OA, OB) { GLD(AR(S), OA, BASE, (J) * 1536); GLD(AR(S) + 4, OB, BASE, (J) * 1536); }
#define WAITT(S, N) asm volatile("s_waitcnt vmcnt(%6)\n\tv_accvgpr_read_b32 %0, a[%c7]\n\tv_accvgpr_read_b32 %1, a[%c8]\n\tv_accvgpr_read_b32 %2, a[%c9]\n\t" \
                              "v_accvgpr_read_b32 %3, a[%c10]\n\tv_accvgpr_read_b32 %4, a[%c11]\n\tv_accvgpr_read_b32 %5, a[%c12]" \
                              : "=v"(ta[0]), "=v"(ta[1]), "=v"(ta[2]), "=v"(tb[0]), "=v"(tb[1]), "=v"(tb[2]) \
                              : "i"(N), "i"(AR(S)), "i"(AR(S) + 1), "i"(AR(S) + 2), "i"(AR(S) + 4), "i"(AR(S) + 5), "i"(AR(S) + 6))
        const unsigned koffb = koff + 384, voffb = voff + 768;
        unsigned u = 0, rg, np = 0;
#define RG0(C) RGOF((unsigned)(wave + 8 * (C)))
        CS_DMA(RG0(0))
        STAMP(52)
        __builtin_amdgcn_s_barrier();
        CBAR();
        STAMP(53)
        const float scale = 0.07216878364870322f;
        double acc[3] = {0.0, 0.0, 0.0};
        bool first = true, chg, none = false;
        unsigned Qp[3], Qn[3];
        float kz4[2], ksc, kres[2];
        v4u ca[2]; v2u cb[2];
        v2f qe[2][3], A[2];
        const char* nb; unsigned oa, ob;
        unsigned g, mg, nrg, tg, late = 0;
        u64 tis;
        unsigned dg, par = 0;
        const unsigned bth = wave < (8) ? 0u - (15) * 128u : 0x80000000u;
        const unsigned grab_adr = lds_addr(&sm.grab);
        asm volatile("v_accvgpr_write_b32 a13, 1\n\tv_accvgpr_write_b32 a14, 2" ::: "a13", "a14", "a15");
#define GRAB(N) { u64 ex_; asm volatile("s_mov_b64 %0, exec\n\ts_mov_b64 exec, 1\n\tds_add_rtn_u32 a15, %1, a%c2\n\ts_mov_b64 exec, %0" : "=&s"(ex_) : "v"(grab_adr), "i"(12 + (N)) : "memory"); }
#define GRABBED() { unsigned t_; asm volatile("s_waitcnt lgkmcnt(0)\n\tv_accvgpr_read_b32 %1, a15\n\ts_nop 0\n\tv_readfirstlane_b32 %0, %1\n\ts_nop 1" : "=s"(g), "=v"(t_)); }
#define NEXTPIECE() { \
            GRABBED() chg = (g >> 5) != u; none = g >= NPC; \
            const bool nv = g >= NPC / 2; \
            nrg = RGOF(g); \
            unsigned boff; asm("s_mul_i32 %0, %1, 0x300" : "=s"(boff) : "s"(((g >> 5) & (NU - 1)) * (GSZ * SKV) + ROWOF(nrg) + 2)); \
            nb = (nv ? vbase : kbase) + boff; \
            oa = nv ? voff : koff; ob = nv ? voffb : koffb; \
            const v2u x_ = TU(g >> 5); \
            tg = __builtin_amdgcn_readfirstlane(x_.x) + (g & 31) * __builtin_amdgcn_readfirstlane(x_.y); }
#define GATE(...) \
            asm volatile(PRIO0 "1:\n\ts_memrealtime vcc\n\ts_waitcnt lgkmcnt(0)\n\ts_lshl_b32 vcc_lo, vcc_lo, 7\n\ts_sub_u32 %0, %1, vcc_lo\n\ts_cmp_lt_u32 %0, 0x40000\n\ts_cbranch_scc0 2f\n\t" \
                         "s_cmp_lt_u32 %0, %2\n\ts_cbranch_scc1 1b\n\ts_sleep %3\n\ts_branch 1b\n2:" PRIO1 : "=&s"(dg) : "s"(tg), "i"(GTH * 128), "i"(GS1), __VA_ARGS__ : "scc", "vcc", "memory");
#define RD6(A) "v_accvgpr_read_b32 %0, a[%c6+" #A "]\n\tv_accvgpr_read_b32 %1, a[%c6+" #A "+1]\n\tv_accvgpr_read_b32 %2, a[%c6+" #A "+2]\n\t" \
               "v_accvgpr_read_b32 %3, a[%c6+" #A "+4]\n\tv_accvgpr_read_b32 %4, a[%c6+" #A "+5]\n\tv_accvgpr_read_b32 %5, a[%c6+" #A "+6]\n"
#define LD2(A) "global_load_dwordx3 a[%c6+" #A ":%c6+" #A "+2], %7, %9 offset:%10 nt\n\tglobal_load_dwordx3 a[%c6+" #A "+4:%c6+" #A "+6], %8, %9 offset:%10 nt"
#define OUT6 "=&v"(ta[0]), "=&v"(ta[1]), "=&v"(ta[2]), "=&v"(tb[0]), "=&v"(tb[1]), "=&v"(tb[2])
#define WAITISSUE(S, TAIL, ...) asm volatile("s_cmp_lt_u32 %13, %14\n\ts_cbranch_scc1 3f\n\ts_waitcnt vmcnt(2)\n3:\n\ts_cmp_lg_u32 %12, 0\n\ts_cbranch_scc1 1f\n\t" \
                              LD2(16) TAIL "\n\ts_waitcnt vmcnt(4)\n\t" RD6(0) "\ts_branch 2f\n1:\n\t" LD2(0) TAIL "\n\ts_waitcnt vmcnt(4)\n\t" RD6(16) "2:" \
                              : OUT6 : "i"(AR(S)), "v"(oa), "v"(ob), "s"(nb), "i"(((S) - 1) * 1536), "v"(grab_adr), "s"(pr), "s"(dg), "s"(bth), __VA_ARGS__ : "scc", "vcc", "memory")
#define LASTT(S) asm volatile("s_waitcnt vmcnt(%c8)\n\ts_cmp_lg_u32 %7, 0\n\ts_cbranch_scc1 1f\n\t" RD6(0) "\ts_branch 2f\n1:\n\t" RD6(16) "2:" : OUT6 : "i"(AR(S)), "s"(pr), "i"(2 - 2 * (S)) : "scc")
#define DRAW "\n\ts_mov_b64 vcc, exec\n\ts_mov_b64 exec, 1\n\tds_add_rtn_u32 a15, %11, a13\n\ts_mov_b64 exec, vcc"
#define LATE0()
#define LATE1()
#define LATEF()
#define K_TAB() { _Pragma("unroll") for (int p = 0; p < 2; p++) { \
            const unsigned* cw = &sm.cs[rg][0]; \
            ca[p] = *(const v4u*)&cw[p * 256 + lane * 4]; cb[p] = *(const v2u*)&cw[512 + p * 128 + lane * 2]; } }
#define K_PRE(P) { \
        float e0, e1, e2, f0, f1, f2; \
        asm("v_dot2_f32_bf16 %0, %6, %12, 0\n\tv_dot2_f32_bf16 %3, %9, %15, 0\n\tv_dot2_f32_bf16 %1, %7, %13, 0\n\tv_dot2_f32_bf16 %4, %10, %16, 0\n\t" \
            "v_dot2_f32_bf16 %2, %8, %14, 0\n\tv_dot2_f32_bf16 %5, %11, %17, 0\n\ts_nop 2" \
            : "=&v"(e0), "=&v"(e1), "=&v"(e2), "=&v"(f0), "=&v"(f1), "=&v"(f2) \
            : "v"(Qp[0]), "v"(Qp[1]), "v"(Qp[2]), "v"(Qn[0]), "v"(Qn[1]), "v"(Qn[2]), \
              "v"(ca[P].x), "v"(ca[P].y), "v"(ca[P].z), "v"(ca[P].w), "v"(cb[P].x), "v"(cb[P].y)); \
        qe[P][0] = (v2f){e0, f0}; qe[P][1] = (v2f){e1, f1}; qe[P][2] = (v2f){e2, f2}; \
        A[P] = (qe[P][0] + qe[P][1] + qe[P][2]) * (v2f){-kz4[P], -kz4[P]}; }
#define K_SUM(P) { \
        _Pragma("unroll") \
        for (int i = 0; i < 3; i++) { v2f x = {(float)ta[i], (float)tb[i]}; A[P] = qe[P][i] * x + A[P]; } \
        kres[P] = A[P].x + A[P].y; }
#define K_RED2() { \
        const bool o1 = lane & 1; \
        float sx = (o1 ? kres[1] : kres[0]) + __int_as_float(__builtin_amdgcn_update_dpp(0, __float_as_int(o1 ? kres[0] : kres[1]), 0xb1, 0xf, 0xf, true)); \
        sx = dpp_add<0x4e>(sx); sx = dpp_add<0x124>(sx); sx = dpp_add<0x128>(sx); \
        const auto sw = __builtin_amdgcn_permlane16_swap(__float_as_uint(sx), __float_as_uint(sx), false, false); \
        sx = __uint_as_float(sw[0]) + __uint_as_float(sw[1]); \
        if ((lane & 30) == 0) sm.ent[u][rg * 4 + 2 * (lane & 1) + rsel] = sx * ksc; }
#define LDV(P) __hip_atomic_load(P, __ATOMIC_RELAXED, __HIP_MEMORY_SCOPE_WORKGROUP)
#define RDYWAIT(SL, SEQ) { while (lds_ld(&sm.rdy[SL]) != (SEQ)) SLEEP(1); CBAR(); }
#define LOADZ() { _Pragma("unroll") for (int p = 0; p < 2; p++) kz4[p] = sm.kz[u % NIN][rg * 4 + 2 * p + rsel].x; ksc = sm.kz[u % NIN][rg * 4 + 2 * (lane & 1) + rsel].y * scale; }
#define LOADQ() { _Pragma("unroll") for (int i = 0; i < 3; i++) { Qp[i] = sm.q[u % NIN][3 * sub + i]; Qn[i] = __builtin_amdgcn_alignbit(Qp[i], Qp[i], 16) ^ 0x80000000u; } }
#define FLUSH(ISV) { \
            mg = __builtin_amdgcn_readfirstlane(mg); \
            while (u >= mg + NOUT) { SLEEP(1); mg = lds_ld(&sm.merged); } \
            CBAR(); \
            if (ISV) { const unsigned aa = lds_addr(&sm.acc[u % NOUT][lane]); \
                asm volatile("ds_add_f64 %0, %1\n\tds_add_f64 %0, %2 offset:512\n\tds_add_f64 %0, %3 offset:1024" :: "v"(aa), "v"(acc[0]), "v"(acc[1]), "v"(acc[2]) : "memory"); \
                acc[0] = 0.0; acc[1] = 0.0; acc[2] = 0.0; } \
            CBAR(); \
            LATEF() \
            LDS_ADD(&sm.cnt[u % NOUT], np) \
            CBAR(); \
            STAMP_UNIT(np) \
            np = 0; }
#define ADVANCE() { first = chg; u = g >> 5; rg = nrg; }

#define REQ0(N, S) { tg = (ts + TOF) * 128 + (wave + 8 * (N)) * P0; \
            const char* kb = kbase + (((N) >> 2) * (GSZ * SKV) + ROWOF(RG0((N) & 3))) * (DH * 4); GATE("s"(kb)) ISSUE(S, 0, kb, koff, koffb) ISSUE((S) + 1, 1, kb, koff, koffb) STAMP(44 + (N)) }
#define USE0(N, S, W) { u = (N) >> 2; rg = RG0((N) & 3); K_TAB() LOADQ() LOADZ() K_PRE(0) K_PRE(1) WAITT(S, W); K_SUM(0) WAITT((S) + 1, W); K_SUM(1) K_RED2() }
#define VMW(N) asm volatile("s_waitcnt vmcnt(" #N ")" ::: "memory");
        REQ0(0, 0) CS_DMA(RG0(1))
        VMW(7) REPACK(RG0(0))
        STAMP(1)
        REQ0(1, 2)
        RDYWAIT(0, 1u)
        USE0(0, 0, 7)
        VMW(4) REPACK(RG0(1))
        REQ0(2, 0) CS_DMA(RG0(2))
        USE0(1, 2, 7)
        REQ0(3, 2)
        VMW(4) REPACK(RG0(2))
        USE0(2, 0, 4)
        REQ0(4, 0) CS_DMA(RG0(3))
        REQ0(5, 4)
        VMW(4) REPACK(RG0(3))
        USE0(3, 2, 4)
        CBAR();
        LDS_ADD(&sm.cnt[0], 4u)
        REQ0(6, 2)
        RDYWAIT(1, 2u)
        USE0(4, 0, 8)
        USE0(5, 4, 4)
        REQ0(7, 0)
        GRAB(1)
        USE0(6, 2, 4)
        rg = RG0(3); np = 3; first = false;
        bool tabok = false;
#pragma unroll 1
        while (u < NU) {
            mg = LDV(&sm.merged);
            if (!tabok) {
                for (;;) { const unsigned tb_ = lds_ld(&sm.tabs); tabok = tb_ == ~0u; if ((tb_ >> rg) & 1u) break; SLEEP(1) }
                CBAR();
            }
            K_TAB()
            if (first) {
                unsigned fl = LDV(&sm.rdy[u % NIN]);
                CBAR();
                LOADQ() LOADZ()
                if (__builtin_amdgcn_readfirstlane(fl) != u + 1) { RDYWAIT(u % NIN, u + 1) LOADQ() LOADZ() }
            } else LOADZ()
            NEXTPIECE()
            K_PRE(0) K_PRE(1)
            const unsigned pr = __builtin_amdgcn_readfirstlane(par); par ^= 1;
            GATE("v"(A[0]), "v"(A[1]), "v"(oa), "v"(ob), "s"(nb), "s"(pr))
            WAITISSUE(0, DRAW, "v"(A[0]));
            LATE0()
            K_SUM(0)
            WAITISSUE(1, "", "v"(A[1]));
            K_SUM(1)
            LATE1()
            K_RED2()
            np++;
            if (chg) FLUSH(false)
            ADVANCE()
        }
#pragma unroll 1
        while (u < 2 * NU) {
            mg = LDV(&sm.merged);
            CBAR();
            v4i pw = *(const v4i*)&sm.ent[u - NU][rg * 4];
            NEXTPIECE()
            if (first && __builtin_amdgcn_readfirstlane(mg) + NU <= u) { while (lds_ld(&sm.merged) + NU <= u) SLEEP(1); CBAR(); pw = *(const v4i*)&sm.ent[u - NU][rg * 4]; }
            const double w0 = pw.x, w1 = pw.y, w2 = pw.z, w3 = pw.w;
            const unsigned pr = __builtin_amdgcn_readfirstlane(par); par ^= 1;
            if (none) { LASTT(0); } else { GATE("s"(pr), "v"(w0), "v"(w1), "v"(w2), "v"(w3), "v"(oa), "v"(ob), "s"(nb)) WAITISSUE(0, DRAW, "v"(w0)); }
            LATE0()
#pragma unroll
            for (int i = 0; i < 3; i++) acc[i] = fma(w1, (double)tb[i], fma(w0, (double)ta[i], acc[i]));
            if (none) { LASTT(1); } else WAITISSUE(1, "", "v"(w2));
#pragma unroll
            for (int i = 0; i < 3; i++) acc[i] = fma(w3, (double)tb[i], fma(w2, (double)ta[i], acc[i]));
            LATE1()
            np++;
            if (chg) FLUSH(true)
            ADVANCE()
        }
        STAMP(2)
        {
            const int head = blockIdx.x;
            const __amdgpu_buffer_rsrc_t rsrc = __builtin_amdgcn_make_buffer_rsrc(ws + WS_PART + (size_t)head * (NSPLIT * 512), 0, NSPLIT * 512, 0x00020000);
            const unsigned t15 = lds_ld(&sm.tag_lo) & 0x7fffu;
            unsigned missing = 3u;
            float L[2], f[2][3];
#pragma unroll
            for (int k = 0; k < 2; k++) { L[k] = -3e38f; f[k][0] = 0.f; f[k][1] = 0.f; f[k][2] = 0.f; }
#define POLL(R) { _Pragma("unroll") for (int k = 0; k < 2; k++) R[k] = __builtin_amdgcn_raw_buffer_load_b64(rsrc, lane * 8 + (((missing >> k) & 1) ? (wave + 8 * k) * 512 : 0x100000), 0, SC1); }
#define TAKE(R) { _Pragma("unroll") for (int k = 0; k < 2; k++) { \
                    if (((missing >> k) & 1) && __builtin_amdgcn_ballot_w64((R[k].y >> 17) == t15) == ~0ull) { \
                        missing &= ~(1u << k); \
                        const u64 bits = __builtin_amdgcn_ballot_w64((R[k].y & 0x10000u) != 0); \
                        L[k] = __uint_as_float((unsigned)bits); \
                        const int ex = (int)((unsigned)(bits >> 32) & 255u) - 141; \
                        f[k][0] = ldexpf((float)(short)(R[k].x & 0xffffu), ex); f[k][1] = ldexpf((float)((int)R[k].x >> 16), ex); f[k][2] = ldexpf((float)(short)(R[k].y & 0xffffu), ex); \
                    } } }
            v2u ra[2], rb[2];
            POLL(ra)
            TAKE(ra)
            if (missing) {
                POLL(ra)
                SLEEP(PHALF)
#pragma unroll 1
                for (;;) {
                    POLL(rb)
                    TAKE(ra)
                    if (!missing) break;
                    SLEEP(0)
                    POLL(ra)
                    TAKE(rb)
                    if (!missing) break;
                    SLEEP(0)
                }
            }
            const float M = fmaxf(L[0], L[1]);
            const float w0 = __builtin_amdgcn_exp2f(L[0] - M), w1 = __builtin_amdgcn_exp2f(L[1] - M);
            v4f* pb = (v4f*)((char*)&sm.cs[0][0] + 65536);
            float* pm = (float*)(pb + NSW * 64);
            pb[wave * 64 + lane] = (v4f){fmaf(w1, f[1][0], w0 * f[0][0]), fmaf(w1, f[1][1], w0 * f[0][1]), fmaf(w1, f[1][2], w0 * f[0][2]), w0 + w1};
            if (lane == 0) pm[wave] = M;
            CBAR();
            { u64 ex_; asm volatile("s_mov_b64 %0, exec\n\ts_mov_b64 exec, 1\n\tds_add_rtn_u32 a15, %1, a13\n\ts_mov_b64 exec, %0" : "=&s"(ex_) : "v"(lds_addr(&sm.cdone)) : "memory"); }
            const v4f ma = *(const v4f*)pm, mb = *(const v4f*)(pm + 4);
            v4f ps[NSW];
#pragma unroll
            for (int w = 0; w < NSW; w++) ps[w] = pb[w * 64 + lane];
            GRABBED()
            if (g == NSW - 1) {
                const float mw[NSW] = {ma.x, ma.y, ma.z, ma.w, mb.x, mb.y, mb.z, mb.w};
                float Mx = mw[0];
#pragma unroll
                for (int w = 1; w < NSW; w++) Mx = fmaxf(Mx, mw[w]);
                v4f sum = {0.f, 0.f, 0.f, 0.f};
#pragma unroll
                for (int w = 0; w < NSW; w++) sum += ps[w] * __builtin_amdgcn_exp2f(mw[w] - Mx);
                const float inv = 1.f / sum.w;
                __hip_bfloat16* o = out + (size_t)head * DH + 3 * lane;
                o[0] = __float2bfloat16(sum.x * inv); o[1] = __float2bfloat16(sum.y * inv); o[2] = __float2bfloat16(sum.z * inv);
            }
        }
        STAMP(62)
        STAMP_DUMP()
        return;
    }

    if (wave == NSW) {
        if (lane < NIN) sm.rdy[lane] = 0;
        if (lane < NOUT) sm.cnt[lane] = 0;
        if (lane == 0) { sm.tabs = 0; sm.grab = 64; sm.tag_rdy = 0; sm.merged = 0; sm.cdone = 0; }
        if (lane < 32) sm.stat[lane] = 0;
        asm volatile("s_waitcnt lgkmcnt(0)" ::: "memory");
        __builtin_amdgcn_s_barrier();
        CBAR();
        STAMPV(24, (unsigned)__builtin_amdgcn_s_memtime()) STAMP(25)
    } else if (wave == NSW + 1) {
        const u64 E = __hip_atomic_load((const u64*)(ws + WS_EPOCH), __ATOMIC_RELAXED, __HIP_MEMORY_SCOPE_AGENT);
        __builtin_amdgcn_s_barrier();
        CBAR();
        const int NB = (4), CAP = (45);
        unsigned lU = 2, lT = (ts + TOF) * 128 + 64 * P0; int lper = PK, dl = 0;
#define OWN(U) (dl * max(NB + 2 - (int)(U), 0) * 32)
#define NEWLINE() { if (lane >= lU && lane <= 2 * NU) sm.tu[lane] = (v2u){lT + (lane - lU) * 32 * lper + (max(lane - NU, 0) - max((int)lU - NU, 0)) * 32 * (PV - PK) + OWN(lane), \
                                                                          (unsigned)(lper + (lane >= NU ? PV - PK : 0) - (lane < NB + 2 ? dl : 0))}; }
        NEWLINE()
        if (lane == 0) sm.tu[1] = (v2u){lT - 32 * P0, (unsigned)P0};
        const u64 E1 = E + 1;
        const unsigned rtag = (unsigned)E1 * 0x9e3779b1u;
        const bool boss = blockIdx.x == 0;
        u64* const slot = (u64*)(ws + WS_REF + (blockIdx.x & 63) * LSTRIDE + (blockIdx.x >> 6) * 16);
        const u64* const slots = (const u64*)(ws + WS_REF + lane * LSTRIDE);
        u64* const dec = (u64*)(ws + WS_DEC);
#define GLDS(P) __hip_atomic_load(P, __ATOMIC_RELAXED, __HIP_MEMORY_SCOPE_AGENT)
#define GSTS(P, V) __hip_atomic_store(P, V, __ATOMIC_RELAXED, __HIP_MEMORY_SCOPE_AGENT)
#define UNTIL(T) while ((unsigned)((T) - NOW() * 128) < 0x40000u) SLEEP(2)
        if (lane < 2) GSTS(slot + lane, lane ? E1 : (u64)ts | ((u64)rtag << 32));
        if (lane == 0) { sm.tag_lo = (unsigned)E1; sm.tag_hi = (unsigned)(E1 >> 32); CBAR(); lds_st(&sm.tag_rdy, 1u); }
        const int K0 = (3), GAIN = (24), BK = (13), BV = (3), XLEAD = (170), XD1 = (100), XD2 = (15), XW = (40), RTH = (6), KI = (0), KD = (0);
        unsigned dv = 0;
        bool got = true;
#define DECISION(K) for (;;) { const u64 x = GLDS(dec + ((K) & 15)); dv = __builtin_amdgcn_readfirstlane((unsigned)x); got = __builtin_amdgcn_readfirstlane((unsigned)(x >> 32)) == rtag + (K); \
                               if (got || lds_ld(&sm.merged) >= 2 * NU - 1) break; STAMPV(41, sm.st[wave][41] + 1) SLEEP(32) }
        if (boss) {
            UNTIL((ts + (330)) * 128)
            for (;;) {
                bool ok = true; float sum = 0.f;
#pragma unroll
                for (int i = 0; i < 4; i++) { const u64 x = GLDS(slots + 2 * i), e = GLDS(slots + 2 * i + 1); ok &= e == E1 && (unsigned)(x >> 32) == rtag; sum += (float)min(max((int)((unsigned)x - ts), -1000), 1000); }
                if (__builtin_amdgcn_ballot_w64(ok) == ~0ull) { dv = ts + (int)(wave_sum(sum) * (1.f / 256)); break; }
                SLEEP(8)
            }
            if (lane == 0) { GSTS(dec, (u64)dv | ((u64)rtag << 32)); GSTS((u64*)(ws + WS_EPOCH), E1); }
        } else {
            UNTIL((ts + (330) + (300)) * 128)
            DECISION(0)
        }
        {
            const int d0 = (min(max((int)(dv - ts), -1000), 1000) + (0)) * 128;
            dl = -d0 / (32 * NB); lT += d0;
            if (got) NEWLINE()
        }
        STAMP(3)
        int sp = 0, sp1 = 0, jump = 0;
        dv = 0;
#pragma unroll 1
        for (unsigned k = K0; k < 2 * NU && got; k++) {
            const unsigned tk = lT + (k - lU) * 32 * lper;
            const unsigned tx = tk - XLEAD * 128;
            if (k + 1 < 2 * NU) {
                UNTIL(tx - XW * 128)
                int sum = 0, n = 0;
                do {
                    const unsigned p = lds_ld(&sm.grab) - 9;
                    const unsigned now = NOW() * 128;
                    const v2u e = TU(p >> 5);
                    sum += (int)(now - __builtin_amdgcn_readfirstlane(e.x) - (p & 31) * __builtin_amdgcn_readfirstlane(e.y)) >> 7; n++;
                } while ((unsigned)(tx - NOW() * 128) < 0x40000u);
                const int l = (int)((float)sum / (float)n) - (k <= NU ? BK + (4) : BK);
                if (l > RTH && lane == 0) GSTS((unsigned*)(ws + WS_REF + (blockIdx.x & 63) * LSTRIDE + 64) + (blockIdx.x >> 6), (unsigned)min(l, 1000) | ((rtag + k) << 10));
            }
            if (!boss && k > K0) {
                UNTIL(tx)
                const unsigned tl = NOW();
                DECISION(k)
                STAMPV(42 + (k >> 1), NOW() - tl)
                if (!got) break;
            }
            sp1 = sp;
            jump = min(dv >> 16, 1000u) * 128; sp = min(dv & 0xffffu, 4000u);
            lper = min(max(lper + min(sp, 40) * KI / 64 - KD, PK), PK + 40);
            const int pk = lper + (k >= NU ? PV - PK : 0);
            lU = k + 1; lT = tk + jump + 32 * (pk + sp);
            if (lane == 0) sm.tu[k] = (v2u){tk + jump + OWN(k), (unsigned)(pk + sp - (k < NB + 2 ? dl : 0))};
            NEWLINE()
            STAMPV(40, max((int)sm.st[wave][40], (int)(NOW() * 128 - tk) / 128 + 1000))
            if (boss && k + 1 < 2 * NU) {
                UNTIL(tx + XD1 * 128)
                const unsigned tl = NOW();
                const v4u x = __builtin_amdgcn_raw_buffer_load_b128(__builtin_amdgcn_make_buffer_rsrc(ws + WS_REF + 64, 0, 64 * LSTRIDE, 0x00020000), lane * LSTRIDE, 0, SC1);
                float a[4];
#pragma unroll
                for (int i = 0; i < 4; i++) a[i] = ((x[i] ^ ((rtag + k) << 10)) >> 10) == 0 ? (float)(x[i] & 1023u) : 0.f;
                asm volatile("" :: "v"(a[0]));
                STAMPV(42 + (k >> 1), NOW() - tl)
                const float mn = -wave_max(-fminf(fminf(a[0], a[1]), fminf(a[2], a[3]))), mx = wave_max(fmaxf(fmaxf(a[0], a[1]), fmaxf(a[2], a[3])));
                const float c = mn + (float)CAP;
                const int tot = (int)wave_sum(a[0] + a[1] + a[2] + a[3]) / 2, cut = (int)wave_sum(fminf(a[0], c) + fminf(a[1], c) + fminf(a[2], c) + fminf(a[3], c)) / 2;
                const int pend = jump + sp * 32 + sp1 * 32 * XLEAD * 4 / (pk + sp1);
                const int nj = GAIN ? max((int)mn * 128 - pend, 0) : 0;
                const int ns = max(max(min(tot * GAIN / 16 - pend - nj, (cut - (int)mn * 128) * GAIN / 16), 0), mx > (float)(XD2 * (2 * NU - (int)k)) ? BV * 128 : 0);
                dv = ((nj >> 7) << 16) | (unsigned)min(ns >> 5, 4000);
                if (lane == 0) GSTS(dec + ((k + 1) & 15), (u64)dv | ((u64)(rtag + k + 1) << 32));
                STAMPV(5 + k, ((tot >> 7) << 20) | (((nj + ns) >> 7) << 10) | (lper - PK + 100))
            }
        }
    } else if (wave == NSW + 2) {
        const int ql = lane < 48 ? lane : 47;
        unsigned c1, c2, s1, s2;
#define QT_LOADS() { int qo_ = ql; asm volatile("" : "+v"(qo_)); \
                     c1 = ((const unsigned*)q_cos)[qo_]; c2 = ((const unsigned*)q_cos)[48 + qo_]; s1 = ((const unsigned*)q_sin)[qo_]; s2 = ((const unsigned*)q_sin)[48 + qo_]; }
        unsigned q1, q2, lkz, lks, lvz, lvs;
#define UNIT_LOADS(HEAD) { \
        q1 = ((const unsigned*)(q + (size_t)(HEAD) * DH))[ql]; q2 = ((const unsigned*)(q + (size_t)(HEAD) * DH))[48 + ql]; \
        const size_t so = ((size_t)(HEAD) * SKV + brow0) / 2 + (lane >> 3) * 128 + (lane & 7); \
        lkz = SLD((const unsigned*)k_zeros + so); lks = SLD((const unsigned*)k_scales + so); \
        lvz = SLD((const unsigned*)v_zeros + so); lvs = SLD((const unsigned*)v_scales + so); }
#define UNIT_FILL(SL, U) { \
            v4f qq; \
            qq.x = bfr(bfr(bflo(q1) * bflo(c1)) + bfr(-bflo(q2) * bflo(s1))); \
            qq.y = bfr(bfr(bflo(q2) * bflo(c2)) + bfr(bflo(q1) * bflo(s2))); \
            qq.z = bfr(bfr(bfhi(q1) * bfhi(c1)) + bfr(-bfhi(q2) * bfhi(s1))); \
            qq.w = bfr(bfr(bfhi(q2) * bfhi(c2)) + bfr(bfhi(q1) * bfhi(s2))); \
            *(v2u*)&sm.q[SL][2 * ql] = (v2u){(__float_as_uint(qq.x) >> 16) | __float_as_uint(qq.y), (__float_as_uint(qq.z) >> 16) | __float_as_uint(qq.w)}; \
            *(v4f*)&sm.kz[SL][2 * lane] = (v4f){bflo(lkz), bflo(lks), bfhi(lkz), bfhi(lks)}; \
            *(v4f*)&sm.vz[SL][2 * lane] = (v4f){bflo(lvz), bflo(lvs), bfhi(lvz), bfhi(lvs)}; \
            CBAR(); \
            if (lane == 0) lds_st(&sm.rdy[SL], (U) + 1); \
            CBAR(); }
        QT_LOADS()
        UNIT_LOADS(rank)
        const unsigned b0 = q1, b1 = q2, b2 = lkz, b3 = lks, b4 = lvz, b5 = lvs;
        __builtin_amdgcn_sched_barrier(0);
        UNIT_LOADS(rank + GSZ)
        const unsigned e0 = q1, e1 = q2, e2 = lkz, e3 = lks, e4 = lvz, e5 = lvs;
        __builtin_amdgcn_sched_barrier(0);
        __builtin_amdgcn_s_barrier();
        __builtin_amdgcn_sched_barrier(0);
        CBAR();
        UNIT_LOADS(rank + 2 * GSZ)
        const unsigned f0 = q1, f1 = q2, f2 = lkz, f3 = lks, f4 = lvz, f5 = lvs;
        __builtin_amdgcn_sched_barrier(0);
        q1 = b0; q2 = b1; lkz = b2; lks = b3; lvz = b4; lvs = b5;
        UNIT_FILL(0, 0)
        __builtin_amdgcn_sched_barrier(0);
        q1 = e0; q2 = e1; lkz = e2; lks = e3; lvz = e4; lvs = e5;
        UNIT_FILL(1, 1)
        __builtin_amdgcn_sched_barrier(0);
        q1 = f0; q2 = f1; lkz = f2; lks = f3; lvz = f4; lvs = f5;
        UNIT_FILL(2, 2)
        CBAR();
#pragma unroll 1
        for (unsigned u = 3; u < NU; u++) {
            UNIT_LOADS(rank + GSZ * u)
            while (u >= lds_ld(&sm.merged) + NIN) SLEEP(4);
            CBAR();
            UNIT_FILL(u % NIN, u)
        }
    } else {
        {
            const unsigned short* sa = (split & 2) ? ((split & 1) ? v_scales : v_zeros) : ((split & 1) ? k_scales : k_zeros);
            const unsigned to = ((rank + GSZ * (lane >> 3)) * SKV + (lane & 7) * 256 + (brow0 & ~63)) * 2;
            asm volatile("global_load_dword a7, %0, %2\n\tglobal_load_dword a7, %1, %2" :: "v"(to), "v"(to + 8 * GSZ * SKV * 2), "s"(sa) : "a7", "memory");
            const unsigned tq = (rank + GSZ * (lane >> 2)) * (DH * 2) + min(lane & 3, 2) * 128;
            asm volatile("global_load_dword a7, %0, %1" :: "v"(tq), "s"(q) : "a7", "memory");
        }
        __builtin_amdgcn_s_barrier();
        CBAR();
        while (lds_ld(&sm.tag_rdy) == 0) SLEEP(1);
        CBAR();
        const unsigned T = lds_ld(&sm.tag_lo);
        const unsigned t15 = T & 0x7fffu;
        const __amdgpu_buffer_rsrc_t rsrc = __builtin_amdgcn_make_buffer_rsrc(ws + WS_PART + (rank * NSPLIT + split) * 512, 0, NH * NSPLIT * 512, 0x00020000);
        v2u* recs = (v2u*)&sm.cs[0][0];
#pragma unroll
        for (int i = 0; i < NOUT * 3; i++) (&sm.acc[0][0])[i * 64 + lane] = 0.0;
        unsigned npub = 0;
        bool direct = false;
#define PUBLISH(N) { \
        _Pragma("unroll 1") for (unsigned i = npub; i < (N); i += 4) { \
            v2u r[4]; \
            _Pragma("unroll") for (int k = 0; k < 4; k++) r[k] = recs[(i + k) * 64 + lane]; \
            _Pragma("unroll") for (int k = 0; k < 4; k++) if (i + k < (N)) __builtin_amdgcn_raw_buffer_store_b64(r[k], rsrc, lane * 8, (i + k) * (GSZ * NSPLIT * 512), SC1); } \
        npub = (N); }
#pragma unroll 1
        for (unsigned u = 0; u < 2 * NU; u++) {
            const unsigned e = u % NU, so = u % NOUT;
            const v4f h = *(const v4f*)&sm.ent[e][128];
            for (;;) {
                if (!direct && lds_ld(&sm.grab) >= NPC - PUBP) { direct = true; wave_sync(); PUBLISH(u - NU) }
                if (lds_ld(&sm.cnt[so]) == 32) break;
                if (direct) SLEEP(0) else SLEEP(1)
            }
            CBAR();
            if (!direct && u == 2 * NU - 1) { direct = true; wave_sync(); PUBLISH(NU - 1) }
            if (u < NU) {
                const float2 lg = *(const float2*)&sm.ent[e][2 * lane];
                const v4f zs = *(const v4f*)&sm.vz[u % NIN][2 * lane];
                const float m = wave_max(fmaxf(lg.x, lg.y));
                const float p0 = __expf(lg.x - m), p1 = __expf(lg.y - m);
                const float x0 = p0 * zs.y, x1 = p1 * zs.w;
                const unsigned eb = max(__float_as_uint(wave_max(fmaxf(fabsf(x0), fabsf(x1)))) >> 23, 21u);
                const float f = __uint_as_float((275u - eb) << 23);
                const float w0 = rintf(x0 * f), w1 = rintf(x1 * f);
                const float den = wave_sum(p0 + p1);
                const float bz = wave_sum(fmaf(w0, zs.x, w1 * zs.z));
                const float L = fmaf(m, 1.4426950408889634f, __builtin_amdgcn_logf(den));
                *(v2u*)&sm.ent[e][2 * lane] = (v2u){(unsigned)(int)w0, (unsigned)(int)w1};
                if (lane == 0) *(v4f*)&sm.ent[e][128] = (v4f){ldexpf(1.f / den, (int)eb - 148), bz, L, 0.f};
            } else {
                const double a0 = sm.acc[so][lane], a1 = sm.acc[so][64 + lane], a2 = sm.acc[so][128 + lane];
                const float n0 = (float)(a0 - (double)h.y) * h.x, n1 = (float)(a1 - (double)h.y) * h.x, n2 = (float)(a2 - (double)h.y) * h.x;
                const unsigned e2 = max(__float_as_uint(wave_maxv(fmaxf(fmaxf(fabsf(n0), fabsf(n1)), fabsf(n2)))) >> 23, 14u);
                const float f2 = __uint_as_float((268u - e2) << 23);
                const int m0 = min(max((int)rintf(n0 * f2), -32767), 32767), m1 = min(max((int)rintf(n1 * f2), -32767), 32767), m2 = min(max((int)rintf(n2 * f2), -32767), 32767);
                const unsigned bit = ((lane < 32 ? __float_as_uint(h.z) : e2) >> (lane & 31)) & 1u;
                const v2u r = {((unsigned)m0 & 0xffffu) | ((unsigned)m1 << 16), ((unsigned)m2 & 0xffffu) | (bit << 16) | (t15 << 17)};
                if (direct) __builtin_amdgcn_raw_buffer_store_b64(r, rsrc, lane * 8, e * (GSZ * NSPLIT * 512), SC1);
                else recs[e * 64 + lane] = r;
                CBAR();
                sm.acc[so][lane] = 0.0; sm.acc[so][64 + lane] = 0.0; sm.acc[so][128 + lane] = 0.0;
            }
            CBAR();
            if (lane == 0) { lds_st(&sm.cnt[so], 0u); CBAR(); lds_st(&sm.merged, u + 1); }
        }
        STAMP(2)
        STAMP_DUMP()
        return;
    }

    STAMP(2)
    STAMP_DUMP()
}

extern "C" void kernel_launch(void* const* d_in, const int* in_sizes, int n_in,
                              void* d_out, int out_size, void* d_ws, size_t ws_size,
                              hipStream_t stream) {
    const unsigned short* q = (const unsigned short*)d_in[0];
    const int* qk = (const int*)d_in[1];
    const unsigned short* k_scales = (const unsigned short*)d_in[2];
    const unsigned short* k_zeros = (const unsigned short*)d_in[3];
    const int* qv = (const int*)d_in[4];
    const unsigned short* v_scales = (const unsigned short*)d_in[5];
    const unsigned short* v_zeros = (const unsigned short*)d_in[6];
    const unsigned short* q_cos = (const unsigned short*)d_in[7];
    const unsigned short* q_sin = (const unsigned short*)d_in[8];
    const unsigned short* k_cos = (const unsigned short*)d_in[9];
    const unsigned short* k_sin = (const unsigned short*)d_in[10];
    unsigned char* ws = (unsigned char*)d_ws;

    attn_kernel<<<NSPLIT * GSZ, NTHREADS, 0, stream>>>(q, qk, k_scales, k_zeros, qv, v_scales, v_zeros,
                                                       q_cos, q_sin, k_cos, k_sin, (__hip_bfloat16*)d_out, ws);
}
```

```cpp
#define PFL 240
#include <hip/hip_runtime.h>
#include <hip/hip_bf16.h>
#include <stdint.h>

#define DH 192
#define HALF 96
#define SKV 2048
#define NH 256
#define NSW 8
#define NWAVES (NSW + 4)
#define NTHREADS (64 * NWAVES)
#define BROWS 128
#define NSPLIT (SKV / BROWS)
#define GSZ 16
#define NU (NH / GSZ)
#define NPC (2 * NU * 32)
#define N1 (NH - GSZ)
#define NIN 4
#define NOUT 4
#define ENTW 132
#define SC1 16
#define LIKELY 1
#define SLD(P) (*(P))
#define LSTRIDE 4352
#define PUBP 2
#define PHALF 9
#define PHI 0x0235712457024671ull
#define KAP 0x0202020220202020ull
#define PHIV PHI
#define KAPV KAP
#define XM 0
#define PRIO 1
#define PRIO0 "s_setprio 0\n"
#define PRIO1 "\n\ts_setprio 2"
#define GTH 10
#define GS1 2

#define WS_EPOCH 0
#define WS_DEC 8192
#define WS_REF 16384
#define WS_LOG 49152
#define WS_PART 1048576
#define WS_FIN (WS_PART + NH * NSPLIT * 512)

typedef unsigned int v2u __attribute__((ext_vector_type(2)));
typedef unsigned int v3u __attribute__((ext_vector_type(3)));
typedef unsigned int v4u __attribute__((ext_vector_type(4)));
typedef int v4i __attribute__((ext_vector_type(4)));
typedef float v2f __attribute__((ext_vector_type(2)));
typedef float v4f __attribute__((ext_vector_type(4)));
typedef unsigned long long u64;

__device__ __forceinline__ float bflo(unsigned u) { return __uint_as_float(u << 16); }
__device__ __forceinline__ float bfhi(unsigned u) { return __uint_as_float(u & 0xffff0000u); }
__device__ __forceinline__ float bfr(float x) {
    __bf16 b = (__bf16)x;
    return __uint_as_float(((unsigned)__builtin_bit_cast(unsigned short, b)) << 16);
}

#define SLEEP(n) SLEEP_(n)
#define SLEEP_(n) asm volatile("s_sleep " #n "\n\tv_add_co_u32 %0, vcc, 1, %0\n\ts_cbranch_vccz 1f\n\ts_endpgm\n1:" : "+v"(spins) :: "vcc", "memory");
#define SPIN0 0xf0000000u
#define CBAR() asm volatile("" ::: "memory")

__device__ __forceinline__ void wave_sync() {
    __builtin_amdgcn_fence(__ATOMIC_RELEASE, "wavefront");
    __builtin_amdgcn_wave_barrier();
    __builtin_amdgcn_fence(__ATOMIC_ACQUIRE, "wavefront");
}
template <int CTRL>
__device__ __forceinline__ float dpp_add(float x) {
    int y = __builtin_amdgcn_update_dpp(0, __float_as_int(x), CTRL, 0xf, 0xf, true);
    return x + __int_as_float(y);
}
template <int CTRL>
__device__ __forceinline__ float dpp_max(float x) {
    int y = __builtin_amdgcn_update_dpp(__float_as_int(x), __float_as_int(x), CTRL, 0xf, 0xf, false);
    return fmaxf(x, __int_as_float(y));
}
__device__ __forceinline__ float row_sum(float x) { x = dpp_add<0x128>(x); x = dpp_add<0x124>(x); x = dpp_add<0x122>(x); return dpp_add<0x121>(x); }
__device__ __forceinline__ float row_max(float m) { m = dpp_max<0x128>(m); m = dpp_max<0x124>(m); m = dpp_max<0x122>(m); return dpp_max<0x121>(m); }
__device__ __forceinline__ float rdlane(float x, int l) { return __int_as_float(__builtin_amdgcn_readlane(__float_as_int(x), l)); }
__device__ __forceinline__ float wave_max(float m) { m = row_max(m); return fmaxf(fmaxf(rdlane(m, 0), rdlane(m, 16)), fmaxf(rdlane(m, 32), rdlane(m, 48))); }
__device__ __forceinline__ float wave_maxv(float m) {
    m = row_max(m);
    const auto a = __builtin_amdgcn_permlane16_swap(__float_as_uint(m), __float_as_uint(m), false, false);
    m = fmaxf(__uint_as_float(a[0]), __uint_as_float(a[1]));
    const auto b = __builtin_amdgcn_permlane32_swap(__float_as_uint(m), __float_as_uint(m), false, false);
    return fmaxf(__uint_as_float(b[0]), __uint_as_float(b[1]));
}
__device__ __forceinline__ float wave_sum(float x) { x = row_sum(x); return (rdlane(x, 0) + rdlane(x, 16)) + (rdlane(x, 32) + rdlane(x, 48)); }
__device__ __forceinline__ unsigned lds_ld(const unsigned* p) { return __builtin_amdgcn_readfirstlane(__hip_atomic_load(p, __ATOMIC_RELAXED, __HIP_MEMORY_SCOPE_WORKGROUP)); }
__device__ __forceinline__ unsigned lds_addr(const void* p) { return (unsigned)(size_t)(const __attribute__((address_space(3))) void*)p; }
#define DMA16(SRC, LDS) asm volatile("s_mov_b32 %0, m0\n\ts_mov_b32 m0, %2\n\ts_nop 0\n\tglobal_load_lds_dwordx4 %1, off\n\ts_mov_b32 m0, %0" : "=&s"(m0keep) : "v"(SRC), "s"(LDS) : "memory")
__device__ __forceinline__ void lds_st(unsigned* p, unsigned v) { __hip_atomic_store(p, v, __ATOMIC_RELAXED, __HIP_MEMORY_SCOPE_WORKGROUP); }
#define NOW() ((unsigned)__builtin_amdgcn_s_memrealtime())

struct __attribute__((aligned(16))) Smem {
    unsigned cs[32][768];
    double acc[NOUT][DH];
    float ent[NU][ENTW];
    unsigned q[NIN][HALF];
    float2 kz[NIN][BROWS];
    float2 vz[NIN][BROWS];
    unsigned rdy[NIN], cnt[NOUT];
    v2u tu[2 * NU + 1];
    unsigned grab, merged, tabs;
    unsigned tag_rdy, tag_lo, tag_hi, cdone;
    int stat[32];
};
#define STAMP(i) {}
#define STAMPV(i, v) {}
#define STAMP_DUMP()
#define STAMP_UNIT(N)

#define TU(I) (*(const volatile v2u __attribute__((address_space(3)))*)(size_t)lds_addr(&sm.tu[I]))
#define LDS_ADD(P, V) { u64 ex_; asm volatile("s_mov_b64 %0, exec\n\ts_mov_b64 exec, 1\n\tds_add_u32 %1, %2\n\ts_mov_b64 exec, %0" : "=&s"(ex_) : "v"(lds_addr(P)), "v"(V) : "memory"); }
#define LDS_OR(P, V) { u64 ex_; asm volatile("s_mov_b64 %0, exec\n\ts_mov_b64 exec, 1\n\tds_or_b32 %1, %2\n\ts_mov_b64 exec, %0" : "=&s"(ex_) : "v"(lds_addr(P)), "v"(V) : "memory"); }

__global__ __launch_bounds__(NTHREADS, 3) void attn_kernel(
    const unsigned short* __restrict__ q, const int* __restrict__ qk,
    const unsigned short* __restrict__ k_scales, const unsigned short* __restrict__ k_zeros,
    const int* __restrict__ qv,
    const unsigned short* __restrict__ v_scales, const unsigned short* __restrict__ v_zeros,
    const unsigned short* __restrict__ q_cos, const unsigned short* __restrict__ q_sin,
    const unsigned short* __restrict__ k_cos, const unsigned short* __restrict__ k_sin,
    __hip_bfloat16* __restrict__ out, unsigned char* __restrict__ ws)
{
    __shared__ Smem sm;
    const int tid = threadIdx.x;
    unsigned spins = SPIN0;
    if ((tid >> 6) == 1) {
        const char* pc0; asm volatile("s_getpc_b64 %0" : "=s"(pc0));
        unsigned ln = (blockIdx.x >> 3) * PFL / 32 + (tid & 63);
        ln = ln >= PFL ? ln - PFL : ln;
        asm volatile("global_load_dword a6, %0, %1" :: "v"(ln * 128u), "s"(pc0) : "a6", "memory");
    }
    asm volatile("" :: "s"(q), "s"(qk), "s"(k_scales), "s"(k_zeros), "s"(qv), "s"(v_scales), "s"(v_zeros), "s"(q_cos), "s"(q_sin), "s"(k_cos), "s"(k_sin), "s"(out), "s"(ws));
    const int hw = __builtin_amdgcn_readfirstlane(tid >> 6);
    const int wave = hw >= 4 ? hw - 4 : hw == 0 ? NSW + 2 : hw == 1 ? NSW : hw == 2 ? NSW + 1 : NSW + 3;
    const int lane = tid & 63;
    STAMP(0)
    const unsigned ts = NOW();
    const int PK = (1266), PV = (1262), TOF = (110), P0 = (1270);
    const int split = (blockIdx.x & 3) * 4 + ((blockIdx.x >> 3) & 3);
    const int rank = (blockIdx.x >> 5) * 2 + ((blockIdx.x >> 2) & 1);
    const int brow0 = split * 16;
#define ROWOF(RG) (((RG) >> 2) * 256 + ((RG) & 3) * 4)

    if (LIKELY ? __builtin_expect(wave < NSW, 1) : wave < NSW) {
        const unsigned rot = 4 * ((unsigned)(PHI >> (60 - 4 * rank)) & 7u), kap = (unsigned)(KAP >> (60 - 4 * rank)) & 3u;
        const unsigned rotv = 4 * ((unsigned)(PHIV >> (60 - 4 * rank)) & 7u), kapv = (unsigned)(KAPV >> (60 - 4 * rank)) & 3u;
#define RGOF(G) ((G) >= NPC / 2 ? (((G) + rotv) & 31u) ^ kapv : (((G) + rot) & 31u) ^ kap)
        const char* cbase[3];
#pragma unroll
        for (int j = 0; j < 3; j++) { const int x = j * 64 + lane; cbase[j] = (x < 96 ? (const char*)k_cos + x * 16 : (const char*)k_sin + (x - 96) * 16) + brow0 * (DH * 2); }
        unsigned m0keep;
#define CS_DMA(RG) { _Pragma("unroll") for (int j = 0; j < 3; j++) DMA16(cbase[j] + ROWOF(RG) * (DH * 2), lds_addr(&sm.cs[0][0]) + (RG) * 3072 + j * 1024); }
#define REPACK(RG) { \
        unsigned* cw = &sm.cs[RG][0]; \
        const unsigned* l = cw + rsel * (DH / 2) + qoff; \
        unsigned r[2][6]; \
        _Pragma("unroll") \
        for (int j = 0; j < 2; j++) { \
            unsigned e[4][2]; \
            _Pragma("unroll") \
            for (int k = 0; k < 4; k++) { \
                const unsigned* a = l + j * DH + (k & 1) * 48 + (k >> 1) * 384;       \
                unsigned x0 = a[0], x1 = a[1]; \
                e[k][0] = __builtin_amdgcn_alignbit(x1, x0, sh); \
                e[k][1] = x1 >> sh; \
            } \
            r[j][0] = (e[0][0] & 0xffffu) | (e[3][0] << 16); \
            r[j][1] = (e[0][0] >> 16) | (e[3][0] & 0xffff0000u); \
            r[j][2] = (e[0][1] & 0xffffu) | (e[3][1] << 16); \
            r[j][3] = (e[1][0] & 0xffffu) | (e[2][0] << 16); \
            r[j][4] = (e[1][0] >> 16) | (e[2][0] & 0xffff0000u); \
            r[j][5] = (e[1][1] & 0xffffu) | (e[2][1] << 16); \
        } \
        wave_sync(); \
        _Pragma("unroll") \
        for (int j = 0; j < 2; j++) { *(v4u*)&cw[j * 256 + lane * 4] = (v4u){r[j][0], r[j][1], r[j][2], r[j][3]}; *(v2u*)&cw[512 + j * 128 + lane * 2] = (v2u){r[j][4], r[j][5]}; } \
        wave_sync(); \
        LDS_OR(&sm.tabs, 1u << (RG)) }
        const int sub = lane & 31;
        const int rsel = lane >> 5;
        const unsigned sh = (sub & 1) * 16;
        const int qoff = 3 * (sub >> 1) + (sub & 1);
        const char* kbase = (const char*)qk + (size_t)(rank * SKV + brow0) * (DH * 4);
        const char* vbase = (const char*)qv + (size_t)(rank * SKV + brow0) * (DH * 4);
        const unsigned koff = (rsel * DH + 3 * sub) * 4, voff = 3 * lane * 4;
#define AR(S) (16 + (S) * 8)
        asm volatile("" ::: "a16", "a17", "a18", "a19", "a20", "a21", "a22", "a23", "a24", "a25", "a26", "a27", "a28", "a29", "a30", "a31",
                            "a32", "a33", "a34", "a35", "a36", "a37", "a38", "a39", "a40", "a41", "a42", "a43", "a44", "a45", "a46", "a47");
        asm volatile("" ::: "a48", "a49", "a50", "a51", "a52", "a53", "a54", "a55", "a56", "a57", "a58", "a59", "a60", "a61", "a62", "a63");
        __builtin_amdgcn_s_waitcnt(0x0f70);
        int ta[3], tb[3];
#define GLD(A, OFF, BASE, IMM) asm volatile("global_load_dwordx3 a[%c0:%c1], %2, %3 offset:%4 nt" :: "i"(A), "i"((A) + 2), "v"(OFF), "s"(BASE), "i"(IMM))
#define ISSUE(S, J, BASE, OA, OB) { GLD(AR(S), OA, BASE, (J) * 1536); GLD(AR(S) + 4, OB, BASE, (J) * 1536); }
#define WAITT(S, N) asm volatile("s_waitcnt vmcnt(%6)\n\tv_accvgpr_read_b32 %0, a[%c7]\n\tv_accvgpr_read_b32 %1, a[%c8]\n\tv_accvgpr_read_b32 %2, a[%c9]\n\t" \
                              "v_accvgpr_read_b32 %3, a[%c10]\n\tv_accvgpr_read_b32 %4, a[%c11]\n\tv_accvgpr_read_b32 %5, a[%c12]" \
                              : "=v"(ta[0]), "=v"(ta[1]), "=v"(ta[2]), "=v"(tb[0]), "=v"(tb[1]), "=v"(tb[2]) \
                              : "i"(N), "i"(AR(S)), "i"(AR(S) + 1), "i"(AR(S) + 2), "i"(AR(S) + 4), "i"(AR(S) + 5), "i"(AR(S) + 6))
        const unsigned koffb = koff + 384, voffb = voff + 768;
        unsigned u = 0, rg, np = 0;
#define RG0(C) RGOF((unsigned)(wave + 8 * (C)))
        CS_DMA(RG0(0))
        STAMP(52)
        __builtin_amdgcn_s_barrier();
        CBAR();
        STAMP(53)
        const float scale = 0.07216878364870322f;
        double acc[3] = {0.0, 0.0, 0.0};
        bool first = true, chg, none = false;
        unsigned Qp[3], Qn[3];
        float kz4[2], ksc, kres[2];
        v4u ca[2]; v2u cb[2];
        v2f qe[2][3], A[2];
        const char* nb; unsigned oa, ob;
        unsigned g, mg, nrg, tg, late = 0;
        u64 tis;
        unsigned dg, par = 0;
        const unsigned bth = wave < (8) ? 0u - (15) * 128u : 0x80000000u;
        const unsigned grab_adr = lds_addr(&sm.grab);
        asm volatile("v_accvgpr_write_b32 a13, 1\n\tv_accvgpr_write_b32 a14, 2" ::: "a13", "a14", "a15");
#define GRAB(N) { u64 ex_; asm volatile("s_mov_b64 %0, exec\n\ts_mov_b64 exec, 1\n\tds_add_rtn_u32 a15, %1, a%c2\n\ts_mov_b64 exec, %0" : "=&s"(ex_) : "v"(grab_adr), "i"(12 + (N)) : "memory"); }
#define GRABBED() { unsigned t_; asm volatile("s_waitcnt lgkmcnt(0)\n\tv_accvgpr_read_b32 %1, a15\n\ts_nop 0\n\tv_readfirstlane_b32 %0, %1\n\ts_nop 1" : "=s"(g), "=v"(t_)); }
#define NEXTPIECE() { \
            GRABBED() chg = (g >> 5) != u; none = g >= NPC; \
            const bool nv = g >= NPC / 2; \
            nrg = RGOF(g); \
            unsigned boff; asm("s_mul_i32 %0, %1, 0x300" : "=s"(boff) : "s"(((g >> 5) & (NU - 1)) * (GSZ * SKV) + ROWOF(nrg) + 2)); \
            nb = (nv ? vbase : kbase) + boff; \
            oa = nv ? voff : koff; ob = nv ? voffb : koffb; \
            const v2u x_ = TU(g >> 5); \
            tg = __builtin_amdgcn_readfirstlane(x_.x) + (g & 31) * __builtin_amdgcn_readfirstlane(x_.y); }
#define GATE(...) \
            asm volatile(PRIO0 "1:\n\ts_memrealtime vcc\n\ts_waitcnt lgkmcnt(0)\n\ts_lshl_b32 vcc_lo, vcc_lo, 7\n\ts_sub_u32 %0, %1, vcc_lo\n\ts_cmp_lt_u32 %0, 0x40000\n\ts_cbranch_scc0 2f\n\t" \
                         "s_cmp_lt_u32 %0, %2\n\ts_cbranch_scc1 1b\n\ts_sleep %3\n\ts_branch 1b\n2:" PRIO1 : "=&s"(dg) : "s"(tg), "i"(GTH * 128), "i"(GS1), __VA_ARGS__ : "scc", "vcc", "memory");
#define RD6(A) "v_accvgpr_read_b32 %0, a[%c6+" #A "]\n\tv_accvgpr_read_b32 %1, a[%c6+" #A "+1]\n\tv_accvgpr_read_b32 %2, a[%c6+" #A "+2]\n\t" \
               "v_accvgpr_read_b32 %3, a[%c6+" #A "+4]\n\tv_accvgpr_read_b32 %4, a[%c6+" #A "+5]\n\tv_accvgpr_read_b32 %5, a[%c6+" #A "+6]\n"
#define LD2(A) "global_load_dwordx3 a[%c6+" #A ":%c6+" #A "+2], %7, %9 offset:%10 nt\n\tglobal_load_dwordx3 a[%c6+" #A "+4:%c6+" #A "+6], %8, %9 offset:%10 nt"
#define OUT6 "=&v"(ta[0]), "=&v"(ta[1]), "=&v"(ta[2]), "=&v"(tb[0]), "=&v"(tb[1]), "=&v"(tb[2])
#define WAITISSUE(S, TAIL, ...) asm volatile("s_cmp_lt_u32 %13, %14\n\ts_cbranch_scc1 3f\n\ts_waitcnt vmcnt(2)\n3:\n\ts_cmp_lg_u32 %12, 0\n\ts_cbranch_scc1 1f\n\t" \
                              LD2(16) TAIL "\n\ts_waitcnt vmcnt(4)\n\t" RD6(0) "\ts_branch 2f\n1:\n\t" LD2(0) TAIL "\n\ts_waitcnt vmcnt(4)\n\t" RD6(16) "2:" \
                              : OUT6 : "i"(AR(S)), "v"(oa), "v"(ob), "s"(nb), "i"(((S) - 1) * 1536), "v"(grab_adr), "s"(pr), "s"(dg), "s"(bth), __VA_ARGS__ : "scc", "vcc", "memory")
#define LASTT(S) asm volatile("s_waitcnt vmcnt(%c8)\n\ts_cmp_lg_u32 %7, 0\n\ts_cbranch_scc1 1f\n\t" RD6(0) "\ts_branch 2f\n1:\n\t" RD6(16) "2:" : OUT6 : "i"(AR(S)), "s"(pr), "i"(2 - 2 * (S)) : "scc")
#define DRAW "\n\ts_mov_b64 vcc, exec\n\ts_mov_b64 exec, 1\n\tds_add_rtn_u32 a15, %11, a13\n\ts_mov_b64 exec, vcc"
#define LATE0()
#define LATE1()
#define LATEF()
#define K_TAB() { _Pragma("unroll") for (int p = 0; p < 2; p++) { \
            const unsigned* cw = &sm.cs[rg][0]; \
            ca[p] = *(const v4u*)&cw[p * 256 + lane * 4]; cb[p] = *(const v2u*)&cw[512 + p * 128 + lane * 2]; } }
#define K_PRE(P) { \
        float e0, e1, e2, f0, f1, f2; \
        asm("v_dot2_f32_bf16 %0, %6, %12, 0\n\tv_dot2_f32_bf16 %3, %9, %15, 0\n\tv_dot2_f32_bf16 %1, %7, %13, 0\n\tv_dot2_f32_bf16 %4, %10, %16, 0\n\t" \
            "v_dot2_f32_bf16 %2, %8, %14, 0\n\tv_dot2_f32_bf16 %5, %11, %17, 0\n\ts_nop 2" \
            : "=&v"(e0), "=&v"(e1), "=&v"(e2), "=&v"(f0), "=&v"(f1), "=&v"(f2) \
            : "v"(Qp[0]), "v"(Qp[1]), "v"(Qp[2]), "v"(Qn[0]), "v"(Qn[1]), "v"(Qn[2]), \
              "v"(ca[P].x), "v"(ca[P].y), "v"(ca[P].z), "v"(ca[P].w), "v"(cb[P].x), "v"(cb[P].y)); \
        qe[P][0] = (v2f){e0, f0}; qe[P][1] = (v2f){e1, f1}; qe[P][2] = (v2f){e2, f2}; \
        A[P] = (qe[P][0] + qe[P][1] + qe[P][2]) * (v2f){-kz4[P], -kz4[P]}; }
#define K_SUM(P) { \
        _Pragma("unroll") \
        for (int i = 0; i < 3; i++) { v2f x = {(float)ta[i], (float)tb[i]}; A[P] = qe[P][i] * x + A[P]; } \
        kres[P] = A[P].x + A[P].y; }
#define K_RED2() { \
        const bool o1 = lane & 1; \
        float sx = (o1 ? kres[1] : kres[0]) + __int_as_float(__builtin_amdgcn_update_dpp(0, __float_as_int(o1 ? kres[0] : kres[1]), 0xb1, 0xf, 0xf, true)); \
        sx = dpp_add<0x4e>(sx); sx = dpp_add<0x124>(sx); sx = dpp_add<0x128>(sx); \
        const auto sw = __builtin_amdgcn_permlane16_swap(__float_as_uint(sx), __float_as_uint(sx), false, false); \
        sx = __uint_as_float(sw[0]) + __uint_as_float(sw[1]); \
        if ((lane & 30) == 0) sm.ent[u][rg * 4 + 2 * (lane & 1) + rsel] = sx * ksc; }
#define LDV(P) __hip_atomic_load(P, __ATOMIC_RELAXED, __HIP_MEMORY_SCOPE_WORKGROUP)
#define RDYWAIT(SL, SEQ) { while (lds_ld(&sm.rdy[SL]) != (SEQ)) SLEEP(1); CBAR(); }
#define LOADZ() { _Pragma("unroll") for (int p = 0; p < 2; p++) kz4[p] = sm.kz[u % NIN][rg * 4 + 2 * p + rsel].x; ksc = sm.kz[u % NIN][rg * 4 + 2 * (lane & 1) + rsel].y * scale; }
#define LOADQ() { _Pragma("unroll") for (int i = 0; i < 3; i++) { Qp[i] = sm.q[u % NIN][3 * sub + i]; Qn[i] = __builtin_amdgcn_alignbit(Qp[i], Qp[i], 16) ^ 0x80000000u; } }
#define FLUSH(ISV) { \
            mg = __builtin_amdgcn_readfirstlane(mg); \
            while (u >= mg + NOUT) { SLEEP(1); mg = lds_ld(&sm.merged); } \
            CBAR(); \
            if (ISV) { const unsigned aa = lds_addr(&sm.acc[u % NOUT][lane]); \
                asm volatile("ds_add_f64 %0, %1\n\tds_add_f64 %0, %2 offset:512\n\tds_add_f64 %0, %3 offset:1024" :: "v"(aa), "v"(acc[0]), "v"(acc[1]), "v"(acc[2]) : "memory"); \
                acc[0] = 0.0; acc[1] = 0.0; acc[2] = 0.0; } \
            CBAR(); \
            LATEF() \
            LDS_ADD(&sm.cnt[u % NOUT], np) \
            CBAR(); \
            STAMP_UNIT(np) \
            np = 0; }
#define ADVANCE() { first = chg; u = g >> 5; rg = nrg; }

#define REQ0(N, S) { tg = (ts + TOF) * 128 + (wave + 8 * (N)) * P0; \
            const char* kb = kbase + (((N) >> 2) * (GSZ * SKV) + ROWOF(RG0((N) & 3))) * (DH * 4); GATE("s"(kb)) ISSUE(S, 0, kb, koff, koffb) ISSUE((S) + 1, 1, kb, koff, koffb) STAMP(44 + (N)) }
#define USE0(N, S, W) { u = (N) >> 2; rg = RG0((N) & 3); K_TAB() LOADQ() LOADZ() K_PRE(0) K_PRE(1) WAITT(S, W); K_SUM(0) WAITT((S) + 1, W); K_SUM(1) K_RED2() }
#define VMW(N) asm volatile("s_waitcnt vmcnt(" #N ")" ::: "memory");
        REQ0(0, 0) CS_DMA(RG0(1))
        VMW(7) REPACK(RG0(0))
        STAMP(1)
        REQ0(1, 2)
        RDYWAIT(0, 1u)
        USE0(0, 0, 7)
        VMW(4) REPACK(RG0(1))
        REQ0(2, 0) CS_DMA(RG0(2))
        USE0(1, 2, 7)
        REQ0(3, 2)
        VMW(4) REPACK(RG0(2))
        USE0(2, 0, 4)
        REQ0(4, 0) CS_DMA(RG0(3))
        REQ0(5, 4)
        VMW(4) REPACK(RG0(3))
        USE0(3, 2, 4)
        CBAR();
        LDS_ADD(&sm.cnt[0], 4u)
        REQ0(6, 2)
        RDYWAIT(1, 2u)
        USE0(4, 0, 8)
        USE0(5, 4, 4)
        REQ0(7, 0)
        GRAB(1)
        USE0(6, 2, 4)
        rg = RG0(3); np = 3; first = false;
        bool tabok = false;
#pragma unroll 1
        while (u < NU) {
            mg = LDV(&sm.merged);
            if (!tabok) {
                for (;;) { const unsigned tb_ = lds_ld(&sm.tabs); tabok = tb_ == ~0u; if ((tb_ >> rg) & 1u) break; SLEEP(1) }
                CBAR();
            }
            K_TAB()
            if (first) {
                unsigned fl = LDV(&sm.rdy[u % NIN]);
                CBAR();
                LOADQ() LOADZ()
                if (__builtin_amdgcn_readfirstlane(fl) != u + 1) { RDYWAIT(u % NIN, u + 1) LOADQ() LOADZ() }
            } else LOADZ()
            NEXTPIECE()
            K_PRE(0) K_PRE(1)
            const unsigned pr = __builtin_amdgcn_readfirstlane(par); par ^= 1;
            GATE("v"(A[0]), "v"(A[1]), "v"(oa), "v"(ob), "s"(nb), "s"(pr))
            WAITISSUE(0, DRAW, "v"(A[0]));
            LATE0()
            K_SUM(0)
            WAITISSUE(1, "", "v"(A[1]));
            K_SUM(1)
            LATE1()
            K_RED2()
            np++;
            if (chg) FLUSH(false)
            ADVANCE()
        }
#pragma unroll 1
        while (u < 2 * NU) {
            mg = LDV(&sm.merged);
            CBAR();
            v4i pw = *(const v4i*)&sm.ent[u - NU][rg * 4];
            NEXTPIECE()
            if (first && __builtin_amdgcn_readfirstlane(mg) + NU <= u) { while (lds_ld(&sm.merged) + NU <= u) SLEEP(1); CBAR(); pw = *(const v4i*)&sm.ent[u - NU][rg * 4]; }
            const double w0 = pw.x, w1 = pw.y, w2 = pw.z, w3 = pw.w;
            const unsigned pr = __builtin_amdgcn_readfirstlane(par); par ^= 1;
            if (none) { LASTT(0); } else { GATE("s"(pr), "v"(w0), "v"(w1), "v"(w2), "v"(w3), "v"(oa), "v"(ob), "s"(nb)) WAITISSUE(0, DRAW, "v"(w0)); }
            LATE0()
#pragma unroll
            for (int i = 0; i < 3; i++) acc[i] = fma(w1, (double)tb[i], fma(w0, (double)ta[i], acc[i]));
            if (none) { LASTT(1); } else WAITISSUE(1, "", "v"(w2));
#pragma unroll
            for (int i = 0; i < 3; i++) acc[i] = fma(w3, (double)tb[i], fma(w2, (double)ta[i], acc[i]));
            LATE1()
            np++;
            if (chg) FLUSH(true)
            ADVANCE()
        }
        STAMP(2)
        {
            const int head = blockIdx.x;
            const __amdgpu_buffer_rsrc_t rsrc = __builtin_amdgcn_make_buffer_rsrc(ws + WS_PART + (size_t)head * (NSPLIT * 512), 0, NSPLIT * 512, 0x00020000);
            const unsigned t15 = lds_ld(&sm.tag_lo) & 0x7fffu;
            unsigned missing = 3u;
            float L[2], f[2][3];
#pragma unroll
            for (int k = 0; k < 2; k++) { L[k] = -3e38f; f[k][0] = 0.f; f[k][1] = 0.f; f[k][2] = 0.f; }
#define POLL(R) { _Pragma("unroll") for (int k = 0; k < 2; k++) R[k] = __builtin_amdgcn_raw_buffer_load_b64(rsrc, lane * 8 + (((missing >> k) & 1) ? (wave + 8 * k) * 512 : 0x100000), 0, SC1); }
#define TAKE(R) { _Pragma("unroll") for (int k = 0; k < 2; k++) { \
                    if (((missing >> k) & 1) && __builtin_amdgcn_ballot_w64((R[k].y >> 17) == t15) == ~0ull) { \
                        missing &= ~(1u << k); \
                        const u64 bits = __builtin_amdgcn_ballot_w64((R[k].y & 0x10000u) != 0); \
                        L[k] = __uint_as_float((unsigned)bits); \
                        const int ex = (int)((unsigned)(bits >> 32) & 255u) - 141; \
                        f[k][0] = ldexpf((float)(short)(R[k].x & 0xffffu), ex); f[k][1] = ldexpf((float)((int)R[k].x >> 16), ex); f[k][2] = ldexpf((float)(short)(R[k].y & 0xffffu), ex); \
                    } } }
            v2u ra[2], rb[2];
            POLL(ra)
            TAKE(ra)
            if (missing) {
                POLL(ra)
                SLEEP(PHALF)
#pragma unroll 1
                for (;;) {
                    POLL(rb)
                    TAKE(ra)
                    if (!missing) break;
                    SLEEP(0)
                    POLL(ra)
                    TAKE(rb)
                    if (!missing) break;
                    SLEEP(0)
                }
            }
            const float M = fmaxf(L[0], L[1]);
            const float w0 = __builtin_amdgcn_exp2f(L[0] - M), w1 = __builtin_amdgcn_exp2f(L[1] - M);
            v4f* pb = (v4f*)((char*)&sm.cs[0][0] + 65536);
            float* pm = (float*)(pb + NSW * 64);
            pb[wave * 64 + lane] = (v4f){fmaf(w1, f[1][0], w0 * f[0][0]), fmaf(w1, f[1][1], w0 * f[0][1]), fmaf(w1, f[1][2], w0 * f[0][2]), w0 + w1};
            if (lane == 0) pm[wave] = M;
            CBAR();
            { u64 ex_; asm volatile("s_mov_b64 %0, exec\n\ts_mov_b64 exec, 1\n\tds_add_rtn_u32 a15, %1, a13\n\ts_mov_b64 exec, %0" : "=&s"(ex_) : "v"(lds_addr(&sm.cdone)) : "memory"); }
            const v4f ma = *(const v4f*)pm, mb = *(const v4f*)(pm + 4);
            v4f ps[NSW];
#pragma unroll
            for (int w = 0; w < NSW; w++) ps[w] = pb[w * 64 + lane];
            GRABBED()
            if (g == NSW - 1) {
                const float mw[NSW] = {ma.x, ma.y, ma.z, ma.w, mb.x, mb.y, mb.z, mb.w};
                float Mx = mw[0];
#pragma unroll
                for (int w = 1; w < NSW; w++) Mx = fmaxf(Mx, mw[w]);
                v4f sum = {0.f, 0.f, 0.f, 0.f};
#pragma unroll
                for (int w = 0; w < NSW; w++) sum += ps[w] * __builtin_amdgcn_exp2f(mw[w] - Mx);
                const float inv = 1.f / sum.w;
                __hip_bfloat16* o = out + (size_t)head * DH + 3 * lane;
                o[0] = __float2bfloat16(sum.x * inv); o[1] = __float2bfloat16(sum.y * inv); o[2] = __float2bfloat16(sum.z * inv);
                if (lane == 0) __hip_atomic_store((unsigned*)(ws + (64u << 20)) + blockIdx.x, NOW(), __ATOMIC_RELAXED, __HIP_MEMORY_SCOPE_AGENT);
            }
        }
        STAMP(62)
        STAMP_DUMP()
        return;
    }

    if (wave == NSW) {
        if (lane < NIN) sm.rdy[lane] = 0;
        if (lane < NOUT) sm.cnt[lane] = 0;
        if (lane == 0) { sm.tabs = 0; sm.grab = 64; sm.tag_rdy = 0; sm.merged = 0; sm.cdone = 0; }
        if (lane < 32) sm.stat[lane] = 0;
        asm volatile("s_waitcnt lgkmcnt(0)" ::: "memory");
        __builtin_amdgcn_s_barrier();
        CBAR();
        STAMPV(24, (unsigned)__builtin_amdgcn_s_memtime()) STAMP(25)
    } else if (wave == NSW + 1) {
        const u64 E = __hip_atomic_load((const u64*)(ws + WS_EPOCH), __ATOMIC_RELAXED, __HIP_MEMORY_SCOPE_AGENT);
        __builtin_amdgcn_s_barrier();
        CBAR();
        const int NB = (4), CAP = (45);
        unsigned lU = 2, lT = (ts + TOF) * 128 + 64 * P0; int lper = PK, dl = 0;
#define OWN(U) (dl * max(NB + 2 - (int)(U), 0) * 32)
#define NEWLINE() { if (lane >= lU && lane <= 2 * NU) sm.tu[lane] = (v2u){lT + (lane - lU) * 32 * lper + (max(lane - NU, 0) - max((int)lU - NU, 0)) * 32 * (PV - PK) + OWN(lane), \
                                                                          (unsigned)(lper + (lane >= NU ? PV - PK : 0) - (lane < NB + 2 ? dl : 0))}; }
        NEWLINE()
        if (lane == 0) sm.tu[1] = (v2u){lT - 32 * P0, (unsigned)P0};
        const u64 E1 = E + 1;
        const unsigned rtag = (unsigned)E1 * 0x9e3779b1u;
        const bool boss = blockIdx.x == 0;
        u64* const slot = (u64*)(ws + WS_REF + (blockIdx.x & 63) * LSTRIDE + (blockIdx.x >> 6) * 16);
        const u64* const slots = (const u64*)(ws + WS_REF + lane * LSTRIDE);
        u64* const dec = (u64*)(ws + WS_DEC);
#define GLDS(P) __hip_atomic_load(P, __ATOMIC_RELAXED, __HIP_MEMORY_SCOPE_AGENT)
#define GSTS(P, V) __hip_atomic_store(P, V, __ATOMIC_RELAXED, __HIP_MEMORY_SCOPE_AGENT)
#define UNTIL(T) while ((unsigned)((T) - NOW() * 128) < 0x40000u) SLEEP(2)
        if (lane < 2) GSTS(slot + lane, lane ? E1 : (u64)ts | ((u64)rtag << 32));
        if (lane == 0) { sm.tag_lo = (unsigned)E1; sm.tag_hi = (unsigned)(E1 >> 32); CBAR(); lds_st(&sm.tag_rdy, 1u); }
        const int K0 = (3), GAIN = (24), BK = (13), BV = (3), XLEAD = (170), XD1 = (100), XD2 = (15), XW = (40), RTH = (6), KI = (0), KD = (0);
        unsigned dv = 0;
        bool got = true;
#define DECISION(K) for (;;) { const u64 x = GLDS(dec + ((K) & 15)); dv = __builtin_amdgcn_readfirstlane((unsigned)x); got = __builtin_amdgcn_readfirstlane((unsigned)(x >> 32)) == rtag + (K); \
                               if (got || lds_ld(&sm.merged) >= 2 * NU - 1) break; STAMPV(41, sm.st[wave][41] + 1) SLEEP(32) }
        float tmin = 0.f, tmax = 0.f; int shA = 0, shB = 0, shC = 0;
        if (boss) {
            UNTIL((ts + (330)) * 128)
            for (;;) {
                bool ok = true; float sum = 0.f; tmin = 0.f; tmax = 0.f;
#pragma unroll
                for (int i = 0; i < 4; i++) { const u64 x = GLDS(slots + 2 * i), e = GLDS(slots + 2 * i + 1); ok &= e == E1 && (unsigned)(x >> 32) == rtag; sum += (float)min(max((int)((unsigned)x - ts), -1000), 1000); tmin = fminf(tmin, (float)(int)((unsigned)x - ts)); tmax = fmaxf(tmax, (float)(int)((unsigned)x - ts)); }
                if (__builtin_amdgcn_ballot_w64(ok) == ~0ull) { dv = ts + (int)(wave_sum(sum) * (1.f / 256)); break; }
                SLEEP(8)
            }
            if (lane == 0) { GSTS(dec, (u64)dv | ((u64)rtag << 32)); GSTS((u64*)(ws + WS_EPOCH), E1); }
        } else {
            UNTIL((ts + (330) + (300)) * 128)
            DECISION(0)
        }
        const unsigned tmean = dv;
        {
            const int d0 = (min(max((int)(dv - ts), -1000), 1000) + (0)) * 128;
            dl = -d0 / (32 * NB); lT += d0;
            if (got) NEWLINE()
        }
        STAMP(3)
        int sp = 0, sp1 = 0, jump = 0;
        dv = 0;
#pragma unroll 1
        for (unsigned k = K0; k < 2 * NU && got; k++) {
            const unsigned tk = lT + (k - lU) * 32 * lper;
            const unsigned tx = tk - XLEAD * 128;
            if (k + 1 < 2 * NU) {
                UNTIL(tx - XW * 128)
                int sum = 0, n = 0;
                do {
                    const unsigned p = lds_ld(&sm.grab) - 9;
                    const unsigned now = NOW() * 128;
                    const v2u e = TU(p >> 5);
                    sum += (int)(now - __builtin_amdgcn_readfirstlane(e.x) - (p & 31) * __builtin_amdgcn_readfirstlane(e.y)) >> 7; n++;
                } while ((unsigned)(tx - NOW() * 128) < 0x40000u);
                const int l = (int)((float)sum / (float)n) - (k <= NU ? BK + (4) : BK);
                if (l > RTH && lane == 0) GSTS((unsigned*)(ws + WS_REF + (blockIdx.x & 63) * LSTRIDE + 64) + (blockIdx.x >> 6), (unsigned)min(l, 1000) | ((rtag + k) << 10));
            }
            if (!boss && k > K0) {
                UNTIL(tx)
                const unsigned tl = NOW();
                DECISION(k)
                STAMPV(42 + (k >> 1), NOW() - tl)
                if (!got) break;
            }
            sp1 = sp;
            jump = min(dv >> 16, 1000u) * 128; sp = min(dv & 0xffffu, 4000u);
            lper = min(max(lper + min(sp, 40) * KI / 64 - KD, PK), PK + 40);
            const int pk = lper + (k >= NU ? PV - PK : 0);
            if (k < 9) shA += jump + sp * 32; else if (k < 18) shB += jump + sp * 32; else shC += jump + sp * 32;
            lU = k + 1; lT = tk + jump + 32 * (pk + sp);
            if (lane == 0) sm.tu[k] = (v2u){tk + jump + OWN(k), (unsigned)(pk + sp - (k < NB + 2 ? dl : 0))};
            NEWLINE()
            STAMPV(40, max((int)sm.st[wave][40], (int)(NOW() * 128 - tk) / 128 + 1000))
            if (boss && k + 1 < 2 * NU) {
                UNTIL(tx + XD1 * 128)
                const unsigned tl = NOW();
                const v4u x = __builtin_amdgcn_raw_buffer_load_b128(__builtin_amdgcn_make_buffer_rsrc(ws + WS_REF + 64, 0, 64 * LSTRIDE, 0x00020000), lane * LSTRIDE, 0, SC1);
                float a[4];
#pragma unroll
                for (int i = 0; i < 4; i++) a[i] = ((x[i] ^ ((rtag + k) << 10)) >> 10) == 0 ? (float)(x[i] & 1023u) : 0.f;
                asm volatile("" :: "v"(a[0]));
                STAMPV(42 + (k >> 1), NOW() - tl)
                const float mn = -wave_max(-fminf(fminf(a[0], a[1]), fminf(a[2], a[3]))), mx = wave_max(fmaxf(fmaxf(a[0], a[1]), fmaxf(a[2], a[3])));
                const float c = mn + (float)CAP;
                const int tot = (int)wave_sum(a[0] + a[1] + a[2] + a[3]) / 2, cut = (int)wave_sum(fminf(a[0], c) + fminf(a[1], c) + fminf(a[2], c) + fminf(a[3], c)) / 2;
                const int pend = jump + sp * 32 + sp1 * 32 * XLEAD * 4 / (pk + sp1);
                const int nj = GAIN ? max((int)mn * 128 - pend, 0) : 0;
                const int ns = max(max(min(tot * GAIN / 16 - pend - nj, (cut - (int)mn * 128) * GAIN / 16), 0), mx > (float)(XD2 * (2 * NU - (int)k)) ? BV * 128 : 0);
                dv = ((nj >> 7) << 16) | (unsigned)min(ns >> 5, 4000);
                if (lane == 0) GSTS(dec + ((k + 1) & 15), (u64)dv | ((u64)(rtag + k + 1) << 32));
                STAMPV(5 + k, ((tot >> 7) << 20) | (((nj + ns) >> 7) << 10) | (lper - PK + 100))
            }
        }
        if (boss) {
            tmin = -wave_max(-tmin); tmax = wave_max(tmax);
            const unsigned t0 = ts + (int)tmin;
            while (NOW() - t0 < 14000u) SLEEP(8)
            float e = 0.f;
#pragma unroll
            for (int i = 0; i < 4; i++) { const unsigned z = GLDS((const unsigned*)(ws + (64u << 20)) + i * 64 + lane) - t0; e = fmaxf(e, z < 14000u ? (float)z : 0.f); }
            e = wave_max(e);
            const int DG = 4;
            const int x = DG == 0 ? 0 : DG == 1 ? shA / 32 : DG == 2 ? shB / 32 : DG == 3 ? shC / 32 : DG == 4 ? (int)(tmean - t0) * 4 : DG == 5 ? (int)(tmax - tmin) * 4 : DG == 6 ? ((int)e - 10000) * 4 : ((int)e - (int)(lT / 128 - (t0 & 0x1ffffffu))) * 4;
            while (NOW() - t0 < 15000u + (unsigned)min(max(x, 0), 20000)) SLEEP(2)
        }
    } else if (wave == NSW + 2) {
        const int ql = lane < 48 ? lane : 47;
        unsigned c1, c2, s1, s2;
#define QT_LOADS() { int qo_ = ql; asm volatile("" : "+v"(qo_)); \
                     c1 = ((const unsigned*)q_cos)[qo_]; c2 = ((const unsigned*)q_cos)[48 + qo_]; s1 = ((const unsigned*)q_sin)[qo_]; s2 = ((const unsigned*)q_sin)[48 + qo_]; }
        unsigned q1, q2, lkz, lks, lvz, lvs;
#define UNIT_LOADS(HEAD) { \
        q1 = ((const unsigned*)(q + (size_t)(HEAD) * DH))[ql]; q2 = ((const unsigned*)(q + (size_t)(HEAD) * DH))[48 + ql]; \
        const size_t so = ((size_t)(HEAD) * SKV + brow0) / 2 + (lane >> 3) * 128 + (lane & 7); \
        lkz = SLD((const unsigned*)k_zeros + so); lks = SLD((const unsigned*)k_scales + so); \
        lvz = SLD((const unsigned*)v_zeros + so); lvs = SLD((const unsigned*)v_scales + so); }
#define UNIT_FILL(SL, U) { \
            v4f qq; \
            qq.x = bfr(bfr(bflo(q1) * bflo(c1)) + bfr(-bflo(q2) * bflo(s1))); \
            qq.y = bfr(bfr(bflo(q2) * bflo(c2)) + bfr(bflo(q1) * bflo(s2))); \
            qq.z = bfr(bfr(bfhi(q1) * bfhi(c1)) + bfr(-bfhi(q2) * bfhi(s1))); \
            qq.w = bfr(bfr(bfhi(q2) * bfhi(c2)) + bfr(bfhi(q1) * bfhi(s2))); \
            *(v2u*)&sm.q[SL][2 * ql] = (v2u){(__float_as_uint(qq.x) >> 16) | __float_as_uint(qq.y), (__float_as_uint(qq.z) >> 16) | __float_as_uint(qq.w)}; \
            *(v4f*)&sm.kz[SL][2 * lane] = (v4f){bflo(lkz), bflo(lks), bfhi(lkz), bfhi(lks)}; \
            *(v4f*)&sm.vz[SL][2 * lane] = (v4f){bflo(lvz), bflo(lvs), bfhi(lvz), bfhi(lvs)}; \
            CBAR(); \
            if (lane == 0) lds_st(&sm.rdy[SL], (U) + 1); \
            CBAR(); }
        QT_LOADS()
        UNIT_LOADS(rank)
        const unsigned b0 = q1, b1 = q2, b2 = lkz, b3 = lks, b4 = lvz, b5 = lvs;
        __builtin_amdgcn_sched_barrier(0);
        UNIT_LOADS(rank + GSZ)
        const unsigned e0 = q1, e1 = q2, e2 = lkz, e3 = lks, e4 = lvz, e5 = lvs;
        __builtin_amdgcn_sched_barrier(0);
        __builtin_amdgcn_s_barrier();
        __builtin_amdgcn_sched_barrier(0);
        CBAR();
        UNIT_LOADS(rank + 2 * GSZ)
        const unsigned f0 = q1, f1 = q2, f2 = lkz, f3 = lks, f4 = lvz, f5 = lvs;
        __builtin_amdgcn_sched_barrier(0);
        q1 = b0; q2 = b1; lkz = b2; lks = b3; lvz = b4; lvs = b5;
        UNIT_FILL(0, 0)
        __builtin_amdgcn_sched_barrier(0);
        q1 = e0; q2 = e1; lkz = e2; lks = e3; lvz = e4; lvs = e5;
        UNIT_FILL(1, 1)
        __builtin_amdgcn_sched_barrier(0);
        q1 = f0; q2 = f1; lkz = f2; lks = f3; lvz = f4; lvs = f5;
        UNIT_FILL(2, 2)
        CBAR();
#pragma unroll 1
        for (unsigned u = 3; u < NU; u++) {
            UNIT_LOADS(rank + GSZ * u)
            while (u >= lds_ld(&sm.merged) + NIN) SLEEP(4);
            CBAR();
            UNIT_FILL(u % NIN, u)
        }
    } else {
        {
            const unsigned short* sa = (split & 2) ? ((split & 1) ? v_scales : v_zeros) : ((split & 1) ? k_scales : k_zeros);
            const unsigned to = ((rank + GSZ * (lane >> 3)) * SKV + (lane & 7) * 256 + (brow0 & ~63)) * 2;
            asm volatile("global_load_dword a7, %0, %2\n\tglobal_load_dword a7, %1, %2" :: "v"(to), "v"(to + 8 * GSZ * SKV * 2), "s"(sa) : "a7", "memory");
            const unsigned tq = (rank + GSZ * (lane >> 2)) * (DH * 2) + min(lane & 3, 2) * 128;
            asm volatile("global_load_dword a7, %0, %1" :: "v"(tq), "s"(q) : "a7", "memory");
        }
        __builtin_amdgcn_s_barrier();
        CBAR();
        while (lds_ld(&sm.tag_rdy) == 0) SLEEP(1);
        CBAR();
        const unsigned T = lds_ld(&sm.tag_lo);
        const unsigned t15 = T & 0x7fffu;
        const __amdgpu_buffer_rsrc_t rsrc = __builtin_amdgcn_make_buffer_rsrc(ws + WS_PART + (rank * NSPLIT + split) * 512, 0, NH * NSPLIT * 512, 0x00020000);
        v2u* recs = (v2u*)&sm.cs[0][0];
#pragma unroll
        for (int i = 0; i < NOUT * 3; i++) (&sm.acc[0][0])[i * 64 + lane] = 0.0;
        unsigned npub = 0;
        bool direct = false;
#define PUBLISH(N) { \
        _Pragma("unroll 1") for (unsigned i = npub; i < (N); i += 4) { \
            v2u r[4]; \
            _Pragma("unroll") for (int k = 0; k < 4; k++) r[k] = recs[(i + k) * 64 + lane]; \
            _Pragma("unroll") for (int k = 0; k < 4; k++) if (i + k < (N)) __builtin_amdgcn_raw_buffer_store_b64(r[k], rsrc, lane * 8, (i + k) * (GSZ * NSPLIT * 512), SC1); } \
        npub = (N); }
#pragma unroll 1
        for (unsigned u = 0; u < 2 * NU; u++) {
            const unsigned e = u % NU, so = u % NOUT;
            const v4f h = *(const v4f*)&sm.ent[e][128];
            for (;;) {
                if (!direct && lds_ld(&sm.grab) >= NPC - PUBP) { direct = true; wave_sync(); PUBLISH(u - NU) }
                if (lds_ld(&sm.cnt[so]) == 32) break;
                if (direct) SLEEP(0) else SLEEP(1)
            }
            CBAR();
            if (!direct && u == 2 * NU - 1) { direct = true; wave_sync(); PUBLISH(NU - 1) }
            if (u < NU) {
                const float2 lg = *(const float2*)&sm.ent[e][2 * lane];
                const v4f zs = *(const v4f*)&sm.vz[u % NIN][2 * lane];
                const float m = wave_max(fmaxf(lg.x, lg.y));
                const float p0 = __expf(lg.x - m), p1 = __expf(lg.y - m);
                const float x0 = p0 * zs.y, x1 = p1 * zs.w;
                const unsigned eb = max(__float_as_uint(wave_max(fmaxf(fabsf(x0), fabsf(x1)))) >> 23, 21u);
                const float f = __uint_as_float((275u - eb) << 23);
                const float w0 = rintf(x0 * f), w1 = rintf(x1 * f);
                const float den = wave_sum(p0 + p1);
                const float bz = wave_sum(fmaf(w0, zs.x, w1 * zs.z));
                const float L = fmaf(m, 1.4426950408889634f, __builtin_amdgcn_logf(den));
                *(v2u*)&sm.ent[e][2 * lane] = (v2u){(unsigned)(int)w0, (unsigned)(int)w1};
                if (lane == 0) *(v4f*)&sm.ent[e][128] = (v4f){ldexpf(1.f / den, (int)eb - 148), bz, L, 0.f};
            } else {
                const double a0 = sm.acc[so][lane], a1 = sm.acc[so][64 + lane], a2 = sm.acc[so][128 + lane];
                const float n0 = (float)(a0 - (double)h.y) * h.x, n1 = (float)(a1 - (double)h.y) * h.x, n2 = (float)(a2 - (double)h.y) * h.x;
                const unsigned e2 = max(__float_as_uint(wave_maxv(fmaxf(fmaxf(fabsf(n0), fabsf(n1)), fabsf(n2)))) >> 23, 14u);
                const float f2 = __uint_as_float((268u - e2) << 23);
                const int m0 = min(max((int)rintf(n0 * f2), -32767), 32767), m1 = min(max((int)rintf(n1 * f2), -32767), 32767), m2 = min(max((int)rintf(n2 * f2), -32767), 32767);
                const unsigned bit = ((lane < 32 ? __float_as_uint(h.z) : e2) >> (lane & 31)) & 1u;
                const v2u r = {((unsigned)m0 & 0xffffu) | ((unsigned)m1 << 16), ((unsigned)m2 & 0xffffu) | (bit << 16) | (t15 << 17)};
                if (direct) __builtin_amdgcn_raw_buffer_store_b64(r, rsrc, lane * 8, e * (GSZ * NSPLIT * 512), SC1);
                else recs[e * 64 + lane] = r;
                CBAR();
                sm.acc[so][lane] = 0.0; sm.acc[so][64 + lane] = 0.0; sm.acc[so][128 + lane] = 0.0;
            }
            CBAR();
            if (lane == 0) { lds_st(&sm.cnt[so], 0u); CBAR(); lds_st(&sm.merged, u + 1); }
        }
        STAMP(2)
        STAMP_DUMP()
        return;
    }

    STAMP(2)
    STAMP_DUMP()
}

extern "C" void kernel_launch(void* const* d_in, const int* in_sizes, int n_in,
                              void* d_out, int out_size, void* d_ws, size_t ws_size,
                              hipStream_t stream) {
    const unsigned short* q = (const unsigned short*)d_in[0];
    const int* qk = (const int*)d_in[1];
    const unsigned short* k_scales = (const unsigned short*)d_in[2];
    const unsigned short* k_zeros = (const unsigned short*)d_in[3];
    const int* qv = (const int*)d_in[4];
    const unsigned short* v_scales = (const unsigned short*)d_in[5];
    const unsigned short* v_zeros = (const unsigned short*)d_in[6];
    const unsigned short* q_cos = (const unsigned short*)d_in[7];
    const unsigned short* q_sin = (const unsigned short*)d_in[8];
    const unsigned short* k_cos = (const unsigned short*)d_in[9];
    const unsigned short* k_sin = (const unsigned short*)d_in[10];
    unsigned char* ws = (unsigned char*)d_ws;

    attn_kernel<<<NSPLIT * GSZ, NTHREADS, 0, stream>>>(q, qk, k_scales, k_zeros, qv, v_scales, v_zeros,
                                                       q_cos, q_sin, k_cos, k_sin, (__hip_bfloat16*)d_out, ws);
}
```

```cpp
#include <hip/hip_runtime.h>
#include <hip/hip_bf16.h>
#include <stdint.h>

#define DH 192
#define HALF 96
#define SKV 2048
#define NH 256
#define NSW 8
#define NWAVES (NSW + 4)
#define NTHREADS (64 * NWAVES)
#define BROWS 128
#define NSPLIT (SKV / BROWS)
#define GSZ 16
#define NU (NH / GSZ)
#define NPC (2 * NU * 32)
#define N1 (NH - GSZ)
#define NIN 4
#define NOUT 4
#define ENTW 132
#define SC1 16
#define LIKELY 1
#define SLD(P) (*(P))
#define LSTRIDE 4352
#define PUBP 2
#define PSL 5
#define PHALF 9
#define PHI 0x0235712457024671ull
#define KAP 0x0202020220202020ull
#define PHIV PHI
#define KAPV KAP
#define XM 0
#define PRIO 1
#define PRIO0 "s_setprio 0\n"
#define PRIO1 "\n\ts_setprio 2"
#define GTH 10
#define GS1 2
#define PFL 157

#define WS_EPOCH 0
#define WS_DEC 8192
#define WS_REF 16384
#define WS_LOG 524288
#define LSTRIDE2 1408
#define WS_PART 1048576
#define WS_FIN (WS_PART + NH * NSPLIT * 512)

typedef unsigned int v2u __attribute__((ext_vector_type(2)));
typedef unsigned int v3u __attribute__((ext_vector_type(3)));
typedef unsigned int v4u __attribute__((ext_vector_type(4)));
typedef int v4i __attribute__((ext_vector_type(4)));
typedef float v2f __attribute__((ext_vector_type(2)));
typedef float v4f __attribute__((ext_vector_type(4)));
typedef unsigned long long u64;

__device__ __forceinline__ float bflo(unsigned u) { return __uint_as_float(u << 16); }
__device__ __forceinline__ float bfhi(unsigned u) { return __uint_as_float(u & 0xffff0000u); }
__device__ __forceinline__ float bfr(float x) {
    __bf16 b = (__bf16)x;
    return __uint_as_float(((unsigned)__builtin_bit_cast(unsigned short, b)) << 16);
}

#define SLEEP(n) SLEEP_(n)
#define SLEEP_(n) asm volatile("s_sleep " #n "\n\tv_add_co_u32 %0, vcc, 1, %0\n\ts_cbranch_vccz 1f\n\ts_endpgm\n1:" : "+v"(spins) :: "vcc", "memory");
#define SPIN0 0xf0000000u
#define CBAR() asm volatile("" ::: "memory")

__device__ __forceinline__ void wave_sync() {
    __builtin_amdgcn_fence(__ATOMIC_RELEASE, "wavefront");
    __builtin_amdgcn_wave_barrier();
    __builtin_amdgcn_fence(__ATOMIC_ACQUIRE, "wavefront");
}
template <int CTRL>
__device__ __forceinline__ float dpp_add(float x) {
    int y = __builtin_amdgcn_update_dpp(0, __float_as_int(x), CTRL, 0xf, 0xf, true);
    return x + __int_as_float(y);
}
template <int CTRL>
__device__ __forceinline__ float dpp_max(float x) {
    int y = __builtin_amdgcn_update_dpp(__float_as_int(x), __float_as_int(x), CTRL, 0xf, 0xf, false);
    return fmaxf(x, __int_as_float(y));
}
__device__ __forceinline__ float row_sum(float x) { x = dpp_add<0x128>(x); x = dpp_add<0x124>(x); x = dpp_add<0x122>(x); return dpp_add<0x121>(x); }
__device__ __forceinline__ float row_max(float m) { m = dpp_max<0x128>(m); m = dpp_max<0x124>(m); m = dpp_max<0x122>(m); return dpp_max<0x121>(m); }
__device__ __forceinline__ float rdlane(float x, int l) { return __int_as_float(__builtin_amdgcn_readlane(__float_as_int(x), l)); }
__device__ __forceinline__ float wave_max(float m) { m = row_max(m); return fmaxf(fmaxf(rdlane(m, 0), rdlane(m, 16)), fmaxf(rdlane(m, 32), rdlane(m, 48))); }
__device__ __forceinline__ float wave_maxv(float m) {
    m = row_max(m);
    const auto a = __builtin_amdgcn_permlane16_swap(__float_as_uint(m), __float_as_uint(m), false, false);
    m = fmaxf(__uint_as_float(a[0]), __uint_as_float(a[1]));
    const auto b = __builtin_amdgcn_permlane32_swap(__float_as_uint(m), __float_as_uint(m), false, false);
    return fmaxf(__uint_as_float(b[0]), __uint_as_float(b[1]));
}
__device__ __forceinline__ float wave_sum(float x) { x = row_sum(x); return (rdlane(x, 0) + rdlane(x, 16)) + (rdlane(x, 32) + rdlane(x, 48)); }
__device__ __forceinline__ unsigned lds_ld(const unsigned* p) { return __builtin_amdgcn_readfirstlane(__hip_atomic_load(p, __ATOMIC_RELAXED, __HIP_MEMORY_SCOPE_WORKGROUP)); }
__device__ __forceinline__ unsigned lds_addr(const void* p) { return (unsigned)(size_t)(const __attribute__((address_space(3))) void*)p; }
#define DMA16(SRC, LDS) asm volatile("s_mov_b32 %0, m0\n\ts_mov_b32 m0, %2\n\ts_nop 0\n\tglobal_load_lds_dwordx4 %1, off\n\ts_mov_b32 m0, %0" : "=&s"(m0keep) : "v"(SRC), "s"(LDS) : "memory")
__device__ __forceinline__ void lds_st(unsigned* p, unsigned v) { __hip_atomic_store(p, v, __ATOMIC_RELAXED, __HIP_MEMORY_SCOPE_WORKGROUP); }
#define NOW() ((unsigned)__builtin_amdgcn_s_memrealtime())

struct __attribute__((aligned(16))) Smem {
    unsigned cs[32][768];
    double acc[NOUT][DH];
    float ent[NU][ENTW];
    unsigned q[NIN][HALF];
    float2 kz[NIN][BROWS];
    float2 vz[NIN][BROWS];
    v2u tu[2 * NU + 2];
    unsigned grab, merged, tabs, cdone;
    unsigned rdy[NIN], cnt[NOUT];
    unsigned tag_rdy, tag_lo, tag_hi, fw_rdy;
    int stat[32];
    float fw[NSPLIT];
};
#define NSTAT 0
#define STAMP(i) {}
#define STAMPV(i, v) {}
#define STAMP_DUMP()
#define STAMP_UNIT(N)

#define TU(I) (*(const volatile v2u __attribute__((address_space(3)))*)(size_t)lds_addr(&sm.tu[I]))
#define LDS_ADD(P, V) { u64 ex_; asm volatile("s_mov_b64 %0, exec\n\ts_mov_b64 exec, 1\n\tds_add_u32 %1, %2\n\ts_mov_b64 exec, %0" : "=&s"(ex_) : "v"(lds_addr(P)), "v"(V) : "memory"); }
#define LDS_OR(P, V) { u64 ex_; asm volatile("s_mov_b64 %0, exec\n\ts_mov_b64 exec, 1\n\tds_or_b32 %1, %2\n\ts_mov_b64 exec, %0" : "=&s"(ex_) : "v"(lds_addr(P)), "v"(V) : "memory"); }

__global__ __launch_bounds__(NTHREADS, 3) void attn_kernel(
    const unsigned short* __restrict__ q, const int* __restrict__ qk,
    const unsigned short* __restrict__ k_scales, const unsigned short* __restrict__ k_zeros,
    const int* __restrict__ qv,
    const unsigned short* __restrict__ v_scales, const unsigned short* __restrict__ v_zeros,
    const unsigned short* __restrict__ q_cos, const unsigned short* __restrict__ q_sin,
    const unsigned short* __restrict__ k_cos, const unsigned short* __restrict__ k_sin,
    __hip_bfloat16* __restrict__ out, unsigned char* __restrict__ ws)
{
    __shared__ Smem sm;
    const int tid = threadIdx.x;
    unsigned spins = SPIN0;
#define TOUCH(OFF, BASE) asm volatile("global_load_dword a6, %0, %1" :: "v"(OFF), "s"(BASE) : "a6", "memory");
    if (tid < 64) { const char* pc0; asm volatile("s_getpc_b64 %0" : "=s"(pc0)); TOUCH(((blockIdx.x >> 3) * ((PFL - 64) / 31) + tid) * 128, pc0) }
    __builtin_amdgcn_sched_barrier(0);
    const int hw = __builtin_amdgcn_readfirstlane(tid >> 6);
    const int lane = tid & 63;
    const int split = (blockIdx.x & 3) * 4 + ((blockIdx.x >> 3) & 3);
    const int rank = (blockIdx.x >> 5) * 2 + ((blockIdx.x >> 2) & 1);
    const int brow0 = split * 16;
#define ROWOF(RG) (((RG) >> 2) * 256 + ((RG) & 3) * 4)
    if (hw == 0) {
        typedef const char* const __attribute__((address_space(4))) * kap_t;
        const char* sa = ((kap_t)__builtin_amdgcn_kernarg_segment_ptr())[(0x5623 >> (4 * (split & 3))) & 7];
        unsigned o1 = ((blockIdx.x >> 5) * 256 + brow0) * (DH * 2) + min(lane, 47) * 128;
        unsigned o2 = ((rank + GSZ * (lane >> 3)) * SKV + (lane & 7) * 256 + (brow0 & ~63)) * 2, o3 = o2 + 8 * GSZ * SKV * 2;
        unsigned o4 = (rank + GSZ * (lane >> 2)) * (DH * 2) + min(lane & 3, 2) * 128;
        asm volatile("" : "+v"(o1), "+v"(o2), "+v"(o3), "+v"(o4));
        TOUCH(o1, k_cos) TOUCH(o1, k_sin) TOUCH(o4, q) TOUCH(o2, sa) TOUCH(o3, sa)
    }
    if (hw == 1 && lane < 16 + NSTAT) (&sm.grab)[lane] = lane ? 0 : 64;
    asm volatile("" :: "s"(q), "s"(qk), "s"(k_scales), "s"(k_zeros), "s"(qv), "s"(v_scales), "s"(v_zeros), "s"(q_cos), "s"(q_sin), "s"(k_cos), "s"(k_sin), "s"(out), "s"(ws));
    asm volatile("s_waitcnt lgkmcnt(0)\n\ts_barrier" ::: "memory");
    const int wave = (int)(0x76543210b9a8ull >> (4 * hw)) & 15;
    STAMP(0)
    const unsigned ts = NOW();
    const int PK = (1266), PV = (1264), TOF = (90), P0 = (1340);
    if (LIKELY ? __builtin_expect(wave < NSW, 1) : wave < NSW) {
        const unsigned rot = 4 * ((unsigned)(PHI >> (60 - 4 * rank)) & 7u), kap = (unsigned)(KAP >> (60 - 4 * rank)) & 3u;
        const unsigned rotv = 4 * ((unsigned)(PHIV >> (60 - 4 * rank)) & 7u), kapv = (unsigned)(KAPV >> (60 - 4 * rank)) & 3u;
#define RGOF(G) ((G) >= NPC / 2 ? (((G) + rotv) & 31u) ^ kapv : (((G) + rot) & 31u) ^ kap)
        const char* cbase[3];
#pragma unroll
        for (int j = 0; j < 3; j++) { const int x = j * 64 + lane; cbase[j] = (x < 96 ? (const char*)k_cos + x * 16 : (const char*)k_sin + (x - 96) * 16) + brow0 * (DH * 2); }
        unsigned m0keep;
#define CS_DMA(RG) { _Pragma("unroll") for (int j = 0; j < 3; j++) DMA16(cbase[j] + ROWOF(RG) * (DH * 2), lds_addr(&sm.cs[0][0]) + (RG) * 3072 + j * 1024); }
#define REPACK(RG) { \
        unsigned* cw = &sm.cs[RG][0]; \
        const unsigned* l = cw + rsel * (DH / 2) + qoff; \
        unsigned r[2][6]; \
        _Pragma("unroll") \
        for (int j = 0; j < 2; j++) { \
            unsigned e[4][2]; \
            _Pragma("unroll") \
            for (int k = 0; k < 4; k++) { \
                const unsigned* a = l + j * DH + (k & 1) * 48 + (k >> 1) * 384;       \
                unsigned x0 = a[0], x1 = a[1]; \
                e[k][0] = __builtin_amdgcn_alignbit(x1, x0, sh); \
                e[k][1] = x1 >> sh; \
            } \
            r[j][0] = (e[0][0] & 0xffffu) | (e[3][0] << 16); \
            r[j][1] = (e[0][0] >> 16) | (e[3][0] & 0xffff0000u); \
            r[j][2] = (e[0][1] & 0xffffu) | (e[3][1] << 16); \
            r[j][3] = (e[1][0] & 0xffffu) | (e[2][0] << 16); \
            r[j][4] = (e[1][0] >> 16) | (e[2][0] & 0xffff0000u); \
            r[j][5] = (e[1][1] & 0xffffu) | (e[2][1] << 16); \
        } \
        wave_sync(); \
        _Pragma("unroll") \
        for (int j = 0; j < 2; j++) { *(v4u*)&cw[j * 256 + lane * 4] = (v4u){r[j][0], r[j][1], r[j][2], r[j][3]}; *(v2u*)&cw[512 + j * 128 + lane * 2] = (v2u){r[j][4], r[j][5]}; } \
        wave_sync(); \
        LDS_OR(&sm.tabs, 1u << (RG)) }
        const int sub = lane & 31;
        const int rsel = lane >> 5;
        const unsigned sh = (sub & 1) * 16;
        const int qoff = 3 * (sub >> 1) + (sub & 1);
        const char* kbase = (const char*)qk + (size_t)(rank * SKV + brow0) * (DH * 4);
        const char* vbase = (const char*)qv + (size_t)(rank * SKV + brow0) * (DH * 4);
        const unsigned koff = (rsel * DH + 3 * sub) * 4, voff = 3 * lane * 4;
#define AR(S) (16 + (S) * 8)
        asm volatile("" ::: "a16", "a17", "a18", "a19", "a20", "a21", "a22", "a23", "a24", "a25", "a26", "a27", "a28", "a29", "a30", "a31",
                            "a32", "a33", "a34", "a35", "a36", "a37", "a38", "a39", "a40", "a41", "a42", "a43", "a44", "a45", "a46", "a47");
        asm volatile("" ::: "a48", "a49", "a50", "a51", "a52", "a53", "a54", "a55", "a56", "a57", "a58", "a59", "a60", "a61", "a62", "a63");
        __builtin_amdgcn_s_waitcnt(0x0f70);
        int ta[3], tb[3];
#define GLD(A, OFF, BASE, IMM) asm volatile("global_load_dwordx3 a[%c0:%c1], %2, %3 offset:%4 nt" :: "i"(A), "i"((A) + 2), "v"(OFF), "s"(BASE), "i"(IMM))
#define ISSUE(S, J, BASE, OA, OB) { GLD(AR(S), OA, BASE, (J) * 1536); GLD(AR(S) + 4, OB, BASE, (J) * 1536); }
#define WAITT(S, N) asm volatile("s_waitcnt vmcnt(%6)\n\tv_accvgpr_read_b32 %0, a[%c7]\n\tv_accvgpr_read_b32 %1, a[%c8]\n\tv_accvgpr_read_b32 %2, a[%c9]\n\t" \
                              "v_accvgpr_read_b32 %3, a[%c10]\n\tv_accvgpr_read_b32 %4, a[%c11]\n\tv_accvgpr_read_b32 %5, a[%c12]" \
                              : "=v"(ta[0]), "=v"(ta[1]), "=v"(ta[2]), "=v"(tb[0]), "=v"(tb[1]), "=v"(tb[2]) \
                              : "i"(N), "i"(AR(S)), "i"(AR(S) + 1), "i"(AR(S) + 2), "i"(AR(S) + 4), "i"(AR(S) + 5), "i"(AR(S) + 6))
        const unsigned koffb = koff + 384, voffb = voff + 768;
        unsigned u = 0, rg, np = 0;
#define RG0(C) RGOF((unsigned)(wave + 8 * (C)))
        CS_DMA(RG0(0))
        STAMP(52)
        STAMP(53)
        const float scale = 0.07216878364870322f;
        double acc[3] = {0.0, 0.0, 0.0};
        bool chg, none = false;
        unsigned Qp[3], Qn[3];
        float kz4[2], ksc, kres[2];
        v4u ca[2]; v2u cb[2];
        v2f qe[2][3], A[2];
        const char* nb; unsigned oa, ob;
        unsigned g, mg, nrg, tg, late = 0;
        u64 tis;
        unsigned dg, par = 0, gu;
        int early;
        const int bth = wave < (8) ? (20) * 128 : 0x3fffffff;
        const unsigned grab_adr = lds_addr(&sm.grab);
        asm volatile("v_accvgpr_write_b32 a13, 1\n\tv_accvgpr_write_b32 a14, 2" ::: "a13", "a14", "a15");
#define GRAB(N) { u64 ex_; asm volatile("s_mov_b64 %0, exec\n\ts_mov_b64 exec, 1\n\tds_add_rtn_u32 a15, %1, a%c2\n\ts_mov_b64 exec, %0" : "=&s"(ex_) : "v"(grab_adr), "i"(12 + (N)) : "memory"); }
#define GRABBED() { unsigned t_; asm volatile("s_waitcnt lgkmcnt(0)\n\tv_accvgpr_read_b32 %1, a15\n\ts_nop 0\n\tv_readfirstlane_b32 %0, %1\n\ts_nop 1" : "=s"(g), "=v"(t_)); }
#define NEXT1() const v2u x_ = TU(g >> 5); u64 tn_; asm volatile("s_memrealtime %0" : "=s"(tn_));
#define NEXT2() { \
            chg = (g >> 5) != u; none = g >= NPC; \
            const bool nv = g >= NPC / 2; \
            nrg = RGOF(g); \
            unsigned boff; asm("s_mul_i32 %0, %1, 0x300" : "=s"(boff) : "s"(((g >> 5) & (NU - 1)) * (GSZ * SKV) + ROWOF(nrg) + 2)); \
            nb = (nv ? vbase : kbase) + boff; \
            oa = nv ? voff : koff; ob = nv ? voffb : koffb; \
            asm volatile("s_waitcnt lgkmcnt(0)" : "+s"(tn_) : "s"(nb), "v"(oa), "v"(ob)); \
            tg = __builtin_amdgcn_readfirstlane(x_.x) + (g & 31) * __builtin_amdgcn_readfirstlane(x_.y); \
            early = none ? 0 : (int)((unsigned)tn_ * 128 - tg) - bth; \
            if (early > 0) asm volatile("s_cmp_lg_u32 %3, 0\n\ts_cbranch_scc1 1f\n\t" L4(32) "\ts_branch 2f\n1:\n\t" L4(16) "2:\n\ts_mov_b64 vcc, exec\n\ts_mov_b64 exec, 1\n\tds_add_rtn_u32 a15, %4, a13\n\ts_mov_b64 exec, vcc" \
                                    :: "v"(oa), "v"(ob), "s"(nb), "s"(pr), "v"(grab_adr) : "scc", "vcc", "memory"); }
#define L4(A) "global_load_dwordx3 a[" #A ":" #A "+2], %0, %2 offset:-1536 nt\n\tglobal_load_dwordx3 a[" #A "+4:" #A "+6], %1, %2 offset:-1536 nt\n\t" \
              "global_load_dwordx3 a[" #A "+8:" #A "+10], %0, %2 nt\n\tglobal_load_dwordx3 a[" #A "+12:" #A "+14], %1, %2 nt\n"
#define GATE(...) \
            asm volatile(PRIO0 "1:\n\ts_memrealtime vcc\n\ts_waitcnt lgkmcnt(0)\n\ts_lshl_b32 vcc_lo, vcc_lo, 7\n\ts_sub_u32 %0, %1, vcc_lo\n\ts_cmp_lt_u32 %0, 0x40000\n\ts_cbranch_scc0 2f\n\t" \
                         "s_cmp_lt_u32 %0, %2\n\ts_cbranch_scc1 1b\n\ts_sleep %3\n\ts_branch 1b\n2:" PRIO1 : "=&s"(dg) : "s"(tg), "i"(GTH * 128), "i"(GS1), __VA_ARGS__ : "scc", "vcc", "memory");
#define RD6(A) "v_accvgpr_read_b32 %0, a[%c6+" #A "]\n\tv_accvgpr_read_b32 %1, a[%c6+" #A "+1]\n\tv_accvgpr_read_b32 %2, a[%c6+" #A "+2]\n\t" \
               "v_accvgpr_read_b32 %3, a[%c6+" #A "+4]\n\tv_accvgpr_read_b32 %4, a[%c6+" #A "+5]\n\tv_accvgpr_read_b32 %5, a[%c6+" #A "+6]\n"
#define LD2(A) "global_load_dwordx3 a[%c6+" #A ":%c6+" #A "+2], %7, %9 offset:%10 nt\n\tglobal_load_dwordx3 a[%c6+" #A "+4:%c6+" #A "+6], %8, %9 offset:%10 nt"
#define OUT6 "=&v"(ta[0]), "=&v"(ta[1]), "=&v"(ta[2]), "=&v"(tb[0]), "=&v"(tb[1]), "=&v"(tb[2])
#define WI(A, B, TAIL) "s_cmp_gt_i32 %13, 0\n\ts_cbranch_scc1 3f\n\ts_waitcnt vmcnt(2)\n\t" LD2(A) TAIL "\n3:\n\ts_waitcnt vmcnt(%c14)\n\t" RD6(B)
#define WAITISSUE(S, TAIL, ...) asm volatile("s_cmp_lg_u32 %12, 0\n\ts_cbranch_scc1 1f\n\t" WI(16, 0, TAIL) "\ts_branch 2f\n1:\n\t" WI(0, 16, TAIL) "2:" \
                              : OUT6 : "i"(AR(S)), "v"(oa), "v"(ob), "s"(nb), "i"(((S) - 1) * 1536), "v"(grab_adr), "s"(pr), "s"(early), "i"(6 - 2 * (S)), __VA_ARGS__ : "scc", "vcc", "memory")
#define LASTT(S) asm volatile("s_waitcnt vmcnt(%c8)\n\ts_cmp_lg_u32 %7, 0\n\ts_cbranch_scc1 1f\n\t" RD6(0) "\ts_branch 2f\n1:\n\t" RD6(16) "2:" : OUT6 : "i"(AR(S)), "s"(pr), "i"(2 - 2 * (S)) : "scc")
#define DRAW "\n\ts_mov_b64 vcc, exec\n\ts_mov_b64 exec, 1\n\tds_add_rtn_u32 a15, %11, a13\n\ts_mov_b64 exec, vcc"
#define LATE0()
#define LATE1()
#define LATEF()
#define K_TAB() { _Pragma("unroll") for (int p = 0; p < 2; p++) { \
            const unsigned* cw = &sm.cs[rg][0]; \
            ca[p] = *(const v4u*)&cw[p * 256 + lane * 4]; cb[p] = *(const v2u*)&cw[512 + p * 128 + lane * 2]; } }
#define K_PRE(P) { \
        float e0, e1, e2, f0, f1, f2; \
        asm("v_dot2_f32_bf16 %0, %6, %12, 0\n\tv_dot2_f32_bf16 %3, %9, %15, 0\n\tv_dot2_f32_bf16 %1, %7, %13, 0\n\tv_dot2_f32_bf16 %4, %10, %16, 0\n\t" \
            "v_dot2_f32_bf16 %2, %8, %14, 0\n\tv_dot2_f32_bf16 %5, %11, %17, 0\n\ts_nop 2" \
            : "=&v"(e0), "=&v"(e1), "=&v"(e2), "=&v"(f0), "=&v"(f1), "=&v"(f2) \
            : "v"(Qp[0]), "v"(Qp[1]), "v"(Qp[2]), "v"(Qn[0]), "v"(Qn[1]), "v"(Qn[2]), \
              "v"(ca[P].x), "v"(ca[P].y), "v"(ca[P].z), "v"(ca[P].w), "v"(cb[P].x), "v"(cb[P].y)); \
        qe[P][0] = (v2f){e0, f0}; qe[P][1] = (v2f){e1, f1}; qe[P][2] = (v2f){e2, f2}; \
        A[P] = (qe[P][0] + qe[P][1] + qe[P][2]) * (v2f){-kz4[P], -kz4[P]}; }
#define K_SUM(P) { \
        _Pragma("unroll") \
        for (int i = 0; i < 3; i++) { v2f x = {(float)ta[i], (float)tb[i]}; A[P] = qe[P][i] * x + A[P]; } \
        kres[P] = A[P].x + A[P].y; }
#define K_RED2() { \
        const bool o1 = lane & 1; \
        float sx = (o1 ? kres[1] : kres[0]) + __int_as_float(__builtin_amdgcn_update_dpp(0, __float_as_int(o1 ? kres[0] : kres[1]), 0xb1, 0xf, 0xf, true)); \
        sx = dpp_add<0x4e>(sx); sx = dpp_add<0x124>(sx); sx = dpp_add<0x128>(sx); \
        const auto sw = __builtin_amdgcn_permlane16_swap(__float_as_uint(sx), __float_as_uint(sx), false, false); \
        sx = __uint_as_float(sw[0]) + __uint_as_float(sw[1]); \
        if ((lane & 30) == 0) sm.ent[u][rg * 4 + 2 * (lane & 1) + rsel] = sx * ksc; }
#define LDV(P) __hip_atomic_load(P, __ATOMIC_RELAXED, __HIP_MEMORY_SCOPE_WORKGROUP)
#define RDYWAIT(SL, SEQ) { while (lds_ld(&sm.rdy[SL]) != (SEQ)) SLEEP(1); CBAR(); }
#define LOADZ() { _Pragma("unroll") for (int p = 0; p < 2; p++) kz4[p] = sm.kz[u % NIN][rg * 4 + 2 * p + rsel].x; ksc = sm.kz[u % NIN][rg * 4 + 2 * (lane & 1) + rsel].y * scale; }
#define LOADQ() { _Pragma("unroll") for (int i = 0; i < 3; i++) { Qp[i] = sm.q[u % NIN][3 * sub + i]; Qn[i] = __builtin_amdgcn_alignbit(Qp[i], Qp[i], 16) ^ 0x80000000u; } }
#define FLUSH(ISV) { \
            mg = __builtin_amdgcn_readfirstlane(mg); \
            while (u >= mg + NOUT) { SLEEP(1); mg = lds_ld(&sm.merged); } \
            CBAR(); \
            if (ISV) { const unsigned aa = lds_addr(&sm.acc[u % NOUT][lane]); \
                asm volatile("ds_add_f64 %0, %1\n\tds_add_f64 %0, %2 offset:512\n\tds_add_f64 %0, %3 offset:1024" :: "v"(aa), "v"(acc[0]), "v"(acc[1]), "v"(acc[2]) : "memory"); \
                acc[0] = 0.0; acc[1] = 0.0; acc[2] = 0.0; } \
            CBAR(); \
            LATEF() \
            LDS_ADD(&sm.cnt[u % NOUT], np) \
            CBAR(); \
            STAMP_UNIT(np) \
            np = 0; }
#define ADVANCE() { u = gu; rg = nrg; }

#define REQ0(N, S) { tg = (ts + TOF) * 128 + (wave + 8 * (N)) * P0; \
            const char* kb = kbase + (((N) >> 2) * (GSZ * SKV) + ROWOF(RG0((N) & 3))) * (DH * 4); GATE("s"(kb)) ISSUE(S, 0, kb, koff, koffb) ISSUE((S) + 1, 1, kb, koff, koffb) STAMP(44 + (N)) }
#define USE0(N, S, W) { u = (N) >> 2; rg = RG0((N) & 3); K_TAB() LOADQ() LOADZ() K_PRE(0) K_PRE(1) WAITT(S, W); K_SUM(0) WAITT((S) + 1, W); K_SUM(1) K_RED2() }
#define VMW(N) asm volatile("s_waitcnt vmcnt(" #N ")" ::: "memory");
        REQ0(0, 0) CS_DMA(RG0(1))
        VMW(7) REPACK(RG0(0))
        STAMP(1)
        REQ0(1, 2)
        RDYWAIT(0, 1u)
        USE0(0, 0, 7)
        VMW(4) REPACK(RG0(1))
        REQ0(2, 0) CS_DMA(RG0(2))
        USE0(1, 2, 7)
        REQ0(3, 2)
        VMW(4) REPACK(RG0(2))
        USE0(2, 0, 4)
        REQ0(4, 0) CS_DMA(RG0(3))
        REQ0(5, 4)
        VMW(4) REPACK(RG0(3))
        USE0(3, 2, 4)
        CBAR();
        LDS_ADD(&sm.cnt[0], 4u)
        REQ0(6, 2)
        RDYWAIT(1, 2u)
        USE0(4, 0, 8)
        USE0(5, 4, 4)
        REQ0(7, 0)
        GRAB(1)
        USE0(6, 2, 4)
        rg = RG0(3); np = 3;
        GRABBED()
#pragma unroll 1
        while (u < NU) {
            const unsigned pr = __builtin_amdgcn_readfirstlane(par); par ^= 1;
            NEXT1()
            const unsigned tb_ = LDV(&sm.tabs), fl = LDV(&sm.rdy[u % NIN]);
            mg = LDV(&sm.merged);
            CBAR();
            K_TAB() LOADQ() LOADZ()
            NEXT2()
            if (__builtin_expect(__builtin_amdgcn_readfirstlane(fl) != u + 1 || !((__builtin_amdgcn_readfirstlane(tb_) >> rg) & 1u), 0)) {
                while (!((lds_ld(&sm.tabs) >> rg) & 1u)) SLEEP(1)
                RDYWAIT(u % NIN, u + 1)
                K_TAB() LOADQ() LOADZ()
            }
            K_PRE(0) K_PRE(1)
            if (early <= 0) GATE("v"(A[0]), "v"(A[1]), "v"(oa), "v"(ob), "s"(nb), "s"(pr))
            WAITISSUE(0, DRAW, "v"(A[0]));
            LATE0()
            K_SUM(0)
            WAITISSUE(1, "", "v"(A[1]));
            K_SUM(1)
            LATE1()
            gu = g >> 5;
            GRABBED()
            K_RED2()
            np++;
            if (chg) FLUSH(false)
            ADVANCE()
        }
#pragma unroll 1
        while (u < 2 * NU) {
            const unsigned pr = __builtin_amdgcn_readfirstlane(par); par ^= 1;
            NEXT1()
            mg = LDV(&sm.merged);
            CBAR();
            v4i pw = *(const v4i*)&sm.ent[u - NU][rg * 4];
            NEXT2()
            if (__builtin_expect(__builtin_amdgcn_readfirstlane(mg) + NU <= u, 0)) { while (lds_ld(&sm.merged) + NU <= u) SLEEP(1); CBAR(); pw = *(const v4i*)&sm.ent[u - NU][rg * 4]; }
            const double w0 = pw.x, w1 = pw.y, w2 = pw.z, w3 = pw.w;
            if (none) { LASTT(0); } else { if (early <= 0) GATE("s"(pr), "v"(w0), "v"(w1), "v"(w2), "v"(w3), "v"(oa), "v"(ob), "s"(nb)) WAITISSUE(0, DRAW, "v"(w0)); }
            LATE0()
#pragma unroll
            for (int i = 0; i < 3; i++) acc[i] = fma(w1, (double)tb[i], fma(w0, (double)ta[i], acc[i]));
            if (none) { LASTT(1); } else WAITISSUE(1, "", "v"(w2));
#pragma unroll
            for (int i = 0; i < 3; i++) acc[i] = fma(w3, (double)tb[i], fma(w2, (double)ta[i], acc[i]));
            LATE1()
            gu = g >> 5;
            GRABBED()
            np++;
            if (chg) FLUSH(true)
            ADVANCE()
        }
        STAMP(2)
        {
            const int head = blockIdx.x;
            while (lds_ld(&sm.fw_rdy) == 0) SLEEP(2)
            CBAR();
            const unsigned tt = lds_ld(&sm.tag_lo) << 18;
            const float wh = sm.fw[wave + 8 * rsel];
            const unsigned ro = (wave + 8 * rsel) * 512 + sub * 16;
            const char* rb = (const char*)ws + WS_PART + head * (NSPLIT * 512);
            v4f* pb = (v4f*)((char*)&sm.cs[0][0] + 16384);
            __hip_bfloat16* o = out + (size_t)head * DH + 6 * sub + 3 * rsel;
            tg = __builtin_amdgcn_readfirstlane(TU(2 * NU).x) + (head >= N1 ? (120) : (120)) * 128;
            GATE("s"(tt), "v"(wh), "v"(ro), "s"(rb), "v"(o))
            STAMP(60)
            STAMPV(57, NOW() - (((NOW() * 128 - __builtin_amdgcn_readfirstlane(TU(2 * NU).x)) >> 7) & 0xfffff))
            v4u R;
#define PLD(A) "global_load_dwordx4 a[" #A ":" #A "+3], %6, %7 sc1\n\t"
#define PCK(A, N, L) "s_waitcnt vmcnt(" #N ")\n\tv_accvgpr_read_b32 %1, a[" #A "+1]\n\tv_accvgpr_read_b32 %3, a[" #A "+3]\n\tv_accvgpr_read_b32 %0, a[" #A "]\n\tv_accvgpr_read_b32 %2, a[" #A "+2]\n\t" \
                     "v_xor_b32 %1, %8, %1\n\tv_xor_b32 %3, %8, %3\n\tv_or_b32 %4, %1, %3\n\tv_cmp_gt_u32 vcc, %9, %4\n\ts_cmp_eq_u64 vcc, exec\n\ts_cbranch_scc1 " L "\n\t"
            { unsigned t_;
              asm volatile("s_cmp_ge_u32 %10, %12\n\ts_cbranch_scc1 1f\n\t" PLD(16) PCK(16, 0, "3f") "1:\n\t" PLD(16) "s_sleep %11\n\t" PLD(20) "s_sleep %11\n\t" PLD(24)
                           "2:\n\t" PCK(16, 2, "3f") PLD(16) PCK(20, 2, "3f") PLD(20) PCK(24, 2, "3f") PLD(24)
                           "v_add_co_u32 %5, vcc, 64, %5\n\ts_cbranch_vccz 2b\n\ts_endpgm\n3:"
                           : "=&v"(R.x), "=&v"(R.y), "=&v"(R.z), "=&v"(R.w), "=&v"(t_), "+v"(spins) : "v"(ro), "s"(rb), "s"(tt), "s"(0x40000u), "s"(head), "i"(PSL), "i"(N1) : "scc", "vcc", "memory"); }
            STAMP(61)
            const float s0 = ldexpf(wh, (int)(R.y >> 10) - 139), s1 = ldexpf(wh, (int)(R.w >> 10) - 139);
            const float f[6] = {s0 * (float)(int)__builtin_amdgcn_sbfe(R.x, 0, 14), s0 * (float)(int)__builtin_amdgcn_sbfe(R.x, 14, 14), s0 * (float)(int)__builtin_amdgcn_sbfe(__builtin_amdgcn_alignbit(R.y, R.x, 28), 0, 14),
                                s1 * (float)(int)__builtin_amdgcn_sbfe(R.z, 0, 14), s1 * (float)(int)__builtin_amdgcn_sbfe(R.z, 14, 14), s1 * (float)(int)__builtin_amdgcn_sbfe(__builtin_amdgcn_alignbit(R.w, R.z, 28), 0, 14)};
            float sv[3];
#pragma unroll
            for (int i = 0; i < 3; i++) {
                const auto sw = __builtin_amdgcn_permlane32_swap(__float_as_uint(f[i]), __float_as_uint(f[i + 3]), false, false);
                sv[i] = __uint_as_float(sw[0]) + __uint_as_float(sw[1]);
            }
            pb[wave * 64 + lane] = (v4f){sv[0], sv[1], sv[2], 0.f};
            CBAR();
            { u64 ex_; asm volatile("s_mov_b64 %0, exec\n\ts_mov_b64 exec, 1\n\tds_add_rtn_u32 a15, %1, a13\n\ts_mov_b64 exec, %0" : "=&s"(ex_) : "v"(grab_adr + 12) : "memory"); }
            v4f ps[NSW];
#pragma unroll
            for (int w = 0; w < NSW; w++) ps[w] = pb[w * 64 + lane];
            GRABBED()
            if (g == NSW - 1) {
                const v4f sum = ((ps[0] + ps[1]) + (ps[2] + ps[3])) + ((ps[4] + ps[5]) + (ps[6] + ps[7]));
                o[0] = __float2bfloat16(sum.x); o[1] = __float2bfloat16(sum.y); o[2] = __float2bfloat16(sum.z);
            }
        }
        STAMP(62)
        STAMP_DUMP()
        return;
    }

    if (wave == NSW) {
        STAMPV(24, (unsigned)__builtin_amdgcn_s_memtime()) STAMP(25)
    } else if (wave == NSW + 1) {
        const u64 E = __hip_atomic_load((const u64*)(ws + WS_EPOCH), __ATOMIC_RELAXED, __HIP_MEMORY_SCOPE_AGENT);
        const int NB = (4), CAP = (45);
        unsigned lU = 2, lT = (ts + TOF) * 128 + 64 * P0; int lper = PK, dl = 0;
#define OWN(U) (dl * max(NB + 2 - (int)(U), 0) * 32)
#define NEWLINE() { if (lane >= lU && lane <= 2 * NU) sm.tu[lane] = (v2u){lT + (lane - lU) * 32 * lper + (max(lane - NU, 0) - max((int)lU - NU, 0)) * 32 * (PV - PK) + OWN(lane), \
                                                                          (unsigned)(lper + (lane >= NU ? PV - PK : 0) - (lane < NB + 2 ? dl : 0))}; }
        NEWLINE()
        if (lane == 0) sm.tu[1] = (v2u){lT - 32 * P0, (unsigned)P0};
        const u64 E1 = E + 1;
        const unsigned rtag = (unsigned)E1 * 0x9e3779b1u;
        const bool boss = blockIdx.x == 0;
        u64* const slot = (u64*)(ws + WS_REF + (blockIdx.x & 63) * LSTRIDE + (blockIdx.x >> 6) * 16);
        const u64* const slots = (const u64*)(ws + WS_REF + lane * LSTRIDE);
        u64* const dec = (u64*)(ws + WS_DEC);
#define GLDS(P) __hip_atomic_load(P, __ATOMIC_RELAXED, __HIP_MEMORY_SCOPE_AGENT)
#define GSTS(P, V) __hip_atomic_store(P, V, __ATOMIC_RELAXED, __HIP_MEMORY_SCOPE_AGENT)
#define UNTIL(T) while ((unsigned)((T) - NOW() * 128) < 0x40000u) SLEEP(2)
        if (lane < 2) GSTS(slot + lane, lane ? E1 : (u64)ts | ((u64)rtag << 32));
        if (lane == 0) { sm.tag_lo = (unsigned)E1; sm.tag_hi = (unsigned)(E1 >> 32); CBAR(); lds_st(&sm.tag_rdy, 1u); }
        const int K0 = (3), GAIN = (24), BK = (13), BV = (3), XLEAD = (250), XD1 = (100), XD2 = (15), XW = (40), RTH = (6), KI = (14), KD = (9);
        unsigned dv = 0;
        bool got = true;
#define DECISION(K) for (;;) { const u64 x = GLDS(dec + ((K) & 15)); dv = __builtin_amdgcn_readfirstlane((unsigned)x); got = __builtin_amdgcn_readfirstlane((unsigned)(x >> 32)) == rtag + (K); \
                               if (got || lds_ld(&sm.merged) >= 2 * NU - 1) break; STAMPV(41, sm.st[wave][41] + 1) SLEEP(32) }
        if (boss) {
            UNTIL((ts + (330)) * 128)
            for (;;) {
                bool ok = true; float sum = 0.f;
#pragma unroll
                for (int i = 0; i < 4; i++) { const u64 x = GLDS(slots + 2 * i), e = GLDS(slots + 2 * i + 1); ok &= e == E1 && (unsigned)(x >> 32) == rtag; sum += (float)min(max((int)((unsigned)x - ts), -1000), 1000); }
                if (__builtin_amdgcn_ballot_w64(ok) == ~0ull) { dv = ts + (int)(wave_sum(sum) * (1.f / 256)); break; }
                SLEEP(8)
            }
            if (lane == 0) { GSTS(dec, (u64)dv | ((u64)rtag << 32)); GSTS((u64*)(ws + WS_EPOCH), E1); }
        } else {
            UNTIL((ts + (330) + (300)) * 128)
            DECISION(0)
        }
        {
            const int d0 = (min(max((int)(dv - ts), -1000), 1000) + (0)) * 128;
            dl = -d0 / (32 * NB); lT += d0;
            if (got) NEWLINE()
        }
        STAMP(3)
        int sp = 0, sp1 = 0, jump = 0, tot1 = 0, tot2 = 0;
        dv = 0;
#pragma unroll 1
        for (unsigned k = K0; k < 2 * NU && got; k++) {
            const unsigned tk = lT + (k - lU) * 32 * lper;
            const unsigned tx = tk - XLEAD * 128;
            if (k + 1 < 2 * NU) {
                UNTIL(tx - XW * 128)
                int sum = 0, n = 0;
                unsigned now;
                asm volatile("s_setprio 3");
                do {
                    u64 t_; unsigned p;
                    asm volatile("ds_read_b32 %1, %2\n\ts_memrealtime %0\n\ts_waitcnt lgkmcnt(0)" : "=s"(t_), "=v"(p) : "v"(lds_addr(&sm.grab)) : "memory");
                    p = __builtin_amdgcn_readfirstlane(p) - 9;
                    now = (unsigned)t_ * 128;
                    const v2u e = TU(p >> 5);
                    sum += (int)(now - __builtin_amdgcn_readfirstlane(e.x) - (p & 31) * __builtin_amdgcn_readfirstlane(e.y)) >> 7; n++;
                } while ((unsigned)(tx - now) < 0x40000u);
                asm volatile("s_setprio 0");
                const int l = (int)((float)sum / (float)n) - BK;
                if (l > RTH && lane == 0) GSTS((unsigned*)(ws + WS_REF + (blockIdx.x & 63) * LSTRIDE + 64) + (blockIdx.x >> 6), (unsigned)min(l, 1000) | ((rtag + k) << 10));
            }
            if (!boss && k > K0) {
                UNTIL(tk - (170) * 128)
                const unsigned tl = NOW();
                DECISION(k)
                STAMPV(42 + (k >> 1), NOW() - tl)
                if (!got) break;
            }
            sp1 = sp;
            jump = (dv >> 18 & 1023u) * 128; sp = dv >> 6 & 4095u;
            lper = PK + (dv & 63u);
            const int pk = lper + (k >= NU ? PV - PK : 0);
            lU = k + 1; lT = tk + jump + 32 * (pk + sp);
            if (lane == 0) sm.tu[k] = (v2u){tk + jump + OWN(k), (unsigned)(pk + sp - (k < NB + 2 ? dl : 0))};
            NEWLINE()
            STAMPV(40, max((int)sm.st[wave][40], (int)(NOW() * 128 - tk) / 128 + 1000))
            if (boss && k + 1 < 2 * NU) {
                UNTIL(tx + XD1 * 128)
                const unsigned tl = NOW();
                const v4u x = __builtin_amdgcn_raw_buffer_load_b128(__builtin_amdgcn_make_buffer_rsrc(ws + WS_REF + 64, 0, 64 * LSTRIDE, 0x00020000), lane * LSTRIDE, 0, SC1);
                float a[4];
#pragma unroll
                for (int i = 0; i < 4; i++) a[i] = ((x[i] ^ ((rtag + k) << 10)) >> 10) == 0 ? (float)(x[i] & 1023u) : 0.f;
                asm volatile("" :: "v"(a[0]));
                STAMPV(42 + (k >> 1), NOW() - tl)
                const float mn = -wave_max(-fminf(fminf(a[0], a[1]), fminf(a[2], a[3]))), mx = wave_max(fmaxf(fmaxf(a[0], a[1]), fmaxf(a[2], a[3])));
                const float c = mn + (float)CAP;
                const int tot = (int)wave_sum(a[0] + a[1] + a[2] + a[3]) / 2, cut = (int)wave_sum(fminf(a[0], c) + fminf(a[1], c) + fminf(a[2], c) + fminf(a[3], c)) / 2;
                const int pend = jump + sp * 32 + sp1 * 32 * XLEAD * 4 / (pk + sp1);
                const int nj = GAIN ? max((int)mn * 128 - pend, 0) : 0;
                const int ns = max(max(min((int)mn * 128 + (tot - (int)mn * 128 - (k < (unsigned)(22) ? (0) * 32 : 0)) * GAIN / 16 - pend - nj, (cut - (int)mn * 128) * GAIN / 16), 0), mx > (float)(XD2 * (2 * NU - (int)k)) ? BV * 128 : 0);
                int dp = lper - PK;
                if (k >= (unsigned)KD && tot >= KI * 32 && tot > tot1 && tot < tot1 + 8 * 128 && tot1 < tot2 + 8 * 128) dp = min(dp + (2), 40);
                if (tot + tot1 < 64 && (1)) dp = max(dp - 1, 0);
                tot2 = tot1; tot1 = tot;
                dv = ((nj >> 7) << 18) | ((unsigned)min(ns >> 5, 4000) << 6) | dp;
                if (lane == 0) GSTS(dec + ((k + 1) & 15), (u64)dv | ((u64)(rtag + k + 1) << 32));
                STAMPV(5 + k, (min(tot >> 5, 4095) << 20) | (min((nj + ns) >> 5, 1023) << 10) | (lper - PK + 100))
            }
        }
    } else if (wave == NSW + 2) {
        const int ql = lane < 48 ? lane : 47;
        unsigned c1, c2, s1, s2;
#define QT_LOADS() { int qo_ = ql; asm volatile("" : "+v"(qo_)); \
                     c1 = ((const unsigned*)q_cos)[qo_]; c2 = ((const unsigned*)q_cos)[48 + qo_]; s1 = ((const unsigned*)q_sin)[qo_]; s2 = ((const unsigned*)q_sin)[48 + qo_]; }
        unsigned q1, q2, lkz, lks, lvz, lvs;
#define UNIT_LOADS(HEAD) { \
        q1 = ((const unsigned*)(q + (size_t)(HEAD) * DH))[ql]; q2 = ((const unsigned*)(q + (size_t)(HEAD) * DH))[48 + ql]; \
        const size_t so = ((size_t)(HEAD) * SKV + brow0) / 2 + (lane >> 3) * 128 + (lane & 7); \
        lkz = SLD((const unsigned*)k_zeros + so); lks = SLD((const unsigned*)k_scales + so); \
        lvz = SLD((const unsigned*)v_zeros + so); lvs = SLD((const unsigned*)v_scales + so); }
#define UNIT_FILL(SL, U) { \
            v4f qq; \
            qq.x = bfr(bfr(bflo(q1) * bflo(c1)) + bfr(-bflo(q2) * bflo(s1))); \
            qq.y = bfr(bfr(bflo(q2) * bflo(c2)) + bfr(bflo(q1) * bflo(s2))); \
            qq.z = bfr(bfr(bfhi(q1) * bfhi(c1)) + bfr(-bfhi(q2) * bfhi(s1))); \
            qq.w = bfr(bfr(bfhi(q2) * bfhi(c2)) + bfr(bfhi(q1) * bfhi(s2))); \
            *(v2u*)&sm.q[SL][2 * ql] = (v2u){(__float_as_uint(qq.x) >> 16) | __float_as_uint(qq.y), (__float_as_uint(qq.z) >> 16) | __float_as_uint(qq.w)}; \
            *(v4f*)&sm.kz[SL][2 * lane] = (v4f){bflo(lkz), bflo(lks), bfhi(lkz), bfhi(lks)}; \
            *(v4f*)&sm.vz[SL][2 * lane] = (v4f){bflo(lvz), bflo(lvs), bfhi(lvz), bfhi(lvs)}; \
            CBAR(); \
            if (lane == 0) lds_st(&sm.rdy[SL], (U) + 1); \
            CBAR(); }
        asm volatile("s_setprio 3");
        QT_LOADS()
#pragma unroll 1
        for (unsigned u = 0; u < NU; u++) {
            UNIT_LOADS(rank + GSZ * u)
            while (u >= lds_ld(&sm.merged) + NIN) SLEEP(4);
            CBAR();
            UNIT_FILL(u % NIN, u)
            if (u < 3) STAMP(5 + u)
            if (u == 1) asm volatile("s_setprio 0");
        }
        while (lds_ld(&sm.merged) < NU) SLEEP(16)
        CBAR();
        const unsigned T = lds_ld(&sm.tag_lo);
        if (lane < NU) __hip_atomic_store((u64*)(ws + WS_LOG + (rank * NSPLIT + split) * LSTRIDE2) + lane, (u64)__float_as_uint(sm.ent[lane][130]) | ((u64)T << 32), __ATOMIC_RELAXED, __HIP_MEMORY_SCOPE_AGENT);
        while (lds_ld(&sm.merged) < NU + 3) SLEEP(16)
        for (;;) {
            const u64 x = __hip_atomic_load((const u64*)(ws + WS_LOG + ((blockIdx.x & 15) * NSPLIT + (lane & 15)) * LSTRIDE2) + (blockIdx.x >> 4), __ATOMIC_RELAXED, __HIP_MEMORY_SCOPE_AGENT);
            if (__builtin_amdgcn_ballot_w64((unsigned)(x >> 32) == T) == ~0ull) {
                const float L = __uint_as_float((unsigned)x);
                const float w = __builtin_amdgcn_exp2f(L - row_max(L));
                sm.fw[lane & 15] = w / row_sum(w);
                CBAR();
                if (lane == 0) lds_st(&sm.fw_rdy, 1u);
                break;
            }
            SLEEP(64)
        }
    } else {
        while (lds_ld(&sm.tag_rdy) == 0) SLEEP(1);
        CBAR();
        const unsigned T = lds_ld(&sm.tag_lo);
        const __amdgpu_buffer_rsrc_t rsrc = __builtin_amdgcn_make_buffer_rsrc(ws + WS_PART + (rank * NSPLIT + split) * 512, 0, NH * NSPLIT * 512, 0x00020000);
        v2u* recs = (v2u*)&sm.cs[0][0];
#pragma unroll
        for (int i = 0; i < NOUT * 3; i++) (&sm.acc[0][0])[i * 64 + lane] = 0.0;
        bool direct = false;
#define PUBLISH(N) { \
        v4u r[8]; \
        _Pragma("unroll") for (int k = 0; k < 8; k++) r[k] = ((const v4u*)recs)[k * 64 + lane]; \
        _Pragma("unroll") for (int k = 0; k < 8; k++) __builtin_amdgcn_raw_buffer_store_b128(r[k], rsrc, 2 * k + (lane >> 5) < (N) ? po : 0x7ffffff0u, k * (2 * GSZ * NSPLIT * 512), SC1); STAMP(4) }
        const unsigned po = (lane >> 5) * (GSZ * NSPLIT * 512) + (lane & 31) * 16;
#pragma unroll 1
        for (unsigned u = 0; u < 2 * NU; u++) {
            const unsigned e = u % NU, so = u % NOUT;
            const v4f h = *(const v4f*)&sm.ent[e][128];
            for (;;) {
                if (!direct && u >= 2 * NU - 3 && (int)(NOW() * 128 - __builtin_amdgcn_readfirstlane(TU(2 * NU).x)) >= (30) * 128) { direct = true; wave_sync(); PUBLISH(u - NU) }
                if (lds_ld(&sm.cnt[so]) == 32) break;
                if (direct) SLEEP(0) else SLEEP(1)
            }
            CBAR();
            STAMP(5)
            if (!direct && u == 2 * NU - 1) { direct = true; wave_sync(); PUBLISH(NU - 1) }
            if (u < NU) {
                const float2 lg = *(const float2*)&sm.ent[e][2 * lane];
                const v4f zs = *(const v4f*)&sm.vz[u % NIN][2 * lane];
                const float m = wave_max(fmaxf(lg.x, lg.y));
                const float p0 = __expf(lg.x - m), p1 = __expf(lg.y - m);
                const float x0 = p0 * zs.y, x1 = p1 * zs.w;
                const unsigned eb = max(__float_as_uint(wave_max(fmaxf(fabsf(x0), fabsf(x1)))) >> 23, 21u);
                const float f = __uint_as_float((275u - eb) << 23);
                const float w0 = rintf(x0 * f), w1 = rintf(x1 * f);
                const float den = wave_sum(p0 + p1);
                const float bz = wave_sum(fmaf(w0, zs.x, w1 * zs.z));
                const float L = fmaf(m, 1.4426950408889634f, __builtin_amdgcn_logf(den));
                *(v2u*)&sm.ent[e][2 * lane] = (v2u){(unsigned)(int)w0, (unsigned)(int)w1};
                if (lane == 0) *(v4f*)&sm.ent[e][128] = (v4f){ldexpf(1.f / den, (int)eb - 148), bz, L, 0.f};
            } else {
                const double a0 = sm.acc[so][lane], a1 = sm.acc[so][64 + lane], a2 = sm.acc[so][128 + lane];
                const float n0 = (float)(a0 - (double)h.y) * h.x, n1 = (float)(a1 - (double)h.y) * h.x, n2 = (float)(a2 - (double)h.y) * h.x;
                const unsigned e2 = max(__float_as_uint(fmaxf(fmaxf(fabsf(n0), fabsf(n1)), fabsf(n2))) >> 23, 12u);
                const float f2 = __uint_as_float((266u - e2) << 23);
                const unsigned m0 = min(max((int)rintf(n0 * f2), -8191), 8191) & 0x3fff, m1 = min(max((int)rintf(n1 * f2), -8191), 8191) & 0x3fff, m2 = min(max((int)rintf(n2 * f2), -8191), 8191) & 0x3fff;
                const v2u r = {m0 | (m1 << 14) | (m2 << 28), (m2 >> 4) | (e2 << 10) | (T << 18)};
                if (direct) {
                    STAMP(6)
                    const v4u r2 = {r.x, r.y, (unsigned)__builtin_amdgcn_update_dpp(0, (int)r.x, 0xf5, 0xf, 0xf, true), (unsigned)__builtin_amdgcn_update_dpp(0, (int)r.y, 0xf5, 0xf, 0xf, true)};
                    __builtin_amdgcn_raw_buffer_store_b128(r2, rsrc, (lane & 1) ? 0x7ffffff0u : lane * 8, e * (GSZ * NSPLIT * 512), SC1);
                }
                else recs[e * 64 + lane] = r;
                CBAR();
                sm.acc[so][lane] = 0.0; sm.acc[so][64 + lane] = 0.0; sm.acc[so][128 + lane] = 0.0;
            }
            CBAR();
            if (lane == 0) { lds_st(&sm.cnt[so], 0u); CBAR(); lds_st(&sm.merged, u + 1); }
        }
        STAMP(2)
        STAMP_DUMP()
        return;
    }

    STAMP(2)
    STAMP_DUMP()
}

extern "C" void kernel_launch(void* const* d_in, const int* in_sizes, int n_in,
                              void* d_out, int out_size, void* d_ws, size_t ws_size,
                              hipStream_t stream) {
    const unsigned short* q = (const unsigned short*)d_in[0];
    const int* qk = (const int*)d_in[1];
    const unsigned short* k_scales = (const unsigned short*)d_in[2];
    const unsigned short* k_zeros = (const unsigned short*)d_in[3];
    const int* qv = (const int*)d_in[4];
    const unsigned short* v_scales = (const unsigned short*)d_in[5];
    const unsigned short* v_zeros = (const unsigned short*)d_in[6];
    const unsigned short* q_cos = (const unsigned short*)d_in[7];
    const unsigned short* q_sin = (const unsigned short*)d_in[8];
    const unsigned short* k_cos = (const unsigned short*)d_in[9];
    const unsigned short* k_sin = (const unsigned short*)d_in[10];
    unsigned char* ws = (unsigned char*)d_ws;

    attn_kernel<<<NSPLIT * GSZ, NTHREADS, 0, stream>>>(q, qk, k_scales, k_zeros, qv, v_scales, v_zeros,
                                                       q_cos, q_sin, k_cos, k_sin, (__hip_bfloat16*)d_out, ws);
}
```

```cpp
#include <hip/hip_runtime.h>
#include <hip/hip_bf16.h>
#include <stdint.h>

#define DH 192
#define HALF 96
#define SKV 2048
#define NH 256
#define NSW 8
#define NWAVES (NSW + 4)
#define NTHREADS (64 * NWAVES)
#define BROWS 128
#define NSPLIT (SKV / BROWS)
#define GSZ 16
#define NU (NH / GSZ)
#define NPC (2 * NU * 32)
#define N1 (NH - GSZ)
#define NIN 4
#define NOUT 4
#define ENTW 132
#define SC1 16
#define LIKELY 1
#define SLD(P) (*(P))
#define LSTRIDE 4352
#define PUBP 2
#define WSL 7
#define PSL 5
#define PHALF 9
#define PHI 0x0235712457024671ull
#define KAP 0x0202020220202020ull
#define PHIV PHI
#define KAPV KAP
#define PRIO 1
#define PRIO0 "s_setprio 0\n"
#define PRIO1 "\n\ts_setprio 2"
#define GTH 10
#define GS1 2
#define PFL 157

#define WS_EPOCH 0
#define WS_DEC 8192
#define WS_REF 16384
#define WS_LOG 524288
#define LSTRIDE2 1408
#define WS_PART 1048576
#define WS_FIN (WS_PART + NH * NSPLIT * 512)

typedef unsigned int v2u __attribute__((ext_vector_type(2)));
typedef unsigned int v3u __attribute__((ext_vector_type(3)));
typedef unsigned int v4u __attribute__((ext_vector_type(4)));
typedef int v4i __attribute__((ext_vector_type(4)));
typedef float v2f __attribute__((ext_vector_type(2)));
typedef float v4f __attribute__((ext_vector_type(4)));
typedef unsigned long long u64;

__device__ __forceinline__ float bflo(unsigned u) { return __uint_as_float(u << 16); }
__device__ __forceinline__ float bfhi(unsigned u) { return __uint_as_float(u & 0xffff0000u); }
__device__ __forceinline__ float bfr(float x) {
    __bf16 b = (__bf16)x;
    return __uint_as_float(((unsigned)__builtin_bit_cast(unsigned short, b)) << 16);
}

#define SLEEP(n) SLEEP_(n)
#define SLEEP_(n) asm volatile("s_sleep " #n "\n\tv_add_co_u32 %0, vcc, 1, %0\n\ts_cbranch_vccz 1f\n\ts_endpgm\n1:" : "+v"(spins) :: "vcc", "memory");
#define SPIN0 0xf0000000u
#define CBAR() asm volatile("" ::: "memory")

__device__ __forceinline__ void wave_sync() {
    __builtin_amdgcn_fence(__ATOMIC_RELEASE, "wavefront");
    __builtin_amdgcn_wave_barrier();
    __builtin_amdgcn_fence(__ATOMIC_ACQUIRE, "wavefront");
}
template <int CTRL>
__device__ __forceinline__ float dpp_add(float x) {
    int y = __builtin_amdgcn_update_dpp(0, __float_as_int(x), CTRL, 0xf, 0xf, true);
    return x + __int_as_float(y);
}
template <int CTRL>
__device__ __forceinline__ float dpp_max(float x) {
    int y = __builtin_amdgcn_update_dpp(__float_as_int(x), __float_as_int(x), CTRL, 0xf, 0xf, false);
    return fmaxf(x, __int_as_float(y));
}
__device__ __forceinline__ float row_sum(float x) { x = dpp_add<0x128>(x); x = dpp_add<0x124>(x); x = dpp_add<0x122>(x); return dpp_add<0x121>(x); }
__device__ __forceinline__ float row_max(float m) { m = dpp_max<0x128>(m); m = dpp_max<0x124>(m); m = dpp_max<0x122>(m); return dpp_max<0x121>(m); }
__device__ __forceinline__ float rdlane(float x, int l) { return __int_as_float(__builtin_amdgcn_readlane(__float_as_int(x), l)); }
__device__ __forceinline__ float wave_max(float m) { m = row_max(m); return fmaxf(fmaxf(rdlane(m, 0), rdlane(m, 16)), fmaxf(rdlane(m, 32), rdlane(m, 48))); }
__device__ __forceinline__ float wave_maxv(float m) {
    m = row_max(m);
    const auto a = __builtin_amdgcn_permlane16_swap(__float_as_uint(m), __float_as_uint(m), false, false);
    m = fmaxf(__uint_as_float(a[0]), __uint_as_float(a[1]));
    const auto b = __builtin_amdgcn_permlane32_swap(__float_as_uint(m), __float_as_uint(m), false, false);
    return fmaxf(__uint_as_float(b[0]), __uint_as_float(b[1]));
}
__device__ __forceinline__ float wave_sum(float x) { x = row_sum(x); return (rdlane(x, 0) + rdlane(x, 16)) + (rdlane(x, 32) + rdlane(x, 48)); }
__device__ __forceinline__ unsigned lds_ld(const unsigned* p) { return __builtin_amdgcn_readfirstlane(__hip_atomic_load(p, __ATOMIC_RELAXED, __HIP_MEMORY_SCOPE_WORKGROUP)); }
__device__ __forceinline__ unsigned lds_addr(const void* p) { return (unsigned)(size_t)(const __attribute__((address_space(3))) void*)p; }
#define DMA16(SRC, LDS) asm volatile("s_mov_b32 %0, m0\n\ts_mov_b32 m0, %2\n\ts_nop 0\n\tglobal_load_lds_dwordx4 %1, off\n\ts_mov_b32 m0, %0" : "=&s"(m0keep) : "v"(SRC), "s"(LDS) : "memory")
__device__ __forceinline__ void lds_st(unsigned* p, unsigned v) { __hip_atomic_store(p, v, __ATOMIC_RELAXED, __HIP_MEMORY_SCOPE_WORKGROUP); }
#define NOW() ((unsigned)__builtin_amdgcn_s_memrealtime())

struct __attribute__((aligned(16))) Smem {
    unsigned cs[32][768];
    double acc[NOUT][DH];
    float ent[NU][ENTW];
    unsigned q[NIN][HALF];
    float2 kz[NIN][BROWS];
    float2 vz[NIN][BROWS];
    v2u tu[2 * NU + 2];
    unsigned grab, merged, tabs, cdone;
    unsigned rdy[NIN], cnt[NOUT];
    unsigned tag_rdy, tag_lo, tag_hi, fw_rdy;
    int stat[32];
    float fw[NSPLIT];
};
#define NSTAT 0
#define STAMP(i) {}
#define STAMPV(i, v) {}
#define STAMP_DUMP()
#define STAMP_UNIT(N)

#define LT(I, V)
#define LTK(K, V)
#define TU(I) (*(const volatile v2u __attribute__((address_space(3)))*)(size_t)lds_addr(&sm.tu[I]))
#define LDS_ADD(P, V) { u64 ex_; asm volatile("s_mov_b64 %0, exec\n\ts_mov_b64 exec, 1\n\tds_add_u32 %1, %2\n\ts_mov_b64 exec, %0" : "=&s"(ex_) : "v"(lds_addr(P)), "v"(V) : "memory"); }
#define LDS_OR(P, V) { u64 ex_; asm volatile("s_mov_b64 %0, exec\n\ts_mov_b64 exec, 1\n\tds_or_b32 %1, %2\n\ts_mov_b64 exec, %0" : "=&s"(ex_) : "v"(lds_addr(P)), "v"(V) : "memory"); }

__global__ __launch_bounds__(NTHREADS, 3) void attn_kernel(
    const unsigned short* __restrict__ q, const int* __restrict__ qk,
    const unsigned short* __restrict__ k_scales, const unsigned short* __restrict__ k_zeros,
    const int* __restrict__ qv,
    const unsigned short* __restrict__ v_scales, const unsigned short* __restrict__ v_zeros,
    const unsigned short* __restrict__ q_cos, const unsigned short* __restrict__ q_sin,
    const unsigned short* __restrict__ k_cos, const unsigned short* __restrict__ k_sin,
    __hip_bfloat16* __restrict__ out, unsigned char* __restrict__ ws)
{
    __shared__ Smem sm;
    const int tid = threadIdx.x;
    unsigned spins = SPIN0;
#define TOUCH(OFF, BASE) asm volatile("global_load_dword a6, %0, %1" :: "v"(OFF), "s"(BASE) : "a6", "memory");
    const unsigned cl = min((blockIdx.x & 24) * 8 + tid, PFL - 1u) * 128;
    if (tid < 64) { const char* pc0; asm volatile("s_getpc_b64 %0" : "=s"(pc0)); TOUCH(cl, pc0) }
    __builtin_amdgcn_sched_barrier(0);
    const int hw = __builtin_amdgcn_readfirstlane(tid >> 6);
    const int lane = tid & 63;
    const int split = (blockIdx.x & 3) * 4 + ((blockIdx.x >> 3) & 3);
    const int rank = (blockIdx.x >> 5) * 2 + ((blockIdx.x >> 2) & 1);
    const int brow0 = split * 16;
#define ROWOF(RG) (((RG) >> 2) * 256 + ((RG) & 3) * 4)
    if (hw == 0) {
        typedef const char* const __attribute__((address_space(4))) * kap_t;
        const char* sa = ((kap_t)__builtin_amdgcn_kernarg_segment_ptr())[(0x5623 >> (4 * (split & 3))) & 7];
        unsigned o1 = ((blockIdx.x >> 5) * 256 + brow0) * (DH * 2) + min(lane, 47) * 128;
        unsigned o2 = ((rank + GSZ * (lane >> 3)) * SKV + (lane & 7) * 256 + (brow0 & ~63)) * 2, o3 = o2 + 8 * GSZ * SKV * 2;
        unsigned o4 = (rank + GSZ * (lane >> 2)) * (DH * 2) + min(lane & 3, 2) * 128;
        asm volatile("" : "+v"(o1), "+v"(o2), "+v"(o3), "+v"(o4));
        TOUCH(o1, k_cos) TOUCH(o1, k_sin) TOUCH(o4, q) TOUCH(o2, sa) TOUCH(o3, sa)
    }
    if (hw == 1 && lane < 16 + NSTAT) (&sm.grab)[lane] = lane ? 0 : 64;
    asm volatile("" :: "s"(q), "s"(qk), "s"(k_scales), "s"(k_zeros), "s"(qv), "s"(v_scales), "s"(v_zeros), "s"(q_cos), "s"(q_sin), "s"(k_cos), "s"(k_sin), "s"(out), "s"(ws));
    asm volatile("s_waitcnt lgkmcnt(0)\n\ts_barrier" ::: "memory");
    const int wave = (int)(0x76543210b9a8ull >> (4 * hw)) & 15;
    STAMP(0)
    const unsigned ts = NOW();
    const int PK = (1266), PV = (1264), TOF = (100), P0 = (1370);
    if (LIKELY ? __builtin_expect(wave < NSW, 1) : wave < NSW) {
        const unsigned rot = 4 * ((unsigned)(PHI >> (60 - 4 * rank)) & 7u), kap = (unsigned)(KAP >> (60 - 4 * rank)) & 3u;
        const unsigned rotv = 4 * ((unsigned)(PHIV >> (60 - 4 * rank)) & 7u), kapv = (unsigned)(KAPV >> (60 - 4 * rank)) & 3u;
#define RGOF(G) ((G) >= NPC / 2 ? (((G) + rotv) & 31u) ^ kapv : (((G) + rot) & 31u) ^ kap)
        const char* cbase[3];
#pragma unroll
        for (int j = 0; j < 3; j++) { const int x = j * 64 + lane; cbase[j] = (x < 96 ? (const char*)k_cos + x * 16 : (const char*)k_sin + (x - 96) * 16) + brow0 * (DH * 2); }
        unsigned m0keep;
#define CS_DMA(RG) { _Pragma("unroll") for (int j = 0; j < 3; j++) DMA16(cbase[j] + ROWOF(RG) * (DH * 2), lds_addr(&sm.cs[0][0]) + (RG) * 3072 + j * 1024); }
#define REPACK(RG) { \
        unsigned* cw = &sm.cs[RG][0]; \
        const unsigned* l = cw + rsel * (DH / 2) + qoff; \
        unsigned r[2][6]; \
        _Pragma("unroll") \
        for (int j = 0; j < 2; j++) { \
            unsigned e[4][2]; \
            _Pragma("unroll") \
            for (int k = 0; k < 4; k++) { \
                const unsigned* a = l + j * DH + (k & 1) * 48 + (k >> 1) * 384;       \
                unsigned x0 = a[0], x1 = a[1]; \
                e[k][0] = __builtin_amdgcn_alignbit(x1, x0, sh); \
                e[k][1] = x1 >> sh; \
            } \
            r[j][0] = (e[0][0] & 0xffffu) | (e[3][0] << 16); \
            r[j][1] = (e[0][0] >> 16) | (e[3][0] & 0xffff0000u); \
            r[j][2] = (e[0][1] & 0xffffu) | (e[3][1] << 16); \
            r[j][3] = (e[1][0] & 0xffffu) | (e[2][0] << 16); \
            r[j][4] = (e[1][0] >> 16) | (e[2][0] & 0xffff0000u); \
            r[j][5] = (e[1][1] & 0xffffu) | (e[2][1] << 16); \
        } \
        wave_sync(); \
        _Pragma("unroll") \
        for (int j = 0; j < 2; j++) { *(v4u*)&cw[j * 256 + lane * 4] = (v4u){r[j][0], r[j][1], r[j][2], r[j][3]}; *(v2u*)&cw[512 + j * 128 + lane * 2] = (v2u){r[j][4], r[j][5]}; } \
        wave_sync(); \
        LDS_OR(&sm.tabs, 1u << (RG)) }
        const int sub = lane & 31;
        const int rsel = lane >> 5;
        const unsigned sh = (sub & 1) * 16;
        const int qoff = 3 * (sub >> 1) + (sub & 1);
        const char* kbase = (const char*)qk + (size_t)(rank * SKV + brow0) * (DH * 4);
        const char* vbase = (const char*)qv + (size_t)(rank * SKV + brow0) * (DH * 4);
        const unsigned koff = (rsel * DH + 3 * sub) * 4, voff = 3 * lane * 4;
#define AR(S) (16 + (S) * 8)
        asm volatile("" ::: "a16", "a17", "a18", "a19", "a20", "a21", "a22", "a23", "a24", "a25", "a26", "a27", "a28", "a29", "a30", "a31",
                            "a32", "a33", "a34", "a35", "a36", "a37", "a38", "a39", "a40", "a41", "a42", "a43", "a44", "a45", "a46", "a47");
        asm volatile("" ::: "a48", "a49", "a50", "a51", "a52", "a53", "a54", "a55", "a56", "a57", "a58", "a59", "a60", "a61", "a62", "a63");
        __builtin_amdgcn_s_waitcnt(0x0f70);
        int ta[3], tb[3];
#define GLD(A, OFF, BASE, IMM) asm volatile("global_load_dwordx3 a[%c0:%c1], %2, %3 offset:%4 nt" :: "i"(A), "i"((A) + 2), "v"(OFF), "s"(BASE), "i"(IMM))
#define ISSUE(S, J, BASE, OA, OB) { GLD(AR(S), OA, BASE, (J) * 1536); GLD(AR(S) + 4, OB, BASE, (J) * 1536); }
#define WAITT(S, N) asm volatile("s_waitcnt vmcnt(%6)\n\tv_accvgpr_read_b32 %0, a[%c7]\n\tv_accvgpr_read_b32 %1, a[%c8]\n\tv_accvgpr_read_b32 %2, a[%c9]\n\t" \
                              "v_accvgpr_read_b32 %3, a[%c10]\n\tv_accvgpr_read_b32 %4, a[%c11]\n\tv_accvgpr_read_b32 %5, a[%c12]" \
                              : "=v"(ta[0]), "=v"(ta[1]), "=v"(ta[2]), "=v"(tb[0]), "=v"(tb[1]), "=v"(tb[2]) \
                              : "i"(N), "i"(AR(S)), "i"(AR(S) + 1), "i"(AR(S) + 2), "i"(AR(S) + 4), "i"(AR(S) + 5), "i"(AR(S) + 6))
        const unsigned koffb = koff + 384, voffb = voff + 768;
        unsigned u = 0, rg, np = 0;
#define RG0(C) RGOF((unsigned)(wave + 8 * (C)))
        CS_DMA(RG0(0))
        STAMP(52)
        STAMP(53)
        const float scale = 0.07216878364870322f;
        double acc[3] = {0.0, 0.0, 0.0};
        bool chg, none = false;
        unsigned Qp[3], Qn[3];
        float kz4[2], ksc, kres[2];
        v4u ca[2]; v2u cb[2];
        v2f qe[2][3], A[2];
        const char* nb; unsigned oa, ob;
        unsigned g, mg, nrg, tg, late = 0;
        u64 tis;
        unsigned dg, par = 0, gu;
        int early;
        const int bth = wave < (8) ? (20) * 128 : 0x3fffffff;
        const unsigned grab_adr = lds_addr(&sm.grab);
        asm volatile("v_accvgpr_write_b32 a13, 1\n\tv_accvgpr_write_b32 a14, 2" ::: "a13", "a14", "a15");
#define GRAB(N) { u64 ex_; asm volatile("s_mov_b64 %0, exec\n\ts_mov_b64 exec, 1\n\tds_add_rtn_u32 a15, %1, a%c2\n\ts_mov_b64 exec, %0" : "=&s"(ex_) : "v"(grab_adr), "i"(12 + (N)) : "memory"); }
#define GRABBED() { unsigned t_; asm volatile("s_waitcnt lgkmcnt(0)\n\tv_accvgpr_read_b32 %1, a15\n\ts_nop 0\n\tv_readfirstlane_b32 %0, %1\n\ts_nop 1" : "=s"(g), "=v"(t_)); }
#define NEXT1() const v2u x_ = TU(g >> 5); u64 tn_; asm volatile("s_memrealtime %0" : "=s"(tn_));
#define NEXT2() { \
            chg = (g >> 5) != u; none = g >= NPC; \
            const bool nv = g >= NPC / 2; \
            nrg = RGOF(g); \
            unsigned boff; asm("s_mul_i32 %0, %1, 0x300" : "=s"(boff) : "s"(((g >> 5) & (NU - 1)) * (GSZ * SKV) + ROWOF(nrg) + 2)); \
            nb = (nv ? vbase : kbase) + boff; \
            oa = nv ? voff : koff; ob = nv ? voffb : koffb; \
            asm volatile("s_waitcnt lgkmcnt(0)" : "+s"(tn_) : "s"(nb), "v"(oa), "v"(ob)); \
            tg = __builtin_amdgcn_readfirstlane(x_.x) + (g & 31) * __builtin_amdgcn_readfirstlane(x_.y); \
            early = none ? 0 : (int)((unsigned)tn_ * 128 - tg) - bth; \
            if (early > 0) asm volatile("s_cmp_lg_u32 %3, 0\n\ts_cbranch_scc1 1f\n\t" L4(32) "\ts_branch 2f\n1:\n\t" L4(16) "2:\n\ts_mov_b64 vcc, exec\n\ts_mov_b64 exec, 1\n\tds_add_rtn_u32 a15, %4, a13\n\ts_mov_b64 exec, vcc" \
                                    :: "v"(oa), "v"(ob), "s"(nb), "s"(pr), "v"(grab_adr) : "scc", "vcc", "memory"); }
#define L4(A) "global_load_dwordx3 a[" #A ":" #A "+2], %0, %2 offset:-1536 nt\n\tglobal_load_dwordx3 a[" #A "+4:" #A "+6], %1, %2 offset:-1536 nt\n\t" \
              "global_load_dwordx3 a[" #A "+8:" #A "+10], %0, %2 nt\n\tglobal_load_dwordx3 a[" #A "+12:" #A "+14], %1, %2 nt\n"
#define GATE(...) \
            asm volatile(PRIO0 "1:\n\ts_memrealtime vcc\n\ts_waitcnt lgkmcnt(0)\n\ts_lshl_b32 vcc_lo, vcc_lo, 7\n\ts_sub_u32 %0, %1, vcc_lo\n\ts_cmp_lt_u32 %0, 0x40000\n\ts_cbranch_scc0 2f\n\t" \
                         "s_cmp_lt_u32 %0, %2\n\ts_cbranch_scc1 1b\n\ts_sleep %3\n\ts_branch 1b\n2:" PRIO1 : "=&s"(dg) : "s"(tg), "i"(GTH * 128), "i"(GS1), __VA_ARGS__ : "scc", "vcc", "memory");
#define RD6(A) "v_accvgpr_read_b32 %0, a[%c6+" #A "]\n\tv_accvgpr_read_b32 %1, a[%c6+" #A "+1]\n\tv_accvgpr_read_b32 %2, a[%c6+" #A "+2]\n\t" \
               "v_accvgpr_read_b32 %3, a[%c6+" #A "+4]\n\tv_accvgpr_read_b32 %4, a[%c6+" #A "+5]\n\tv_accvgpr_read_b32 %5, a[%c6+" #A "+6]\n"
#define LD2(A) "global_load_dwordx3 a[%c6+" #A ":%c6+" #A "+2], %7, %9 offset:%10 nt\n\tglobal_load_dwordx3 a[%c6+" #A "+4:%c6+" #A "+6], %8, %9 offset:%10 nt"
#define OUT6 "=&v"(ta[0]), "=&v"(ta[1]), "=&v"(ta[2]), "=&v"(tb[0]), "=&v"(tb[1]), "=&v"(tb[2])
#define WI(A, B, TAIL) "s_cmp_gt_i32 %13, 0\n\ts_cbranch_scc1 3f\n\ts_waitcnt vmcnt(2)\n\t" LD2(A) TAIL "\n3:\n\ts_waitcnt vmcnt(%c14)\n\t" RD6(B)
#define WAITISSUE(S, TAIL, ...) asm volatile("s_cmp_lg_u32 %12, 0\n\ts_cbranch_scc1 1f\n\t" WI(16, 0, TAIL) "\ts_branch 2f\n1:\n\t" WI(0, 16, TAIL) "2:" \
                              : OUT6 : "i"(AR(S)), "v"(oa), "v"(ob), "s"(nb), "i"(((S) - 1) * 1536), "v"(grab_adr), "s"(pr), "s"(early), "i"(6 - 2 * (S)), __VA_ARGS__ : "scc", "vcc", "memory")
#define LASTT(S) asm volatile("s_waitcnt vmcnt(%c8)\n\ts_cmp_lg_u32 %7, 0\n\ts_cbranch_scc1 1f\n\t" RD6(0) "\ts_branch 2f\n1:\n\t" RD6(16) "2:" : OUT6 : "i"(AR(S)), "s"(pr), "i"(2 - 2 * (S)) : "scc")
#define DRAW "\n\ts_mov_b64 vcc, exec\n\ts_mov_b64 exec, 1\n\tds_add_rtn_u32 a15, %11, a13\n\ts_mov_b64 exec, vcc"
#define LATE0()
#define LATE1()
#define LATEF()
#define LATEF()
#define K_TAB() { _Pragma("unroll") for (int p = 0; p < 2; p++) { \
            const unsigned* cw = &sm.cs[rg][0]; \
            ca[p] = *(const v4u*)&cw[p * 256 + lane * 4]; cb[p] = *(const v2u*)&cw[512 + p * 128 + lane * 2]; } }
#define K_PRE(P) { \
        float e0, e1, e2, f0, f1, f2; \
        asm("v_dot2_f32_bf16 %0, %6, %12, 0\n\tv_dot2_f32_bf16 %3, %9, %15, 0\n\tv_dot2_f32_bf16 %1, %7, %13, 0\n\tv_dot2_f32_bf16 %4, %10, %16, 0\n\t" \
            "v_dot2_f32_bf16 %2, %8, %14, 0\n\tv_dot2_f32_bf16 %5, %11, %17, 0\n\ts_nop 2" \
            : "=&v"(e0), "=&v"(e1), "=&v"(e2), "=&v"(f0), "=&v"(f1), "=&v"(f2) \
            : "v"(Qp[0]), "v"(Qp[1]), "v"(Qp[2]), "v"(Qn[0]), "v"(Qn[1]), "v"(Qn[2]), \
              "v"(ca[P].x), "v"(ca[P].y), "v"(ca[P].z), "v"(ca[P].w), "v"(cb[P].x), "v"(cb[P].y)); \
        qe[P][0] = (v2f){e0, f0}; qe[P][1] = (v2f){e1, f1}; qe[P][2] = (v2f){e2, f2}; \
        A[P] = (qe[P][0] + qe[P][1] + qe[P][2]) * (v2f){-kz4[P], -kz4[P]}; }
#define K_SUM(P) { \
        _Pragma("unroll") \
        for (int i = 0; i < 3; i++) { v2f x = {(float)ta[i], (float)tb[i]}; A[P] = qe[P][i] * x + A[P]; } \
        kres[P] = A[P].x + A[P].y; }
#define K_RED2() { \
        const bool o1 = lane & 1; \
        float sx = (o1 ? kres[1] : kres[0]) + __int_as_float(__builtin_amdgcn_update_dpp(0, __float_as_int(o1 ? kres[0] : kres[1]), 0xb1, 0xf, 0xf, true)); \
        sx = dpp_add<0x4e>(sx); sx = dpp_add<0x124>(sx); sx = dpp_add<0x128>(sx); \
        const auto sw = __builtin_amdgcn_permlane16_swap(__float_as_uint(sx), __float_as_uint(sx), false, false); \
        sx = __uint_as_float(sw[0]) + __uint_as_float(sw[1]); \
        if ((lane & 30) == 0) sm.ent[u][rg * 4 + 2 * (lane & 1) + rsel] = sx * ksc; }
#define LDV(P) __hip_atomic_load(P, __ATOMIC_RELAXED, __HIP_MEMORY_SCOPE_WORKGROUP)
#define RDYWAIT(SL, SEQ) { while (lds_ld(&sm.rdy[SL]) != (SEQ)) SLEEP(1); CBAR(); }
#define LOADZ() { _Pragma("unroll") for (int p = 0; p < 2; p++) kz4[p] = sm.kz[u % NIN][rg * 4 + 2 * p + rsel].x; ksc = sm.kz[u % NIN][rg * 4 + 2 * (lane & 1) + rsel].y * scale; }
#define LOADQ() { _Pragma("unroll") for (int i = 0; i < 3; i++) { Qp[i] = sm.q[u % NIN][3 * sub + i]; Qn[i] = __builtin_amdgcn_alignbit(Qp[i], Qp[i], 16) ^ 0x80000000u; } }
#define FLUSH(ISV) { \
            mg = __builtin_amdgcn_readfirstlane(mg); \
            while (u >= mg + NOUT) { SLEEP(1); mg = lds_ld(&sm.merged); } \
            CBAR(); \
            if (ISV) { const unsigned aa = lds_addr(&sm.acc[u % NOUT][lane]); \
                asm volatile("ds_add_f64 %0, %1\n\tds_add_f64 %0, %2 offset:512\n\tds_add_f64 %0, %3 offset:1024" :: "v"(aa), "v"(acc[0]), "v"(acc[1]), "v"(acc[2]) : "memory"); \
                acc[0] = 0.0; acc[1] = 0.0; acc[2] = 0.0; } \
            CBAR(); \
            LATEF() \
            LDS_ADD(&sm.cnt[u % NOUT], np) \
            CBAR(); \
            STAMP_UNIT(np) \
            np = 0; }
#define ADVANCE() { u = gu; rg = nrg; }

#define REQ0(N, S) { tg = (ts + TOF) * 128 + (wave + 8 * (N)) * P0; \
            const char* kb = kbase + (((N) >> 2) * (GSZ * SKV) + ROWOF(RG0((N) & 3))) * (DH * 4); GATE("s"(kb)) ISSUE(S, 0, kb, koff, koffb) ISSUE((S) + 1, 1, kb, koff, koffb) STAMP(44 + (N)) }
#define USE0(N, S, W) { u = (N) >> 2; rg = RG0((N) & 3); K_TAB() LOADQ() LOADZ() K_PRE(0) K_PRE(1) WAITT(S, W); K_SUM(0) WAITT((S) + 1, W); K_SUM(1) K_RED2() }
#define VMW(N) asm volatile("s_waitcnt vmcnt(" #N ")" ::: "memory");
        REQ0(0, 0) CS_DMA(RG0(1))
        VMW(7) REPACK(RG0(0))
        STAMP(1)
        REQ0(1, 2)
        RDYWAIT(0, 1u)
        USE0(0, 0, 7)
        VMW(4) REPACK(RG0(1))
        REQ0(2, 0) CS_DMA(RG0(2))
        USE0(1, 2, 7)
        REQ0(3, 2)
        VMW(4) REPACK(RG0(2))
        USE0(2, 0, 4)
        REQ0(4, 0) CS_DMA(RG0(3))
        REQ0(5, 4)
        VMW(4) REPACK(RG0(3))
        USE0(3, 2, 4)
        CBAR();
        LDS_ADD(&sm.cnt[0], 4u)
        REQ0(6, 2)
        RDYWAIT(1, 2u)
        USE0(4, 0, 8)
        USE0(5, 4, 4)
        REQ0(7, 0)
        GRAB(1)
        USE0(6, 2, 4)
        rg = RG0(3); np = 3;
        GRABBED()
#pragma unroll 1
        while (u < NU) {
            const unsigned pr = __builtin_amdgcn_readfirstlane(par); par ^= 1;
            NEXT1()
            const unsigned tb_ = LDV(&sm.tabs), fl = LDV(&sm.rdy[u % NIN]);
            mg = LDV(&sm.merged);
            CBAR();
            K_TAB() LOADQ() LOADZ()
            NEXT2()
            if (__builtin_expect(__builtin_amdgcn_readfirstlane(fl) != u + 1 || !((__builtin_amdgcn_readfirstlane(tb_) >> rg) & 1u), 0)) {
                while (!((lds_ld(&sm.tabs) >> rg) & 1u)) SLEEP(1)
                RDYWAIT(u % NIN, u + 1)
                K_TAB() LOADQ() LOADZ()
            }
            K_PRE(0) K_PRE(1)
            if (early <= 0) GATE("v"(A[0]), "v"(A[1]), "v"(oa), "v"(ob), "s"(nb), "s"(pr))
            WAITISSUE(0, DRAW, "v"(A[0]));
            LATE0()
            K_SUM(0)
            WAITISSUE(1, "", "v"(A[1]));
            K_SUM(1)
            LATE1()
            gu = g >> 5;
            GRABBED()
            K_RED2()
            np++;
            if (chg) FLUSH(false)
            ADVANCE()
        }
#pragma unroll 1
        while (u < 2 * NU) {
            const unsigned pr = __builtin_amdgcn_readfirstlane(par); par ^= 1;
            NEXT1()
            mg = LDV(&sm.merged);
            CBAR();
            v4i pw = *(const v4i*)&sm.ent[u - NU][rg * 4];
            NEXT2()
            if (__builtin_expect(__builtin_amdgcn_readfirstlane(mg) + NU <= u, 0)) { while (lds_ld(&sm.merged) + NU <= u) SLEEP(1); CBAR(); pw = *(const v4i*)&sm.ent[u - NU][rg * 4]; }
            const double w0 = pw.x, w1 = pw.y, w2 = pw.z, w3 = pw.w;
            if (none) { LASTT(0); } else { if (early <= 0) GATE("s"(pr), "v"(w0), "v"(w1), "v"(w2), "v"(w3), "v"(oa), "v"(ob), "s"(nb)) WAITISSUE(0, DRAW, "v"(w0)); }
            LATE0()
#pragma unroll
            for (int i = 0; i < 3; i++) acc[i] = fma(w1, (double)tb[i], fma(w0, (double)ta[i], acc[i]));
            if (none) { LASTT(1); } else WAITISSUE(1, "", "v"(w2));
#pragma unroll
            for (int i = 0; i < 3; i++) acc[i] = fma(w3, (double)tb[i], fma(w2, (double)ta[i], acc[i]));
            LATE1()
            gu = g >> 5;
            GRABBED()
            np++;
            if (chg) FLUSH(true)
            ADVANCE()
        }
        STAMP(2)
        {
            const int head = blockIdx.x;
            while (lds_ld(&sm.fw_rdy) == 0) SLEEP(2)
            CBAR();
            const unsigned tt = lds_ld(&sm.tag_lo) << 18;
            const float wh = sm.fw[wave + 8 * rsel];
            const unsigned ro = (wave + 8 * rsel) * 512 + sub * 16;
            const char* rb = (const char*)ws + WS_PART + head * (NSPLIT * 512);
            v4f* pb = (v4f*)((char*)&sm.cs[0][0] + 16384);
            __hip_bfloat16* o = out + (size_t)head * DH + 6 * sub + 3 * rsel;
            tg = __builtin_amdgcn_readfirstlane(TU(2 * NU).x) + (head >= N1 ? (120) : (120)) * 128;
            GATE("s"(tt), "v"(wh), "v"(ro), "s"(rb), "v"(o))
            STAMP(60)
            STAMPV(57, NOW() - (((NOW() * 128 - __builtin_amdgcn_readfirstlane(TU(2 * NU).x)) >> 7) & 0xfffff))
            v4u R;
#define PLD(A) "global_load_dwordx4 a[" #A ":" #A "+3], %6, %7 sc1\n\t"
#define PCK(A, N, L) "s_waitcnt vmcnt(" #N ")\n\tv_accvgpr_read_b32 %1, a[" #A "+1]\n\tv_accvgpr_read_b32 %3, a[" #A "+3]\n\tv_accvgpr_read_b32 %0, a[" #A "]\n\tv_accvgpr_read_b32 %2, a[" #A "+2]\n\t" \
                     "v_xor_b32 %1, %8, %1\n\tv_xor_b32 %3, %8, %3\n\tv_or_b32 %4, %1, %3\n\tv_cmp_gt_u32 vcc, %9, %4\n\ts_cmp_eq_u64 vcc, exec\n\ts_cbranch_scc1 " L "\n\t"
            { unsigned t_;
              asm volatile("s_cmp_ge_u32 %10, %12\n\ts_cbranch_scc1 1f\n\t" PLD(16) PCK(16, 0, "3f") "1:\n\t" PLD(16) "s_sleep %11\n\t" PLD(20) "s_sleep %11\n\t" PLD(24)
                           "2:\n\t" PCK(16, 2, "3f") PLD(16) PCK(20, 2, "3f") PLD(20) PCK(24, 2, "3f") PLD(24)
                           "v_add_co_u32 %5, vcc, 64, %5\n\ts_cbranch_vccz 2b\n\ts_endpgm\n3:"
                           : "=&v"(R.x), "=&v"(R.y), "=&v"(R.z), "=&v"(R.w), "=&v"(t_), "+v"(spins) : "v"(ro), "s"(rb), "s"(tt), "s"(0x40000u), "s"(head), "i"(PSL), "i"(N1) : "scc", "vcc", "memory"); }
            STAMP(61)
            const float s0 = ldexpf(wh, (int)(R.y >> 10) - 139), s1 = ldexpf(wh, (int)(R.w >> 10) - 139);
            const float f[6] = {s0 * (float)(int)__builtin_amdgcn_sbfe(R.x, 0, 14), s0 * (float)(int)__builtin_amdgcn_sbfe(R.x, 14, 14), s0 * (float)(int)__builtin_amdgcn_sbfe(__builtin_amdgcn_alignbit(R.y, R.x, 28), 0, 14),
                                s1 * (float)(int)__builtin_amdgcn_sbfe(R.z, 0, 14), s1 * (float)(int)__builtin_amdgcn_sbfe(R.z, 14, 14), s1 * (float)(int)__builtin_amdgcn_sbfe(__builtin_amdgcn_alignbit(R.w, R.z, 28), 0, 14)};
            float sv[3];
#pragma unroll
            for (int i = 0; i < 3; i++) {
                const auto sw = __builtin_amdgcn_permlane32_swap(__float_as_uint(f[i]), __float_as_uint(f[i + 3]), false, false);
                sv[i] = __uint_as_float(sw[0]) + __uint_as_float(sw[1]);
            }
            pb[wave * 64 + lane] = (v4f){sv[0], sv[1], sv[2], 0.f};
            CBAR();
            { u64 ex_; asm volatile("s_mov_b64 %0, exec\n\ts_mov_b64 exec, 1\n\tds_add_rtn_u32 a15, %1, a13\n\ts_mov_b64 exec, %0" : "=&s"(ex_) : "v"(grab_adr + 12) : "memory"); }
            v4f ps[NSW];
#pragma unroll
            for (int w = 0; w < NSW; w++) ps[w] = pb[w * 64 + lane];
            GRABBED()
            if (g == NSW - 1) {
                const v4f sum = ((ps[0] + ps[1]) + (ps[2] + ps[3])) + ((ps[4] + ps[5]) + (ps[6] + ps[7]));
                o[0] = __float2bfloat16(sum.x); o[1] = __float2bfloat16(sum.y); o[2] = __float2bfloat16(sum.z);
                LT(1, NOW())
            }
        }
        STAMP(62)
        STAMP_DUMP()
        return;
    }

    if (wave == NSW) {
        STAMPV(24, (unsigned)__builtin_amdgcn_s_memtime()) STAMP(25)
    } else if (wave == NSW + 1) {
        u64 E; asm volatile("s_setprio 3\n\ts_load_dwordx2 %0, %1, 0\n\ts_waitcnt lgkmcnt(0)" : "=s"(E) : "s"(ws + WS_EPOCH) : "memory");
        const int NB = (3), CAP = (45);
        const int K0 = (3), GAIN = (24), BK = (3), FLOOR = (3), XM = (250), XD1 = (100), FTH = (15), XW = (40), RTH = (6), KI = (14), KD = (9), XR = (170);
        unsigned t2 = (ts + TOF) * 128 + 64 * P0;
        int off = 0, off1 = 0, hold = 0, cum = 0, dp = 0, sp = 0;
#define NOM(U) (((int)(U) - 2) * 32 * PK + max((int)(U) - NU, 0) * 32 * (PV - PK))
#define NEWLINE(K) { if (lane >= (K) && lane <= 2 * NU) sm.tu[lane] = (v2u){t2 + NOM(lane) + (cum + (lane - (int)(K)) * dp + (lane > (K) ? sp + off : off1)) * 32, \
                                                                            (unsigned)((lane >= NU ? PV : PK) + dp + (lane == (K) ? sp + off - off1 : 0))}; }
        NEWLINE(2)
        if (lane == 0) sm.tu[1] = (v2u){t2 - 32 * P0, (unsigned)P0};
        const u64 E1 = E + 1;
        const unsigned rtag = (unsigned)E1 * 0x9e3779b1u;
        const bool boss = blockIdx.x == 0;
        u64* const slot = (u64*)(ws + WS_REF + (blockIdx.x & 63) * LSTRIDE + (blockIdx.x >> 6) * 16);
        const u64* const slots = (const u64*)(ws + WS_REF + lane * LSTRIDE);
        u64* const dec = (u64*)(ws + WS_DEC);
#define GLDS(P) __hip_atomic_load(P, __ATOMIC_RELAXED, __HIP_MEMORY_SCOPE_AGENT)
#define GSTS(P, V) __hip_atomic_store(P, V, __ATOMIC_RELAXED, __HIP_MEMORY_SCOPE_AGENT)
#define UNTIL(T) while ((unsigned)((T) - NOW() * 128) < 0x40000u) SLEEP(2)
        if (lane < 2) GSTS(slot + lane, lane ? E1 : (u64)ts | ((u64)rtag << 32));
        if (lane == 0) { sm.tag_lo = (unsigned)E1; CBAR(); lds_st(&sm.tag_rdy, 1u); }
        unsigned dv = 0, npoll = 0;
        bool joined = false;
#define JOIN(T0) { const int d0 = min(max((int)((T0) - ts), -1000), 1000) + (0); off -= d0 * 4; t2 += d0 * 128; joined = true; }
        unsigned pw_ = 0, tw_ = 0, now, p; int prov = 0;
#define LOOK() { u64 t_; asm volatile("ds_read_b32 %1, %2\n\ts_memrealtime %0\n\ts_waitcnt lgkmcnt(0)" : "=s"(t_), "=v"(p) : "v"(lds_addr(&sm.grab)) : "memory"); \
                 p = __builtin_amdgcn_readfirstlane(p) - 8; now = (unsigned)t_ * 128; }
#define LATENESS(P, T) ({ const v2u e = TU((P) >> 5); (int)((T) - __builtin_amdgcn_readfirstlane(e.x) - ((P) & 31) * __builtin_amdgcn_readfirstlane(e.y)); })
#define WATCH(T) for (;;) { \
            LOOK() \
            if (__builtin_expect(now - tw_ > (80) * 128u && pw_ >= 64 && p < NPC - 8, 0)) { \
                const int lw = max(LATENESS(pw_, tw_), 0); \
                if (LATENESS(p, now) - lw > (80) * 128) \
                    for (;;) { \
                        const int d_ = min(max(LATENESS(p, now) - lw, 0) >> 5, 1600); \
                        if (lane <= 2 * NU) atomicAdd((unsigned*)&sm.tu[lane], d_ * 32u); \
                        off += d_; prov += d_; hold = 3; \
                        if (p != pw_ || prov > 8000) break; \
                        SLEEP(1) \
                        LOOK() \
                    } \
                } \
            if (p != pw_) { pw_ = p; tw_ = now; } \
            if ((unsigned)((T) - now) >= 0x40000u) break; \
            if ((unsigned)((T) - now) > 25 * 128) SLEEP(WSL) else SLEEP(1) }
        if (boss) {
            TOUCH((lane & 31) * 128, dec)
            UNTIL((ts + (330)) * 128)
            for (;;) {
                bool ok = true; float sum = 0.f;
#pragma unroll
                for (int i = 0; i < 4; i++) { const u64 x = GLDS(slots + 2 * i), e = GLDS(slots + 2 * i + 1); ok &= e == E1 && (unsigned)(x >> 32) == rtag; sum += (float)min(max((int)((unsigned)x - ts), -1000), 1000); }
                if (__builtin_amdgcn_ballot_w64(ok) == ~0ull) { dv = ts + (int)(wave_sum(sum) * (1.f / 256)); break; }
                SLEEP(8)
            }
            if (lane == 0) { GSTS(dec, (u64)dv | ((u64)rtag << 32)); GSTS((u64*)(ws + WS_EPOCH), E1); }
            JOIN(dv)
            dv = 0;
        }
        int sp1 = 0, tot1 = 0, tot2 = 0;
#pragma unroll 1
        for (unsigned k = 2; k < 2 * NU; k++) {
            const unsigned tk = t2 + NOM(k) + cum * 32;
            if (k >= K0 && k + 1 < 2 * NU) {
                WATCH(tk - (XM + XW) * 128)
                int sum = 0, n = 0;
                do { LOOK() sum += LATENESS(p, now) >> 7; n++; } while ((unsigned)(tk - XM * 128 - now) < 0x40000u);
                const int l = (int)((float)sum * __builtin_amdgcn_rcpf((float)n)) - BK + prov / 4;
                if (l > RTH && lane == 0) GSTS((unsigned*)(ws + WS_REF + (blockIdx.x & 63) * LSTRIDE + 64) + (blockIdx.x >> 6), (unsigned)min(l, 1000) | ((rtag + k) << 10));
            }
            const int cum0 = cum;
            bool got = boss;
            if (!boss && (k > K0 || !joined)) {
                WATCH(tk - XR * 128)
                const u64 x = GLDS(dec + (joined ? k * 16 : 0));
                dv = __builtin_amdgcn_readfirstlane((unsigned)x);
                got = __builtin_amdgcn_readfirstlane((unsigned)(x >> 32)) == rtag;
                npoll += !got;
                if (got && !joined) { JOIN(dv) got = false; }
            }
            sp1 = sp; sp = 0;
            if (got && k > K0) { cum = dv >> 18; sp = dv >> 6 & 4095u; dp = dv & 63u; }
            const int yld = cum - cum0 + sp;
            { const int pj = min(prov, cum - cum0); off -= pj; prov -= pj; off1 = off;
              const int ps = min(prov, sp); off -= ps; prov -= ps;
              if (hold) hold--; else prov = 0;
              const int r_ = off - prov; off -= r_ > 40 || r_ < -40 ? r_ / NB : r_; }
            NEWLINE(k)
            cum = min(cum + sp + dp, 16383);
            if (boss) dv = (cum << 18) | dp;
            if (boss && k >= K0 && k + 1 < 2 * NU && (int)(NOW() * 128 - tk) < -60 * 128) {
                WATCH(tk - (XM - XD1) * 128)
                const v4u x = __builtin_amdgcn_raw_buffer_load_b128(__builtin_amdgcn_make_buffer_rsrc(ws + WS_REF + 64, 0, 64 * LSTRIDE, 0x00020000), lane * LSTRIDE, 0, SC1);
                float a[4];
#pragma unroll
                for (int i = 0; i < 4; i++) a[i] = ((x[i] ^ ((rtag + k) << 10)) >> 10) == 0 ? (float)(x[i] & 1023u) : 0.f;
                const int tot = (int)wave_sum(a[0] + a[1] + a[2] + a[3]) >> 6;
                float mn = 0.f;
                if (tot >= 40) {
#pragma unroll 1
                    for (float st = 512.f; st >= 4.f; st *= 0.5f) {
                        const float x_ = mn + st;
                        const int c_ = __builtin_popcountll(__builtin_amdgcn_ballot_w64(a[0] >= x_)) + __builtin_popcountll(__builtin_amdgcn_ballot_w64(a[1] >= x_)) +
                                       __builtin_popcountll(__builtin_amdgcn_ballot_w64(a[2] >= x_)) + __builtin_popcountll(__builtin_amdgcn_ballot_w64(a[3] >= x_));
                        if (c_ >= (224)) mn = x_;
                    }
                }
                const float c = mn + (float)CAP, fth = (float)(FTH * (2 * NU - (int)k));
                const int cut = (int)wave_sum(fminf(a[0], c) + fminf(a[1], c) + fminf(a[2], c) + fminf(a[3], c)) >> 6, m4 = (int)mn * 4;
                const bool far = __builtin_amdgcn_ballot_w64(fmaxf(fmaxf(a[0], a[1]), fmaxf(a[2], a[3])) > fth) != 0;
                const int pend = yld + sp1 * XM / 320;
                const int nj = max(m4 - pend, 0);
                const int ns = min(max(max(min(m4 + (tot - m4) * GAIN / 16 - pend - nj, (cut - m4) * GAIN / 16), 0), far ? FLOOR * 4 : 0), 4000);
                int ndp = dp;
                if (k >= (unsigned)KD && tot >= KI && tot > tot1 && tot < tot1 + 32 && tot1 < tot2 + 32) ndp = min(dp + (2), 40);
                if (tot + tot1 < 2 && (1)) ndp = max(dp - 1, 0);
                tot2 = tot1; tot1 = tot;
                dv = (min(cum + nj, 16383) << 18) | (ns << 6) | ndp;
                if ((int)(NOW() * 128 - tk) > (60) * 128) dv = (cum << 18) | dp;
                else if (lane == 0) GSTS(dec + (k + 1) * 16, (u64)dv | ((u64)rtag << 32));
                LTK(k + 33, (int)(NOW() * 128 - tk) / 128 + 1000)
                LTK(k + 1, (min(tot, 4095) << 20) | (min(nj + ns, 1023) << 10) | (dp + 100))
                STAMPV(5 + k, (min(tot, 4095) << 20) | (min(nj + ns, 1023) << 10) | (dp + 100))
            }
        }
        WATCH(p >= NPC - 8 ? now - 128 : now + 4096)
    } else if (wave == NSW + 2) {
        const int ql = lane < 48 ? lane : 47;
        unsigned c1, c2, s1, s2;
#define QT_LOADS() { int qo_ = ql; asm volatile("" : "+v"(qo_)); \
                     c1 = ((const unsigned*)q_cos)[qo_]; c2 = ((const unsigned*)q_cos)[48 + qo_]; s1 = ((const unsigned*)q_sin)[qo_]; s2 = ((const unsigned*)q_sin)[48 + qo_]; }
        unsigned q1, q2, lkz, lks, lvz, lvs;
#define UNIT_LOADS(HEAD) { \
        q1 = ((const unsigned*)(q + (size_t)(HEAD) * DH))[ql]; q2 = ((const unsigned*)(q + (size_t)(HEAD) * DH))[48 + ql]; \
        const size_t so = ((size_t)(HEAD) * SKV + brow0) / 2 + (lane >> 3) * 128 + (lane & 7); \
        lkz = SLD((const unsigned*)k_zeros + so); lks = SLD((const unsigned*)k_scales + so); \
        lvz = SLD((const unsigned*)v_zeros + so); lvs = SLD((const unsigned*)v_scales + so); }
#define UNIT_FILL(SL, U) { \
            v4f qq; \
            qq.x = bfr(bfr(bflo(q1) * bflo(c1)) + bfr(-bflo(q2) * bflo(s1))); \
            qq.y = bfr(bfr(bflo(q2) * bflo(c2)) + bfr(bflo(q1) * bflo(s2))); \
            qq.z = bfr(bfr(bfhi(q1) * bfhi(c1)) + bfr(-bfhi(q2) * bfhi(s1))); \
            qq.w = bfr(bfr(bfhi(q2) * bfhi(c2)) + bfr(bfhi(q1) * bfhi(s2))); \
            *(v2u*)&sm.q[SL][2 * ql] = (v2u){(__float_as_uint(qq.x) >> 16) | __float_as_uint(qq.y), (__float_as_uint(qq.z) >> 16) | __float_as_uint(qq.w)}; \
            *(v4f*)&sm.kz[SL][2 * lane] = (v4f){bflo(lkz), bflo(lks), bfhi(lkz), bfhi(lks)}; \
            *(v4f*)&sm.vz[SL][2 * lane] = (v4f){bflo(lvz), bflo(lvs), bfhi(lvz), bfhi(lvs)}; \
            CBAR(); \
            if (lane == 0) lds_st(&sm.rdy[SL], (U) + 1); \
            CBAR(); }
        asm volatile("s_setprio 3");
        QT_LOADS()
#pragma unroll 1
        for (unsigned u = 0; u < NU; u++) {
            UNIT_LOADS(rank + GSZ * u)
            while (u >= lds_ld(&sm.merged) + NIN) SLEEP(4);
            CBAR();
            UNIT_FILL(u % NIN, u)
            if (u < 3) STAMP(5 + u)
            if (u == 1) asm volatile("s_setprio 0");
        }
        while (lds_ld(&sm.merged) < NU) SLEEP(16)
        CBAR();
        const unsigned T = lds_ld(&sm.tag_lo);
        if (lane < NU) __hip_atomic_store((u64*)(ws + WS_LOG + (rank * NSPLIT + split) * LSTRIDE2) + lane, (u64)__float_as_uint(sm.ent[lane][130]) | ((u64)T << 32), __ATOMIC_RELAXED, __HIP_MEMORY_SCOPE_AGENT);
        while (lds_ld(&sm.merged) < NU + 3) SLEEP(16)
        for (;;) {
            const u64 x = __hip_atomic_load((const u64*)(ws + WS_LOG + ((blockIdx.x & 15) * NSPLIT + (lane & 15)) * LSTRIDE2) + (blockIdx.x >> 4), __ATOMIC_RELAXED, __HIP_MEMORY_SCOPE_AGENT);
            if (__builtin_amdgcn_ballot_w64((unsigned)(x >> 32) == T) == ~0ull) {
                const float L = __uint_as_float((unsigned)x);
                const float w = __builtin_amdgcn_exp2f(L - row_max(L));
                sm.fw[lane & 15] = w / row_sum(w);
                CBAR();
                if (lane == 0) lds_st(&sm.fw_rdy, 1u);
                break;
            }
            SLEEP(64)
        }
    } else {
        while (lds_ld(&sm.tag_rdy) == 0) SLEEP(1);
        CBAR();
        const unsigned T = lds_ld(&sm.tag_lo);
        const __amdgpu_buffer_rsrc_t rsrc = __builtin_amdgcn_make_buffer_rsrc(ws + WS_PART + (rank * NSPLIT + split) * 512, 0, NH * NSPLIT * 512, 0x00020000);
        v2u* recs = (v2u*)&sm.cs[0][0];
#pragma unroll
        for (int i = 0; i < NOUT * 3; i++) (&sm.acc[0][0])[i * 64 + lane] = 0.0;
        bool direct = false;
#define PUBLISH(N) { \
        v4u r[8]; \
        _Pragma("unroll") for (int k = 0; k < 8; k++) r[k] = ((const v4u*)recs)[k * 64 + lane]; \
        _Pragma("unroll") for (int k = 0; k < 8; k++) __builtin_amdgcn_raw_buffer_store_b128(r[k], rsrc, 2 * k + (lane >> 5) < (N) ? po : 0x7ffffff0u, k * (2 * GSZ * NSPLIT * 512), SC1); STAMP(4) }
        const unsigned po = (lane >> 5) * (GSZ * NSPLIT * 512) + (lane & 31) * 16;
#pragma unroll 1
        for (unsigned u = 0; u < 2 * NU; u++) {
            const unsigned e = u % NU, so = u % NOUT;
            const v4f h = *(const v4f*)&sm.ent[e][128];
            for (;;) {
                if (!direct && u >= 2 * NU - 3 && (int)(NOW() * 128 - __builtin_amdgcn_readfirstlane(TU(2 * NU).x)) >= (30) * 128) { direct = true; wave_sync(); PUBLISH(u - NU) }
                if (lds_ld(&sm.cnt[so]) == 32) break;
                if (direct) SLEEP(0) else SLEEP(1)
            }
            CBAR();
            STAMP(5)
            if (!direct && u == 2 * NU - 1) { direct = true; wave_sync(); PUBLISH(NU - 1) }
            if (u < NU) {
                const float2 lg = *(const float2*)&sm.ent[e][2 * lane];
                const v4f zs = *(const v4f*)&sm.vz[u % NIN][2 * lane];
                const float m = wave_max(fmaxf(lg.x, lg.y));
                const float p0 = __expf(lg.x - m), p1 = __expf(lg.y - m);
                const float x0 = p0 * zs.y, x1 = p1 * zs.w;
                const unsigned eb = max(__float_as_uint(wave_max(fmaxf(fabsf(x0), fabsf(x1)))) >> 23, 21u);
                const float f = __uint_as_float((275u - eb) << 23);
                const float w0 = rintf(x0 * f), w1 = rintf(x1 * f);
                const float den = wave_sum(p0 + p1);
                const float bz = wave_sum(fmaf(w0, zs.x, w1 * zs.z));
                const float L = fmaf(m, 1.4426950408889634f, __builtin_amdgcn_logf(den));
                *(v2u*)&sm.ent[e][2 * lane] = (v2u){(unsigned)(int)w0, (unsigned)(int)w1};
                if (lane == 0) *(v4f*)&sm.ent[e][128] = (v4f){ldexpf(1.f / den, (int)eb - 148), bz, L, 0.f};
            } else {
                const double a0 = sm.acc[so][lane], a1 = sm.acc[so][64 + lane], a2 = sm.acc[so][128 + lane];
                const float n0 = (float)(a0 - (double)h.y) * h.x, n1 = (float)(a1 - (double)h.y) * h.x, n2 = (float)(a2 - (double)h.y) * h.x;
                const unsigned e2 = max(__float_as_uint(fmaxf(fmaxf(fabsf(n0), fabsf(n1)), fabsf(n2))) >> 23, 12u);
                const float f2 = __uint_as_float((266u - e2) << 23);
                const unsigned m0 = min(max((int)rintf(n0 * f2), -8191), 8191) & 0x3fff, m1 = min(max((int)rintf(n1 * f2), -8191), 8191) & 0x3fff, m2 = min(max((int)rintf(n2 * f2), -8191), 8191) & 0x3fff;
                const v2u r = {m0 | (m1 << 14) | (m2 << 28), (m2 >> 4) | (e2 << 10) | (T << 18)};
                if (direct) {
                    STAMP(6)
                    const v4u r2 = {r.x, r.y, (unsigned)__builtin_amdgcn_update_dpp(0, (int)r.x, 0xf5, 0xf, 0xf, true), (unsigned)__builtin_amdgcn_update_dpp(0, (int)r.y, 0xf5, 0xf, 0xf, true)};
                    __builtin_amdgcn_raw_buffer_store_b128(r2, rsrc, (lane & 1) ? 0x7ffffff0u : lane * 8, e * (GSZ * NSPLIT * 512), SC1);
                }
                else recs[e * 64 + lane] = r;
                CBAR();
                sm.acc[so][lane] = 0.0; sm.acc[so][64 + lane] = 0.0; sm.acc[so][128 + lane] = 0.0;
            }
            CBAR();
            if (lane == 0) { lds_st(&sm.cnt[so], 0u); CBAR(); lds_st(&sm.merged, u + 1); }
        }
        LT(2, NOW()) LT(0, (unsigned)tent) LT(3, ts) LT(4, NOW() - (((NOW() * 128 - __builtin_amdgcn_readfirstlane(TU(2 * NU).x)) >> 7) & 0xfffff))
        STAMP(2)
        STAMP_DUMP()
        return;
    }

    STAMP(2)
    STAMP_DUMP()
}

extern "C" void kernel_launch(void* const* d_in, const int* in_sizes, int n_in,
                              void* d_out, int out_size, void* d_ws, size_t ws_size,
                              hipStream_t stream) {
    const unsigned short* q = (const unsigned short*)d_in[0];
    const int* qk = (const int*)d_in[1];
    const unsigned short* k_scales = (const unsigned short*)d_in[2];
    const unsigned short* k_zeros = (const unsigned short*)d_in[3];
    const int* qv = (const int*)d_in[4];
    const unsigned short* v_scales = (const unsigned short*)d_in[5];
    const unsigned short* v_zeros = (const unsigned short*)d_in[6];
    const unsigned short* q_cos = (const unsigned short*)d_in[7];
    const unsigned short* q_sin = (const unsigned short*)d_in[8];
    const unsigned short* k_cos = (const unsigned short*)d_in[9];
    const unsigned short* k_sin = (const unsigned short*)d_in[10];
    unsigned char* ws = (unsigned char*)d_ws;

    attn_kernel<<<NSPLIT * GSZ, NTHREADS, 0, stream>>>(q, qk, k_scales, k_zeros, qv, v_scales, v_zeros,
                                                       q_cos, q_sin, k_cos, k_sin, (__hip_bfloat16*)d_out, ws);
}
```

```cpp
#define PFL 182
#include <hip/hip_runtime.h>
#include <hip/hip_bf16.h>
#include <stdint.h>

#define DH 192
#define HALF 96
#define SKV 2048
#define NH 256
#define NSW 8
#define NWAVES (NSW + 4)
#define NTHREADS (64 * NWAVES)
#define BROWS 128
#define NSPLIT (SKV / BROWS)
#define GSZ 16
#define NU (NH / GSZ)
#define NPC (2 * NU * 32)
#define N1 (NH - GSZ)
#define NIN 4
#define NOUT 4
#define ENTW 132
#define SC1 16
#define LIKELY 1
#define SLD(P) (*(P))
#define LSTRIDE 4352
#define PUBP 2
#define WSL 7
#define PSL 5
#define PHALF 9
#define PHI 0x0235712457024671ull
#define KAP 0x0202020220202020ull
#define PHIV PHI
#define KAPV KAP
#define PRIO 1
#define PRIO0 "s_setprio 0\n"
#define PRIO1 "\n\ts_setprio 2"
#define GTH 10
#define GS1 2

#define WS_EPOCH 0
#define WS_DEC 8192
#define WS_REF 16384
#define WS_LOG 524288
#define LSTRIDE2 1408
#define WS_PART 1048576
#define WS_FIN (WS_PART + NH * NSPLIT * 512)

typedef unsigned int v2u __attribute__((ext_vector_type(2)));
typedef unsigned int v3u __attribute__((ext_vector_type(3)));
typedef unsigned int v4u __attribute__((ext_vector_type(4)));
typedef int v4i __attribute__((ext_vector_type(4)));
typedef float v2f __attribute__((ext_vector_type(2)));
typedef float v4f __attribute__((ext_vector_type(4)));
typedef unsigned long long u64;

__device__ __forceinline__ float bflo(unsigned u) { return __uint_as_float(u << 16); }
__device__ __forceinline__ float bfhi(unsigned u) { return __uint_as_float(u & 0xffff0000u); }
__device__ __forceinline__ float bfr(float x) {
    __bf16 b = (__bf16)x;
    return __uint_as_float(((unsigned)__builtin_bit_cast(unsigned short, b)) << 16);
}

#define SLEEP(n) SLEEP_(n)
#define SLEEP_(n) asm volatile("s_sleep " #n "\n\tv_add_co_u32 %0, vcc, 1, %0\n\ts_cbranch_vccz 1f\n\ts_endpgm\n1:" : "+v"(spins) :: "vcc", "memory");
#define SPIN0 0xf0000000u
#define CBAR() asm volatile("" ::: "memory")

__device__ __forceinline__ void wave_sync() {
    __builtin_amdgcn_fence(__ATOMIC_RELEASE, "wavefront");
    __builtin_amdgcn_wave_barrier();
    __builtin_amdgcn_fence(__ATOMIC_ACQUIRE, "wavefront");
}
template <int CTRL>
__device__ __forceinline__ float dpp_add(float x) {
    int y = __builtin_amdgcn_update_dpp(0, __float_as_int(x), CTRL, 0xf, 0xf, true);
    return x + __int_as_float(y);
}
template <int CTRL>
__device__ __forceinline__ float dpp_max(float x) {
    int y = __builtin_amdgcn_update_dpp(__float_as_int(x), __float_as_int(x), CTRL, 0xf, 0xf, false);
    return fmaxf(x, __int_as_float(y));
}
__device__ __forceinline__ float row_sum(float x) { x = dpp_add<0x128>(x); x = dpp_add<0x124>(x); x = dpp_add<0x122>(x); return dpp_add<0x121>(x); }
__device__ __forceinline__ float row_max(float m) { m = dpp_max<0x128>(m); m = dpp_max<0x124>(m); m = dpp_max<0x122>(m); return dpp_max<0x121>(m); }
__device__ __forceinline__ float rdlane(float x, int l) { return __int_as_float(__builtin_amdgcn_readlane(__float_as_int(x), l)); }
__device__ __forceinline__ float wave_max(float m) { m = row_max(m); return fmaxf(fmaxf(rdlane(m, 0), rdlane(m, 16)), fmaxf(rdlane(m, 32), rdlane(m, 48))); }
__device__ __forceinline__ float wave_maxv(float m) {
    m = row_max(m);
    const auto a = __builtin_amdgcn_permlane16_swap(__float_as_uint(m), __float_as_uint(m), false, false);
    m = fmaxf(__uint_as_float(a[0]), __uint_as_float(a[1]));
    const auto b = __builtin_amdgcn_permlane32_swap(__float_as_uint(m), __float_as_uint(m), false, false);
    return fmaxf(__uint_as_float(b[0]), __uint_as_float(b[1]));
}
__device__ __forceinline__ float wave_sum(float x) { x = row_sum(x); return (rdlane(x, 0) + rdlane(x, 16)) + (rdlane(x, 32) + rdlane(x, 48)); }
__device__ __forceinline__ unsigned lds_ld(const unsigned* p) { return __builtin_amdgcn_readfirstlane(__hip_atomic_load(p, __ATOMIC_RELAXED, __HIP_MEMORY_SCOPE_WORKGROUP)); }
__device__ __forceinline__ unsigned lds_addr(const void* p) { return (unsigned)(size_t)(const __attribute__((address_space(3))) void*)p; }
#define DMA16(SRC, LDS) asm volatile("s_mov_b32 %0, m0\n\ts_mov_b32 m0, %2\n\ts_nop 0\n\tglobal_load_lds_dwordx4 %1, off\n\ts_mov_b32 m0, %0" : "=&s"(m0keep) : "v"(SRC), "s"(LDS) : "memory")
__device__ __forceinline__ void lds_st(unsigned* p, unsigned v) { __hip_atomic_store(p, v, __ATOMIC_RELAXED, __HIP_MEMORY_SCOPE_WORKGROUP); }
#define NOW() ((unsigned)__builtin_amdgcn_s_memrealtime())

struct __attribute__((aligned(16))) Smem {
    unsigned cs[32][768];
    double acc[NOUT][DH];
    float ent[NU][ENTW];
    unsigned q[NIN][HALF];
    float2 kz[NIN][BROWS];
    float2 vz[NIN][BROWS];
    v2u tu[2 * NU + 2];
    unsigned grab, merged, tabs, cdone;
    unsigned rdy[NIN], cnt[NOUT];
    unsigned tag_rdy, tag_lo, tag_hi, fw_rdy;
    int stat[32];
    float fw[NSPLIT];
};
#define NSTAT 0
#define STAMP(i) {}
#define STAMPV(i, v) {}
#define STAMP_DUMP()
#define STAMP_UNIT(N)

#define LT(I, V)
#define LTK(K, V)
#define TU(I) (*(const volatile v2u __attribute__((address_space(3)))*)(size_t)lds_addr(&sm.tu[I]))
#define LDS_ADD(P, V) { u64 ex_; asm volatile("s_mov_b64 %0, exec\n\ts_mov_b64 exec, 1\n\tds_add_u32 %1, %2\n\ts_mov_b64 exec, %0" : "=&s"(ex_) : "v"(lds_addr(P)), "v"(V) : "memory"); }
#define LDS_OR(P, V) { u64 ex_; asm volatile("s_mov_b64 %0, exec\n\ts_mov_b64 exec, 1\n\tds_or_b32 %1, %2\n\ts_mov_b64 exec, %0" : "=&s"(ex_) : "v"(lds_addr(P)), "v"(V) : "memory"); }

__global__ __launch_bounds__(NTHREADS, 3) void attn_kernel(
    const unsigned short* __restrict__ q, const int* __restrict__ qk,
    const unsigned short* __restrict__ k_scales, const unsigned short* __restrict__ k_zeros,
    const int* __restrict__ qv,
    const unsigned short* __restrict__ v_scales, const unsigned short* __restrict__ v_zeros,
    const unsigned short* __restrict__ q_cos, const unsigned short* __restrict__ q_sin,
    const unsigned short* __restrict__ k_cos, const unsigned short* __restrict__ k_sin,
    __hip_bfloat16* __restrict__ out, unsigned char* __restrict__ ws)
{
    __shared__ Smem sm;
    const int tid = threadIdx.x;
    unsigned spins = SPIN0;
    u64 tent_; asm volatile("s_memrealtime %0" : "=s"(tent_));
#define TOUCH(OFF, BASE) asm volatile("global_load_dword a6, %0, %1" :: "v"(OFF), "s"(BASE) : "a6", "memory");
    const unsigned cl = min((blockIdx.x & 24) * 8 + tid, PFL - 1u) * 128;
    if (tid < 64) { const char* pc0; asm volatile("s_getpc_b64 %0" : "=s"(pc0)); TOUCH(cl, pc0) }
    __builtin_amdgcn_sched_barrier(0);
    const int hw = __builtin_amdgcn_readfirstlane(tid >> 6);
    const int lane = tid & 63;
    const int split = (blockIdx.x & 3) * 4 + ((blockIdx.x >> 3) & 3);
    const int rank = (blockIdx.x >> 5) * 2 + ((blockIdx.x >> 2) & 1);
    const int brow0 = split * 16;
#define ROWOF(RG) (((RG) >> 2) * 256 + ((RG) & 3) * 4)
    if (hw == 0) {
        typedef const char* const __attribute__((address_space(4))) * kap_t;
        const char* sa = ((kap_t)__builtin_amdgcn_kernarg_segment_ptr())[(0x5623 >> (4 * (split & 3))) & 7];
        unsigned o1 = ((blockIdx.x >> 5) * 256 + brow0) * (DH * 2) + min(lane, 47) * 128;
        unsigned o2 = ((rank + GSZ * (lane >> 3)) * SKV + (lane & 7) * 256 + (brow0 & ~63)) * 2, o3 = o2 + 8 * GSZ * SKV * 2;
        unsigned o4 = (rank + GSZ * (lane >> 2)) * (DH * 2) + min(lane & 3, 2) * 128;
        asm volatile("" : "+v"(o1), "+v"(o2), "+v"(o3), "+v"(o4));
        TOUCH(o1, k_cos) TOUCH(o1, k_sin) TOUCH(o4, q) TOUCH(o2, sa) TOUCH(o3, sa)
    }
    if (hw == 1 && lane < 16 + NSTAT) (&sm.grab)[lane] = lane ? 0 : 64;
    asm volatile("" :: "s"(q), "s"(qk), "s"(k_scales), "s"(k_zeros), "s"(qv), "s"(v_scales), "s"(v_zeros), "s"(q_cos), "s"(q_sin), "s"(k_cos), "s"(k_sin), "s"(out), "s"(ws));
    asm volatile("s_waitcnt lgkmcnt(0)\n\ts_barrier" ::: "memory");
    asm volatile("" : "+s"(tent_));
    const int wave = (int)(0x76543210b9a8ull >> (4 * hw)) & 15;
    STAMP(0)
    const unsigned ts = NOW();
    const int PK = (1268), PV = (1268), TOF = (100), P0 = (1370);
    if (LIKELY ? __builtin_expect(wave < NSW, 1) : wave < NSW) {
        const unsigned rot = 4 * ((unsigned)(PHI >> (60 - 4 * rank)) & 7u), kap = (unsigned)(KAP >> (60 - 4 * rank)) & 3u;
        const unsigned rotv = 4 * ((unsigned)(PHIV >> (60 - 4 * rank)) & 7u), kapv = (unsigned)(KAPV >> (60 - 4 * rank)) & 3u;
#define RGOF(G) ((G) >= NPC / 2 ? (((G) + rotv) & 31u) ^ kapv : (((G) + rot) & 31u) ^ kap)
        const char* cbase[3];
#pragma unroll
        for (int j = 0; j < 3; j++) { const int x = j * 64 + lane; cbase[j] = (x < 96 ? (const char*)k_cos + x * 16 : (const char*)k_sin + (x - 96) * 16) + brow0 * (DH * 2); }
        unsigned m0keep;
#define CS_DMA(RG) { _Pragma("unroll") for (int j = 0; j < 3; j++) DMA16(cbase[j] + ROWOF(RG) * (DH * 2), lds_addr(&sm.cs[0][0]) + (RG) * 3072 + j * 1024); }
#define REPACK(RG) { \
        unsigned* cw = &sm.cs[RG][0]; \
        const unsigned* l = cw + rsel * (DH / 2) + qoff; \
        unsigned r[2][6]; \
        _Pragma("unroll") \
        for (int j = 0; j < 2; j++) { \
            unsigned e[4][2]; \
            _Pragma("unroll") \
            for (int k = 0; k < 4; k++) { \
                const unsigned* a = l + j * DH + (k & 1) * 48 + (k >> 1) * 384;       \
                unsigned x0 = a[0], x1 = a[1]; \
                e[k][0] = __builtin_amdgcn_alignbit(x1, x0, sh); \
                e[k][1] = x1 >> sh; \
            } \
            r[j][0] = (e[0][0] & 0xffffu) | (e[3][0] << 16); \
            r[j][1] = (e[0][0] >> 16) | (e[3][0] & 0xffff0000u); \
            r[j][2] = (e[0][1] & 0xffffu) | (e[3][1] << 16); \
            r[j][3] = (e[1][0] & 0xffffu) | (e[2][0] << 16); \
            r[j][4] = (e[1][0] >> 16) | (e[2][0] & 0xffff0000u); \
            r[j][5] = (e[1][1] & 0xffffu) | (e[2][1] << 16); \
        } \
        wave_sync(); \
        _Pragma("unroll") \
        for (int j = 0; j < 2; j++) { *(v4u*)&cw[j * 256 + lane * 4] = (v4u){r[j][0], r[j][1], r[j][2], r[j][3]}; *(v2u*)&cw[512 + j * 128 + lane * 2] = (v2u){r[j][4], r[j][5]}; } \
        wave_sync(); \
        LDS_OR(&sm.tabs, 1u << (RG)) }
        const int sub = lane & 31;
        const int rsel = lane >> 5;
        const unsigned sh = (sub & 1) * 16;
        const int qoff = 3 * (sub >> 1) + (sub & 1);
        const char* kbase = (const char*)qk + (size_t)(rank * SKV + brow0) * (DH * 4);
        const char* vbase = (const char*)qv + (size_t)(rank * SKV + brow0) * (DH * 4);
        const unsigned koff = (rsel * DH + 3 * sub) * 4, voff = 3 * lane * 4;
#define AR(S) (16 + (S) * 8)
        asm volatile("" ::: "a16", "a17", "a18", "a19", "a20", "a21", "a22", "a23", "a24", "a25", "a26", "a27", "a28", "a29", "a30", "a31",
                            "a32", "a33", "a34", "a35", "a36", "a37", "a38", "a39", "a40", "a41", "a42", "a43", "a44", "a45", "a46", "a47");
        asm volatile("" ::: "a48", "a49", "a50", "a51", "a52", "a53", "a54", "a55", "a56", "a57", "a58", "a59", "a60", "a61", "a62", "a63");
        __builtin_amdgcn_s_waitcnt(0x0f70);
        int ta[3], tb[3];
#define GLD(A, OFF, BASE, IMM) asm volatile("global_load_dwordx3 a[%c0:%c1], %2, %3 offset:%4 nt" :: "i"(A), "i"((A) + 2), "v"(OFF), "s"(BASE), "i"(IMM))
#define ISSUE(S, J, BASE, OA, OB) { GLD(AR(S), OA, BASE, (J) * 1536); GLD(AR(S) + 4, OB, BASE, (J) * 1536); }
#define WAITT(S, N) asm volatile("s_waitcnt vmcnt(%6)\n\tv_accvgpr_read_b32 %0, a[%c7]\n\tv_accvgpr_read_b32 %1, a[%c8]\n\tv_accvgpr_read_b32 %2, a[%c9]\n\t" \
                              "v_accvgpr_read_b32 %3, a[%c10]\n\tv_accvgpr_read_b32 %4, a[%c11]\n\tv_accvgpr_read_b32 %5, a[%c12]" \
                              : "=v"(ta[0]), "=v"(ta[1]), "=v"(ta[2]), "=v"(tb[0]), "=v"(tb[1]), "=v"(tb[2]) \
                              : "i"(N), "i"(AR(S)), "i"(AR(S) + 1), "i"(AR(S) + 2), "i"(AR(S) + 4), "i"(AR(S) + 5), "i"(AR(S) + 6))
        const unsigned koffb = koff + 384, voffb = voff + 768;
        unsigned u = 0, rg, np = 0;
#define RG0(C) RGOF((unsigned)(wave + 8 * (C)))
        CS_DMA(RG0(0))
        STAMP(52)
        STAMP(53)
        const float scale = 0.07216878364870322f;
        double acc[3] = {0.0, 0.0, 0.0};
        bool chg, none = false;
        unsigned Qp[3], Qn[3];
        float kz4[2], ksc, kres[2];
        v4u ca[2]; v2u cb[2];
        v2f qe[2][3], A[2];
        const char* nb; unsigned oa, ob;
        unsigned g, mg, nrg, tg, late = 0;
        u64 tis;
        unsigned dg, par = 0, gu;
        int early;
        const int bth = wave < (8) ? (20) * 128 : 0x3fffffff;
        const unsigned grab_adr = lds_addr(&sm.grab);
        asm volatile("v_accvgpr_write_b32 a13, 1\n\tv_accvgpr_write_b32 a14, 2" ::: "a13", "a14", "a15");
#define GRAB(N) { u64 ex_; asm volatile("s_mov_b64 %0, exec\n\ts_mov_b64 exec, 1\n\tds_add_rtn_u32 a15, %1, a%c2\n\ts_mov_b64 exec, %0" : "=&s"(ex_) : "v"(grab_adr), "i"(12 + (N)) : "memory"); }
#define GRABBED() { unsigned t_; asm volatile("s_waitcnt lgkmcnt(0)\n\tv_accvgpr_read_b32 %1, a15\n\ts_nop 0\n\tv_readfirstlane_b32 %0, %1\n\ts_nop 1" : "=s"(g), "=v"(t_)); }
#define NEXT1() const v2u x_ = TU(g >> 5); u64 tn_; asm volatile("s_memrealtime %0" : "=s"(tn_));
#define NEXT2() { \
            chg = (g >> 5) != u; none = g >= NPC; \
            const bool nv = g >= NPC / 2; \
            nrg = RGOF(g); \
            unsigned boff; asm("s_mul_i32 %0, %1, 0x300" : "=s"(boff) : "s"(((g >> 5) & (NU - 1)) * (GSZ * SKV) + ROWOF(nrg) + 2)); \
            nb = (nv ? vbase : kbase) + boff; \
            oa = nv ? voff : koff; ob = nv ? voffb : koffb; \
            asm volatile("s_waitcnt lgkmcnt(0)" : "+s"(tn_) : "s"(nb), "v"(oa), "v"(ob)); \
            tg = __builtin_amdgcn_readfirstlane(x_.x) + (g & 31) * __builtin_amdgcn_readfirstlane(x_.y); \
            early = none ? 0 : (int)((unsigned)tn_ * 128 - tg) - bth; \
            if (early > 0) asm volatile("s_cmp_lg_u32 %3, 0\n\ts_cbranch_scc1 1f\n\t" L4(32) "\ts_branch 2f\n1:\n\t" L4(16) "2:\n\ts_mov_b64 vcc, exec\n\ts_mov_b64 exec, 1\n\tds_add_rtn_u32 a15, %4, a13\n\ts_mov_b64 exec, vcc" \
                                    :: "v"(oa), "v"(ob), "s"(nb), "s"(pr), "v"(grab_adr) : "scc", "vcc", "memory"); }
#define L4(A) "global_load_dwordx3 a[" #A ":" #A "+2], %0, %2 offset:-1536 nt\n\tglobal_load_dwordx3 a[" #A "+4:" #A "+6], %1, %2 offset:-1536 nt\n\t" \
              "global_load_dwordx3 a[" #A "+8:" #A "+10], %0, %2 nt\n\tglobal_load_dwordx3 a[" #A "+12:" #A "+14], %1, %2 nt\n"
#define GATE(...) \
            asm volatile(PRIO0 "1:\n\ts_memrealtime vcc\n\ts_waitcnt lgkmcnt(0)\n\ts_lshl_b32 vcc_lo, vcc_lo, 7\n\ts_sub_u32 %0, %1, vcc_lo\n\ts_cmp_lt_u32 %0, 0x40000\n\ts_cbranch_scc0 2f\n\t" \
                         "s_cmp_lt_u32 %0, %2\n\ts_cbranch_scc1 1b\n\ts_sleep %3\n\ts_branch 1b\n2:" PRIO1 : "=&s"(dg) : "s"(tg), "i"(GTH * 128), "i"(GS1), __VA_ARGS__ : "scc", "vcc", "memory");
#define RD6(A) "v_accvgpr_read_b32 %0, a[%c6+" #A "]\n\tv_accvgpr_read_b32 %1, a[%c6+" #A "+1]\n\tv_accvgpr_read_b32 %2, a[%c6+" #A "+2]\n\t" \
               "v_accvgpr_read_b32 %3, a[%c6+" #A "+4]\n\tv_accvgpr_read_b32 %4, a[%c6+" #A "+5]\n\tv_accvgpr_read_b32 %5, a[%c6+" #A "+6]\n"
#define LD2(A) "global_load_dwordx3 a[%c6+" #A ":%c6+" #A "+2], %7, %9 offset:%10 nt\n\tglobal_load_dwordx3 a[%c6+" #A "+4:%c6+" #A "+6], %8, %9 offset:%10 nt"
#define OUT6 "=&v"(ta[0]), "=&v"(ta[1]), "=&v"(ta[2]), "=&v"(tb[0]), "=&v"(tb[1]), "=&v"(tb[2])
#define WI(A, B, TAIL) "s_cmp_gt_i32 %13, 0\n\ts_cbranch_scc1 3f\n\ts_waitcnt vmcnt(2)\n\t" LD2(A) TAIL "\n3:\n\ts_waitcnt vmcnt(%c14)\n\t" RD6(B)
#define WAITISSUE(S, TAIL, ...) asm volatile("s_cmp_lg_u32 %12, 0\n\ts_cbranch_scc1 1f\n\t" WI(16, 0, TAIL) "\ts_branch 2f\n1:\n\t" WI(0, 16, TAIL) "2:" \
                              : OUT6 : "i"(AR(S)), "v"(oa), "v"(ob), "s"(nb), "i"(((S) - 1) * 1536), "v"(grab_adr), "s"(pr), "s"(early), "i"(6 - 2 * (S)), __VA_ARGS__ : "scc", "vcc", "memory")
#define LASTT(S) asm volatile("s_waitcnt vmcnt(%c8)\n\ts_cmp_lg_u32 %7, 0\n\ts_cbranch_scc1 1f\n\t" RD6(0) "\ts_branch 2f\n1:\n\t" RD6(16) "2:" : OUT6 : "i"(AR(S)), "s"(pr), "i"(2 - 2 * (S)) : "scc")
#define DRAW "\n\ts_mov_b64 vcc, exec\n\ts_mov_b64 exec, 1\n\tds_add_rtn_u32 a15, %11, a13\n\ts_mov_b64 exec, vcc"
#define LATE0()
#define LATE1()
#define LATEF()
#define LATEF()
#define K_TAB() { _Pragma("unroll") for (int p = 0; p < 2; p++) { \
            const unsigned* cw = &sm.cs[rg][0]; \
            ca[p] = *(const v4u*)&cw[p * 256 + lane * 4]; cb[p] = *(const v2u*)&cw[512 + p * 128 + lane * 2]; } }
#define K_PRE(P) { \
        float e0, e1, e2, f0, f1, f2; \
        asm("v_dot2_f32_bf16 %0, %6, %12, 0\n\tv_dot2_f32_bf16 %3, %9, %15, 0\n\tv_dot2_f32_bf16 %1, %7, %13, 0\n\tv_dot2_f32_bf16 %4, %10, %16, 0\n\t" \
            "v_dot2_f32_bf16 %2, %8, %14, 0\n\tv_dot2_f32_bf16 %5, %11, %17, 0\n\ts_nop 2" \
            : "=&v"(e0), "=&v"(e1), "=&v"(e2), "=&v"(f0), "=&v"(f1), "=&v"(f2) \
            : "v"(Qp[0]), "v"(Qp[1]), "v"(Qp[2]), "v"(Qn[0]), "v"(Qn[1]), "v"(Qn[2]), \
              "v"(ca[P].x), "v"(ca[P].y), "v"(ca[P].z), "v"(ca[P].w), "v"(cb[P].x), "v"(cb[P].y)); \
        qe[P][0] = (v2f){e0, f0}; qe[P][1] = (v2f){e1, f1}; qe[P][2] = (v2f){e2, f2}; \
        A[P] = (qe[P][0] + qe[P][1] + qe[P][2]) * (v2f){-kz4[P], -kz4[P]}; }
#define K_SUM(P) { \
        _Pragma("unroll") \
        for (int i = 0; i < 3; i++) { v2f x = {(float)ta[i], (float)tb[i]}; A[P] = qe[P][i] * x + A[P]; } \
        kres[P] = A[P].x + A[P].y; }
#define K_RED2() { \
        const bool o1 = lane & 1; \
        float sx = (o1 ? kres[1] : kres[0]) + __int_as_float(__builtin_amdgcn_update_dpp(0, __float_as_int(o1 ? kres[0] : kres[1]), 0xb1, 0xf, 0xf, true)); \
        sx = dpp_add<0x4e>(sx); sx = dpp_add<0x124>(sx); sx = dpp_add<0x128>(sx); \
        const auto sw = __builtin_amdgcn_permlane16_swap(__float_as_uint(sx), __float_as_uint(sx), false, false); \
        sx = __uint_as_float(sw[0]) + __uint_as_float(sw[1]); \
        if ((lane & 30) == 0) sm.ent[u][rg * 4 + 2 * (lane & 1) + rsel] = sx * ksc; }
#define LDV(P) __hip_atomic_load(P, __ATOMIC_RELAXED, __HIP_MEMORY_SCOPE_WORKGROUP)
#define RDYWAIT(SL, SEQ) { while (lds_ld(&sm.rdy[SL]) != (SEQ)) SLEEP(1); CBAR(); }
#define LOADZ() { _Pragma("unroll") for (int p = 0; p < 2; p++) kz4[p] = sm.kz[u % NIN][rg * 4 + 2 * p + rsel].x; ksc = sm.kz[u % NIN][rg * 4 + 2 * (lane & 1) + rsel].y * scale; }
#define LOADQ() { _Pragma("unroll") for (int i = 0; i < 3; i++) { Qp[i] = sm.q[u % NIN][3 * sub + i]; Qn[i] = __builtin_amdgcn_alignbit(Qp[i], Qp[i], 16) ^ 0x80000000u; } }
#define FLUSH(ISV) { \
            mg = __builtin_amdgcn_readfirstlane(mg); \
            while (u >= mg + NOUT) { SLEEP(1); mg = lds_ld(&sm.merged); } \
            CBAR(); \
            if (ISV) { const unsigned aa = lds_addr(&sm.acc[u % NOUT][lane]); \
                asm volatile("ds_add_f64 %0, %1\n\tds_add_f64 %0, %2 offset:512\n\tds_add_f64 %0, %3 offset:1024" :: "v"(aa), "v"(acc[0]), "v"(acc[1]), "v"(acc[2]) : "memory"); \
                acc[0] = 0.0; acc[1] = 0.0; acc[2] = 0.0; } \
            CBAR(); \
            LATEF() \
            LDS_ADD(&sm.cnt[u % NOUT], np) \
            CBAR(); \
            STAMP_UNIT(np) \
            np = 0; }
#define ADVANCE() { u = gu; rg = nrg; }

#define REQ0(N, S) { tg = (ts + TOF) * 128 + (wave + 8 * (N)) * P0; \
            const char* kb = kbase + (((N) >> 2) * (GSZ * SKV) + ROWOF(RG0((N) & 3))) * (DH * 4); GATE("s"(kb)) ISSUE(S, 0, kb, koff, koffb) ISSUE((S) + 1, 1, kb, koff, koffb) STAMP(44 + (N)) }
#define USE0(N, S, W) { u = (N) >> 2; rg = RG0((N) & 3); K_TAB() LOADQ() LOADZ() K_PRE(0) K_PRE(1) WAITT(S, W); K_SUM(0) WAITT((S) + 1, W); K_SUM(1) K_RED2() }
#define VMW(N) asm volatile("s_waitcnt vmcnt(" #N ")" ::: "memory");
        REQ0(0, 0) CS_DMA(RG0(1))
        VMW(7) REPACK(RG0(0))
        STAMP(1)
        REQ0(1, 2)
        RDYWAIT(0, 1u)
        USE0(0, 0, 7)
        VMW(4) REPACK(RG0(1))
        REQ0(2, 0) CS_DMA(RG0(2))
        USE0(1, 2, 7)
        REQ0(3, 2)
        VMW(4) REPACK(RG0(2))
        USE0(2, 0, 4)
        REQ0(4, 0) CS_DMA(RG0(3))
        REQ0(5, 4)
        VMW(4) REPACK(RG0(3))
        USE0(3, 2, 4)
        CBAR();
        LDS_ADD(&sm.cnt[0], 4u)
        REQ0(6, 2)
        RDYWAIT(1, 2u)
        USE0(4, 0, 8)
        USE0(5, 4, 4)
        REQ0(7, 0)
        GRAB(1)
        USE0(6, 2, 4)
        rg = RG0(3); np = 3;
        GRABBED()
#pragma unroll 1
        while (u < NU) {
            const unsigned pr = __builtin_amdgcn_readfirstlane(par); par ^= 1;
            NEXT1()
            const unsigned tb_ = LDV(&sm.tabs), fl = LDV(&sm.rdy[u % NIN]);
            mg = LDV(&sm.merged);
            CBAR();
            K_TAB() LOADQ() LOADZ()
            NEXT2()
            if (__builtin_expect(__builtin_amdgcn_readfirstlane(fl) != u + 1 || !((__builtin_amdgcn_readfirstlane(tb_) >> rg) & 1u), 0)) {
                while (!((lds_ld(&sm.tabs) >> rg) & 1u)) SLEEP(1)
                RDYWAIT(u % NIN, u + 1)
                K_TAB() LOADQ() LOADZ()
            }
            K_PRE(0) K_PRE(1)
            if (early <= 0) GATE("v"(A[0]), "v"(A[1]), "v"(oa), "v"(ob), "s"(nb), "s"(pr))
            WAITISSUE(0, DRAW, "v"(A[0]));
            LATE0()
            K_SUM(0)
            WAITISSUE(1, "", "v"(A[1]));
            K_SUM(1)
            LATE1()
            gu = g >> 5;
            GRABBED()
            K_RED2()
            np++;
            if (chg) FLUSH(false)
            ADVANCE()
        }
#pragma unroll 1
        while (u < 2 * NU) {
            const unsigned pr = __builtin_amdgcn_readfirstlane(par); par ^= 1;
            NEXT1()
            mg = LDV(&sm.merged);
            CBAR();
            v4i pw = *(const v4i*)&sm.ent[u - NU][rg * 4];
            NEXT2()
            if (__builtin_expect(__builtin_amdgcn_readfirstlane(mg) + NU <= u, 0)) { while (lds_ld(&sm.merged) + NU <= u) SLEEP(1); CBAR(); pw = *(const v4i*)&sm.ent[u - NU][rg * 4]; }
            const double w0 = pw.x, w1 = pw.y, w2 = pw.z, w3 = pw.w;
            if (none) { LASTT(0); } else { if (early <= 0) GATE("s"(pr), "v"(w0), "v"(w1), "v"(w2), "v"(w3), "v"(oa), "v"(ob), "s"(nb)) WAITISSUE(0, DRAW, "v"(w0)); }
            LATE0()
#pragma unroll
            for (int i = 0; i < 3; i++) acc[i] = fma(w1, (double)tb[i], fma(w0, (double)ta[i], acc[i]));
            if (none) { LASTT(1); } else WAITISSUE(1, "", "v"(w2));
#pragma unroll
            for (int i = 0; i < 3; i++) acc[i] = fma(w3, (double)tb[i], fma(w2, (double)ta[i], acc[i]));
            LATE1()
            gu = g >> 5;
            GRABBED()
            np++;
            if (chg) FLUSH(true)
            ADVANCE()
        }
        STAMP(2)
        {
            const int head = blockIdx.x;
            while (lds_ld(&sm.fw_rdy) == 0) SLEEP(2)
            CBAR();
            const unsigned tt = lds_ld(&sm.tag_lo) << 18;
            const float wh = sm.fw[wave + 8 * rsel];
            const unsigned ro = (wave + 8 * rsel) * 512 + sub * 16;
            const char* rb = (const char*)ws + WS_PART + head * (NSPLIT * 512);
            v4f* pb = (v4f*)((char*)&sm.cs[0][0] + 16384);
            __hip_bfloat16* o = out + (size_t)head * DH + 6 * sub + 3 * rsel;
            tg = __builtin_amdgcn_readfirstlane(TU(2 * NU).x) + (head >= N1 ? (120) : (120)) * 128;
            GATE("s"(tt), "v"(wh), "v"(ro), "s"(rb), "v"(o))
            STAMP(60)
            STAMPV(57, NOW() - (((NOW() * 128 - __builtin_amdgcn_readfirstlane(TU(2 * NU).x)) >> 7) & 0xfffff))
            v4u R;
#define PLD(A) "global_load_dwordx4 a[" #A ":" #A "+3], %6, %7 sc1\n\t"
#define PCK(A, N, L) "s_waitcnt vmcnt(" #N ")\n\tv_accvgpr_read_b32 %1, a[" #A "+1]\n\tv_accvgpr_read_b32 %3, a[" #A "+3]\n\tv_accvgpr_read_b32 %0, a[" #A "]\n\tv_accvgpr_read_b32 %2, a[" #A "+2]\n\t" \
                     "v_xor_b32 %1, %8, %1\n\tv_xor_b32 %3, %8, %3\n\tv_or_b32 %4, %1, %3\n\tv_cmp_gt_u32 vcc, %9, %4\n\ts_cmp_eq_u64 vcc, exec\n\ts_cbranch_scc1 " L "\n\t"
            { unsigned t_;
              asm volatile("s_cmp_ge_u32 %10, %12\n\ts_cbranch_scc1 1f\n\t" PLD(16) PCK(16, 0, "3f") "1:\n\t" PLD(16) "s_sleep %11\n\t" PLD(20) "s_sleep %11\n\t" PLD(24)
                           "2:\n\t" PCK(16, 2, "3f") PLD(16) PCK(20, 2, "3f") PLD(20) PCK(24, 2, "3f") PLD(24)
                           "v_add_co_u32 %5, vcc, 64, %5\n\ts_cbranch_vccz 2b\n\ts_endpgm\n3:"
                           : "=&v"(R.x), "=&v"(R.y), "=&v"(R.z), "=&v"(R.w), "=&v"(t_), "+v"(spins) : "v"(ro), "s"(rb), "s"(tt), "s"(0x40000u), "s"(head), "i"(PSL), "i"(N1) : "scc", "vcc", "memory"); }
            STAMP(61)
            const float s0 = ldexpf(wh, (int)(R.y >> 10) - 139), s1 = ldexpf(wh, (int)(R.w >> 10) - 139);
            const float f[6] = {s0 * (float)(int)__builtin_amdgcn_sbfe(R.x, 0, 14), s0 * (float)(int)__builtin_amdgcn_sbfe(R.x, 14, 14), s0 * (float)(int)__builtin_amdgcn_sbfe(__builtin_amdgcn_alignbit(R.y, R.x, 28), 0, 14),
                                s1 * (float)(int)__builtin_amdgcn_sbfe(R.z, 0, 14), s1 * (float)(int)__builtin_amdgcn_sbfe(R.z, 14, 14), s1 * (float)(int)__builtin_amdgcn_sbfe(__builtin_amdgcn_alignbit(R.w, R.z, 28), 0, 14)};
            float sv[3];
#pragma unroll
            for (int i = 0; i < 3; i++) {
                const auto sw = __builtin_amdgcn_permlane32_swap(__float_as_uint(f[i]), __float_as_uint(f[i + 3]), false, false);
                sv[i] = __uint_as_float(sw[0]) + __uint_as_float(sw[1]);
            }
            pb[wave * 64 + lane] = (v4f){sv[0], sv[1], sv[2], 0.f};
            CBAR();
            { u64 ex_; asm volatile("s_mov_b64 %0, exec\n\ts_mov_b64 exec, 1\n\tds_add_rtn_u32 a15, %1, a13\n\ts_mov_b64 exec, %0" : "=&s"(ex_) : "v"(grab_adr + 12) : "memory"); }
            v4f ps[NSW];
#pragma unroll
            for (int w = 0; w < NSW; w++) ps[w] = pb[w * 64 + lane];
            GRABBED()
            if (g == NSW - 1) {
                const v4f sum = ((ps[0] + ps[1]) + (ps[2] + ps[3])) + ((ps[4] + ps[5]) + (ps[6] + ps[7]));
                o[0] = __float2bfloat16(sum.x); o[1] = __float2bfloat16(sum.y); o[2] = __float2bfloat16(sum.z);
                if (lane == 0) __hip_atomic_store((unsigned*)(ws + (64u << 20)) + blockIdx.x, NOW(), __ATOMIC_RELAXED, __HIP_MEMORY_SCOPE_AGENT);
                LT(1, NOW())
            }
        }
        STAMP(62)
        STAMP_DUMP()
        return;
    }

    if (wave == NSW) {
        STAMPV(24, (unsigned)__builtin_amdgcn_s_memtime()) STAMP(25)
    } else if (wave == NSW + 1) {
        u64 E; asm volatile("s_setprio 3\n\ts_load_dwordx2 %0, %1, 0\n\ts_waitcnt lgkmcnt(0)" : "=s"(E) : "s"(ws + WS_EPOCH) : "memory");
        const int NB = (3), CAP = (45);
        const int K0 = (3), GAIN = (24), BK = (3), FLOOR = (3), XM = (250), XD1 = (100), FTH = (15), XW = (40), RTH = (6), KI = (14), KD = (9), XR = (170);
        unsigned t2 = (ts + TOF) * 128 + 64 * P0;
        int off = 0, off1 = 0, hold = 0, cum = 0, dp = 0, sp = 0;
#define NOM(U) (((int)(U) - 2) * 32 * PK + max((int)(U) - NU, 0) * 32 * (PV - PK))
#define NEWLINE(K) { if (lane >= (K) && lane <= 2 * NU) sm.tu[lane] = (v2u){t2 + NOM(lane) + (cum + (lane - (int)(K)) * dp + (lane > (K) ? sp + off : off1)) * 32, \
                                                                            (unsigned)((lane >= NU ? PV : PK) + dp + (lane == (K) ? sp + off - off1 : 0))}; }
        NEWLINE(2)
        if (lane == 0) sm.tu[1] = (v2u){t2 - 32 * P0, (unsigned)P0};
        const u64 E1 = E + 1;
        const unsigned rtag = (unsigned)E1 * 0x9e3779b1u;
        const bool boss = blockIdx.x == 0;
        u64* const slot = (u64*)(ws + WS_REF + (blockIdx.x & 63) * LSTRIDE + (blockIdx.x >> 6) * 16);
        const u64* const slots = (const u64*)(ws + WS_REF + lane * LSTRIDE);
        u64* const dec = (u64*)(ws + WS_DEC);
#define GLDS(P) __hip_atomic_load(P, __ATOMIC_RELAXED, __HIP_MEMORY_SCOPE_AGENT)
#define GSTS(P, V) __hip_atomic_store(P, V, __ATOMIC_RELAXED, __HIP_MEMORY_SCOPE_AGENT)
#define UNTIL(T) while ((unsigned)((T) - NOW() * 128) < 0x40000u) SLEEP(2)
        if (lane < 2) GSTS(slot + lane, lane ? E1 : (u64)ts | ((u64)rtag << 32));
        if (lane == 0) { sm.tag_lo = (unsigned)E1; CBAR(); lds_st(&sm.tag_rdy, 1u); }
        unsigned dv = 0, npoll = 0;
        bool joined = false;
#define JOIN(T0) { const int d0 = min(max((int)((T0) - ts), -1000), 1000) + (0); off -= d0 * 4; t2 += d0 * 128; joined = true; }
        unsigned pw_ = 0, tw_ = 0, now, p; int prov = 0;
#define LOOK() { u64 t_; asm volatile("ds_read_b32 %1, %2\n\ts_memrealtime %0\n\ts_waitcnt lgkmcnt(0)" : "=s"(t_), "=v"(p) : "v"(lds_addr(&sm.grab)) : "memory"); \
                 p = __builtin_amdgcn_readfirstlane(p) - 8; now = (unsigned)t_ * 128; }
#define LATENESS(P, T) ({ const v2u e = TU((P) >> 5); (int)((T) - __builtin_amdgcn_readfirstlane(e.x) - ((P) & 31) * __builtin_amdgcn_readfirstlane(e.y)); })
#define WATCH(T) for (;;) { \
            LOOK() \
            if (__builtin_expect(now - tw_ > (80) * 128u && pw_ >= 64 && p < NPC - 8, 0)) { \
                const int lw = max(LATENESS(pw_, tw_), 0); \
                if (LATENESS(p, now) - lw > (80) * 128) \
                    for (;;) { \
                        const int d_ = min(max(LATENESS(p, now) - lw, 0) >> 5, 1600); \
                        if (lane <= 2 * NU) atomicAdd((unsigned*)&sm.tu[lane], d_ * 32u); \
                        off += d_; prov += d_; hold = 3; stl += d_; \
                        if (p != pw_ || prov > 8000) break; \
                        SLEEP(1) \
                        LOOK() \
                    } \
                } \
            if (p != pw_) { pw_ = p; tw_ = now; } \
            if ((unsigned)((T) - now) >= 0x40000u) break; \
            if ((unsigned)((T) - now) > 25 * 128) SLEEP(WSL) else SLEEP(1) }
        float tmin = 0.f; int c9 = 0, c18 = 0, stl = 0; unsigned tmean = 0;
        if (boss) {
            TOUCH((lane & 31) * 128, dec)
            UNTIL((ts + (330)) * 128)
            for (;;) {
                bool ok = true; float sum = 0.f; tmin = 0.f;
#pragma unroll
                for (int i = 0; i < 4; i++) { const u64 x = GLDS(slots + 2 * i), e = GLDS(slots + 2 * i + 1); ok &= e == E1 && (unsigned)(x >> 32) == rtag; sum += (float)min(max((int)((unsigned)x - ts), -1000), 1000); tmin = fminf(tmin, (float)(int)((unsigned)x - ts)); }
                if (__builtin_amdgcn_ballot_w64(ok) == ~0ull) { dv = ts + (int)(wave_sum(sum) * (1.f / 256)); break; }
                SLEEP(8)
            }
            if (lane == 0) { GSTS(dec, (u64)dv | ((u64)rtag << 32)); GSTS((u64*)(ws + WS_EPOCH), E1); }
            JOIN(dv)
            tmean = dv;
            dv = 0;
        }
        int sp1 = 0, tot1 = 0, tot2 = 0;
#pragma unroll 1
        for (unsigned k = 2; k < 2 * NU; k++) {
            const unsigned tk = t2 + NOM(k) + cum * 32;
            if (k >= K0 && k + 1 < 2 * NU) {
                WATCH(tk - (XM + XW) * 128)
                int sum = 0, n = 0;
                do { LOOK() sum += LATENESS(p, now) >> 7; n++; } while ((unsigned)(tk - XM * 128 - now) < 0x40000u);
                const int l = (int)((float)sum * __builtin_amdgcn_rcpf((float)n)) - BK + prov / 4;
                if (l > RTH && lane == 0) GSTS((unsigned*)(ws + WS_REF + (blockIdx.x & 63) * LSTRIDE + 64) + (blockIdx.x >> 6), (unsigned)min(l, 1000) | ((rtag + k) << 10));
            }
            const int cum0 = cum;
            if (k == 9) c9 = cum; if (k == 18) c18 = cum;
            bool got = boss;
            if (!boss && (k > K0 || !joined)) {
                WATCH(tk - XR * 128)
                const u64 x = GLDS(dec + (joined ? k * 16 : 0));
                dv = __builtin_amdgcn_readfirstlane((unsigned)x);
                got = __builtin_amdgcn_readfirstlane((unsigned)(x >> 32)) == rtag;
                npoll += !got;
                if (got && !joined) { JOIN(dv) got = false; }
            }
            sp1 = sp; sp = 0;
            if (got && k > K0) { cum = dv >> 18; sp = dv >> 6 & 4095u; dp = dv & 63u; }
            const int yld = cum - cum0 + sp;
            { const int pj = min(prov, cum - cum0); off -= pj; prov -= pj; off1 = off;
              const int ps = min(prov, sp); off -= ps; prov -= ps;
              if (hold) hold--; else prov = 0;
              const int r_ = off - prov; off -= r_ > 40 || r_ < -40 ? r_ / NB : r_; }
            NEWLINE(k)
            cum = min(cum + sp + dp, 16383);
            if (boss) dv = (cum << 18) | dp;
            if (boss && k >= K0 && k + 1 < 2 * NU && (int)(NOW() * 128 - tk) < -60 * 128) {
                WATCH(tk - (XM - XD1) * 128)
                const v4u x = __builtin_amdgcn_raw_buffer_load_b128(__builtin_amdgcn_make_buffer_rsrc(ws + WS_REF + 64, 0, 64 * LSTRIDE, 0x00020000), lane * LSTRIDE, 0, SC1);
                float a[4];
#pragma unroll
                for (int i = 0; i < 4; i++) a[i] = ((x[i] ^ ((rtag + k) << 10)) >> 10) == 0 ? (float)(x[i] & 1023u) : 0.f;
                const int tot = (int)wave_sum(a[0] + a[1] + a[2] + a[3]) >> 6;
                float mn = 0.f;
                if (tot >= 40) {
#pragma unroll 1
                    for (float st = 512.f; st >= 4.f; st *= 0.5f) {
                        const float x_ = mn + st;
                        const int c_ = __builtin_popcountll(__builtin_amdgcn_ballot_w64(a[0] >= x_)) + __builtin_popcountll(__builtin_amdgcn_ballot_w64(a[1] >= x_)) +
                                       __builtin_popcountll(__builtin_amdgcn_ballot_w64(a[2] >= x_)) + __builtin_popcountll(__builtin_amdgcn_ballot_w64(a[3] >= x_));
                        if (c_ >= (224)) mn = x_;
                    }
                }
                const float c = mn + (float)CAP, fth = (float)(FTH * (2 * NU - (int)k));
                const int cut = (int)wave_sum(fminf(a[0], c) + fminf(a[1], c) + fminf(a[2], c) + fminf(a[3], c)) >> 6, m4 = (int)mn * 4;
                const bool far = __builtin_amdgcn_ballot_w64(fmaxf(fmaxf(a[0], a[1]), fmaxf(a[2], a[3])) > fth) != 0;
                const int pend = yld + sp1 * XM / 320;
                const int nj = max(m4 - pend, 0);
                const int ns = min(max(max(min(m4 + (tot - m4) * GAIN / 16 - pend - nj, (cut - m4) * GAIN / 16), 0), far ? FLOOR * 4 : 0), 4000);
                int ndp = dp;
                if (k >= (unsigned)KD && tot >= KI && tot > tot1 && tot < tot1 + 32 && tot1 < tot2 + 32) ndp = min(dp + (2), 40);
                if (tot + tot1 < 2 && (1)) ndp = max(dp - 1, 0);
                tot2 = tot1; tot1 = tot;
                dv = (min(cum + nj, 16383) << 18) | (ns << 6) | ndp;
                if ((int)(NOW() * 128 - tk) > (60) * 128) dv = (cum << 18) | dp;
                else if (lane == 0) GSTS(dec + (k + 1) * 16, (u64)dv | ((u64)rtag << 32));
                LTK(k + 33, (int)(NOW() * 128 - tk) / 128 + 1000)
                LTK(k + 1, (min(tot, 4095) << 20) | (min(nj + ns, 1023) << 10) | (dp + 100))
                STAMPV(5 + k, (min(tot, 4095) << 20) | (min(nj + ns, 1023) << 10) | (dp + 100))
            }
        }
        WATCH(p >= NPC - 8 ? now - 128 : now + 4096)
        if (boss) {
            tmin = -wave_max(-tmin);
            const unsigned t0 = (unsigned)tent_ + (int)tmin;
            const unsigned t32 = NOW() - (int)(NOW() * 128 - __builtin_amdgcn_readfirstlane(TU(2 * NU).x)) / 128 - t0;
            while (NOW() - t0 < 14000u) SLEEP(8)
            float e = 0.f;
#pragma unroll
            for (int i = 0; i < 4; i++) { const unsigned z = GLDS((const unsigned*)(ws + (64u << 20)) + i * 64 + lane) - t0; e = fmaxf(e, z < 14000u ? (float)z : 0.f); }
            e = wave_max(e);
            const int DG = 0;
            const int x = DG == 0 ? 0 : DG == 1 ? c9 : DG == 2 ? c18 - c9 : DG == 3 ? cum - c18 : DG == 4 ? stl : DG == 6 ? ((int)e - 10000) * 4 : DG == 7 ? ((int)e - (int)t32) * 4 : (int)(tmean - t0) * 4;
            while (NOW() - t0 < 15000u + (unsigned)min(max(x, 0), 20000)) SLEEP(2)
        }
    } else if (wave == NSW + 2) {
        const int ql = lane < 48 ? lane : 47;
        unsigned c1, c2, s1, s2;
#define QT_LOADS() { int qo_ = ql; asm volatile("" : "+v"(qo_)); \
                     c1 = ((const unsigned*)q_cos)[qo_]; c2 = ((const unsigned*)q_cos)[48 + qo_]; s1 = ((const unsigned*)q_sin)[qo_]; s2 = ((const unsigned*)q_sin)[48 + qo_]; }
        unsigned q1, q2, lkz, lks, lvz, lvs;
#define UNIT_LOADS(HEAD) { \
        q1 = ((const unsigned*)(q + (size_t)(HEAD) * DH))[ql]; q2 = ((const unsigned*)(q + (size_t)(HEAD) * DH))[48 + ql]; \
        const size_t so = ((size_t)(HEAD) * SKV + brow0) / 2 + (lane >> 3) * 128 + (lane & 7); \
        lkz = SLD((const unsigned*)k_zeros + so); lks = SLD((const unsigned*)k_scales + so); \
        lvz = SLD((const unsigned*)v_zeros + so); lvs = SLD((const unsigned*)v_scales + so); }
#define UNIT_FILL(SL, U) { \
            v4f qq; \
            qq.x = bfr(bfr(bflo(q1) * bflo(c1)) + bfr(-bflo(q2) * bflo(s1))); \
            qq.y = bfr(bfr(bflo(q2) * bflo(c2)) + bfr(bflo(q1) * bflo(s2))); \
            qq.z = bfr(bfr(bfhi(q1) * bfhi(c1)) + bfr(-bfhi(q2) * bfhi(s1))); \
            qq.w = bfr(bfr(bfhi(q2) * bfhi(c2)) + bfr(bfhi(q1) * bfhi(s2))); \
            *(v2u*)&sm.q[SL][2 * ql] = (v2u){(__float_as_uint(qq.x) >> 16) | __float_as_uint(qq.y), (__float_as_uint(qq.z) >> 16) | __float_as_uint(qq.w)}; \
            *(v4f*)&sm.kz[SL][2 * lane] = (v4f){bflo(lkz), bflo(lks), bfhi(lkz), bfhi(lks)}; \
            *(v4f*)&sm.vz[SL][2 * lane] = (v4f){bflo(lvz), bflo(lvs), bfhi(lvz), bfhi(lvs)}; \
            CBAR(); \
            if (lane == 0) lds_st(&sm.rdy[SL], (U) + 1); \
            CBAR(); }
        asm volatile("s_setprio 3");
        QT_LOADS()
#pragma unroll 1
        for (unsigned u = 0; u < NU; u++) {
            UNIT_LOADS(rank + GSZ * u)
            while (u >= lds_ld(&sm.merged) + NIN) SLEEP(4);
            CBAR();
            UNIT_FILL(u % NIN, u)
            if (u < 3) STAMP(5 + u)
            if (u == 1) asm volatile("s_setprio 0");
        }
        while (lds_ld(&sm.merged) < NU) SLEEP(16)
        CBAR();
        const unsigned T = lds_ld(&sm.tag_lo);
        if (lane < NU) __hip_atomic_store((u64*)(ws + WS_LOG + (rank * NSPLIT + split) * LSTRIDE2) + lane, (u64)__float_as_uint(sm.ent[lane][130]) | ((u64)T << 32), __ATOMIC_RELAXED, __HIP_MEMORY_SCOPE_AGENT);
        while (lds_ld(&sm.merged) < NU + 3) SLEEP(16)
        for (;;) {
            const u64 x = __hip_atomic_load((const u64*)(ws + WS_LOG + ((blockIdx.x & 15) * NSPLIT + (lane & 15)) * LSTRIDE2) + (blockIdx.x >> 4), __ATOMIC_RELAXED, __HIP_MEMORY_SCOPE_AGENT);
            if (__builtin_amdgcn_ballot_w64((unsigned)(x >> 32) == T) == ~0ull) {
                const float L = __uint_as_float((unsigned)x);
                const float w = __builtin_amdgcn_exp2f(L - row_max(L));
                sm.fw[lane & 15] = w / row_sum(w);
                CBAR();
                if (lane == 0) lds_st(&sm.fw_rdy, 1u);
                break;
            }
            SLEEP(64)
        }
    } else {
        while (lds_ld(&sm.tag_rdy) == 0) SLEEP(1);
        CBAR();
        const unsigned T = lds_ld(&sm.tag_lo);
        const __amdgpu_buffer_rsrc_t rsrc = __builtin_amdgcn_make_buffer_rsrc(ws + WS_PART + (rank * NSPLIT + split) * 512, 0, NH * NSPLIT * 512, 0x00020000);
        v2u* recs = (v2u*)&sm.cs[0][0];
#pragma unroll
        for (int i = 0; i < NOUT * 3; i++) (&sm.acc[0][0])[i * 64 + lane] = 0.0;
        bool direct = false;
#define PUBLISH(N) { \
        v4u r[8]; \
        _Pragma("unroll") for (int k = 0; k < 8; k++) r[k] = ((const v4u*)recs)[k * 64 + lane]; \
        _Pragma("unroll") for (int k = 0; k < 8; k++) __builtin_amdgcn_raw_buffer_store_b128(r[k], rsrc, 2 * k + (lane >> 5) < (N) ? po : 0x7ffffff0u, k * (2 * GSZ * NSPLIT * 512), SC1); STAMP(4) }
        const unsigned po = (lane >> 5) * (GSZ * NSPLIT * 512) + (lane & 31) * 16;
#pragma unroll 1
        for (unsigned u = 0; u < 2 * NU; u++) {
            const unsigned e = u % NU, so = u % NOUT;
            const v4f h = *(const v4f*)&sm.ent[e][128];
            for (;;) {
                if (!direct && u >= 2 * NU - 3 && (int)(NOW() * 128 - __builtin_amdgcn_readfirstlane(TU(2 * NU).x)) >= (30) * 128) { direct = true; wave_sync(); PUBLISH(u - NU) }
                if (lds_ld(&sm.cnt[so]) == 32) break;
                if (direct) SLEEP(0) else SLEEP(1)
            }
            CBAR();
            STAMP(5)
            if (!direct && u == 2 * NU - 1) { direct = true; wave_sync(); PUBLISH(NU - 1) }
            if (u < NU) {
                const float2 lg = *(const float2*)&sm.ent[e][2 * lane];
                const v4f zs = *(const v4f*)&sm.vz[u % NIN][2 * lane];
                const float m = wave_max(fmaxf(lg.x, lg.y));
                const float p0 = __expf(lg.x - m), p1 = __expf(lg.y - m);
                const float x0 = p0 * zs.y, x1 = p1 * zs.w;
                const unsigned eb = max(__float_as_uint(wave_max(fmaxf(fabsf(x0), fabsf(x1)))) >> 23, 21u);
                const float f = __uint_as_float((275u - eb) << 23);
                const float w0 = rintf(x0 * f), w1 = rintf(x1 * f);
                const float den = wave_sum(p0 + p1);
                const float bz = wave_sum(fmaf(w0, zs.x, w1 * zs.z));
                const float L = fmaf(m, 1.4426950408889634f, __builtin_amdgcn_logf(den));
                *(v2u*)&sm.ent[e][2 * lane] = (v2u){(unsigned)(int)w0, (unsigned)(int)w1};
                if (lane == 0) *(v4f*)&sm.ent[e][128] = (v4f){ldexpf(1.f / den, (int)eb - 148), bz, L, 0.f};
            } else {
                const double a0 = sm.acc[so][lane], a1 = sm.acc[so][64 + lane], a2 = sm.acc[so][128 + lane];
                const float n0 = (float)(a0 - (double)h.y) * h.x, n1 = (float)(a1 - (double)h.y) * h.x, n2 = (float)(a2 - (double)h.y) * h.x;
                const unsigned e2 = max(__float_as_uint(fmaxf(fmaxf(fabsf(n0), fabsf(n1)), fabsf(n2))) >> 23, 12u);
                const float f2 = __uint_as_float((266u - e2) << 23);
                const unsigned m0 = min(max((int)rintf(n0 * f2), -8191), 8191) & 0x3fff, m1 = min(max((int)rintf(n1 * f2), -8191), 8191) & 0x3fff, m2 = min(max((int)rintf(n2 * f2), -8191), 8191) & 0x3fff;
                const v2u r = {m0 | (m1 << 14) | (m2 << 28), (m2 >> 4) | (e2 << 10) | (T << 18)};
                if (direct) {
                    STAMP(6)
                    const v4u r2 = {r.x, r.y, (unsigned)__builtin_amdgcn_update_dpp(0, (int)r.x, 0xf5, 0xf, 0xf, true), (unsigned)__builtin_amdgcn_update_dpp(0, (int)r.y, 0xf5, 0xf, 0xf, true)};
                    __builtin_amdgcn_raw_buffer_store_b128(r2, rsrc, (lane & 1) ? 0x7ffffff0u : lane * 8, e * (GSZ * NSPLIT * 512), SC1);
                }
                else recs[e * 64 + lane] = r;
                CBAR();
                sm.acc[so][lane] = 0.0; sm.acc[so][64 + lane] = 0.0; sm.acc[so][128 + lane] = 0.0;
            }
            CBAR();
            if (lane == 0) { lds_st(&sm.cnt[so], 0u); CBAR(); lds_st(&sm.merged, u + 1); }
        }
        LT(2, NOW()) LT(0, (unsigned)tent) LT(3, ts) LT(4, NOW() - (((NOW() * 128 - __builtin_amdgcn_readfirstlane(TU(2 * NU).x)) >> 7) & 0xfffff))
        STAMP(2)
        STAMP_DUMP()
        return;
    }

    STAMP(2)
    STAMP_DUMP()
}

extern "C" void kernel_launch(void* const* d_in, const int* in_sizes, int n_in,
                              void* d_out, int out_size, void* d_ws, size_t ws_size,
                              hipStream_t stream) {
    const unsigned short* q = (const unsigned short*)d_in[0];
    const int* qk = (const int*)d_in[1];
    const unsigned short* k_scales = (const unsigned short*)d_in[2];
    const unsigned short* k_zeros = (const unsigned short*)d_in[3];
    const int* qv = (const int*)d_in[4];
    const unsigned short* v_scales = (const unsigned short*)d_in[5];
    const unsigned short* v_zeros = (const unsigned short*)d_in[6];
    const unsigned short* q_cos = (const unsigned short*)d_in[7];
    const unsigned short* q_sin = (const unsigned short*)d_in[8];
    const unsigned short* k_cos = (const unsigned short*)d_in[9];
    const unsigned short* k_sin = (const unsigned short*)d_in[10];
    unsigned char* ws = (unsigned char*)d_ws;

    attn_kernel<<<NSPLIT * GSZ, NTHREADS, 0, stream>>>(q, qk, k_scales, k_zeros, qv, v_scales, v_zeros,
                                                       q_cos, q_sin, k_cos, k_sin, (__hip_bfloat16*)d_out, ws);
}
```

```cpp
#include <hip/hip_runtime.h>
#include <hip/hip_bf16.h>
#include <stdint.h>

#define DH 192
#define HALF 96
#define SKV 2048
#define NH 256
#define NSW 8
#define NWAVES (NSW + 4)
#define NTHREADS (64 * NWAVES)
#define BROWS 128
#define NSPLIT (SKV / BROWS)
#define GSZ 16
#define NU (NH / GSZ)
#define NPC (2 * NU * 32)
#define N1 (NH - GSZ)
#define NIN 4
#define NOUT 4
#define ENTW 132
#define VORD 0x9876543210afedcbull
#define VENT(V) ((unsigned)(VORD >> (4 * ((V) & 15))) & 15u)
#define SC1 16
#define LIKELY 1
#define SLD(P) (*(P))
#define LSTRIDE 4352
#define PUBP 2
#define KT 1
#define WSL 7
#define PSL 5
#define PHALF 9
#define PHI 0x0235712457024671ull
#define KAP 0x0202020220202020ull
#define PHIV PHI
#define KAPV KAP
#define PRIO 1
#define PRIO0 "s_setprio 0\n"
#define PRIO1 "\n\ts_setprio 2"
#define GTH 10
#define GS1 2
#define PFL 194

#define WS_EPOCH 0
#define WS_DEC 8192
#define WS_REF 16384
#define WS_LOG 524288
#define LSTRIDE2 1408
#define WS_PART 1048576
#define WS_FIN (WS_PART + NH * NSPLIT * 512)

typedef unsigned int v2u __attribute__((ext_vector_type(2)));
typedef unsigned int v3u __attribute__((ext_vector_type(3)));
typedef unsigned int v4u __attribute__((ext_vector_type(4)));
typedef int v4i __attribute__((ext_vector_type(4)));
typedef float v2f __attribute__((ext_vector_type(2)));
typedef float v4f __attribute__((ext_vector_type(4)));
typedef unsigned long long u64;

__device__ __forceinline__ float bflo(unsigned u) { return __uint_as_float(u << 16); }
__device__ __forceinline__ float bfhi(unsigned u) { return __uint_as_float(u & 0xffff0000u); }
__device__ __forceinline__ float bfr(float x) {
    __bf16 b = (__bf16)x;
    return __uint_as_float(((unsigned)__builtin_bit_cast(unsigned short, b)) << 16);
}

#define SLEEP(n) SLEEP_(n)
#define SLEEP_(n) asm volatile("s_sleep " #n "\n\tv_add_co_u32 %0, vcc, 1, %0\n\ts_cbranch_vccz 1f\n\ts_endpgm\n1:" : "+v"(spins) :: "vcc", "memory");
#define SPIN0 0xf0000000u
#define CBAR() asm volatile("" ::: "memory")

__device__ __forceinline__ void wave_sync() {
    __builtin_amdgcn_fence(__ATOMIC_RELEASE, "wavefront");
    __builtin_amdgcn_wave_barrier();
    __builtin_amdgcn_fence(__ATOMIC_ACQUIRE, "wavefront");
}
template <int CTRL>
__device__ __forceinline__ float dpp_add(float x) {
    int y = __builtin_amdgcn_update_dpp(0, __float_as_int(x), CTRL, 0xf, 0xf, true);
    return x + __int_as_float(y);
}
template <int CTRL>
__device__ __forceinline__ float dpp_max(float x) {
    int y = __builtin_amdgcn_update_dpp(__float_as_int(x), __float_as_int(x), CTRL, 0xf, 0xf, false);
    return fmaxf(x, __int_as_float(y));
}
__device__ __forceinline__ float row_sum(float x) { x = dpp_add<0x128>(x); x = dpp_add<0x124>(x); x = dpp_add<0x122>(x); return dpp_add<0x121>(x); }
__device__ __forceinline__ float row_max(float m) { m = dpp_max<0x128>(m); m = dpp_max<0x124>(m); m = dpp_max<0x122>(m); return dpp_max<0x121>(m); }
__device__ __forceinline__ float rdlane(float x, int l) { return __int_as_float(__builtin_amdgcn_readlane(__float_as_int(x), l)); }
__device__ __forceinline__ float wave_max(float m) { m = row_max(m); return fmaxf(fmaxf(rdlane(m, 0), rdlane(m, 16)), fmaxf(rdlane(m, 32), rdlane(m, 48))); }
__device__ __forceinline__ float wave_maxv(float m) {
    m = row_max(m);
    const auto a = __builtin_amdgcn_permlane16_swap(__float_as_uint(m), __float_as_uint(m), false, false);
    m = fmaxf(__uint_as_float(a[0]), __uint_as_float(a[1]));
    const auto b = __builtin_amdgcn_permlane32_swap(__float_as_uint(m), __float_as_uint(m), false, false);
    return fmaxf(__uint_as_float(b[0]), __uint_as_float(b[1]));
}
__device__ __forceinline__ float wave_sum(float x) { x = row_sum(x); return (rdlane(x, 0) + rdlane(x, 16)) + (rdlane(x, 32) + rdlane(x, 48)); }
__device__ __forceinline__ unsigned lds_ld(const unsigned* p) { return __builtin_amdgcn_readfirstlane(__hip_atomic_load(p, __ATOMIC_RELAXED, __HIP_MEMORY_SCOPE_WORKGROUP)); }
__device__ __forceinline__ unsigned lds_addr(const void* p) { return (unsigned)(size_t)(const __attribute__((address_space(3))) void*)p; }
#define DMA16(SRC, LDS) asm volatile("s_mov_b32 %0, m0\n\ts_mov_b32 m0, %2\n\ts_nop 0\n\tglobal_load_lds_dwordx4 %1, off\n\ts_mov_b32 m0, %0" : "=&s"(m0keep) : "v"(SRC), "s"(LDS) : "memory")
__device__ __forceinline__ void lds_st(unsigned* p, unsigned v) { __hip_atomic_store(p, v, __ATOMIC_RELAXED, __HIP_MEMORY_SCOPE_WORKGROUP); }
#define NOW() ((unsigned)__builtin_amdgcn_s_memrealtime())

struct __attribute__((aligned(16))) Smem {
    unsigned cs[32][768];
    double acc[NOUT][DH];
    float ent[NU][ENTW];
    unsigned q[NIN][HALF];
    float2 kz[NIN][BROWS];
    float2 vz[NIN][BROWS];
    v2u tu[2 * NU + 2];
    unsigned grab, merged, tabs, cdone;
    unsigned rdy[NIN], cnt[NOUT];
    unsigned tag_rdy, tag_lo, tag_hi, fw_rdy;
    int stat[32];
    float fw[NSPLIT];
};
#define NSTAT 0
#define STAMP(i) {}
#define STAMPV(i, v) {}
#define STAMP_DUMP()
#define STAMP_UNIT(N)

#define LT(I, V)
#define LTK(K, V)
#define TU(I) (*(const volatile v2u __attribute__((address_space(3)))*)(size_t)lds_addr(&sm.tu[I]))
#define LDS_ADD(P, V) { u64 ex_; asm volatile("s_mov_b64 %0, exec\n\ts_mov_b64 exec, 1\n\tds_add_u32 %1, %2\n\ts_mov_b64 exec, %0" : "=&s"(ex_) : "v"(lds_addr(P)), "v"(V) : "memory"); }
#define LDS_OR(P, V) { u64 ex_; asm volatile("s_mov_b64 %0, exec\n\ts_mov_b64 exec, 1\n\tds_or_b32 %1, %2\n\ts_mov_b64 exec, %0" : "=&s"(ex_) : "v"(lds_addr(P)), "v"(V) : "memory"); }

__global__ __launch_bounds__(NTHREADS, 3) void attn_kernel(
    const unsigned short* __restrict__ q, const int* __restrict__ qk,
    const unsigned short* __restrict__ k_scales, const unsigned short* __restrict__ k_zeros,
    const int* __restrict__ qv,
    const unsigned short* __restrict__ v_scales, const unsigned short* __restrict__ v_zeros,
    const unsigned short* __restrict__ q_cos, const unsigned short* __restrict__ q_sin,
    const unsigned short* __restrict__ k_cos, const unsigned short* __restrict__ k_sin,
    __hip_bfloat16* __restrict__ out, unsigned char* __restrict__ ws)
{
    __shared__ Smem sm;
    const int tid = threadIdx.x;
    unsigned spins = SPIN0;
#define TOUCH(OFF, BASE) asm volatile("global_load_dword a6, %0, %1" :: "v"(OFF), "s"(BASE) : "a6", "memory");
    const unsigned cl = min((blockIdx.x & 24) * 8 + tid, PFL - 1u) * 128;
    if (tid < 64) { const char* pc0; asm volatile("s_getpc_b64 %0" : "=s"(pc0)); TOUCH(cl, pc0) }
    __builtin_amdgcn_sched_barrier(0);
    const int hw = __builtin_amdgcn_readfirstlane(tid >> 6);
    const int lane = tid & 63;
    const int split = (blockIdx.x & 3) * 4 + ((blockIdx.x >> 3) & 3);
    const int rank = (blockIdx.x >> 5) * 2 + ((blockIdx.x >> 2) & 1);
    const int brow0 = split * 16;
#define ROWOF(RG) (((RG) >> 2) * 256 + ((RG) & 3) * 4)
    if (hw == 0) {
        typedef const char* const __attribute__((address_space(4))) * kap_t;
        const char* sa = ((kap_t)__builtin_amdgcn_kernarg_segment_ptr())[(0x5623 >> (4 * (split & 3))) & 7];
        unsigned o1 = ((blockIdx.x >> 5) * 256 + brow0) * (DH * 2) + min(lane, 47) * 128;
        unsigned o2 = ((rank + GSZ * (lane >> 3)) * SKV + (lane & 7) * 256 + (brow0 & ~63)) * 2, o3 = o2 + 8 * GSZ * SKV * 2;
        unsigned o4 = (rank + GSZ * (lane >> 2)) * (DH * 2) + min(lane & 3, 2) * 128;
        asm volatile("" : "+v"(o1), "+v"(o2), "+v"(o3), "+v"(o4));
        asm volatile("global_load_dword a6, %0, %1 nt\n\tglobal_load_dword a6, %0, %2 nt" :: "v"((lane & 1) * (NH * SKV * DH * 4u - 128u * NH) + blockIdx.x * 128), "s"(qk), "s"(qv) : "a6", "memory");
        TOUCH(o1, k_cos) TOUCH(o1, k_sin) TOUCH(o4, q) TOUCH(o2, sa) TOUCH(o3, sa)
    }
    if (hw == 1 && lane < 16 + NSTAT) (&sm.grab)[lane] = lane ? 0 : 64;
    asm volatile("" :: "s"(q), "s"(qk), "s"(k_scales), "s"(k_zeros), "s"(qv), "s"(v_scales), "s"(v_zeros), "s"(q_cos), "s"(q_sin), "s"(k_cos), "s"(k_sin), "s"(out), "s"(ws));
    asm volatile("s_waitcnt lgkmcnt(0)\n\ts_barrier" ::: "memory");
    const int wave = (int)(0x76543210b9a8ull >> (4 * hw)) & 15;
    STAMP(0)
    const unsigned ts = NOW();
    const int PK = (1266), PV = (1264), TOF = (100), P0 = (1380);
    if (LIKELY ? __builtin_expect(wave < NSW, 1) : wave < NSW) {
        const unsigned rot = 4 * ((unsigned)(PHI >> (60 - 4 * rank)) & 7u), kap = (unsigned)(KAP >> (60 - 4 * rank)) & 3u;
        const unsigned rotv = 4 * ((unsigned)(PHIV >> (60 - 4 * rank)) & 7u), kapv = (unsigned)(KAPV >> (60 - 4 * rank)) & 3u;
#define RGOF(G) ((G) >= NPC / 2 ? (((G) + rotv) & 31u) ^ kapv : (((G) + rot) & 31u) ^ kap)
        const char* cbase[3];
#pragma unroll
        for (int j = 0; j < 3; j++) { const int x = j * 64 + lane; cbase[j] = (x < 96 ? (const char*)k_cos + x * 16 : (const char*)k_sin + (x - 96) * 16) + brow0 * (DH * 2); }
        unsigned m0keep;
#define CS_DMA(RG) { _Pragma("unroll") for (int j = 0; j < 3; j++) DMA16(cbase[j] + ROWOF(RG) * (DH * 2), lds_addr(&sm.cs[0][0]) + (RG) * 3072 + j * 1024); }
#define REPACK(RG) { \
        unsigned* cw = &sm.cs[RG][0]; \
        const unsigned* l = cw + rsel * (DH / 2) + qoff; \
        unsigned r[2][6]; \
        _Pragma("unroll") \
        for (int j = 0; j < 2; j++) { \
            unsigned e[4][2]; \
            _Pragma("unroll") \
            for (int k = 0; k < 4; k++) { \
                const unsigned* a = l + j * DH + (k & 1) * 48 + (k >> 1) * 384;       \
                unsigned x0 = a[0], x1 = a[1]; \
                e[k][0] = __builtin_amdgcn_alignbit(x1, x0, sh); \
                e[k][1] = x1 >> sh; \
            } \
            r[j][0] = (e[0][0] & 0xffffu) | (e[3][0] << 16); \
            r[j][1] = (e[0][0] >> 16) | (e[3][0] & 0xffff0000u); \
            r[j][2] = (e[0][1] & 0xffffu) | (e[3][1] << 16); \
            r[j][3] = (e[1][0] & 0xffffu) | (e[2][0] << 16); \
            r[j][4] = (e[1][0] >> 16) | (e[2][0] & 0xffff0000u); \
            r[j][5] = (e[1][1] & 0xffffu) | (e[2][1] << 16); \
        } \
        wave_sync(); \
        _Pragma("unroll") \
        for (int j = 0; j < 2; j++) { *(v4u*)&cw[j * 256 + lane * 4] = (v4u){r[j][0], r[j][1], r[j][2], r[j][3]}; *(v2u*)&cw[512 + j * 128 + lane * 2] = (v2u){r[j][4], r[j][5]}; } \
        wave_sync(); \
        LDS_OR(&sm.tabs, 1u << (RG)) }
        const int sub = lane & 31;
        const int rsel = lane >> 5;
        const unsigned sh = (sub & 1) * 16;
        const int qoff = 3 * (sub >> 1) + (sub & 1);
        const char* kbase = (const char*)qk + (size_t)(rank * SKV + brow0) * (DH * 4);
        const char* vbase = (const char*)qv + (size_t)(rank * SKV + brow0) * (DH * 4);
        const unsigned koff = (rsel * DH + 3 * sub) * 4, voff = 3 * lane * 4;
#define AR(S) (16 + (S) * 8)
        asm volatile("" ::: "a16", "a17", "a18", "a19", "a20", "a21", "a22", "a23", "a24", "a25", "a26", "a27", "a28", "a29", "a30", "a31",
                            "a32", "a33", "a34", "a35", "a36", "a37", "a38", "a39", "a40", "a41", "a42", "a43", "a44", "a45", "a46", "a47");
        asm volatile("" ::: "a48", "a49", "a50", "a51", "a52", "a53", "a54", "a55", "a56", "a57", "a58", "a59", "a60", "a61", "a62", "a63");
        __builtin_amdgcn_s_waitcnt(0x0f70);
        int ta[3], tb[3];
#define GLD(A, OFF, BASE, IMM) asm volatile("global_load_dwordx3 a[%c0:%c1], %2, %3 offset:%4 nt" :: "i"(A), "i"((A) + 2), "v"(OFF), "s"(BASE), "i"(IMM))
#define ISSUE(S, J, BASE, OA, OB) { GLD(AR(S), OA, BASE, (J) * 1536); GLD(AR(S) + 4, OB, BASE, (J) * 1536); }
#define WAITT(S, N) asm volatile("s_waitcnt vmcnt(%6)\n\tv_accvgpr_read_b32 %0, a[%c7]\n\tv_accvgpr_read_b32 %1, a[%c8]\n\tv_accvgpr_read_b32 %2, a[%c9]\n\t" \
                              "v_accvgpr_read_b32 %3, a[%c10]\n\tv_accvgpr_read_b32 %4, a[%c11]\n\tv_accvgpr_read_b32 %5, a[%c12]" \
                              : "=v"(ta[0]), "=v"(ta[1]), "=v"(ta[2]), "=v"(tb[0]), "=v"(tb[1]), "=v"(tb[2]) \
                              : "i"(N), "i"(AR(S)), "i"(AR(S) + 1), "i"(AR(S) + 2), "i"(AR(S) + 4), "i"(AR(S) + 5), "i"(AR(S) + 6))
        const unsigned koffb = koff + 384, voffb = voff + 768;
        unsigned u = 0, rg, np = 0;
#define RG0(C) RGOF((unsigned)(wave + 8 * (C)))
        CS_DMA(RG0(0))
        STAMP(52)
        STAMP(53)
        const float scale = 0.07216878364870322f;
        double acc[3] = {0.0, 0.0, 0.0};
        bool chg, none = false;
        unsigned Qp[3], Qn[3];
        float kz4[2], ksc, kres[2];
        v4u ca[2]; v2u cb[2];
        v2f qe[2][3], A[2];
        const char* nb; unsigned oa, ob;
        unsigned g, mg, nrg, tg, late = 0;
        u64 tis;
        unsigned dg, par = 0, gu;
        int early;
        const int bth = wave < (2) ? (20) * 128 : 0x3fffffff;
        const unsigned grab_adr = lds_addr(&sm.grab);
        asm volatile("v_accvgpr_write_b32 a13, 1\n\tv_accvgpr_write_b32 a14, 2" ::: "a13", "a14", "a15");
#define GRAB(N) { u64 ex_; asm volatile("s_mov_b64 %0, exec\n\ts_mov_b64 exec, 1\n\tds_add_rtn_u32 a15, %1, a%c2\n\ts_mov_b64 exec, %0" : "=&s"(ex_) : "v"(grab_adr), "i"(12 + (N)) : "memory"); }
#define GRABBED() { unsigned t_; asm volatile("s_waitcnt lgkmcnt(0)\n\tv_accvgpr_read_b32 %1, a15\n\ts_nop 0\n\tv_readfirstlane_b32 %0, %1\n\ts_nop 1" : "=s"(g), "=v"(t_)); }
#define NEXT1() const v2u x_ = TU(g >> 5); u64 tn_; asm volatile("s_memrealtime %0" : "=s"(tn_));
#define NEXT2() { \
            chg = (g >> 5) != u; none = g >= NPC; \
            const bool nv = g >= NPC / 2; \
            nrg = RGOF(g); \
            unsigned boff; asm("s_mul_i32 %0, %1, 0x300" : "=s"(boff) : "s"((nv ? VENT(g >> 5) : g >> 5) * (GSZ * SKV) + ROWOF(nrg) + 2)); \
            nb = (nv ? vbase : kbase) + boff; \
            oa = nv ? voff : koff; ob = nv ? voffb : koffb; \
            asm volatile("s_waitcnt lgkmcnt(0)" : "+s"(tn_) : "s"(nb), "v"(oa), "v"(ob)); \
            tg = __builtin_amdgcn_readfirstlane(x_.x) + (g & 31) * __builtin_amdgcn_readfirstlane(x_.y); \
            early = none ? 0 : (int)((unsigned)tn_ * 128 - tg) - bth; \
            if (early > 0) asm volatile("s_cmp_lg_u32 %3, 0\n\ts_cbranch_scc1 1f\n\t" L4(32) "\ts_branch 2f\n1:\n\t" L4(16) "2:\n\ts_mov_b64 vcc, exec\n\ts_mov_b64 exec, 1\n\tds_add_rtn_u32 a15, %4, a13\n\ts_mov_b64 exec, vcc" \
                                    :: "v"(oa), "v"(ob), "s"(nb), "s"(pr), "v"(grab_adr) : "scc", "vcc", "memory"); }
#define L4(A) "global_load_dwordx3 a[" #A ":" #A "+2], %0, %2 offset:-1536 nt\n\tglobal_load_dwordx3 a[" #A "+4:" #A "+6], %1, %2 offset:-1536 nt\n\t" \
              "global_load_dwordx3 a[" #A "+8:" #A "+10], %0, %2 nt\n\tglobal_load_dwordx3 a[" #A "+12:" #A "+14], %1, %2 nt\n"
#define GATE(...) \
            asm volatile(PRIO0 "1:\n\ts_memrealtime vcc\n\ts_waitcnt lgkmcnt(0)\n\ts_lshl_b32 vcc_lo, vcc_lo, 7\n\ts_sub_u32 %0, %1, vcc_lo\n\ts_cmp_lt_u32 %0, 0x40000\n\ts_cbranch_scc0 2f\n\t" \
                         "s_cmp_lt_u32 %0, %2\n\ts_cbranch_scc1 1b\n\ts_sleep %3\n\ts_branch 1b\n2:" PRIO1 : "=&s"(dg) : "s"(tg), "i"(GTH * 128), "i"(GS1), __VA_ARGS__ : "scc", "vcc", "memory");
#define RD6(A) "v_accvgpr_read_b32 %0, a[%c6+" #A "]\n\tv_accvgpr_read_b32 %1, a[%c6+" #A "+1]\n\tv_accvgpr_read_b32 %2, a[%c6+" #A "+2]\n\t" \
               "v_accvgpr_read_b32 %3, a[%c6+" #A "+4]\n\tv_accvgpr_read_b32 %4, a[%c6+" #A "+5]\n\tv_accvgpr_read_b32 %5, a[%c6+" #A "+6]\n"
#define LD2(A) "global_load_dwordx3 a[%c6+" #A ":%c6+" #A "+2], %7, %9 offset:%10 nt\n\tglobal_load_dwordx3 a[%c6+" #A "+4:%c6+" #A "+6], %8, %9 offset:%10 nt"
#define OUT6 "=&v"(ta[0]), "=&v"(ta[1]), "=&v"(ta[2]), "=&v"(tb[0]), "=&v"(tb[1]), "=&v"(tb[2])
#define WI(A, B, TAIL) "s_cmp_gt_i32 %13, 0\n\ts_cbranch_scc1 3f\n\ts_waitcnt vmcnt(2)\n\t" LD2(A) TAIL "\n3:\n\ts_waitcnt vmcnt(%c14)\n\t" RD6(B)
#define WAITISSUE(S, TAIL, ...) asm volatile("s_cmp_lg_u32 %12, 0\n\ts_cbranch_scc1 1f\n\t" WI(16, 0, TAIL) "\ts_branch 2f\n1:\n\t" WI(0, 16, TAIL) "2:" \
                              : OUT6 : "i"(AR(S)), "v"(oa), "v"(ob), "s"(nb), "i"(((S) - 1) * 1536), "v"(grab_adr), "s"(pr), "s"(early), "i"(6 - 2 * (S)), __VA_ARGS__ : "scc", "vcc", "memory")
#define LASTT(S) asm volatile("s_waitcnt vmcnt(%c8)\n\ts_cmp_lg_u32 %7, 0\n\ts_cbranch_scc1 1f\n\t" RD6(0) "\ts_branch 2f\n1:\n\t" RD6(16) "2:" : OUT6 : "i"(AR(S)), "s"(pr), "i"(2 - 2 * (S)) : "scc")
#define DRAW "\n\ts_mov_b64 vcc, exec\n\ts_mov_b64 exec, 1\n\tds_add_rtn_u32 a15, %11, a13\n\ts_mov_b64 exec, vcc"
#define LATE0()
#define LATE1()
#define LATEF()
#define LATEF()
#define K_TAB() { _Pragma("unroll") for (int p = 0; p < 2; p++) { \
            const unsigned* cw = &sm.cs[rg][0]; \
            ca[p] = *(const v4u*)&cw[p * 256 + lane * 4]; cb[p] = *(const v2u*)&cw[512 + p * 128 + lane * 2]; } }
#define K_PRE(P) { \
        float e0, e1, e2, f0, f1, f2; \
        asm("v_dot2_f32_bf16 %0, %6, %12, 0\n\tv_dot2_f32_bf16 %3, %9, %15, 0\n\tv_dot2_f32_bf16 %1, %7, %13, 0\n\tv_dot2_f32_bf16 %4, %10, %16, 0\n\t" \
            "v_dot2_f32_bf16 %2, %8, %14, 0\n\tv_dot2_f32_bf16 %5, %11, %17, 0\n\ts_nop 2" \
            : "=&v"(e0), "=&v"(e1), "=&v"(e2), "=&v"(f0), "=&v"(f1), "=&v"(f2) \
            : "v"(Qp[0]), "v"(Qp[1]), "v"(Qp[2]), "v"(Qn[0]), "v"(Qn[1]), "v"(Qn[2]), \
              "v"(ca[P].x), "v"(ca[P].y), "v"(ca[P].z), "v"(ca[P].w), "v"(cb[P].x), "v"(cb[P].y)); \
        qe[P][0] = (v2f){e0, f0}; qe[P][1] = (v2f){e1, f1}; qe[P][2] = (v2f){e2, f2}; \
        A[P] = (qe[P][0] + qe[P][1] + qe[P][2]) * (v2f){-kz4[P], -kz4[P]}; }
#define K_SUM(P) { \
        _Pragma("unroll") \
        for (int i = 0; i < 3; i++) { v2f x = {(float)ta[i], (float)tb[i]}; A[P] = qe[P][i] * x + A[P]; } \
        kres[P] = A[P].x + A[P].y; }
#define K_RED2() { \
        const bool o1 = lane & 1; \
        float sx = (o1 ? kres[1] : kres[0]) + __int_as_float(__builtin_amdgcn_update_dpp(0, __float_as_int(o1 ? kres[0] : kres[1]), 0xb1, 0xf, 0xf, true)); \
        sx = dpp_add<0x4e>(sx); sx = dpp_add<0x124>(sx); sx = dpp_add<0x128>(sx); \
        const auto sw = __builtin_amdgcn_permlane16_swap(__float_as_uint(sx), __float_as_uint(sx), false, false); \
        sx = __uint_as_float(sw[0]) + __uint_as_float(sw[1]); \
        if ((lane & 30) == 0) sm.ent[u][rg * 4 + 2 * (lane & 1) + rsel] = sx * ksc; }
#define LDV(P) __hip_atomic_load(P, __ATOMIC_RELAXED, __HIP_MEMORY_SCOPE_WORKGROUP)
#define RDYWAIT(SL, SEQ) { while (lds_ld(&sm.rdy[SL]) != (SEQ)) SLEEP(1); CBAR(); }
#define LOADZ() { _Pragma("unroll") for (int p = 0; p < 2; p++) kz4[p] = sm.kz[u % NIN][rg * 4 + 2 * p + rsel].x; ksc = sm.kz[u % NIN][rg * 4 + 2 * (lane & 1) + rsel].y * scale; }
#define LOADQ() { _Pragma("unroll") for (int i = 0; i < 3; i++) { Qp[i] = sm.q[u % NIN][3 * sub + i]; Qn[i] = __builtin_amdgcn_alignbit(Qp[i], Qp[i], 16) ^ 0x80000000u; } }
#define FLUSH(ISV) { \
            mg = __builtin_amdgcn_readfirstlane(mg); \
            while (u >= mg + NOUT) { SLEEP(1); mg = lds_ld(&sm.merged); } \
            CBAR(); \
            if (ISV) { const unsigned aa = lds_addr(&sm.acc[u % NOUT][lane]); \
                asm volatile("ds_add_f64 %0, %1\n\tds_add_f64 %0, %2 offset:512\n\tds_add_f64 %0, %3 offset:1024" :: "v"(aa), "v"(acc[0]), "v"(acc[1]), "v"(acc[2]) : "memory"); \
                acc[0] = 0.0; acc[1] = 0.0; acc[2] = 0.0; } \
            CBAR(); \
            LATEF() \
            LDS_ADD(&sm.cnt[u % NOUT], np) \
            CBAR(); \
            STAMP_UNIT(np) \
            np = 0; }
#define ADVANCE() { u = gu; rg = nrg; }

#define REQ0(N, S) { tg = (ts + TOF) * 128 + (wave + 8 * (N)) * P0; \
            const char* kb = kbase + (((N) >> 2) * (GSZ * SKV) + ROWOF(RG0((N) & 3))) * (DH * 4); GATE("s"(kb)) ISSUE(S, 0, kb, koff, koffb) ISSUE((S) + 1, 1, kb, koff, koffb) STAMP(44 + (N)) }
#define USE0(N, S, W) { u = (N) >> 2; rg = RG0((N) & 3); K_TAB() LOADQ() LOADZ() K_PRE(0) K_PRE(1) WAITT(S, W); K_SUM(0) WAITT((S) + 1, W); K_SUM(1) K_RED2() }
#define VMW(N) asm volatile("s_waitcnt vmcnt(" #N ")" ::: "memory");
        REQ0(0, 0) CS_DMA(RG0(1))
        VMW(7) REPACK(RG0(0))
        STAMP(1)
        REQ0(1, 2)
        RDYWAIT(0, 1u)
        USE0(0, 0, 7)
        VMW(4) REPACK(RG0(1))
        REQ0(2, 0) CS_DMA(RG0(2))
        USE0(1, 2, 7)
        REQ0(3, 2)
        VMW(4) REPACK(RG0(2))
        USE0(2, 0, 4)
        REQ0(4, 0) CS_DMA(RG0(3))
        REQ0(5, 4)
        VMW(4) REPACK(RG0(3))
        USE0(3, 2, 4)
        CBAR();
        LDS_ADD(&sm.cnt[0], 4u)
        REQ0(6, 2)
        RDYWAIT(1, 2u)
        USE0(4, 0, 8)
        USE0(5, 4, 4)
        REQ0(7, 0)
        GRAB(1)
        USE0(6, 2, 4)
        rg = RG0(3); np = 3;
        GRABBED()
#pragma unroll 1
        while (u < NU) {
            const unsigned pr = __builtin_amdgcn_readfirstlane(par); par ^= 1;
            NEXT1()
            const unsigned tb_ = LDV(&sm.tabs), fl = LDV(&sm.rdy[u % NIN]);
            mg = LDV(&sm.merged);
            CBAR();
            K_TAB() LOADQ() LOADZ()
            NEXT2()
            if (__builtin_expect(__builtin_amdgcn_readfirstlane(fl) != u + 1 || !((__builtin_amdgcn_readfirstlane(tb_) >> rg) & 1u), 0)) {
                while (!((lds_ld(&sm.tabs) >> rg) & 1u)) SLEEP(1)
                RDYWAIT(u % NIN, u + 1)
                K_TAB() LOADQ() LOADZ()
            }
            K_PRE(0) K_PRE(1)
            if (early <= 0) GATE("v"(A[0]), "v"(A[1]), "v"(oa), "v"(ob), "s"(nb), "s"(pr))
            WAITISSUE(0, DRAW, "v"(A[0]));
            LATE0()
            K_SUM(0)
            WAITISSUE(1, "", "v"(A[1]));
            K_SUM(1)
            LATE1()
            gu = g >> 5;
            GRABBED()
            K_RED2()
            np++;
            if (chg) FLUSH(false)
            ADVANCE()
        }
#pragma unroll 1
        while (u < 2 * NU) {
            const unsigned pr = __builtin_amdgcn_readfirstlane(par); par ^= 1;
            NEXT1()
            mg = LDV(&sm.merged);
            CBAR();
            const unsigned ve = VENT(u);
            v4i pw = *(const v4i*)&sm.ent[ve][rg * 4];
            NEXT2()
            if (__builtin_expect(__builtin_amdgcn_readfirstlane(mg) <= ve, 0)) { while (lds_ld(&sm.merged) <= ve) SLEEP(1); CBAR(); pw = *(const v4i*)&sm.ent[ve][rg * 4]; }
            const double w0 = pw.x, w1 = pw.y, w2 = pw.z, w3 = pw.w;
            if (none) { LASTT(0); } else { if (early <= 0) GATE("s"(pr), "v"(w0), "v"(w1), "v"(w2), "v"(w3), "v"(oa), "v"(ob), "s"(nb)) WAITISSUE(0, DRAW, "v"(w0)); }
            LATE0()
#pragma unroll
            for (int i = 0; i < 3; i++) acc[i] = fma(w1, (double)tb[i], fma(w0, (double)ta[i], acc[i]));
            if (none) { LASTT(1); } else WAITISSUE(1, "", "v"(w2));
#pragma unroll
            for (int i = 0; i < 3; i++) acc[i] = fma(w3, (double)tb[i], fma(w2, (double)ta[i], acc[i]));
            LATE1()
            gu = g >> 5;
            GRABBED()
            np++;
            if (chg) FLUSH(true)
            ADVANCE()
        }
        STAMP(2)
        {
            const int head = blockIdx.x;
            while (lds_ld(&sm.fw_rdy) == 0) SLEEP(2)
            CBAR();
            const unsigned tt = lds_ld(&sm.tag_lo) << 18;
            const float wh = sm.fw[wave + 8 * rsel];
            const unsigned ro = (wave + 8 * rsel) * 512 + sub * 16;
            const char* rb = (const char*)ws + WS_PART + head * (NSPLIT * 512);
            v4f* pb = (v4f*)((char*)&sm.cs[0][0] + 16384);
            __hip_bfloat16* o = out + (size_t)head * DH + 6 * sub + 3 * rsel;
            tg = __builtin_amdgcn_readfirstlane(TU(2 * NU).x) + ((head >> 4) == VENT(15) ? (120) : (120)) * 128;
            GATE("s"(tt), "v"(wh), "v"(ro), "s"(rb), "v"(o))
            STAMP(60)
            STAMPV(57, NOW() - (((NOW() * 128 - __builtin_amdgcn_readfirstlane(TU(2 * NU).x)) >> 7) & 0xfffff))
            v4u R;
#define PLD(A) "global_load_dwordx4 a[" #A ":" #A "+3], %6, %7 sc1\n\t"
#define PCK(A, N, L) "s_waitcnt vmcnt(" #N ")\n\tv_accvgpr_read_b32 %1, a[" #A "+1]\n\tv_accvgpr_read_b32 %3, a[" #A "+3]\n\tv_accvgpr_read_b32 %0, a[" #A "]\n\tv_accvgpr_read_b32 %2, a[" #A "+2]\n\t" \
                     "v_xor_b32 %1, %8, %1\n\tv_xor_b32 %3, %8, %3\n\tv_or_b32 %4, %1, %3\n\tv_cmp_gt_u32 vcc, %9, %4\n\ts_cmp_eq_u64 vcc, exec\n\ts_cbranch_scc1 " L "\n\t"
            { unsigned t_;
              asm volatile("s_cmp_eq_u32 %10, %12\n\ts_cbranch_scc1 1f\n\t" PLD(16) PCK(16, 0, "3f") "1:\n\t" PLD(16) "s_sleep %11\n\t" PLD(20) "s_sleep %11\n\t" PLD(24)
                           "2:\n\t" PCK(16, 2, "3f") PLD(16) PCK(20, 2, "3f") PLD(20) PCK(24, 2, "3f") PLD(24)
                           "v_add_co_u32 %5, vcc, 64, %5\n\ts_cbranch_vccz 2b\n\ts_endpgm\n3:"
                           : "=&v"(R.x), "=&v"(R.y), "=&v"(R.z), "=&v"(R.w), "=&v"(t_), "+v"(spins) : "v"(ro), "s"(rb), "s"(tt), "s"(0x40000u), "s"(head >> 4), "i"(PSL), "i"(VENT(15)) : "scc", "vcc", "memory"); }
            STAMP(61)
            const float s0 = ldexpf(wh, (int)(R.y >> 10) - 139), s1 = ldexpf(wh, (int)(R.w >> 10) - 139);
            const float f[6] = {s0 * (float)(int)__builtin_amdgcn_sbfe(R.x, 0, 14), s0 * (float)(int)__builtin_amdgcn_sbfe(R.x, 14, 14), s0 * (float)(int)__builtin_amdgcn_sbfe(__builtin_amdgcn_alignbit(R.y, R.x, 28), 0, 14),
                                s1 * (float)(int)__builtin_amdgcn_sbfe(R.z, 0, 14), s1 * (float)(int)__builtin_amdgcn_sbfe(R.z, 14, 14), s1 * (float)(int)__builtin_amdgcn_sbfe(__builtin_amdgcn_alignbit(R.w, R.z, 28), 0, 14)};
            float sv[3];
#pragma unroll
            for (int i = 0; i < 3; i++) {
                const auto sw = __builtin_amdgcn_permlane32_swap(__float_as_uint(f[i]), __float_as_uint(f[i + 3]), false, false);
                sv[i] = __uint_as_float(sw[0]) + __uint_as_float(sw[1]);
            }
            pb[wave * 64 + lane] = (v4f){sv[0], sv[1], sv[2], 0.f};
            CBAR();
            { u64 ex_; asm volatile("s_mov_b64 %0, exec\n\ts_mov_b64 exec, 1\n\tds_add_rtn_u32 a15, %1, a13\n\ts_mov_b64 exec, %0" : "=&s"(ex_) : "v"(grab_adr + 12) : "memory"); }
            v4f ps[NSW];
#pragma unroll
            for (int w = 0; w < NSW; w++) ps[w] = pb[w * 64 + lane];
            GRABBED()
            if (g == NSW - 1) {
                const v4f sum = ((ps[0] + ps[1]) + (ps[2] + ps[3])) + ((ps[4] + ps[5]) + (ps[6] + ps[7]));
                o[0] = __float2bfloat16(sum.x); o[1] = __float2bfloat16(sum.y); o[2] = __float2bfloat16(sum.z);
                LT(1, NOW())
            }
        }
        STAMP(62)
        STAMP_DUMP()
        return;
    }

    if (wave == NSW) {
        STAMPV(24, (unsigned)__builtin_amdgcn_s_memtime()) STAMP(25)
    } else if (wave == NSW + 1) {
        u64 E; asm volatile("s_setprio 3\n\ts_load_dwordx2 %0, %1, 0\n\ts_waitcnt lgkmcnt(0)" : "=s"(E) : "s"(ws + WS_EPOCH) : "memory");
        const int NB = (3), CAP = (45);
        const int K0 = (3), GAIN = (24), BK = (3), FLOOR = (3), XM = (250), XD1 = (100), FTH = (15), XW = (40), RTH = (6), KI = (14), KD = (9), XR = (170);
        unsigned t2 = (ts + TOF) * 128 + 64 * P0;
        int off = 0, off1 = 0, hold = 0, cum = 0, dp = 0, sp = 0;
#define NOM(U) (((int)(U) - 2) * 32 * PK + max((int)(U) - NU, 0) * 32 * (PV - PK))
#define NEWLINE(K) { if (lane >= (K) && lane <= 2 * NU) sm.tu[lane] = (v2u){t2 + NOM(lane) + (cum + (lane - (int)(K)) * dp + (lane > (K) ? sp + off : off1)) * 32, \
                                                                            (unsigned)((lane >= NU ? PV : PK) + dp + (lane == (K) ? sp + off - off1 : 0))}; }
        NEWLINE(2)
        if (lane == 0) sm.tu[1] = (v2u){t2 - 32 * P0, (unsigned)P0};
        const u64 E1 = E + 1;
        const unsigned rtag = (unsigned)E1 * 0x9e3779b1u;
        const bool boss = blockIdx.x == 0;
        u64* const slot = (u64*)(ws + WS_REF + (blockIdx.x & 63) * LSTRIDE + (blockIdx.x >> 6) * 16);
        const u64* const slots = (const u64*)(ws + WS_REF + lane * LSTRIDE);
        u64* const dec = (u64*)(ws + WS_DEC);
#define GLDS(P) __hip_atomic_load(P, __ATOMIC_RELAXED, __HIP_MEMORY_SCOPE_AGENT)
#define GSTS(P, V) __hip_atomic_store(P, V, __ATOMIC_RELAXED, __HIP_MEMORY_SCOPE_AGENT)
#define UNTIL(T) while ((unsigned)((T) - NOW() * 128) < 0x40000u) SLEEP(2)
        if (lane < 2) GSTS(slot + lane, lane ? E1 : (u64)ts | ((u64)rtag << 32));
        if (lane == 0) { sm.tag_lo = (unsigned)E1; CBAR(); lds_st(&sm.tag_rdy, 1u); }
        unsigned dv = 0, npoll = 0;
        bool joined = false;
#define JOIN(T0) { const int d0 = min(max((int)((T0) - ts), -1000), 1000) + (0); off -= d0 * 4; t2 += d0 * 128; joined = true; }
        unsigned pw_ = 0, tw_ = 0, now, p; int prov = 0;
#define LOOK() { u64 t_; asm volatile("ds_read_b32 %1, %2\n\ts_memrealtime %0\n\ts_waitcnt lgkmcnt(0)" : "=s"(t_), "=v"(p) : "v"(lds_addr(&sm.grab)) : "memory"); \
                 p = __builtin_amdgcn_readfirstlane(p) - 8; now = (unsigned)t_ * 128; }
#define LATENESS(P, T) ({ const v2u e = TU((P) >> 5); (int)((T) - __builtin_amdgcn_readfirstlane(e.x) - ((P) & 31) * __builtin_amdgcn_readfirstlane(e.y)); })
#define WATCH(T) for (;;) { \
            LOOK() \
            if (__builtin_expect(now - tw_ > (80) * 128u && pw_ >= 64 && p < NPC - 8, 0)) { \
                const int lw = max(LATENESS(pw_, tw_), 0); \
                if (LATENESS(p, now) - lw > (80) * 128) \
                    for (;;) { \
                        const int d_ = min(max(LATENESS(p, now) - lw, 0) >> 5, 1600); \
                        if (lane <= 2 * NU) atomicAdd((unsigned*)&sm.tu[lane], d_ * 32u); \
                        off += d_; prov += d_; hold = 3; \
                        if (p != pw_ || prov > 8000) break; \
                        SLEEP(1) \
                        LOOK() \
                    } \
                } \
            if (p != pw_) { pw_ = p; tw_ = now; } \
            if ((unsigned)((T) - now) >= 0x40000u) break; \
            if ((unsigned)((T) - now) > 25 * 128) SLEEP(WSL) else SLEEP(1) }
        if (boss) {
            TOUCH((lane & 31) * 128, dec)
            UNTIL((ts + (330)) * 128)
            for (;;) {
                bool ok = true; float sum = 0.f;
#pragma unroll
                for (int i = 0; i < 4; i++) { const u64 x = GLDS(slots + 2 * i), e = GLDS(slots + 2 * i + 1); ok &= e == E1 && (unsigned)(x >> 32) == rtag; sum += (float)min(max((int)((unsigned)x - ts), -1000), 1000); }
                if (__builtin_amdgcn_ballot_w64(ok) == ~0ull) { dv = ts + (int)(wave_sum(sum) * (1.f / 256)); break; }
                SLEEP(8)
            }
            if (lane == 0) { GSTS(dec, (u64)dv | ((u64)rtag << 32)); GSTS((u64*)(ws + WS_EPOCH), E1); }
            JOIN(dv)
            dv = 0;
        }
        int sp1 = 0, tot1 = 0, tot2 = 0;
#pragma unroll 1
        for (unsigned k = 2; k < 2 * NU; k++) {
            const unsigned tk = t2 + NOM(k) + cum * 32;
            if (k >= K0 && k + 1 < 2 * NU) {
                WATCH(tk - (XM + XW) * 128)
                int sum = 0, n = 0;
                do { LOOK() sum += LATENESS(p, now) >> 7; n++; } while ((unsigned)(tk - XM * 128 - now) < 0x40000u);
                const int l = (int)((float)sum * __builtin_amdgcn_rcpf((float)n)) - BK + prov / 4;
                if (l > RTH && lane == 0) GSTS((unsigned*)(ws + WS_REF + (blockIdx.x & 63) * LSTRIDE + 64) + (blockIdx.x >> 6), (unsigned)min(l, 1000) | ((rtag + k) << 10));
            }
            const int cum0 = cum;
            bool got = boss;
            if (!boss && (k > K0 || !joined)) {
                WATCH(tk - XR * 128)
                const u64 x = GLDS(dec + (joined ? k * 16 : 0));
                dv = __builtin_amdgcn_readfirstlane((unsigned)x);
                got = __builtin_amdgcn_readfirstlane((unsigned)(x >> 32)) == rtag;
                npoll += !got;
                if (got && !joined) { JOIN(dv) got = false; }
            }
            sp1 = sp; sp = 0;
            if (got && k > K0) { cum = dv >> 18; sp = dv >> 6 & 4095u; dp = dv & 63u; }
            const int yld = cum - cum0 + sp;
            { const int pj = min(prov, cum - cum0); off -= pj; prov -= pj; off1 = off;
              const int ps = min(prov, sp); off -= ps; prov -= ps;
              if (hold) hold--; else prov = 0;
              const int r_ = off - prov; off -= r_ > 40 || r_ < -40 ? r_ / NB : r_; }
            NEWLINE(k)
            cum = min(cum + sp + dp, 16383);
            if (boss) dv = (cum << 18) | dp;
            if (boss && k >= K0 && k + 1 < 2 * NU && (int)(NOW() * 128 - tk) < -60 * 128) {
                WATCH(tk - (XM - XD1) * 128)
                const v4u x = __builtin_amdgcn_raw_buffer_load_b128(__builtin_amdgcn_make_buffer_rsrc(ws + WS_REF + 64, 0, 64 * LSTRIDE, 0x00020000), lane * LSTRIDE, 0, SC1);
                float a[4];
#pragma unroll
                for (int i = 0; i < 4; i++) a[i] = ((x[i] ^ ((rtag + k) << 10)) >> 10) == 0 ? (float)(x[i] & 1023u) : 0.f;
                const int tot = (int)wave_sum(a[0] + a[1] + a[2] + a[3]) >> 6;
                float mn = 0.f;
                if (tot >= 40) {
#pragma unroll 1
                    for (float st = 512.f; st >= 4.f; st *= 0.5f) {
                        const float x_ = mn + st;
                        const int c_ = __builtin_popcountll(__builtin_amdgcn_ballot_w64(a[0] >= x_)) + __builtin_popcountll(__builtin_amdgcn_ballot_w64(a[1] >= x_)) +
                                       __builtin_popcountll(__builtin_amdgcn_ballot_w64(a[2] >= x_)) + __builtin_popcountll(__builtin_amdgcn_ballot_w64(a[3] >= x_));
                        if (c_ >= (224)) mn = x_;
                    }
                }
                const float c = mn + (float)CAP, fth = (float)(FTH * (2 * NU - (int)k));
                const int cut = (int)wave_sum(fminf(a[0], c) + fminf(a[1], c) + fminf(a[2], c) + fminf(a[3], c)) >> 6, m4 = (int)mn * 4;
                const bool far = __builtin_amdgcn_ballot_w64(fmaxf(fmaxf(a[0], a[1]), fmaxf(a[2], a[3])) > fth) != 0;
                const int pend = yld + sp1 * XM / 320;
                const int nj = max(m4 - pend, 0);
                const int ns = min(max(max(min(m4 + (tot - m4) * GAIN / 16 - pend - nj, (cut - m4) * GAIN / 16), 0), far ? FLOOR * 4 : 0), 4000);
                int ndp = dp;
                if (k >= (unsigned)KD && tot >= KI && tot > tot1 && tot < tot1 + 32 && tot1 < tot2 + 32) ndp = min(dp + (2), 40);
                if (tot + tot1 < 2 && (1)) ndp = max(dp - 1, 0);
                tot2 = tot1; tot1 = tot;
                dv = (min(cum + nj, 16383) << 18) | (ns << 6) | ndp;
                if ((int)(NOW() * 128 - tk) > (60) * 128) dv = (cum << 18) | dp;
                else if (lane == 0) GSTS(dec + (k + 1) * 16, (u64)dv | ((u64)rtag << 32));
                LTK(k + 33, (int)(NOW() * 128 - tk) / 128 + 1000)
                LTK(k + 1, (min(tot, 4095) << 20) | (min(nj + ns, 1023) << 10) | (dp + 100))
                STAMPV(5 + k, (min(tot, 4095) << 20) | (min(nj + ns, 1023) << 10) | (dp + 100))
            }
        }
        WATCH(p >= NPC - 8 ? now - 128 : now + 4096)
    } else if (wave == NSW + 2) {
        const int ql = lane < 48 ? lane : 47;
        unsigned c1, c2, s1, s2;
#define QT_LOADS() { int qo_ = ql; asm volatile("" : "+v"(qo_)); \
                     c1 = ((const unsigned*)q_cos)[qo_]; c2 = ((const unsigned*)q_cos)[48 + qo_]; s1 = ((const unsigned*)q_sin)[qo_]; s2 = ((const unsigned*)q_sin)[48 + qo_]; }
        unsigned q1, q2, lkz, lks, lvz, lvs;
#define UNIT_LOADS(HEAD) { \
        q1 = ((const unsigned*)(q + (size_t)(HEAD) * DH))[ql]; q2 = ((const unsigned*)(q + (size_t)(HEAD) * DH))[48 + ql]; \
        const size_t so = ((size_t)(HEAD) * SKV + brow0) / 2 + (lane >> 3) * 128 + (lane & 7); \
        lkz = SLD((const unsigned*)k_zeros + so); lks = SLD((const unsigned*)k_scales + so); \
        lvz = SLD((const unsigned*)v_zeros + so); lvs = SLD((const unsigned*)v_scales + so); }
#define UNIT_FILL(SL, U) { \
            v4f qq; \
            qq.x = bfr(bfr(bflo(q1) * bflo(c1)) + bfr(-bflo(q2) * bflo(s1))); \
            qq.y = bfr(bfr(bflo(q2) * bflo(c2)) + bfr(bflo(q1) * bflo(s2))); \
            qq.z = bfr(bfr(bfhi(q1) * bfhi(c1)) + bfr(-bfhi(q2) * bfhi(s1))); \
            qq.w = bfr(bfr(bfhi(q2) * bfhi(c2)) + bfr(bfhi(q1) * bfhi(s2))); \
            *(v2u*)&sm.q[SL][2 * ql] = (v2u){(__float_as_uint(qq.x) >> 16) | __float_as_uint(qq.y), (__float_as_uint(qq.z) >> 16) | __float_as_uint(qq.w)}; \
            *(v4f*)&sm.kz[SL][2 * lane] = (v4f){bflo(lkz), bflo(lks), bfhi(lkz), bfhi(lks)}; \
            *(v4f*)&sm.vz[SL][2 * lane] = (v4f){bflo(lvz), bflo(lvs), bfhi(lvz), bfhi(lvs)}; \
            CBAR(); \
            if (lane == 0) lds_st(&sm.rdy[SL], (U) + 1); \
            CBAR(); }
        asm volatile("s_setprio 3");
        QT_LOADS()
#pragma unroll 1
        for (unsigned u = 0; u < NU; u++) {
            UNIT_LOADS(rank + GSZ * u)
            while (u >= lds_ld(&sm.merged) + NIN) SLEEP(4);
            CBAR();
            UNIT_FILL(u % NIN, u)
            if (u < 3) STAMP(5 + u)
            if (u == 1) asm volatile("s_setprio 0");
        }
        while (lds_ld(&sm.merged) < NU) SLEEP(16)
        CBAR();
        const unsigned T = lds_ld(&sm.tag_lo);
        if (lane < NU) __hip_atomic_store((u64*)(ws + WS_LOG + (rank * NSPLIT + split) * LSTRIDE2) + lane, (u64)__float_as_uint(sm.ent[lane][130]) | ((u64)T << 32), __ATOMIC_RELAXED, __HIP_MEMORY_SCOPE_AGENT);
        while (lds_ld(&sm.merged) < NU + 3) SLEEP(16)
        for (;;) {
            const u64 x = __hip_atomic_load((const u64*)(ws + WS_LOG + ((blockIdx.x & 15) * NSPLIT + (lane & 15)) * LSTRIDE2) + (blockIdx.x >> 4), __ATOMIC_RELAXED, __HIP_MEMORY_SCOPE_AGENT);
            if (__builtin_amdgcn_ballot_w64((unsigned)(x >> 32) == T) == ~0ull) {
                const float L = __uint_as_float((unsigned)x);
                const float w = __builtin_amdgcn_exp2f(L - row_max(L));
                sm.fw[lane & 15] = w / row_sum(w);
                CBAR();
                if (lane == 0) lds_st(&sm.fw_rdy, 1u);
                break;
            }
            SLEEP(64)
        }
    } else {
        while (lds_ld(&sm.tag_rdy) == 0) SLEEP(1);
        CBAR();
        const unsigned T = lds_ld(&sm.tag_lo);
        const __amdgpu_buffer_rsrc_t rsrc = __builtin_amdgcn_make_buffer_rsrc(ws + WS_PART + (rank * NSPLIT + split) * 512, 0, NH * NSPLIT * 512, 0x00020000);
        v2u* recs = (v2u*)&sm.cs[0][0];
#pragma unroll
        for (int i = 0; i < NOUT * 3; i++) (&sm.acc[0][0])[i * 64 + lane] = 0.0;
        bool direct = false;
#define PUBLISH(N) { \
        v4u r[8]; \
        _Pragma("unroll") for (int k = 0; k < 8; k++) r[k] = ((const v4u*)recs)[k * 64 + lane]; \
        _Pragma("unroll") for (int k = 0; k < 8; k++) __builtin_amdgcn_raw_buffer_store_b128(r[k], rsrc, 2 * k + (lane >> 5) < (N) ? po + (lane < 32 ? VENT(2 * k) : VENT(2 * k + 1)) * (GSZ * NSPLIT * 512) : 0x7ffffff0u, 0, SC1); STAMP(4) }
        const unsigned po = (lane & 31) * 16;
#pragma unroll 1
        for (unsigned u = 0; u < 2 * NU; u++) {
            const unsigned e = u < NU ? u : VENT(u), so = u % NOUT;
            const v4f h = *(const v4f*)&sm.ent[e][128];
            for (;;) {
                if (!direct && u >= 2 * NU - 3 && (int)(NOW() * 128 - __builtin_amdgcn_readfirstlane(TU(2 * NU).x)) >= (30) * 128) { direct = true; wave_sync(); PUBLISH(u - NU) }
                if (lds_ld(&sm.cnt[so]) == 32) break;
                if (direct) SLEEP(0) else SLEEP(1)
            }
            CBAR();
            STAMP(5)
            if (!direct && u == 2 * NU - 1) { direct = true; wave_sync(); PUBLISH(NU - 1) }
            if (u < NU) {
                const float2 lg = *(const float2*)&sm.ent[e][2 * lane];
                const v4f zs = *(const v4f*)&sm.vz[u % NIN][2 * lane];
                const float m = wave_max(fmaxf(lg.x, lg.y));
                const float p0 = __expf(lg.x - m), p1 = __expf(lg.y - m);
                const float x0 = p0 * zs.y, x1 = p1 * zs.w;
                const unsigned eb = max(__float_as_uint(wave_max(fmaxf(fabsf(x0), fabsf(x1)))) >> 23, 21u);
                const float f = __uint_as_float((275u - eb) << 23);
                const float w0 = rintf(x0 * f), w1 = rintf(x1 * f);
                const float den = wave_sum(p0 + p1);
                const float bz = wave_sum(fmaf(w0, zs.x, w1 * zs.z));
                const float L = fmaf(m, 1.4426950408889634f, __builtin_amdgcn_logf(den));
                *(v2u*)&sm.ent[e][2 * lane] = (v2u){(unsigned)(int)w0, (unsigned)(int)w1};
                if (lane == 0) *(v4f*)&sm.ent[e][128] = (v4f){ldexpf(1.f / den, (int)eb - 148), bz, L, 0.f};
            } else {
                const double a0 = sm.acc[so][lane], a1 = sm.acc[so][64 + lane], a2 = sm.acc[so][128 + lane];
                const float n0 = (float)(a0 - (double)h.y) * h.x, n1 = (float)(a1 - (double)h.y) * h.x, n2 = (float)(a2 - (double)h.y) * h.x;
                const unsigned e2 = max(__float_as_uint(fmaxf(fmaxf(fabsf(n0), fabsf(n1)), fabsf(n2))) >> 23, 12u);
                const float f2 = __uint_as_float((266u - e2) << 23);
                const unsigned m0 = min(max((int)rintf(n0 * f2), -8191), 8191) & 0x3fff, m1 = min(max((int)rintf(n1 * f2), -8191), 8191) & 0x3fff, m2 = min(max((int)rintf(n2 * f2), -8191), 8191) & 0x3fff;
                const v2u r = {m0 | (m1 << 14) | (m2 << 28), (m2 >> 4) | (e2 << 10) | (T << 18)};
                if (direct) {
                    STAMP(6)
                    const v4u r2 = {r.x, r.y, (unsigned)__builtin_amdgcn_update_dpp(0, (int)r.x, 0xf5, 0xf, 0xf, true), (unsigned)__builtin_amdgcn_update_dpp(0, (int)r.y, 0xf5, 0xf, 0xf, true)};
                    __builtin_amdgcn_raw_buffer_store_b128(r2, rsrc, (lane & 1) ? 0x7ffffff0u : lane * 8, e * (GSZ * NSPLIT * 512), SC1);
                }
                else recs[(u - NU) * 64 + lane] = r;
                CBAR();
                sm.acc[so][lane] = 0.0; sm.acc[so][64 + lane] = 0.0; sm.acc[so][128 + lane] = 0.0;
            }
            CBAR();
            if (lane == 0) { lds_st(&sm.cnt[so], 0u); CBAR(); lds_st(&sm.merged, u + 1); }
        }
        LT(2, NOW()) LT(0, (unsigned)tent) LT(3, ts) LT(4, NOW() - (((NOW() * 128 - __builtin_amdgcn_readfirstlane(TU(2 * NU).x)) >> 7) & 0xfffff))
        STAMP(2)
        STAMP_DUMP()
        return;
    }

    STAMP(2)
    STAMP_DUMP()
}

extern "C" void kernel_launch(void* const* d_in, const int* in_sizes, int n_in,
                              void* d_out, int out_size, void* d_ws, size_t ws_size,
                              hipStream_t stream) {
    const unsigned short* q = (const unsigned short*)d_in[0];
    const int* qk = (const int*)d_in[1];
    const unsigned short* k_scales = (const unsigned short*)d_in[2];
    const unsigned short* k_zeros = (const unsigned short*)d_in[3];
    const int* qv = (const int*)d_in[4];
    const unsigned short* v_scales = (const unsigned short*)d_in[5];
    const unsigned short* v_zeros = (const unsigned short*)d_in[6];
    const unsigned short* q_cos = (const unsigned short*)d_in[7];
    const unsigned short* q_sin = (const unsigned short*)d_in[8];
    const unsigned short* k_cos = (const unsigned short*)d_in[9];
    const unsigned short* k_sin = (const unsigned short*)d_in[10];
    unsigned char* ws = (unsigned char*)d_ws;

    attn_kernel<<<NSPLIT * GSZ, NTHREADS, 0, stream>>>(q, qk, k_scales, k_zeros, qv, v_scales, v_zeros,
                                                       q_cos, q_sin, k_cos, k_sin, (__hip_bfloat16*)d_out, ws);
}
```

```cpp
#define PFL 183
#define TPSN 0
#define TP2 0
#include <hip/hip_runtime.h>
#include <hip/hip_bf16.h>
#include <stdint.h>

#define DH 192
#define HALF 96
#define SKV 2048
#define NH 256
#define NSW 8
#define NWAVES (NSW + 4)
#define NTHREADS (64 * NWAVES)
#define BROWS 128
#define NSPLIT (SKV / BROWS)
#define GSZ 16
#define NU (NH / GSZ)
#define NPC (2 * NU * 32)
#define N1 (NH - GSZ)
#define NIN 4
#define NOUT 4
#define ENTW 132
#define VORD 0x9876543210afedcbull
#define VENT(V) ((unsigned)(VORD >> (4 * ((V) & 15))) & 15u)
#define SC1 16
#define LIKELY 1
#define SLD(P) (*(P))
#define LSTRIDE 4352
#define PUBP 2
#define TPTN 0
#define POL0 ""
#define POL1 "nt"
#define POL2 "sc1"
#define POL3 "sc0 sc1"
#define POL4 "sc1 nt"
#define POL5 "sc0 nt"
#define POLX_(N) POL##N
#define POLX(N) POLX_(N)
#define TPT POLX(TPTN)
#define TPS POLX(TPSN)
#define KT 1
#define WSL 7
#define PSL 5
#define PHALF 9
#define PHI 0x0235712457024671ull
#define KAP 0x0202020220202020ull
#define PHIV PHI
#define KAPV KAP
#define PRIO 1
#define PRIO0 "s_setprio 0\n"
#define PRIO1 "\n\ts_setprio 2"
#define GTH 10
#define GS1 2

#define WS_EPOCH 0
#define WS_DEC 8192
#define WS_REF 16384
#define WS_LOG 524288
#define LSTRIDE2 1408
#define WS_PART 1048576
#define WS_FIN (WS_PART + NH * NSPLIT * 512)

typedef unsigned int v2u __attribute__((ext_vector_type(2)));
typedef unsigned int v3u __attribute__((ext_vector_type(3)));
typedef unsigned int v4u __attribute__((ext_vector_type(4)));
typedef int v4i __attribute__((ext_vector_type(4)));
typedef float v2f __attribute__((ext_vector_type(2)));
typedef float v4f __attribute__((ext_vector_type(4)));
typedef unsigned long long u64;

__device__ __forceinline__ float bflo(unsigned u) { return __uint_as_float(u << 16); }
__device__ __forceinline__ float bfhi(unsigned u) { return __uint_as_float(u & 0xffff0000u); }
__device__ __forceinline__ float bfr(float x) {
    __bf16 b = (__bf16)x;
    return __uint_as_float(((unsigned)__builtin_bit_cast(unsigned short, b)) << 16);
}

#define SLEEP(n) SLEEP_(n)
#define SLEEP_(n) asm volatile("s_sleep " #n "\n\tv_add_co_u32 %0, vcc, 1, %0\n\ts_cbranch_vccz 1f\n\ts_endpgm\n1:" : "+v"(spins) :: "vcc", "memory");
#define SPIN0 0xf0000000u
#define CBAR() asm volatile("" ::: "memory")

__device__ __forceinline__ void wave_sync() {
    __builtin_amdgcn_fence(__ATOMIC_RELEASE, "wavefront");
    __builtin_amdgcn_wave_barrier();
    __builtin_amdgcn_fence(__ATOMIC_ACQUIRE, "wavefront");
}
template <int CTRL>
__device__ __forceinline__ float dpp_add(float x) {
    int y = __builtin_amdgcn_update_dpp(0, __float_as_int(x), CTRL, 0xf, 0xf, true);
    return x + __int_as_float(y);
}
template <int CTRL>
__device__ __forceinline__ float dpp_max(float x) {
    int y = __builtin_amdgcn_update_dpp(__float_as_int(x), __float_as_int(x), CTRL, 0xf, 0xf, false);
    return fmaxf(x, __int_as_float(y));
}
__device__ __forceinline__ float row_sum(float x) { x = dpp_add<0x128>(x); x = dpp_add<0x124>(x); x = dpp_add<0x122>(x); return dpp_add<0x121>(x); }
__device__ __forceinline__ float row_max(float m) { m = dpp_max<0x128>(m); m = dpp_max<0x124>(m); m = dpp_max<0x122>(m); return dpp_max<0x121>(m); }
__device__ __forceinline__ float rdlane(float x, int l) { return __int_as_float(__builtin_amdgcn_readlane(__float_as_int(x), l)); }
__device__ __forceinline__ float wave_max(float m) { m = row_max(m); return fmaxf(fmaxf(rdlane(m, 0), rdlane(m, 16)), fmaxf(rdlane(m, 32), rdlane(m, 48))); }
__device__ __forceinline__ float wave_maxv(float m) {
    m = row_max(m);
    const auto a = __builtin_amdgcn_permlane16_swap(__float_as_uint(m), __float_as_uint(m), false, false);
    m = fmaxf(__uint_as_float(a[0]), __uint_as_float(a[1]));
    const auto b = __builtin_amdgcn_permlane32_swap(__float_as_uint(m), __float_as_uint(m), false, false);
    return fmaxf(__uint_as_float(b[0]), __uint_as_float(b[1]));
}
__device__ __forceinline__ float wave_sum(float x) { x = row_sum(x); return (rdlane(x, 0) + rdlane(x, 16)) + (rdlane(x, 32) + rdlane(x, 48)); }
__device__ __forceinline__ unsigned lds_ld(const unsigned* p) { return __builtin_amdgcn_readfirstlane(__hip_atomic_load(p, __ATOMIC_RELAXED, __HIP_MEMORY_SCOPE_WORKGROUP)); }
__device__ __forceinline__ unsigned lds_addr(const void* p) { return (unsigned)(size_t)(const __attribute__((address_space(3))) void*)p; }
#define DMA16(SRC, LDS) asm volatile("s_mov_b32 %0, m0\n\ts_mov_b32 m0, %2\n\ts_nop 0\n\tglobal_load_lds_dwordx4 %1, off\n\ts_mov_b32 m0, %0" : "=&s"(m0keep) : "v"(SRC), "s"(LDS) : "memory")
__device__ __forceinline__ void lds_st(unsigned* p, unsigned v) { __hip_atomic_store(p, v, __ATOMIC_RELAXED, __HIP_MEMORY_SCOPE_WORKGROUP); }
#define NOW() ((unsigned)__builtin_amdgcn_s_memrealtime())

struct __attribute__((aligned(16))) Smem {
    unsigned cs[32][768];
    double acc[NOUT][DH];
    float ent[NU][ENTW];
    unsigned q[NIN][HALF];
    float2 kz[NIN][BROWS];
    float2 vz[NIN][BROWS];
    v2u tu[2 * NU + 2];
    unsigned grab, merged, tabs, cdone;
    unsigned rdy[NIN], cnt[NOUT];
    unsigned tag_rdy, tag_lo, tag_hi, fw_rdy;
    int stat[32];
    float fw[NSPLIT];
};
#define NSTAT 0
#define STAMP(i) {}
#define STAMPV(i, v) {}
#define STAMP_DUMP()
#define STAMP_UNIT(N)

#define LT(I, V)
#define LTK(K, V)
#define TU(I) (*(const volatile v2u __attribute__((address_space(3)))*)(size_t)lds_addr(&sm.tu[I]))
#define LDS_ADD(P, V) { u64 ex_; asm volatile("s_mov_b64 %0, exec\n\ts_mov_b64 exec, 1\n\tds_add_u32 %1, %2\n\ts_mov_b64 exec, %0" : "=&s"(ex_) : "v"(lds_addr(P)), "v"(V) : "memory"); }
#define LDS_OR(P, V) { u64 ex_; asm volatile("s_mov_b64 %0, exec\n\ts_mov_b64 exec, 1\n\tds_or_b32 %1, %2\n\ts_mov_b64 exec, %0" : "=&s"(ex_) : "v"(lds_addr(P)), "v"(V) : "memory"); }

__global__ __launch_bounds__(NTHREADS, 3) void attn_kernel(
    const unsigned short* __restrict__ q, const int* __restrict__ qk,
    const unsigned short* __restrict__ k_scales, const unsigned short* __restrict__ k_zeros,
    const int* __restrict__ qv,
    const unsigned short* __restrict__ v_scales, const unsigned short* __restrict__ v_zeros,
    const unsigned short* __restrict__ q_cos, const unsigned short* __restrict__ q_sin,
    const unsigned short* __restrict__ k_cos, const unsigned short* __restrict__ k_sin,
    __hip_bfloat16* __restrict__ out, unsigned char* __restrict__ ws)
{
    __shared__ Smem sm;
    const int tid = threadIdx.x;
    unsigned spins = SPIN0;
#define TOUCH(OFF, BASE) asm volatile("global_load_dword a6, %0, %1" :: "v"(OFF), "s"(BASE) : "a6", "memory");
    const unsigned cl = min((blockIdx.x & 24) * 8 + tid, PFL - 1u) * 128;
    if (tid < 64) { const char* pc0; asm volatile("s_getpc_b64 %0" : "=s"(pc0)); TOUCH(cl, pc0) }
    __builtin_amdgcn_sched_barrier(0);
    const int hw = __builtin_amdgcn_readfirstlane(tid >> 6);
    const int lane = tid & 63;
    const int split = (blockIdx.x & 3) * 4 + ((blockIdx.x >> 3) & 3);
    const int rank = (blockIdx.x >> 5) * 2 + ((blockIdx.x >> 2) & 1);
    const int brow0 = split * 16;
#define ROWOF(RG) (((RG) >> 2) * 256 + ((RG) & 3) * 4)
    if (hw == 0) {
        typedef const char* const __attribute__((address_space(4))) * kap_t;
        const char* sa = ((kap_t)__builtin_amdgcn_kernarg_segment_ptr())[(0x5623 >> (4 * (split & 3))) & 7];
        unsigned o1 = ((blockIdx.x >> 5) * 256 + brow0) * (DH * 2) + min(lane, 47) * 128;
        unsigned o2 = ((rank + GSZ * (lane >> 3)) * SKV + (lane & 7) * 256 + (brow0 & ~63)) * 2, o3 = o2 + 8 * GSZ * SKV * 2;
        unsigned o4 = (rank + GSZ * (lane >> 2)) * (DH * 2) + min(lane & 3, 2) * 128;
        asm volatile("" : "+v"(o1), "+v"(o2), "+v"(o3), "+v"(o4));
        asm volatile("global_load_dword a6, %0, %1 nt\n\tglobal_load_dword a6, %0, %2 nt" :: "v"((lane & 1) * (NH * SKV * DH * 4u - 128u * NH) + blockIdx.x * 128), "s"(qk), "s"(qv) : "a6", "memory");
#define TOUCHP(OFF, BASE, POL) asm volatile("global_load_dword a6, %0, %1 " POL :: "v"(OFF), "s"(BASE) : "a6", "memory");
        TOUCHP(o1, k_cos, TPT) TOUCHP(o1, k_sin, TPT) TOUCH(o4, q) TOUCHP(o2, sa, TPS) TOUCHP(o3, sa, TPS)
    }
    if (hw == 1 && lane < 16 + NSTAT) (&sm.grab)[lane] = lane ? 0 : 64;
    asm volatile("" :: "s"(q), "s"(qk), "s"(k_scales), "s"(k_zeros), "s"(qv), "s"(v_scales), "s"(v_zeros), "s"(q_cos), "s"(q_sin), "s"(k_cos), "s"(k_sin), "s"(out), "s"(ws));
    asm volatile("s_waitcnt lgkmcnt(0)\n\ts_barrier" ::: "memory");
    const int wave = (int)(0x76543210b9a8ull >> (4 * hw)) & 15;
    STAMP(0)
    const unsigned ts = NOW();
    const int PK = (1268), PV = (1268), TOF = (100), P0 = (1370);
    if (LIKELY ? __builtin_expect(wave < NSW, 1) : wave < NSW) {
        const unsigned rot = 4 * ((unsigned)(PHI >> (60 - 4 * rank)) & 7u), kap = (unsigned)(KAP >> (60 - 4 * rank)) & 3u;
        const unsigned rotv = 4 * ((unsigned)(PHIV >> (60 - 4 * rank)) & 7u), kapv = (unsigned)(KAPV >> (60 - 4 * rank)) & 3u;
#define RGOF(G) ((G) >= NPC / 2 ? (((G) + rotv) & 31u) ^ kapv : (((G) + rot) & 31u) ^ kap)
        const char* cbase[3];
#pragma unroll
        for (int j = 0; j < 3; j++) { const int x = j * 64 + lane; cbase[j] = (x < 96 ? (const char*)k_cos + x * 16 : (const char*)k_sin + (x - 96) * 16) + brow0 * (DH * 2); }
        unsigned m0keep;
#define CS_DMA(RG) { _Pragma("unroll") for (int j = 0; j < 3; j++) DMA16(cbase[j] + ROWOF(RG) * (DH * 2), lds_addr(&sm.cs[0][0]) + (RG) * 3072 + j * 1024); }
#define REPACK(RG) { \
        unsigned* cw = &sm.cs[RG][0]; \
        const unsigned* l = cw + rsel * (DH / 2) + qoff; \
        unsigned r[2][6]; \
        _Pragma("unroll") \
        for (int j = 0; j < 2; j++) { \
            unsigned e[4][2]; \
            _Pragma("unroll") \
            for (int k = 0; k < 4; k++) { \
                const unsigned* a = l + j * DH + (k & 1) * 48 + (k >> 1) * 384;       \
                unsigned x0 = a[0], x1 = a[1]; \
                e[k][0] = __builtin_amdgcn_alignbit(x1, x0, sh); \
                e[k][1] = x1 >> sh; \
            } \
            r[j][0] = (e[0][0] & 0xffffu) | (e[3][0] << 16); \
            r[j][1] = (e[0][0] >> 16) | (e[3][0] & 0xffff0000u); \
            r[j][2] = (e[0][1] & 0xffffu) | (e[3][1] << 16); \
            r[j][3] = (e[1][0] & 0xffffu) | (e[2][0] << 16); \
            r[j][4] = (e[1][0] >> 16) | (e[2][0] & 0xffff0000u); \
            r[j][5] = (e[1][1] & 0xffffu) | (e[2][1] << 16); \
        } \
        wave_sync(); \
        _Pragma("unroll") \
        for (int j = 0; j < 2; j++) { *(v4u*)&cw[j * 256 + lane * 4] = (v4u){r[j][0], r[j][1], r[j][2], r[j][3]}; *(v2u*)&cw[512 + j * 128 + lane * 2] = (v2u){r[j][4], r[j][5]}; } \
        wave_sync(); \
        LDS_OR(&sm.tabs, 1u << (RG)) }
        const int sub = lane & 31;
        const int rsel = lane >> 5;
        const unsigned sh = (sub & 1) * 16;
        const int qoff = 3 * (sub >> 1) + (sub & 1);
        const char* kbase = (const char*)qk + (size_t)(rank * SKV + brow0) * (DH * 4);
        const char* vbase = (const char*)qv + (size_t)(rank * SKV + brow0) * (DH * 4);
        const unsigned koff = (rsel * DH + 3 * sub) * 4, voff = 3 * lane * 4;
#define AR(S) (16 + (S) * 8)
        asm volatile("" ::: "a16", "a17", "a18", "a19", "a20", "a21", "a22", "a23", "a24", "a25", "a26", "a27", "a28", "a29", "a30", "a31",
                            "a32", "a33", "a34", "a35", "a36", "a37", "a38", "a39", "a40", "a41", "a42", "a43", "a44", "a45", "a46", "a47");
        asm volatile("" ::: "a48", "a49", "a50", "a51", "a52", "a53", "a54", "a55", "a56", "a57", "a58", "a59", "a60", "a61", "a62", "a63");
        __builtin_amdgcn_s_waitcnt(0x0f70);
        int ta[3], tb[3];
#define GLD(A, OFF, BASE, IMM) asm volatile("global_load_dwordx3 a[%c0:%c1], %2, %3 offset:%4 nt" :: "i"(A), "i"((A) + 2), "v"(OFF), "s"(BASE), "i"(IMM))
#define ISSUE(S, J, BASE, OA, OB) { GLD(AR(S), OA, BASE, (J) * 1536); GLD(AR(S) + 4, OB, BASE, (J) * 1536); }
#define WAITT(S, N) asm volatile("s_waitcnt vmcnt(%6)\n\tv_accvgpr_read_b32 %0, a[%c7]\n\tv_accvgpr_read_b32 %1, a[%c8]\n\tv_accvgpr_read_b32 %2, a[%c9]\n\t" \
                              "v_accvgpr_read_b32 %3, a[%c10]\n\tv_accvgpr_read_b32 %4, a[%c11]\n\tv_accvgpr_read_b32 %5, a[%c12]" \
                              : "=v"(ta[0]), "=v"(ta[1]), "=v"(ta[2]), "=v"(tb[0]), "=v"(tb[1]), "=v"(tb[2]) \
                              : "i"(N), "i"(AR(S)), "i"(AR(S) + 1), "i"(AR(S) + 2), "i"(AR(S) + 4), "i"(AR(S) + 5), "i"(AR(S) + 6))
        const unsigned koffb = koff + 384, voffb = voff + 768;
        unsigned u = 0, rg, np = 0;
#define RG0(C) RGOF((unsigned)(wave + 8 * (C)))
        CS_DMA(RG0(0))
        STAMP(52)
        STAMP(53)
        const float scale = 0.07216878364870322f;
        double acc[3] = {0.0, 0.0, 0.0};
        bool chg, none = false;
        unsigned Qp[3], Qn[3];
        float kz4[2], ksc, kres[2];
        v4u ca[2]; v2u cb[2];
        v2f qe[2][3], A[2];
        const char* nb; unsigned oa, ob;
        unsigned g, mg, nrg, tg, late = 0;
        u64 tis;
        unsigned dg, par = 0, gu;
        int early;
        const int bth = wave < (2) ? (20) * 128 : 0x3fffffff;
        const unsigned grab_adr = lds_addr(&sm.grab);
        asm volatile("v_accvgpr_write_b32 a13, 1\n\tv_accvgpr_write_b32 a14, 2" ::: "a13", "a14", "a15");
#define GRAB(N) { u64 ex_; asm volatile("s_mov_b64 %0, exec\n\ts_mov_b64 exec, 1\n\tds_add_rtn_u32 a15, %1, a%c2\n\ts_mov_b64 exec, %0" : "=&s"(ex_) : "v"(grab_adr), "i"(12 + (N)) : "memory"); }
#define GRABBED() { unsigned t_; asm volatile("s_waitcnt lgkmcnt(0)\n\tv_accvgpr_read_b32 %1, a15\n\ts_nop 0\n\tv_readfirstlane_b32 %0, %1\n\ts_nop 1" : "=s"(g), "=v"(t_)); }
#define NEXT1() const v2u x_ = TU(g >> 5); u64 tn_; asm volatile("s_memrealtime %0" : "=s"(tn_));
#define NEXT2() { \
            chg = (g >> 5) != u; none = g >= NPC; \
            const bool nv = g >= NPC / 2; \
            nrg = RGOF(g); \
            unsigned boff; asm("s_mul_i32 %0, %1, 0x300" : "=s"(boff) : "s"((nv ? VENT(g >> 5) : g >> 5) * (GSZ * SKV) + ROWOF(nrg) + 2)); \
            nb = (nv ? vbase : kbase) + boff; \
            oa = nv ? voff : koff; ob = nv ? voffb : koffb; \
            asm volatile("s_waitcnt lgkmcnt(0)" : "+s"(tn_) : "s"(nb), "v"(oa), "v"(ob)); \
            tg = __builtin_amdgcn_readfirstlane(x_.x) + (g & 31) * __builtin_amdgcn_readfirstlane(x_.y); \
            early = none ? 0 : (int)((unsigned)tn_ * 128 - tg) - bth; \
            if (early > 0) asm volatile("s_cmp_lg_u32 %3, 0\n\ts_cbranch_scc1 1f\n\t" L4(32) "\ts_branch 2f\n1:\n\t" L4(16) "2:\n\ts_mov_b64 vcc, exec\n\ts_mov_b64 exec, 1\n\tds_add_rtn_u32 a15, %4, a13\n\ts_mov_b64 exec, vcc" \
                                    :: "v"(oa), "v"(ob), "s"(nb), "s"(pr), "v"(grab_adr) : "scc", "vcc", "memory"); }
#define L4(A) "global_load_dwordx3 a[" #A ":" #A "+2], %0, %2 offset:-1536 nt\n\tglobal_load_dwordx3 a[" #A "+4:" #A "+6], %1, %2 offset:-1536 nt\n\t" \
              "global_load_dwordx3 a[" #A "+8:" #A "+10], %0, %2 nt\n\tglobal_load_dwordx3 a[" #A "+12:" #A "+14], %1, %2 nt\n"
#define GATE(...) \
            asm volatile(PRIO0 "1:\n\ts_memrealtime vcc\n\ts_waitcnt lgkmcnt(0)\n\ts_lshl_b32 vcc_lo, vcc_lo, 7\n\ts_sub_u32 %0, %1, vcc_lo\n\ts_cmp_lt_u32 %0, 0x40000\n\ts_cbranch_scc0 2f\n\t" \
                         "s_cmp_lt_u32 %0, %2\n\ts_cbranch_scc1 1b\n\ts_sleep %3\n\ts_branch 1b\n2:" PRIO1 : "=&s"(dg) : "s"(tg), "i"(GTH * 128), "i"(GS1), __VA_ARGS__ : "scc", "vcc", "memory");
#define RD6(A) "v_accvgpr_read_b32 %0, a[%c6+" #A "]\n\tv_accvgpr_read_b32 %1, a[%c6+" #A "+1]\n\tv_accvgpr_read_b32 %2, a[%c6+" #A "+2]\n\t" \
               "v_accvgpr_read_b32 %3, a[%c6+" #A "+4]\n\tv_accvgpr_read_b32 %4, a[%c6+" #A "+5]\n\tv_accvgpr_read_b32 %5, a[%c6+" #A "+6]\n"
#define LD2(A) "global_load_dwordx3 a[%c6+" #A ":%c6+" #A "+2], %7, %9 offset:%10 nt\n\tglobal_load_dwordx3 a[%c6+" #A "+4:%c6+" #A "+6], %8, %9 offset:%10 nt"
#define OUT6 "=&v"(ta[0]), "=&v"(ta[1]), "=&v"(ta[2]), "=&v"(tb[0]), "=&v"(tb[1]), "=&v"(tb[2])
#define WI(A, B, TAIL) "s_cmp_gt_i32 %13, 0\n\ts_cbranch_scc1 3f\n\ts_waitcnt vmcnt(2)\n\t" LD2(A) TAIL "\n3:\n\ts_waitcnt vmcnt(%c14)\n\t" RD6(B)
#define WAITISSUE(S, TAIL, ...) asm volatile("s_cmp_lg_u32 %12, 0\n\ts_cbranch_scc1 1f\n\t" WI(16, 0, TAIL) "\ts_branch 2f\n1:\n\t" WI(0, 16, TAIL) "2:" \
                              : OUT6 : "i"(AR(S)), "v"(oa), "v"(ob), "s"(nb), "i"(((S) - 1) * 1536), "v"(grab_adr), "s"(pr), "s"(early), "i"(6 - 2 * (S)), __VA_ARGS__ : "scc", "vcc", "memory")
#define LASTT(S) asm volatile("s_waitcnt vmcnt(%c8)\n\ts_cmp_lg_u32 %7, 0\n\ts_cbranch_scc1 1f\n\t" RD6(0) "\ts_branch 2f\n1:\n\t" RD6(16) "2:" : OUT6 : "i"(AR(S)), "s"(pr), "i"(2 - 2 * (S)) : "scc")
#define DRAW "\n\ts_mov_b64 vcc, exec\n\ts_mov_b64 exec, 1\n\tds_add_rtn_u32 a15, %11, a13\n\ts_mov_b64 exec, vcc"
#define LATE0()
#define LATE1()
#define LATEF()
#define LATEF()
#define K_TAB() { _Pragma("unroll") for (int p = 0; p < 2; p++) { \
            const unsigned* cw = &sm.cs[rg][0]; \
            ca[p] = *(const v4u*)&cw[p * 256 + lane * 4]; cb[p] = *(const v2u*)&cw[512 + p * 128 + lane * 2]; } }
#define K_PRE(P) { \
        float e0, e1, e2, f0, f1, f2; \
        asm("v_dot2_f32_bf16 %0, %6, %12, 0\n\tv_dot2_f32_bf16 %3, %9, %15, 0\n\tv_dot2_f32_bf16 %1, %7, %13, 0\n\tv_dot2_f32_bf16 %4, %10, %16, 0\n\t" \
            "v_dot2_f32_bf16 %2, %8, %14, 0\n\tv_dot2_f32_bf16 %5, %11, %17, 0\n\ts_nop 2" \
            : "=&v"(e0), "=&v"(e1), "=&v"(e2), "=&v"(f0), "=&v"(f1), "=&v"(f2) \
            : "v"(Qp[0]), "v"(Qp[1]), "v"(Qp[2]), "v"(Qn[0]), "v"(Qn[1]), "v"(Qn[2]), \
              "v"(ca[P].x), "v"(ca[P].y), "v"(ca[P].z), "v"(ca[P].w), "v"(cb[P].x), "v"(cb[P].y)); \
        qe[P][0] = (v2f){e0, f0}; qe[P][1] = (v2f){e1, f1}; qe[P][2] = (v2f){e2, f2}; \
        A[P] = (qe[P][0] + qe[P][1] + qe[P][2]) * (v2f){-kz4[P], -kz4[P]}; }
#define K_SUM(P) { \
        _Pragma("unroll") \
        for (int i = 0; i < 3; i++) { v2f x = {(float)ta[i], (float)tb[i]}; A[P] = qe[P][i] * x + A[P]; } \
        kres[P] = A[P].x + A[P].y; }
#define K_RED2() { \
        const bool o1 = lane & 1; \
        float sx = (o1 ? kres[1] : kres[0]) + __int_as_float(__builtin_amdgcn_update_dpp(0, __float_as_int(o1 ? kres[0] : kres[1]), 0xb1, 0xf, 0xf, true)); \
        sx = dpp_add<0x4e>(sx); sx = dpp_add<0x124>(sx); sx = dpp_add<0x128>(sx); \
        const auto sw = __builtin_amdgcn_permlane16_swap(__float_as_uint(sx), __float_as_uint(sx), false, false); \
        sx = __uint_as_float(sw[0]) + __uint_as_float(sw[1]); \
        if ((lane & 30) == 0) sm.ent[u][rg * 4 + 2 * (lane & 1) + rsel] = sx * ksc; }
#define LDV(P) __hip_atomic_load(P, __ATOMIC_RELAXED, __HIP_MEMORY_SCOPE_WORKGROUP)
#define RDYWAIT(SL, SEQ) { while (lds_ld(&sm.rdy[SL]) != (SEQ)) SLEEP(1); CBAR(); }
#define LOADZ() { _Pragma("unroll") for (int p = 0; p < 2; p++) kz4[p] = sm.kz[u % NIN][rg * 4 + 2 * p + rsel].x; ksc = sm.kz[u % NIN][rg * 4 + 2 * (lane & 1) + rsel].y * scale; }
#define LOADQ() { _Pragma("unroll") for (int i = 0; i < 3; i++) { Qp[i] = sm.q[u % NIN][3 * sub + i]; Qn[i] = __builtin_amdgcn_alignbit(Qp[i], Qp[i], 16) ^ 0x80000000u; } }
#define FLUSH(ISV) { \
            mg = __builtin_amdgcn_readfirstlane(mg); \
            while (u >= mg + NOUT) { SLEEP(1); mg = lds_ld(&sm.merged); } \
            CBAR(); \
            if (ISV) { const unsigned aa = lds_addr(&sm.acc[u % NOUT][lane]); \
                asm volatile("ds_add_f64 %0, %1\n\tds_add_f64 %0, %2 offset:512\n\tds_add_f64 %0, %3 offset:1024" :: "v"(aa), "v"(acc[0]), "v"(acc[1]), "v"(acc[2]) : "memory"); \
                acc[0] = 0.0; acc[1] = 0.0; acc[2] = 0.0; } \
            CBAR(); \
            LATEF() \
            LDS_ADD(&sm.cnt[u % NOUT], np) \
            CBAR(); \
            STAMP_UNIT(np) \
            np = 0; }
#define ADVANCE() { u = gu; rg = nrg; }

#define REQ0(N, S) { tg = (ts + TOF) * 128 + (wave + 8 * (N)) * P0; \
            const char* kb = kbase + (((N) >> 2) * (GSZ * SKV) + ROWOF(RG0((N) & 3))) * (DH * 4); GATE("s"(kb)) ISSUE(S, 0, kb, koff, koffb) ISSUE((S) + 1, 1, kb, koff, koffb) STAMP(44 + (N)) }
#define USE0(N, S, W) { u = (N) >> 2; rg = RG0((N) & 3); K_TAB() LOADQ() LOADZ() K_PRE(0) K_PRE(1) WAITT(S, W); K_SUM(0) WAITT((S) + 1, W); K_SUM(1) K_RED2() }
#define VMW(N) asm volatile("s_waitcnt vmcnt(" #N ")" ::: "memory");
        REQ0(0, 0) CS_DMA(RG0(1))
        VMW(7) REPACK(RG0(0))
        STAMP(1)
        REQ0(1, 2)
        RDYWAIT(0, 1u)
        USE0(0, 0, 7)
        VMW(4) REPACK(RG0(1))
        REQ0(2, 0) CS_DMA(RG0(2))
        USE0(1, 2, 7)
        REQ0(3, 2)
        VMW(4) REPACK(RG0(2))
        USE0(2, 0, 4)
        REQ0(4, 0) CS_DMA(RG0(3))
        REQ0(5, 4)
        VMW(4) REPACK(RG0(3))
        USE0(3, 2, 4)
        CBAR();
        LDS_ADD(&sm.cnt[0], 4u)
        REQ0(6, 2)
        RDYWAIT(1, 2u)
        USE0(4, 0, 8)
        USE0(5, 4, 4)
        REQ0(7, 0)
        GRAB(1)
        USE0(6, 2, 4)
        rg = RG0(3); np = 3;
        GRABBED()
#pragma unroll 1
        while (u < NU) {
            const unsigned pr = __builtin_amdgcn_readfirstlane(par); par ^= 1;
            NEXT1()
            const unsigned tb_ = LDV(&sm.tabs), fl = LDV(&sm.rdy[u % NIN]);
            mg = LDV(&sm.merged);
            CBAR();
            K_TAB() LOADQ() LOADZ()
            NEXT2()
            if (__builtin_expect(__builtin_amdgcn_readfirstlane(fl) != u + 1 || !((__builtin_amdgcn_readfirstlane(tb_) >> rg) & 1u), 0)) {
                while (!((lds_ld(&sm.tabs) >> rg) & 1u)) SLEEP(1)
                RDYWAIT(u % NIN, u + 1)
                K_TAB() LOADQ() LOADZ()
            }
            K_PRE(0) K_PRE(1)
            if (early <= 0) GATE("v"(A[0]), "v"(A[1]), "v"(oa), "v"(ob), "s"(nb), "s"(pr))
            WAITISSUE(0, DRAW, "v"(A[0]));
            LATE0()
            K_SUM(0)
            WAITISSUE(1, "", "v"(A[1]));
            K_SUM(1)
            LATE1()
            gu = g >> 5;
            GRABBED()
            K_RED2()
            np++;
            if (chg) FLUSH(false)
            ADVANCE()
        }
#pragma unroll 1
        while (u < 2 * NU) {
            const unsigned pr = __builtin_amdgcn_readfirstlane(par); par ^= 1;
            NEXT1()
            mg = LDV(&sm.merged);
            CBAR();
            const unsigned ve = VENT(u);
            v4i pw = *(const v4i*)&sm.ent[ve][rg * 4];
            NEXT2()
            if (__builtin_expect(__builtin_amdgcn_readfirstlane(mg) <= ve, 0)) { while (lds_ld(&sm.merged) <= ve) SLEEP(1); CBAR(); pw = *(const v4i*)&sm.ent[ve][rg * 4]; }
            const double w0 = pw.x, w1 = pw.y, w2 = pw.z, w3 = pw.w;
            if (none) { LASTT(0); } else { if (early <= 0) GATE("s"(pr), "v"(w0), "v"(w1), "v"(w2), "v"(w3), "v"(oa), "v"(ob), "s"(nb)) WAITISSUE(0, DRAW, "v"(w0)); }
            LATE0()
#pragma unroll
            for (int i = 0; i < 3; i++) acc[i] = fma(w1, (double)tb[i], fma(w0, (double)ta[i], acc[i]));
            if (none) { LASTT(1); } else WAITISSUE(1, "", "v"(w2));
#pragma unroll
            for (int i = 0; i < 3; i++) acc[i] = fma(w3, (double)tb[i], fma(w2, (double)ta[i], acc[i]));
            LATE1()
            gu = g >> 5;
            GRABBED()
            np++;
            if (chg) FLUSH(true)
            ADVANCE()
        }
        STAMP(2)
        {
            const int head = blockIdx.x;
            while (lds_ld(&sm.fw_rdy) == 0) SLEEP(2)
            CBAR();
            const unsigned tt = lds_ld(&sm.tag_lo) << 18;
            const float wh = sm.fw[wave + 8 * rsel];
            const unsigned ro = (wave + 8 * rsel) * 512 + sub * 16;
            const char* rb = (const char*)ws + WS_PART + head * (NSPLIT * 512);
            v4f* pb = (v4f*)((char*)&sm.cs[0][0] + 16384);
            __hip_bfloat16* o = out + (size_t)head * DH + 6 * sub + 3 * rsel;
            tg = __builtin_amdgcn_readfirstlane(TU(2 * NU).x) + ((head >> 4) == VENT(15) ? (120) : (120)) * 128;
            GATE("s"(tt), "v"(wh), "v"(ro), "s"(rb), "v"(o))
            STAMP(60)
            STAMPV(57, NOW() - (((NOW() * 128 - __builtin_amdgcn_readfirstlane(TU(2 * NU).x)) >> 7) & 0xfffff))
            v4u R;
#define PLD(A) "global_load_dwordx4 a[" #A ":" #A "+3], %6, %7 sc1\n\t"
#define PCK(A, N, L) "s_waitcnt vmcnt(" #N ")\n\tv_accvgpr_read_b32 %1, a[" #A "+1]\n\tv_accvgpr_read_b32 %3, a[" #A "+3]\n\tv_accvgpr_read_b32 %0, a[" #A "]\n\tv_accvgpr_read_b32 %2, a[" #A "+2]\n\t" \
                     "v_xor_b32 %1, %8, %1\n\tv_xor_b32 %3, %8, %3\n\tv_or_b32 %4, %1, %3\n\tv_cmp_gt_u32 vcc, %9, %4\n\ts_cmp_eq_u64 vcc, exec\n\ts_cbranch_scc1 " L "\n\t"
            { unsigned t_;
              asm volatile("s_cmp_eq_u32 %10, %12\n\ts_cbranch_scc1 1f\n\t" PLD(16) PCK(16, 0, "3f") "1:\n\t" PLD(16) "s_sleep %11\n\t" PLD(20) "s_sleep %11\n\t" PLD(24)
                           "2:\n\t" PCK(16, 2, "3f") PLD(16) PCK(20, 2, "3f") PLD(20) PCK(24, 2, "3f") PLD(24)
                           "v_add_co_u32 %5, vcc, 64, %5\n\ts_cbranch_vccz 2b\n\ts_endpgm\n3:"
                           : "=&v"(R.x), "=&v"(R.y), "=&v"(R.z), "=&v"(R.w), "=&v"(t_), "+v"(spins) : "v"(ro), "s"(rb), "s"(tt), "s"(0x40000u), "s"(head >> 4), "i"(PSL), "i"(VENT(15)) : "scc", "vcc", "memory"); }
            STAMP(61)
            const float s0 = ldexpf(wh, (int)(R.y >> 10) - 139), s1 = ldexpf(wh, (int)(R.w >> 10) - 139);
            const float f[6] = {s0 * (float)(int)__builtin_amdgcn_sbfe(R.x, 0, 14), s0 * (float)(int)__builtin_amdgcn_sbfe(R.x, 14, 14), s0 * (float)(int)__builtin_amdgcn_sbfe(__builtin_amdgcn_alignbit(R.y, R.x, 28), 0, 14),
                                s1 * (float)(int)__builtin_amdgcn_sbfe(R.z, 0, 14), s1 * (float)(int)__builtin_amdgcn_sbfe(R.z, 14, 14), s1 * (float)(int)__builtin_amdgcn_sbfe(__builtin_amdgcn_alignbit(R.w, R.z, 28), 0, 14)};
            float sv[3];
#pragma unroll
            for (int i = 0; i < 3; i++) {
                const auto sw = __builtin_amdgcn_permlane32_swap(__float_as_uint(f[i]), __float_as_uint(f[i + 3]), false, false);
                sv[i] = __uint_as_float(sw[0]) + __uint_as_float(sw[1]);
            }
            pb[wave * 64 + lane] = (v4f){sv[0], sv[1], sv[2], 0.f};
            CBAR();
            { u64 ex_; asm volatile("s_mov_b64 %0, exec\n\ts_mov_b64 exec, 1\n\tds_add_rtn_u32 a15, %1, a13\n\ts_mov_b64 exec, %0" : "=&s"(ex_) : "v"(grab_adr + 12) : "memory"); }
            v4f ps[NSW];
#pragma unroll
            for (int w = 0; w < NSW; w++) ps[w] = pb[w * 64 + lane];
            GRABBED()
            if (g == NSW - 1) {
                const v4f sum = ((ps[0] + ps[1]) + (ps[2] + ps[3])) + ((ps[4] + ps[5]) + (ps[6] + ps[7]));
                o[0] = __float2bfloat16(sum.x); o[1] = __float2bfloat16(sum.y); o[2] = __float2bfloat16(sum.z);
                LT(1, NOW())
            }
        }
        STAMP(62)
        STAMP_DUMP()
        return;
    }

    if (wave == NSW) {
        STAMPV(24, (unsigned)__builtin_amdgcn_s_memtime()) STAMP(25)
    } else if (wave == NSW + 1) {
        u64 E; asm volatile("s_setprio 3\n\ts_load_dwordx2 %0, %1, 0\n\ts_waitcnt lgkmcnt(0)" : "=s"(E) : "s"(ws + WS_EPOCH) : "memory");
        const int NB = (3), CAP = (45);
        const int K0 = (3), GAIN = (24), BK = (3), FLOOR = (3), XM = (250), XD1 = (100), FTH = (15), XW = (40), RTH = (6), KI = (14), KD = (9), XR = (170);
        unsigned t2 = (ts + TOF) * 128 + 64 * P0;
        int off = 0, off1 = 0, hold = 0, cum = 0, dp = 0, sp = 0;
#define NOM(U) (((int)(U) - 2) * 32 * PK + max((int)(U) - NU, 0) * 32 * (PV - PK))
#define NEWLINE(K) { if (lane >= (K) && lane <= 2 * NU) sm.tu[lane] = (v2u){t2 + NOM(lane) + (cum + (lane - (int)(K)) * dp + (lane > (K) ? sp + off : off1)) * 32, \
                                                                            (unsigned)((lane >= NU ? PV : PK) + dp + (lane == (K) ? sp + off - off1 : 0))}; }
        NEWLINE(2)
        if (lane == 0) sm.tu[1] = (v2u){t2 - 32 * P0, (unsigned)P0};
        const u64 E1 = E + 1;
        const unsigned rtag = (unsigned)E1 * 0x9e3779b1u;
        const bool boss = blockIdx.x == 0;
        u64* const slot = (u64*)(ws + WS_REF + (blockIdx.x & 63) * LSTRIDE + (blockIdx.x >> 6) * 16);
        const u64* const slots = (const u64*)(ws + WS_REF + lane * LSTRIDE);
        u64* const dec = (u64*)(ws + WS_DEC);
#define GLDS(P) __hip_atomic_load(P, __ATOMIC_RELAXED, __HIP_MEMORY_SCOPE_AGENT)
#define GSTS(P, V) __hip_atomic_store(P, V, __ATOMIC_RELAXED, __HIP_MEMORY_SCOPE_AGENT)
#define UNTIL(T) while ((unsigned)((T) - NOW() * 128) < 0x40000u) SLEEP(2)
        if (lane < 2) GSTS(slot + lane, lane ? E1 : (u64)ts | ((u64)rtag << 32));
        if (lane == 0) { sm.tag_lo = (unsigned)E1; CBAR(); lds_st(&sm.tag_rdy, 1u); }
        unsigned dv = 0, npoll = 0;
        bool joined = false;
#define JOIN(T0) { const int d0 = min(max((int)((T0) - ts), -1000), 1000) + (0); off -= d0 * 4; t2 += d0 * 128; joined = true; }
        unsigned pw_ = 0, tw_ = 0, now, p; int prov = 0;
#define LOOK() { u64 t_; asm volatile("ds_read_b32 %1, %2\n\ts_memrealtime %0\n\ts_waitcnt lgkmcnt(0)" : "=s"(t_), "=v"(p) : "v"(lds_addr(&sm.grab)) : "memory"); \
                 p = __builtin_amdgcn_readfirstlane(p) - 8; now = (unsigned)t_ * 128; }
#define LATENESS(P, T) ({ const v2u e = TU((P) >> 5); (int)((T) - __builtin_amdgcn_readfirstlane(e.x) - ((P) & 31) * __builtin_amdgcn_readfirstlane(e.y)); })
#define WATCH(T) for (;;) { \
            LOOK() \
            if (__builtin_expect(now - tw_ > (80) * 128u && pw_ >= 64 && p < NPC - 8, 0)) { \
                const int lw = max(LATENESS(pw_, tw_), 0); \
                if (LATENESS(p, now) - lw > (80) * 128) \
                    for (;;) { \
                        const int d_ = min(max(LATENESS(p, now) - lw, 0) >> 5, 1600); \
                        if (lane <= 2 * NU) atomicAdd((unsigned*)&sm.tu[lane], d_ * 32u); \
                        off += d_; prov += d_; hold = 3; \
                        if (p != pw_ || prov > 8000) break; \
                        SLEEP(1) \
                        LOOK() \
                    } \
                } \
            if (p != pw_) { pw_ = p; tw_ = now; } \
            if ((unsigned)((T) - now) >= 0x40000u) break; \
            if ((unsigned)((T) - now) > 25 * 128) SLEEP(WSL) else SLEEP(1) }
        if (boss) {
            TOUCH((lane & 31) * 128, dec)
            UNTIL((ts + (330)) * 128)
            for (;;) {
                bool ok = true; float sum = 0.f;
#pragma unroll
                for (int i = 0; i < 4; i++) { const u64 x = GLDS(slots + 2 * i), e = GLDS(slots + 2 * i + 1); ok &= e == E1 && (unsigned)(x >> 32) == rtag; sum += (float)min(max((int)((unsigned)x - ts), -1000), 1000); }
                if (__builtin_amdgcn_ballot_w64(ok) == ~0ull) { dv = ts + (int)(wave_sum(sum) * (1.f / 256)); break; }
                SLEEP(8)
            }
            if (lane == 0) { GSTS(dec, (u64)dv | ((u64)rtag << 32)); GSTS((u64*)(ws + WS_EPOCH), E1); }
            JOIN(dv)
            dv = 0;
        }
        int sp1 = 0, tot1 = 0, tot2 = 0;
#pragma unroll 1
        for (unsigned k = 2; k < 2 * NU; k++) {
            const unsigned tk = t2 + NOM(k) + cum * 32;
            if (k >= K0 && k + 1 < 2 * NU) {
                WATCH(tk - (XM + XW) * 128)
                int sum = 0, n = 0;
                do { LOOK() sum += LATENESS(p, now) >> 7; n++; } while ((unsigned)(tk - XM * 128 - now) < 0x40000u);
                const int l = (int)((float)sum * __builtin_amdgcn_rcpf((float)n)) - BK + prov / 4;
                if (l > RTH && lane == 0) GSTS((unsigned*)(ws + WS_REF + (blockIdx.x & 63) * LSTRIDE + 64) + (blockIdx.x >> 6), (unsigned)min(l, 1000) | ((rtag + k) << 10));
            }
            const int cum0 = cum;
            bool got = boss;
            if (!boss && (k > K0 || !joined)) {
                WATCH(tk - XR * 128)
                const u64 x = GLDS(dec + (joined ? k * 16 : 0));
                dv = __builtin_amdgcn_readfirstlane((unsigned)x);
                got = __builtin_amdgcn_readfirstlane((unsigned)(x >> 32)) == rtag;
                npoll += !got;
                if (got && !joined) { JOIN(dv) got = false; }
            }
            sp1 = sp; sp = 0;
            if (got && k > K0) { cum = dv >> 18; sp = dv >> 6 & 4095u; dp = dv & 63u; }
            const int yld = cum - cum0 + sp;
            { const int pj = min(prov, cum - cum0); off -= pj; prov -= pj; off1 = off;
              const int ps = min(prov, sp); off -= ps; prov -= ps;
              if (hold) hold--; else prov = 0;
              const int r_ = off - prov; off -= r_ > 40 || r_ < -40 ? r_ / NB : r_; }
            NEWLINE(k)
            cum = min(cum + sp + dp, 16383);
            if (boss) dv = (cum << 18) | dp;
            if (boss && k >= K0 && k + 1 < 2 * NU && (int)(NOW() * 128 - tk) < -60 * 128) {
                WATCH(tk - (XM - XD1) * 128)
                const v4u x = __builtin_amdgcn_raw_buffer_load_b128(__builtin_amdgcn_make_buffer_rsrc(ws + WS_REF + 64, 0, 64 * LSTRIDE, 0x00020000), lane * LSTRIDE, 0, SC1);
                float a[4];
#pragma unroll
                for (int i = 0; i < 4; i++) a[i] = ((x[i] ^ ((rtag + k) << 10)) >> 10) == 0 ? (float)(x[i] & 1023u) : 0.f;
                const int tot = (int)wave_sum(a[0] + a[1] + a[2] + a[3]) >> 6;
                float mn = 0.f;
                if (tot >= 40) {
#pragma unroll 1
                    for (float st = 512.f; st >= 4.f; st *= 0.5f) {
                        const float x_ = mn + st;
                        const int c_ = __builtin_popcountll(__builtin_amdgcn_ballot_w64(a[0] >= x_)) + __builtin_popcountll(__builtin_amdgcn_ballot_w64(a[1] >= x_)) +
                                       __builtin_popcountll(__builtin_amdgcn_ballot_w64(a[2] >= x_)) + __builtin_popcountll(__builtin_amdgcn_ballot_w64(a[3] >= x_));
                        if (c_ >= (224)) mn = x_;
                    }
                }
                const float c = mn + (float)CAP, fth = (float)(FTH * (2 * NU - (int)k));
                const int cut = (int)wave_sum(fminf(a[0], c) + fminf(a[1], c) + fminf(a[2], c) + fminf(a[3], c)) >> 6, m4 = (int)mn * 4;
                const bool far = __builtin_amdgcn_ballot_w64(fmaxf(fmaxf(a[0], a[1]), fmaxf(a[2], a[3])) > fth) != 0;
                const int pend = yld + sp1 * XM / 320;
                const int nj = max(m4 - pend, 0);
                const int ns = min(max(max(min(m4 + (tot - m4) * GAIN / 16 - pend - nj, (cut - m4) * GAIN / 16), 0), far ? FLOOR * 4 : 0), 4000);
                int ndp = dp;
                if (k >= (unsigned)KD && tot >= KI && tot > tot1 && tot < tot1 + 32 && tot1 < tot2 + 32) ndp = min(dp + (2), 40);
                if (tot + tot1 < 2 && (1)) ndp = max(dp - 1, 0);
                tot2 = tot1; tot1 = tot;
                dv = (min(cum + nj, 16383) << 18) | (ns << 6) | ndp;
                if ((int)(NOW() * 128 - tk) > (60) * 128) dv = (cum << 18) | dp;
                else if (lane == 0) GSTS(dec + (k + 1) * 16, (u64)dv | ((u64)rtag << 32));
                LTK(k + 33, (int)(NOW() * 128 - tk) / 128 + 1000)
                LTK(k + 1, (min(tot, 4095) << 20) | (min(nj + ns, 1023) << 10) | (dp + 100))
                STAMPV(5 + k, (min(tot, 4095) << 20) | (min(nj + ns, 1023) << 10) | (dp + 100))
            }
        }
        WATCH(p >= NPC - 8 ? now - 128 : now + 4096)
    } else if (wave == NSW + 2) {
        const int ql = lane < 48 ? lane : 47;
        unsigned c1, c2, s1, s2;
#define QT_LOADS() { int qo_ = ql; asm volatile("" : "+v"(qo_)); \
                     c1 = ((const unsigned*)q_cos)[qo_]; c2 = ((const unsigned*)q_cos)[48 + qo_]; s1 = ((const unsigned*)q_sin)[qo_]; s2 = ((const unsigned*)q_sin)[48 + qo_]; }
        unsigned q1, q2, lkz, lks, lvz, lvs;
#define UNIT_LOADS(HEAD) { \
        q1 = ((const unsigned*)(q + (size_t)(HEAD) * DH))[ql]; q2 = ((const unsigned*)(q + (size_t)(HEAD) * DH))[48 + ql]; \
        const size_t so = ((size_t)(HEAD) * SKV + brow0) / 2 + (lane >> 3) * 128 + (lane & 7); \
        lkz = SLD((const unsigned*)k_zeros + so); lks = SLD((const unsigned*)k_scales + so); \
        lvz = SLD((const unsigned*)v_zeros + so); lvs = SLD((const unsigned*)v_scales + so); }
#define UNIT_FILL(SL, U) { \
            v4f qq; \
            qq.x = bfr(bfr(bflo(q1) * bflo(c1)) + bfr(-bflo(q2) * bflo(s1))); \
            qq.y = bfr(bfr(bflo(q2) * bflo(c2)) + bfr(bflo(q1) * bflo(s2))); \
            qq.z = bfr(bfr(bfhi(q1) * bfhi(c1)) + bfr(-bfhi(q2) * bfhi(s1))); \
            qq.w = bfr(bfr(bfhi(q2) * bfhi(c2)) + bfr(bfhi(q1) * bfhi(s2))); \
            *(v2u*)&sm.q[SL][2 * ql] = (v2u){(__float_as_uint(qq.x) >> 16) | __float_as_uint(qq.y), (__float_as_uint(qq.z) >> 16) | __float_as_uint(qq.w)}; \
            *(v4f*)&sm.kz[SL][2 * lane] = (v4f){bflo(lkz), bflo(lks), bfhi(lkz), bfhi(lks)}; \
            *(v4f*)&sm.vz[SL][2 * lane] = (v4f){bflo(lvz), bflo(lvs), bfhi(lvz), bfhi(lvs)}; \
            CBAR(); \
            if (lane == 0) lds_st(&sm.rdy[SL], (U) + 1); \
            CBAR(); }
        asm volatile("s_setprio 3");
        QT_LOADS()
#pragma unroll 1
        for (unsigned u = 0; u < NU; u++) {
            UNIT_LOADS(rank + GSZ * u)
            while (u >= lds_ld(&sm.merged) + NIN) SLEEP(4);
            CBAR();
            UNIT_FILL(u % NIN, u)
            if (u < 3) STAMP(5 + u)
            if (u == 1) asm volatile("s_setprio 0");
        }
        while (lds_ld(&sm.merged) < NU) SLEEP(16)
        CBAR();
        const unsigned T = lds_ld(&sm.tag_lo);
        if (lane < NU) __hip_atomic_store((u64*)(ws + WS_LOG + (rank * NSPLIT + split) * LSTRIDE2) + lane, (u64)__float_as_uint(sm.ent[lane][130]) | ((u64)T << 32), __ATOMIC_RELAXED, __HIP_MEMORY_SCOPE_AGENT);
        while (lds_ld(&sm.merged) < NU + 3) SLEEP(16)
        for (;;) {
            const u64 x = __hip_atomic_load((const u64*)(ws + WS_LOG + ((blockIdx.x & 15) * NSPLIT + (lane & 15)) * LSTRIDE2) + (blockIdx.x >> 4), __ATOMIC_RELAXED, __HIP_MEMORY_SCOPE_AGENT);
            if (__builtin_amdgcn_ballot_w64((unsigned)(x >> 32) == T) == ~0ull) {
                const float L = __uint_as_float((unsigned)x);
                const float w = __builtin_amdgcn_exp2f(L - row_max(L));
                sm.fw[lane & 15] = w / row_sum(w);
                CBAR();
                if (lane == 0) lds_st(&sm.fw_rdy, 1u);
                break;
            }
            SLEEP(64)
        }
    } else {
        while (lds_ld(&sm.tag_rdy) == 0) SLEEP(1);
        CBAR();
        const unsigned T = lds_ld(&sm.tag_lo);
        const __amdgpu_buffer_rsrc_t rsrc = __builtin_amdgcn_make_buffer_rsrc(ws + WS_PART + (rank * NSPLIT + split) * 512, 0, NH * NSPLIT * 512, 0x00020000);
        v2u* recs = (v2u*)&sm.cs[0][0];
#pragma unroll
        for (int i = 0; i < NOUT * 3; i++) (&sm.acc[0][0])[i * 64 + lane] = 0.0;
        bool direct = false;
#define PUBLISH(N) { \
        v4u r[8]; \
        _Pragma("unroll") for (int k = 0; k < 8; k++) r[k] = ((const v4u*)recs)[k * 64 + lane]; \
        _Pragma("unroll") for (int k = 0; k < 8; k++) __builtin_amdgcn_raw_buffer_store_b128(r[k], rsrc, 2 * k + (lane >> 5) < (N) ? po + (lane < 32 ? VENT(2 * k) : VENT(2 * k + 1)) * (GSZ * NSPLIT * 512) : 0x7ffffff0u, 0, SC1); STAMP(4) }
        const unsigned po = (lane & 31) * 16;
#pragma unroll 1
        for (unsigned u = 0; u < 2 * NU; u++) {
            const unsigned e = u < NU ? u : VENT(u), so = u % NOUT;
            const v4f h = *(const v4f*)&sm.ent[e][128];
            for (;;) {
                if (!direct && u >= 2 * NU - 3 && (int)(NOW() * 128 - __builtin_amdgcn_readfirstlane(TU(2 * NU).x)) >= (30) * 128) { direct = true; wave_sync(); PUBLISH(u - NU) }
                if (lds_ld(&sm.cnt[so]) == 32) break;
                if (direct) SLEEP(0) else SLEEP(1)
            }
            CBAR();
            STAMP(5)
            if (!direct && u == 2 * NU - 1) { direct = true; wave_sync(); PUBLISH(NU - 1) }
            if (u < NU) {
                const float2 lg = *(const float2*)&sm.ent[e][2 * lane];
                const v4f zs = *(const v4f*)&sm.vz[u % NIN][2 * lane];
                const float m = wave_max(fmaxf(lg.x, lg.y));
                const float p0 = __expf(lg.x - m), p1 = __expf(lg.y - m);
                const float x0 = p0 * zs.y, x1 = p1 * zs.w;
                const unsigned eb = max(__float_as_uint(wave_max(fmaxf(fabsf(x0), fabsf(x1)))) >> 23, 21u);
                const float f = __uint_as_float((275u - eb) << 23);
                const float w0 = rintf(x0 * f), w1 = rintf(x1 * f);
                const float den = wave_sum(p0 + p1);
                const float bz = wave_sum(fmaf(w0, zs.x, w1 * zs.z));
                const float L = fmaf(m, 1.4426950408889634f, __builtin_amdgcn_logf(den));
                *(v2u*)&sm.ent[e][2 * lane] = (v2u){(unsigned)(int)w0, (unsigned)(int)w1};
                if (lane == 0) *(v4f*)&sm.ent[e][128] = (v4f){ldexpf(1.f / den, (int)eb - 148), bz, L, 0.f};
            } else {
                const double a0 = sm.acc[so][lane], a1 = sm.acc[so][64 + lane], a2 = sm.acc[so][128 + lane];
                const float n0 = (float)(a0 - (double)h.y) * h.x, n1 = (float)(a1 - (double)h.y) * h.x, n2 = (float)(a2 - (double)h.y) * h.x;
                const unsigned e2 = max(__float_as_uint(fmaxf(fmaxf(fabsf(n0), fabsf(n1)), fabsf(n2))) >> 23, 12u);
                const float f2 = __uint_as_float((266u - e2) << 23);
                const unsigned m0 = min(max((int)rintf(n0 * f2), -8191), 8191) & 0x3fff, m1 = min(max((int)rintf(n1 * f2), -8191), 8191) & 0x3fff, m2 = min(max((int)rintf(n2 * f2), -8191), 8191) & 0x3fff;
                const v2u r = {m0 | (m1 << 14) | (m2 << 28), (m2 >> 4) | (e2 << 10) | (T << 18)};
                if (direct) {
                    STAMP(6)
                    const v4u r2 = {r.x, r.y, (unsigned)__builtin_amdgcn_update_dpp(0, (int)r.x, 0xf5, 0xf, 0xf, true), (unsigned)__builtin_amdgcn_update_dpp(0, (int)r.y, 0xf5, 0xf, 0xf, true)};
                    __builtin_amdgcn_raw_buffer_store_b128(r2, rsrc, (lane & 1) ? 0x7ffffff0u : lane * 8, e * (GSZ * NSPLIT * 512), SC1);
                }
                else recs[(u - NU) * 64 + lane] = r;
                CBAR();
                sm.acc[so][lane] = 0.0; sm.acc[so][64 + lane] = 0.0; sm.acc[so][128 + lane] = 0.0;
            }
            CBAR();
            if (lane == 0) { lds_st(&sm.cnt[so], 0u); CBAR(); lds_st(&sm.merged, u + 1); }
        }
        LT(2, NOW()) LT(0, (unsigned)tent) LT(3, ts) LT(4, NOW() - (((NOW() * 128 - __builtin_amdgcn_readfirstlane(TU(2 * NU).x)) >> 7) & 0xfffff))
        STAMP(2)
        STAMP_DUMP()
        return;
    }

    STAMP(2)
    STAMP_DUMP()
}

extern "C" void kernel_launch(void* const* d_in, const int* in_sizes, int n_in,
                              void* d_out, int out_size, void* d_ws, size_t ws_size,
                              hipStream_t stream) {
    const unsigned short* q = (const unsigned short*)d_in[0];
    const int* qk = (const int*)d_in[1];
    const unsigned short* k_scales = (const unsigned short*)d_in[2];
    const unsigned short* k_zeros = (const unsigned short*)d_in[3];
    const int* qv = (const int*)d_in[4];
    const unsigned short* v_scales = (const unsigned short*)d_in[5];
    const unsigned short* v_zeros = (const unsigned short*)d_in[6];
    const unsigned short* q_cos = (const unsigned short*)d_in[7];
    const unsigned short* q_sin = (const unsigned short*)d_in[8];
    const unsigned short* k_cos = (const unsigned short*)d_in[9];
    const unsigned short* k_sin = (const unsigned short*)d_in[10];
    unsigned char* ws = (unsigned char*)d_ws;

    attn_kernel<<<NSPLIT * GSZ, NTHREADS, 0, stream>>>(q, qk, k_scales, k_zeros, qv, v_scales, v_zeros,
                                                       q_cos, q_sin, k_cos, k_sin, (__hip_bfloat16*)d_out, ws);
}
```

```cpp
#define PFL 183
#define TPSN 0
#define TP2 0
#include <hip/hip_runtime.h>
#include <hip/hip_bf16.h>
#include <stdint.h>

#define DH 192
#define HALF 96
#define SKV 2048
#define NH 256
#define NSW 8
#define NWAVES (NSW + 4)
#define NTHREADS (64 * NWAVES)
#define BROWS 128
#define NSPLIT (SKV / BROWS)
#define GSZ 16
#define NU (NH / GSZ)
#define NPC (2 * NU * 32)
#define N1 (NH - GSZ)
#define NIN 4
#define NOUT 4
#define ENTW 132
#define VORD 0x9876543210afedcbull
#define VENT(V) ((unsigned)(VORD >> (4 * ((V) & 15))) & 15u)
#define SC1 16
#define LIKELY 1
#define SLD(P) (*(P))
#define LSTRIDE 4352
#define PUBP 2
#define TPTN 0
#define POL0 ""
#define POL1 "nt"
#define POL2 "sc1"
#define POL3 "sc0 sc1"
#define POL4 "sc1 nt"
#define POL5 "sc0 nt"
#define POLX_(N) POL##N
#define POLX(N) POLX_(N)
#define TPT POLX(TPTN)
#define TPS POLX(TPSN)
#define KT 1
#define WSL 7
#define PSL 5
#define PHALF 9
#define PHI 0x0235712457024671ull
#define KAP 0x0202020220202020ull
#define PHIV PHI
#define KAPV KAP
#define PRIO 1
#define PRIO0 "s_setprio 0\n"
#define PRIO1 "\n\ts_setprio 2"
#define GTH 10
#define GS1 2

#define WS_EPOCH 0
#define WS_DEC 8192
#define WS_REF 16384
#define WS_LOG 524288
#define LSTRIDE2 1408
#define WS_PART 1048576
#define WS_FIN (WS_PART + NH * NSPLIT * 512)

typedef unsigned int v2u __attribute__((ext_vector_type(2)));
typedef unsigned int v3u __attribute__((ext_vector_type(3)));
typedef unsigned int v4u __attribute__((ext_vector_type(4)));
typedef int v4i __attribute__((ext_vector_type(4)));
typedef float v2f __attribute__((ext_vector_type(2)));
typedef float v4f __attribute__((ext_vector_type(4)));
typedef unsigned long long u64;

__device__ __forceinline__ float bflo(unsigned u) { return __uint_as_float(u << 16); }
__device__ __forceinline__ float bfhi(unsigned u) { return __uint_as_float(u & 0xffff0000u); }
__device__ __forceinline__ float bfr(float x) {
    __bf16 b = (__bf16)x;
    return __uint_as_float(((unsigned)__builtin_bit_cast(unsigned short, b)) << 16);
}

#define SLEEP(n) SLEEP_(n)
#define SLEEP_(n) asm volatile("s_sleep " #n "\n\tv_add_co_u32 %0, vcc, 1, %0\n\ts_cbranch_vccz 1f\n\ts_endpgm\n1:" : "+v"(spins) :: "vcc", "memory");
#define SPIN0 0xf0000000u
#define CBAR() asm volatile("" ::: "memory")

__device__ __forceinline__ void wave_sync() {
    __builtin_amdgcn_fence(__ATOMIC_RELEASE, "wavefront");
    __builtin_amdgcn_wave_barrier();
    __builtin_amdgcn_fence(__ATOMIC_ACQUIRE, "wavefront");
}
template <int CTRL>
__device__ __forceinline__ float dpp_add(float x) {
    int y = __builtin_amdgcn_update_dpp(0, __float_as_int(x), CTRL, 0xf, 0xf, true);
    return x + __int_as_float(y);
}
template <int CTRL>
__device__ __forceinline__ float dpp_max(float x) {
    int y = __builtin_amdgcn_update_dpp(__float_as_int(x), __float_as_int(x), CTRL, 0xf, 0xf, false);
    return fmaxf(x, __int_as_float(y));
}
__device__ __forceinline__ float row_sum(float x) { x = dpp_add<0x128>(x); x = dpp_add<0x124>(x); x = dpp_add<0x122>(x); return dpp_add<0x121>(x); }
__device__ __forceinline__ float row_max(float m) { m = dpp_max<0x128>(m); m = dpp_max<0x124>(m); m = dpp_max<0x122>(m); return dpp_max<0x121>(m); }
__device__ __forceinline__ float rdlane(float x, int l) { return __int_as_float(__builtin_amdgcn_readlane(__float_as_int(x), l)); }
__device__ __forceinline__ float wave_max(float m) { m = row_max(m); return fmaxf(fmaxf(rdlane(m, 0), rdlane(m, 16)), fmaxf(rdlane(m, 32), rdlane(m, 48))); }
__device__ __forceinline__ float wave_maxv(float m) {
    m = row_max(m);
    const auto a = __builtin_amdgcn_permlane16_swap(__float_as_uint(m), __float_as_uint(m), false, false);
    m = fmaxf(__uint_as_float(a[0]), __uint_as_float(a[1]));
    const auto b = __builtin_amdgcn_permlane32_swap(__float_as_uint(m), __float_as_uint(m), false, false);
    return fmaxf(__uint_as_float(b[0]), __uint_as_float(b[1]));
}
__device__ __forceinline__ float wave_sum(float x) { x = row_sum(x); return (rdlane(x, 0) + rdlane(x, 16)) + (rdlane(x, 32) + rdlane(x, 48)); }
__device__ __forceinline__ unsigned lds_ld(const unsigned* p) { return __builtin_amdgcn_readfirstlane(__hip_atomic_load(p, __ATOMIC_RELAXED, __HIP_MEMORY_SCOPE_WORKGROUP)); }
__device__ __forceinline__ unsigned lds_addr(const void* p) { return (unsigned)(size_t)(const __attribute__((address_space(3))) void*)p; }
#define DMA16(SRC, LDS) asm volatile("s_mov_b32 %0, m0\n\ts_mov_b32 m0, %2\n\ts_nop 0\n\tglobal_load_lds_dwordx4 %1, off\n\ts_mov_b32 m0, %0" : "=&s"(m0keep) : "v"(SRC), "s"(LDS) : "memory")
__device__ __forceinline__ void lds_st(unsigned* p, unsigned v) { __hip_atomic_store(p, v, __ATOMIC_RELAXED, __HIP_MEMORY_SCOPE_WORKGROUP); }
#define NOW() ((unsigned)__builtin_amdgcn_s_memrealtime())

struct __attribute__((aligned(16))) Smem {
    unsigned cs[32][768];
    double acc[NOUT][DH];
    float ent[NU][ENTW];
    unsigned q[NIN][HALF];
    float2 kz[NIN][BROWS];
    float2 vz[NIN][BROWS];
    v2u tu[2 * NU + 2];
    unsigned grab, merged, tabs, cdone;
    unsigned rdy[NIN], cnt[NOUT];
    unsigned tag_rdy, tag_lo, tag_hi, fw_rdy;
    int stat[32];
    float fw[NSPLIT];
};
#define NSTAT 0
#define STAMP(i) {}
#define STAMPV(i, v) {}
#define STAMP_DUMP()
#define STAMP_UNIT(N)

#define LT(I, V)
#define LTK(K, V)
#define TU(I) (*(const volatile v2u __attribute__((address_space(3)))*)(size_t)lds_addr(&sm.tu[I]))
#define LDS_ADD(P, V) { u64 ex_; asm volatile("s_mov_b64 %0, exec\n\ts_mov_b64 exec, 1\n\tds_add_u32 %1, %2\n\ts_mov_b64 exec, %0" : "=&s"(ex_) : "v"(lds_addr(P)), "v"(V) : "memory"); }
#define LDS_OR(P, V) { u64 ex_; asm volatile("s_mov_b64 %0, exec\n\ts_mov_b64 exec, 1\n\tds_or_b32 %1, %2\n\ts_mov_b64 exec, %0" : "=&s"(ex_) : "v"(lds_addr(P)), "v"(V) : "memory"); }

__global__ __launch_bounds__(NTHREADS, 3) void attn_kernel(
    const unsigned short* __restrict__ q, const int* __restrict__ qk,
    const unsigned short* __restrict__ k_scales, const unsigned short* __restrict__ k_zeros,
    const int* __restrict__ qv,
    const unsigned short* __restrict__ v_scales, const unsigned short* __restrict__ v_zeros,
    const unsigned short* __restrict__ q_cos, const unsigned short* __restrict__ q_sin,
    const unsigned short* __restrict__ k_cos, const unsigned short* __restrict__ k_sin,
    __hip_bfloat16* __restrict__ out, unsigned char* __restrict__ ws)
{
    __shared__ Smem sm;
    const int tid = threadIdx.x;
    unsigned spins = SPIN0;
    u64 tent_; asm volatile("s_memrealtime %0" : "=s"(tent_));
#define TOUCH(OFF, BASE) asm volatile("global_load_dword a6, %0, %1" :: "v"(OFF), "s"(BASE) : "a6", "memory");
    const unsigned cl = min((blockIdx.x & 24) * 8 + tid, PFL - 1u) * 128;
    if (tid < 64) { const char* pc0; asm volatile("s_getpc_b64 %0" : "=s"(pc0)); TOUCH(cl, pc0) }
    __builtin_amdgcn_sched_barrier(0);
    const int hw = __builtin_amdgcn_readfirstlane(tid >> 6);
    const int lane = tid & 63;
    const int split = (blockIdx.x & 3) * 4 + ((blockIdx.x >> 3) & 3);
    const int rank = (blockIdx.x >> 5) * 2 + ((blockIdx.x >> 2) & 1);
    const int brow0 = split * 16;
#define ROWOF(RG) (((RG) >> 2) * 256 + ((RG) & 3) * 4)
    if (hw == 0) {
        typedef const char* const __attribute__((address_space(4))) * kap_t;
        const char* sa = ((kap_t)__builtin_amdgcn_kernarg_segment_ptr())[(0x5623 >> (4 * (split & 3))) & 7];
        unsigned o1 = ((blockIdx.x >> 5) * 256 + brow0) * (DH * 2) + min(lane, 47) * 128;
        unsigned o2 = ((rank + GSZ * (lane >> 3)) * SKV + (lane & 7) * 256 + (brow0 & ~63)) * 2, o3 = o2 + 8 * GSZ * SKV * 2;
        unsigned o4 = (rank + GSZ * (lane >> 2)) * (DH * 2) + min(lane & 3, 2) * 128;
        asm volatile("" : "+v"(o1), "+v"(o2), "+v"(o3), "+v"(o4));
        asm volatile("global_load_dword a6, %0, %1 nt\n\tglobal_load_dword a6, %0, %2 nt" :: "v"((lane & 1) * (NH * SKV * DH * 4u - 128u * NH) + blockIdx.x * 128), "s"(qk), "s"(qv) : "a6", "memory");
#define TOUCHP(OFF, BASE, POL) asm volatile("global_load_dword a6, %0, %1 " POL :: "v"(OFF), "s"(BASE) : "a6", "memory");
        TOUCHP(o1, k_cos, TPT) TOUCHP(o1, k_sin, TPT) TOUCH(o4, q) TOUCHP(o2, sa, TPS) TOUCHP(o3, sa, TPS)
    }
    if (hw == 1 && lane < 16 + NSTAT) (&sm.grab)[lane] = lane ? 0 : 64;
    asm volatile("" :: "s"(q), "s"(qk), "s"(k_scales), "s"(k_zeros), "s"(qv), "s"(v_scales), "s"(v_zeros), "s"(q_cos), "s"(q_sin), "s"(k_cos), "s"(k_sin), "s"(out), "s"(ws));
    asm volatile("s_waitcnt lgkmcnt(0)\n\ts_barrier" ::: "memory");
    asm volatile("" : "+s"(tent_));
    const int wave = (int)(0x76543210b9a8ull >> (4 * hw)) & 15;
    STAMP(0)
    const unsigned ts = NOW();
    const int PK = (1268), PV = (1268), TOF = (100), P0 = (1370);
    if (LIKELY ? __builtin_expect(wave < NSW, 1) : wave < NSW) {
        const unsigned rot = 4 * ((unsigned)(PHI >> (60 - 4 * rank)) & 7u), kap = (unsigned)(KAP >> (60 - 4 * rank)) & 3u;
        const unsigned rotv = 4 * ((unsigned)(PHIV >> (60 - 4 * rank)) & 7u), kapv = (unsigned)(KAPV >> (60 - 4 * rank)) & 3u;
#define RGOF(G) ((G) >= NPC / 2 ? (((G) + rotv) & 31u) ^ kapv : (((G) + rot) & 31u) ^ kap)
        const char* cbase[3];
#pragma unroll
        for (int j = 0; j < 3; j++) { const int x = j * 64 + lane; cbase[j] = (x < 96 ? (const char*)k_cos + x * 16 : (const char*)k_sin + (x - 96) * 16) + brow0 * (DH * 2); }
        unsigned m0keep;
#define CS_DMA(RG) { _Pragma("unroll") for (int j = 0; j < 3; j++) DMA16(cbase[j] + ROWOF(RG) * (DH * 2), lds_addr(&sm.cs[0][0]) + (RG) * 3072 + j * 1024); }
#define REPACK(RG) { \
        unsigned* cw = &sm.cs[RG][0]; \
        const unsigned* l = cw + rsel * (DH / 2) + qoff; \
        unsigned r[2][6]; \
        _Pragma("unroll") \
        for (int j = 0; j < 2; j++) { \
            unsigned e[4][2]; \
            _Pragma("unroll") \
            for (int k = 0; k < 4; k++) { \
                const unsigned* a = l + j * DH + (k & 1) * 48 + (k >> 1) * 384;       \
                unsigned x0 = a[0], x1 = a[1]; \
                e[k][0] = __builtin_amdgcn_alignbit(x1, x0, sh); \
                e[k][1] = x1 >> sh; \
            } \
            r[j][0] = (e[0][0] & 0xffffu) | (e[3][0] << 16); \
            r[j][1] = (e[0][0] >> 16) | (e[3][0] & 0xffff0000u); \
            r[j][2] = (e[0][1] & 0xffffu) | (e[3][1] << 16); \
            r[j][3] = (e[1][0] & 0xffffu) | (e[2][0] << 16); \
            r[j][4] = (e[1][0] >> 16) | (e[2][0] & 0xffff0000u); \
            r[j][5] = (e[1][1] & 0xffffu) | (e[2][1] << 16); \
        } \
        wave_sync(); \
        _Pragma("unroll") \
        for (int j = 0; j < 2; j++) { *(v4u*)&cw[j * 256 + lane * 4] = (v4u){r[j][0], r[j][1], r[j][2], r[j][3]}; *(v2u*)&cw[512 + j * 128 + lane * 2] = (v2u){r[j][4], r[j][5]}; } \
        wave_sync(); \
        LDS_OR(&sm.tabs, 1u << (RG)) }
        const int sub = lane & 31;
        const int rsel = lane >> 5;
        const unsigned sh = (sub & 1) * 16;
        const int qoff = 3 * (sub >> 1) + (sub & 1);
        const char* kbase = (const char*)qk + (size_t)(rank * SKV + brow0) * (DH * 4);
        const char* vbase = (const char*)qv + (size_t)(rank * SKV + brow0) * (DH * 4);
        const unsigned koff = (rsel * DH + 3 * sub) * 4, voff = 3 * lane * 4;
#define AR(S) (16 + (S) * 8)
        asm volatile("" ::: "a16", "a17", "a18", "a19", "a20", "a21", "a22", "a23", "a24", "a25", "a26", "a27", "a28", "a29", "a30", "a31",
                            "a32", "a33", "a34", "a35", "a36", "a37", "a38", "a39", "a40", "a41", "a42", "a43", "a44", "a45", "a46", "a47");
        asm volatile("" ::: "a48", "a49", "a50", "a51", "a52", "a53", "a54", "a55", "a56", "a57", "a58", "a59", "a60", "a61", "a62", "a63");
        __builtin_amdgcn_s_waitcnt(0x0f70);
        int ta[3], tb[3];
#define GLD(A, OFF, BASE, IMM) asm volatile("global_load_dwordx3 a[%c0:%c1], %2, %3 offset:%4 nt" :: "i"(A), "i"((A) + 2), "v"(OFF), "s"(BASE), "i"(IMM))
#define ISSUE(S, J, BASE, OA, OB) { GLD(AR(S), OA, BASE, (J) * 1536); GLD(AR(S) + 4, OB, BASE, (J) * 1536); }
#define WAITT(S, N) asm volatile("s_waitcnt vmcnt(%6)\n\tv_accvgpr_read_b32 %0, a[%c7]\n\tv_accvgpr_read_b32 %1, a[%c8]\n\tv_accvgpr_read_b32 %2, a[%c9]\n\t" \
                              "v_accvgpr_read_b32 %3, a[%c10]\n\tv_accvgpr_read_b32 %4, a[%c11]\n\tv_accvgpr_read_b32 %5, a[%c12]" \
                              : "=v"(ta[0]), "=v"(ta[1]), "=v"(ta[2]), "=v"(tb[0]), "=v"(tb[1]), "=v"(tb[2]) \
                              : "i"(N), "i"(AR(S)), "i"(AR(S) + 1), "i"(AR(S) + 2), "i"(AR(S) + 4), "i"(AR(S) + 5), "i"(AR(S) + 6))
        const unsigned koffb = koff + 384, voffb = voff + 768;
        unsigned u = 0, rg, np = 0;
#define RG0(C) RGOF((unsigned)(wave + 8 * (C)))
        CS_DMA(RG0(0))
        STAMP(52)
        STAMP(53)
        const float scale = 0.07216878364870322f;
        double acc[3] = {0.0, 0.0, 0.0};
        bool chg, none = false;
        unsigned Qp[3], Qn[3];
        float kz4[2], ksc, kres[2];
        v4u ca[2]; v2u cb[2];
        v2f qe[2][3], A[2];
        const char* nb; unsigned oa, ob;
        unsigned g, mg, nrg, tg, late = 0;
        u64 tis;
        unsigned dg, par = 0, gu;
        int early;
        const int bth = wave < (2) ? (20) * 128 : 0x3fffffff;
        const unsigned grab_adr = lds_addr(&sm.grab);
        asm volatile("v_accvgpr_write_b32 a13, 1\n\tv_accvgpr_write_b32 a14, 2" ::: "a13", "a14", "a15");
#define GRAB(N) { u64 ex_; asm volatile("s_mov_b64 %0, exec\n\ts_mov_b64 exec, 1\n\tds_add_rtn_u32 a15, %1, a%c2\n\ts_mov_b64 exec, %0" : "=&s"(ex_) : "v"(grab_adr), "i"(12 + (N)) : "memory"); }
#define GRABBED() { unsigned t_; asm volatile("s_waitcnt lgkmcnt(0)\n\tv_accvgpr_read_b32 %1, a15\n\ts_nop 0\n\tv_readfirstlane_b32 %0, %1\n\ts_nop 1" : "=s"(g), "=v"(t_)); }
#define NEXT1() const v2u x_ = TU(g >> 5); u64 tn_; asm volatile("s_memrealtime %0" : "=s"(tn_));
#define NEXT2() { \
            chg = (g >> 5) != u; none = g >= NPC; \
            const bool nv = g >= NPC / 2; \
            nrg = RGOF(g); \
            unsigned boff; asm("s_mul_i32 %0, %1, 0x300" : "=s"(boff) : "s"((nv ? VENT(g >> 5) : g >> 5) * (GSZ * SKV) + ROWOF(nrg) + 2)); \
            nb = (nv ? vbase : kbase) + boff; \
            oa = nv ? voff : koff; ob = nv ? voffb : koffb; \
            asm volatile("s_waitcnt lgkmcnt(0)" : "+s"(tn_) : "s"(nb), "v"(oa), "v"(ob)); \
            tg = __builtin_amdgcn_readfirstlane(x_.x) + (g & 31) * __builtin_amdgcn_readfirstlane(x_.y); \
            early = none ? 0 : (int)((unsigned)tn_ * 128 - tg) - bth; \
            if (early > 0) asm volatile("s_cmp_lg_u32 %3, 0\n\ts_cbranch_scc1 1f\n\t" L4(32) "\ts_branch 2f\n1:\n\t" L4(16) "2:\n\ts_mov_b64 vcc, exec\n\ts_mov_b64 exec, 1\n\tds_add_rtn_u32 a15, %4, a13\n\ts_mov_b64 exec, vcc" \
                                    :: "v"(oa), "v"(ob), "s"(nb), "s"(pr), "v"(grab_adr) : "scc", "vcc", "memory"); }
#define L4(A) "global_load_dwordx3 a[" #A ":" #A "+2], %0, %2 offset:-1536 nt\n\tglobal_load_dwordx3 a[" #A "+4:" #A "+6], %1, %2 offset:-1536 nt\n\t" \
              "global_load_dwordx3 a[" #A "+8:" #A "+10], %0, %2 nt\n\tglobal_load_dwordx3 a[" #A "+12:" #A "+14], %1, %2 nt\n"
#define GATE(...) \
            asm volatile(PRIO0 "1:\n\ts_memrealtime vcc\n\ts_waitcnt lgkmcnt(0)\n\ts_lshl_b32 vcc_lo, vcc_lo, 7\n\ts_sub_u32 %0, %1, vcc_lo\n\ts_cmp_lt_u32 %0, 0x40000\n\ts_cbranch_scc0 2f\n\t" \
                         "s_cmp_lt_u32 %0, %2\n\ts_cbranch_scc1 1b\n\ts_sleep %3\n\ts_branch 1b\n2:" PRIO1 : "=&s"(dg) : "s"(tg), "i"(GTH * 128), "i"(GS1), __VA_ARGS__ : "scc", "vcc", "memory");
#define RD6(A) "v_accvgpr_read_b32 %0, a[%c6+" #A "]\n\tv_accvgpr_read_b32 %1, a[%c6+" #A "+1]\n\tv_accvgpr_read_b32 %2, a[%c6+" #A "+2]\n\t" \
               "v_accvgpr_read_b32 %3, a[%c6+" #A "+4]\n\tv_accvgpr_read_b32 %4, a[%c6+" #A "+5]\n\tv_accvgpr_read_b32 %5, a[%c6+" #A "+6]\n"
#define LD2(A) "global_load_dwordx3 a[%c6+" #A ":%c6+" #A "+2], %7, %9 offset:%10 nt\n\tglobal_load_dwordx3 a[%c6+" #A "+4:%c6+" #A "+6], %8, %9 offset:%10 nt"
#define OUT6 "=&v"(ta[0]), "=&v"(ta[1]), "=&v"(ta[2]), "=&v"(tb[0]), "=&v"(tb[1]), "=&v"(tb[2])
#define WI(A, B, TAIL) "s_cmp_gt_i32 %13, 0\n\ts_cbranch_scc1 3f\n\ts_waitcnt vmcnt(2)\n\t" LD2(A) TAIL "\n3:\n\ts_waitcnt vmcnt(%c14)\n\t" RD6(B)
#define WAITISSUE(S, TAIL, ...) asm volatile("s_cmp_lg_u32 %12, 0\n\ts_cbranch_scc1 1f\n\t" WI(16, 0, TAIL) "\ts_branch 2f\n1:\n\t" WI(0, 16, TAIL) "2:" \
                              : OUT6 : "i"(AR(S)), "v"(oa), "v"(ob), "s"(nb), "i"(((S) - 1) * 1536), "v"(grab_adr), "s"(pr), "s"(early), "i"(6 - 2 * (S)), __VA_ARGS__ : "scc", "vcc", "memory")
#define LASTT(S) asm volatile("s_waitcnt vmcnt(%c8)\n\ts_cmp_lg_u32 %7, 0\n\ts_cbranch_scc1 1f\n\t" RD6(0) "\ts_branch 2f\n1:\n\t" RD6(16) "2:" : OUT6 : "i"(AR(S)), "s"(pr), "i"(2 - 2 * (S)) : "scc")
#define DRAW "\n\ts_mov_b64 vcc, exec\n\ts_mov_b64 exec, 1\n\tds_add_rtn_u32 a15, %11, a13\n\ts_mov_b64 exec, vcc"
#define LATE0()
#define LATE1()
#define LATEF()
#define LATEF()
#define K_TAB() { _Pragma("unroll") for (int p = 0; p < 2; p++) { \
            const unsigned* cw = &sm.cs[rg][0]; \
            ca[p] = *(const v4u*)&cw[p * 256 + lane * 4]; cb[p] = *(const v2u*)&cw[512 + p * 128 + lane * 2]; } }
#define K_PRE(P) { \
        float e0, e1, e2, f0, f1, f2; \
        asm("v_dot2_f32_bf16 %0, %6, %12, 0\n\tv_dot2_f32_bf16 %3, %9, %15, 0\n\tv_dot2_f32_bf16 %1, %7, %13, 0\n\tv_dot2_f32_bf16 %4, %10, %16, 0\n\t" \
            "v_dot2_f32_bf16 %2, %8, %14, 0\n\tv_dot2_f32_bf16 %5, %11, %17, 0\n\ts_nop 2" \
            : "=&v"(e0), "=&v"(e1), "=&v"(e2), "=&v"(f0), "=&v"(f1), "=&v"(f2) \
            : "v"(Qp[0]), "v"(Qp[1]), "v"(Qp[2]), "v"(Qn[0]), "v"(Qn[1]), "v"(Qn[2]), \
              "v"(ca[P].x), "v"(ca[P].y), "v"(ca[P].z), "v"(ca[P].w), "v"(cb[P].x), "v"(cb[P].y)); \
        qe[P][0] = (v2f){e0, f0}; qe[P][1] = (v2f){e1, f1}; qe[P][2] = (v2f){e2, f2}; \
        A[P] = (qe[P][0] + qe[P][1] + qe[P][2]) * (v2f){-kz4[P], -kz4[P]}; }
#define K_SUM(P) { \
        _Pragma("unroll") \
        for (int i = 0; i < 3; i++) { v2f x = {(float)ta[i], (float)tb[i]}; A[P] = qe[P][i] * x + A[P]; } \
        kres[P] = A[P].x + A[P].y; }
#define K_RED2() { \
        const bool o1 = lane & 1; \
        float sx = (o1 ? kres[1] : kres[0]) + __int_as_float(__builtin_amdgcn_update_dpp(0, __float_as_int(o1 ? kres[0] : kres[1]), 0xb1, 0xf, 0xf, true)); \
        sx = dpp_add<0x4e>(sx); sx = dpp_add<0x124>(sx); sx = dpp_add<0x128>(sx); \
        const auto sw = __builtin_amdgcn_permlane16_swap(__float_as_uint(sx), __float_as_uint(sx), false, false); \
        sx = __uint_as_float(sw[0]) + __uint_as_float(sw[1]); \
        if ((lane & 30) == 0) sm.ent[u][rg * 4 + 2 * (lane & 1) + rsel] = sx * ksc; }
#define LDV(P) __hip_atomic_load(P, __ATOMIC_RELAXED, __HIP_MEMORY_SCOPE_WORKGROUP)
#define RDYWAIT(SL, SEQ) { while (lds_ld(&sm.rdy[SL]) != (SEQ)) SLEEP(1); CBAR(); }
#define LOADZ() { _Pragma("unroll") for (int p = 0; p < 2; p++) kz4[p] = sm.kz[u % NIN][rg * 4 + 2 * p + rsel].x; ksc = sm.kz[u % NIN][rg * 4 + 2 * (lane & 1) + rsel].y * scale; }
#define LOADQ() { _Pragma("unroll") for (int i = 0; i < 3; i++) { Qp[i] = sm.q[u % NIN][3 * sub + i]; Qn[i] = __builtin_amdgcn_alignbit(Qp[i], Qp[i], 16) ^ 0x80000000u; } }
#define FLUSH(ISV) { \
            mg = __builtin_amdgcn_readfirstlane(mg); \
            while (u >= mg + NOUT) { SLEEP(1); mg = lds_ld(&sm.merged); } \
            CBAR(); \
            if (ISV) { const unsigned aa = lds_addr(&sm.acc[u % NOUT][lane]); \
                asm volatile("ds_add_f64 %0, %1\n\tds_add_f64 %0, %2 offset:512\n\tds_add_f64 %0, %3 offset:1024" :: "v"(aa), "v"(acc[0]), "v"(acc[1]), "v"(acc[2]) : "memory"); \
                acc[0] = 0.0; acc[1] = 0.0; acc[2] = 0.0; } \
            CBAR(); \
            LATEF() \
            LDS_ADD(&sm.cnt[u % NOUT], np) \
            CBAR(); \
            STAMP_UNIT(np) \
            np = 0; }
#define ADVANCE() { u = gu; rg = nrg; }

#define REQ0(N, S) { tg = (ts + TOF) * 128 + (wave + 8 * (N)) * P0; \
            const char* kb = kbase + (((N) >> 2) * (GSZ * SKV) + ROWOF(RG0((N) & 3))) * (DH * 4); GATE("s"(kb)) ISSUE(S, 0, kb, koff, koffb) ISSUE((S) + 1, 1, kb, koff, koffb) STAMP(44 + (N)) }
#define USE0(N, S, W) { u = (N) >> 2; rg = RG0((N) & 3); K_TAB() LOADQ() LOADZ() K_PRE(0) K_PRE(1) WAITT(S, W); K_SUM(0) WAITT((S) + 1, W); K_SUM(1) K_RED2() }
#define VMW(N) asm volatile("s_waitcnt vmcnt(" #N ")" ::: "memory");
        REQ0(0, 0) CS_DMA(RG0(1))
        VMW(7) REPACK(RG0(0))
        STAMP(1)
        REQ0(1, 2)
        RDYWAIT(0, 1u)
        USE0(0, 0, 7)
        VMW(4) REPACK(RG0(1))
        REQ0(2, 0) CS_DMA(RG0(2))
        USE0(1, 2, 7)
        REQ0(3, 2)
        VMW(4) REPACK(RG0(2))
        USE0(2, 0, 4)
        REQ0(4, 0) CS_DMA(RG0(3))
        REQ0(5, 4)
        VMW(4) REPACK(RG0(3))
        USE0(3, 2, 4)
        CBAR();
        LDS_ADD(&sm.cnt[0], 4u)
        REQ0(6, 2)
        RDYWAIT(1, 2u)
        USE0(4, 0, 8)
        USE0(5, 4, 4)
        REQ0(7, 0)
        GRAB(1)
        USE0(6, 2, 4)
        rg = RG0(3); np = 3;
        GRABBED()
#pragma unroll 1
        while (u < NU) {
            const unsigned pr = __builtin_amdgcn_readfirstlane(par); par ^= 1;
            NEXT1()
            const unsigned tb_ = LDV(&sm.tabs), fl = LDV(&sm.rdy[u % NIN]);
            mg = LDV(&sm.merged);
            CBAR();
            K_TAB() LOADQ() LOADZ()
            NEXT2()
            if (__builtin_expect(__builtin_amdgcn_readfirstlane(fl) != u + 1 || !((__builtin_amdgcn_readfirstlane(tb_) >> rg) & 1u), 0)) {
                while (!((lds_ld(&sm.tabs) >> rg) & 1u)) SLEEP(1)
                RDYWAIT(u % NIN, u + 1)
                K_TAB() LOADQ() LOADZ()
            }
            K_PRE(0) K_PRE(1)
            if (early <= 0) GATE("v"(A[0]), "v"(A[1]), "v"(oa), "v"(ob), "s"(nb), "s"(pr))
            WAITISSUE(0, DRAW, "v"(A[0]));
            LATE0()
            K_SUM(0)
            WAITISSUE(1, "", "v"(A[1]));
            K_SUM(1)
            LATE1()
            gu = g >> 5;
            GRABBED()
            K_RED2()
            np++;
            if (chg) FLUSH(false)
            ADVANCE()
        }
#pragma unroll 1
        while (u < 2 * NU) {
            const unsigned pr = __builtin_amdgcn_readfirstlane(par); par ^= 1;
            NEXT1()
            mg = LDV(&sm.merged);
            CBAR();
            const unsigned ve = VENT(u);
            v4i pw = *(const v4i*)&sm.ent[ve][rg * 4];
            NEXT2()
            if (__builtin_expect(__builtin_amdgcn_readfirstlane(mg) <= ve, 0)) { while (lds_ld(&sm.merged) <= ve) SLEEP(1); CBAR(); pw = *(const v4i*)&sm.ent[ve][rg * 4]; }
            const double w0 = pw.x, w1 = pw.y, w2 = pw.z, w3 = pw.w;
            if (none) { LASTT(0); } else { if (early <= 0) GATE("s"(pr), "v"(w0), "v"(w1), "v"(w2), "v"(w3), "v"(oa), "v"(ob), "s"(nb)) WAITISSUE(0, DRAW, "v"(w0)); }
            LATE0()
#pragma unroll
            for (int i = 0; i < 3; i++) acc[i] = fma(w1, (double)tb[i], fma(w0, (double)ta[i], acc[i]));
            if (none) { LASTT(1); } else WAITISSUE(1, "", "v"(w2));
#pragma unroll
            for (int i = 0; i < 3; i++) acc[i] = fma(w3, (double)tb[i], fma(w2, (double)ta[i], acc[i]));
            LATE1()
            gu = g >> 5;
            GRABBED()
            np++;
            if (chg) FLUSH(true)
            ADVANCE()
        }
        STAMP(2)
        {
            const int head = blockIdx.x;
            while (lds_ld(&sm.fw_rdy) == 0) SLEEP(2)
            CBAR();
            const unsigned tt = lds_ld(&sm.tag_lo) << 18;
            const float wh = sm.fw[wave + 8 * rsel];
            const unsigned ro = (wave + 8 * rsel) * 512 + sub * 16;
            const char* rb = (const char*)ws + WS_PART + head * (NSPLIT * 512);
            v4f* pb = (v4f*)((char*)&sm.cs[0][0] + 16384);
            __hip_bfloat16* o = out + (size_t)head * DH + 6 * sub + 3 * rsel;
            tg = __builtin_amdgcn_readfirstlane(TU(2 * NU).x) + ((head >> 4) == VENT(15) ? (120) : (120)) * 128;
            GATE("s"(tt), "v"(wh), "v"(ro), "s"(rb), "v"(o))
            STAMP(60)
            STAMPV(57, NOW() - (((NOW() * 128 - __builtin_amdgcn_readfirstlane(TU(2 * NU).x)) >> 7) & 0xfffff))
            v4u R;
#define PLD(A) "global_load_dwordx4 a[" #A ":" #A "+3], %6, %7 sc1\n\t"
#define PCK(A, N, L) "s_waitcnt vmcnt(" #N ")\n\tv_accvgpr_read_b32 %1, a[" #A "+1]\n\tv_accvgpr_read_b32 %3, a[" #A "+3]\n\tv_accvgpr_read_b32 %0, a[" #A "]\n\tv_accvgpr_read_b32 %2, a[" #A "+2]\n\t" \
                     "v_xor_b32 %1, %8, %1\n\tv_xor_b32 %3, %8, %3\n\tv_or_b32 %4, %1, %3\n\tv_cmp_gt_u32 vcc, %9, %4\n\ts_cmp_eq_u64 vcc, exec\n\ts_cbranch_scc1 " L "\n\t"
            { unsigned t_;
              asm volatile("s_cmp_eq_u32 %10, %12\n\ts_cbranch_scc1 1f\n\t" PLD(16) PCK(16, 0, "3f") "1:\n\t" PLD(16) "s_sleep %11\n\t" PLD(20) "s_sleep %11\n\t" PLD(24)
                           "2:\n\t" PCK(16, 2, "3f") PLD(16) PCK(20, 2, "3f") PLD(20) PCK(24, 2, "3f") PLD(24)
                           "v_add_co_u32 %5, vcc, 64, %5\n\ts_cbranch_vccz 2b\n\ts_endpgm\n3:"
                           : "=&v"(R.x), "=&v"(R.y), "=&v"(R.z), "=&v"(R.w), "=&v"(t_), "+v"(spins) : "v"(ro), "s"(rb), "s"(tt), "s"(0x40000u), "s"(head >> 4), "i"(PSL), "i"(VENT(15)) : "scc", "vcc", "memory"); }
            STAMP(61)
            const float s0 = ldexpf(wh, (int)(R.y >> 10) - 139), s1 = ldexpf(wh, (int)(R.w >> 10) - 139);
            const float f[6] = {s0 * (float)(int)__builtin_amdgcn_sbfe(R.x, 0, 14), s0 * (float)(int)__builtin_amdgcn_sbfe(R.x, 14, 14), s0 * (float)(int)__builtin_amdgcn_sbfe(__builtin_amdgcn_alignbit(R.y, R.x, 28), 0, 14),
                                s1 * (float)(int)__builtin_amdgcn_sbfe(R.z, 0, 14), s1 * (float)(int)__builtin_amdgcn_sbfe(R.z, 14, 14), s1 * (float)(int)__builtin_amdgcn_sbfe(__builtin_amdgcn_alignbit(R.w, R.z, 28), 0, 14)};
            float sv[3];
#pragma unroll
            for (int i = 0; i < 3; i++) {
                const auto sw = __builtin_amdgcn_permlane32_swap(__float_as_uint(f[i]), __float_as_uint(f[i + 3]), false, false);
                sv[i] = __uint_as_float(sw[0]) + __uint_as_float(sw[1]);
            }
            pb[wave * 64 + lane] = (v4f){sv[0], sv[1], sv[2], 0.f};
            CBAR();
            { u64 ex_; asm volatile("s_mov_b64 %0, exec\n\ts_mov_b64 exec, 1\n\tds_add_rtn_u32 a15, %1, a13\n\ts_mov_b64 exec, %0" : "=&s"(ex_) : "v"(grab_adr + 12) : "memory"); }
            v4f ps[NSW];
#pragma unroll
            for (int w = 0; w < NSW; w++) ps[w] = pb[w * 64 + lane];
            GRABBED()
            if (g == NSW - 1) {
                const v4f sum = ((ps[0] + ps[1]) + (ps[2] + ps[3])) + ((ps[4] + ps[5]) + (ps[6] + ps[7]));
                o[0] = __float2bfloat16(sum.x); o[1] = __float2bfloat16(sum.y); o[2] = __float2bfloat16(sum.z);
                if (lane == 0) __hip_atomic_store((unsigned*)(ws + (64u << 20)) + blockIdx.x, NOW(), __ATOMIC_RELAXED, __HIP_MEMORY_SCOPE_AGENT);
                LT(1, NOW())
            }
        }
        STAMP(62)
        STAMP_DUMP()
        return;
    }

    if (wave == NSW) {
        STAMPV(24, (unsigned)__builtin_amdgcn_s_memtime()) STAMP(25)
    } else if (wave == NSW + 1) {
        u64 E; asm volatile("s_setprio 3\n\ts_load_dwordx2 %0, %1, 0\n\ts_waitcnt lgkmcnt(0)" : "=s"(E) : "s"(ws + WS_EPOCH) : "memory");
        const int NB = (3), CAP = (45);
        const int K0 = (3), GAIN = (24), BK = (3), FLOOR = (3), XM = (250), XD1 = (100), FTH = (15), XW = (40), RTH = (6), KI = (14), KD = (9), XR = (170);
        unsigned t2 = (ts + TOF) * 128 + 64 * P0;
        int off = 0, off1 = 0, hold = 0, cum = 0, dp = 0, sp = 0;
#define NOM(U) (((int)(U) - 2) * 32 * PK + max((int)(U) - NU, 0) * 32 * (PV - PK))
#define NEWLINE(K) { if (lane >= (K) && lane <= 2 * NU) sm.tu[lane] = (v2u){t2 + NOM(lane) + (cum + (lane - (int)(K)) * dp + (lane > (K) ? sp + off : off1)) * 32, \
                                                                            (unsigned)((lane >= NU ? PV : PK) + dp + (lane == (K) ? sp + off - off1 : 0))}; }
        NEWLINE(2)
        if (lane == 0) sm.tu[1] = (v2u){t2 - 32 * P0, (unsigned)P0};
        const u64 E1 = E + 1;
        const unsigned rtag = (unsigned)E1 * 0x9e3779b1u;
        const bool boss = blockIdx.x == 0;
        u64* const slot = (u64*)(ws + WS_REF + (blockIdx.x & 63) * LSTRIDE + (blockIdx.x >> 6) * 16);
        const u64* const slots = (const u64*)(ws + WS_REF + lane * LSTRIDE);
        u64* const dec = (u64*)(ws + WS_DEC);
#define GLDS(P) __hip_atomic_load(P, __ATOMIC_RELAXED, __HIP_MEMORY_SCOPE_AGENT)
#define GSTS(P, V) __hip_atomic_store(P, V, __ATOMIC_RELAXED, __HIP_MEMORY_SCOPE_AGENT)
#define UNTIL(T) while ((unsigned)((T) - NOW() * 128) < 0x40000u) SLEEP(2)
        if (lane < 2) GSTS(slot + lane, lane ? E1 : (u64)ts | ((u64)rtag << 32));
        if (lane == 0) { sm.tag_lo = (unsigned)E1; CBAR(); lds_st(&sm.tag_rdy, 1u); }
        unsigned dv = 0, npoll = 0;
        bool joined = false;
#define JOIN(T0) { const int d0 = min(max((int)((T0) - ts), -1000), 1000) + (0); off -= d0 * 4; t2 += d0 * 128; joined = true; }
        unsigned pw_ = 0, tw_ = 0, now, p; int prov = 0;
#define LOOK() { u64 t_; asm volatile("ds_read_b32 %1, %2\n\ts_memrealtime %0\n\ts_waitcnt lgkmcnt(0)" : "=s"(t_), "=v"(p) : "v"(lds_addr(&sm.grab)) : "memory"); \
                 p = __builtin_amdgcn_readfirstlane(p) - 8; now = (unsigned)t_ * 128; }
#define LATENESS(P, T) ({ const v2u e = TU((P) >> 5); (int)((T) - __builtin_amdgcn_readfirstlane(e.x) - ((P) & 31) * __builtin_amdgcn_readfirstlane(e.y)); })
#define WATCH(T) for (;;) { \
            LOOK() \
            if (__builtin_expect(now - tw_ > (80) * 128u && pw_ >= 64 && p < NPC - 8, 0)) { \
                const int lw = max(LATENESS(pw_, tw_), 0); \
                if (LATENESS(p, now) - lw > (80) * 128) \
                    for (;;) { \
                        const int d_ = min(max(LATENESS(p, now) - lw, 0) >> 5, 1600); \
                        if (lane <= 2 * NU) atomicAdd((unsigned*)&sm.tu[lane], d_ * 32u); \
                        off += d_; prov += d_; hold = 3; stl += d_; \
                        if (p != pw_ || prov > 8000) break; \
                        SLEEP(1) \
                        LOOK() \
                    } \
                } \
            if (p != pw_) { pw_ = p; tw_ = now; } \
            if ((unsigned)((T) - now) >= 0x40000u) break; \
            if ((unsigned)((T) - now) > 25 * 128) SLEEP(WSL) else SLEEP(1) }
        float tmin = 0.f; int c9 = 0, c18 = 0, stl = 0; unsigned tmean = 0;
        if (boss) {
            TOUCH((lane & 31) * 128, dec)
            UNTIL((ts + (330)) * 128)
            for (;;) {
                bool ok = true; float sum = 0.f; tmin = 0.f;
#pragma unroll
                for (int i = 0; i < 4; i++) { const u64 x = GLDS(slots + 2 * i), e = GLDS(slots + 2 * i + 1); ok &= e == E1 && (unsigned)(x >> 32) == rtag; sum += (float)min(max((int)((unsigned)x - ts), -1000), 1000); tmin = fminf(tmin, (float)(int)((unsigned)x - ts)); }
                if (__builtin_amdgcn_ballot_w64(ok) == ~0ull) { dv = ts + (int)(wave_sum(sum) * (1.f / 256)); break; }
                SLEEP(8)
            }
            if (lane == 0) { GSTS(dec, (u64)dv | ((u64)rtag << 32)); GSTS((u64*)(ws + WS_EPOCH), E1); }
            JOIN(dv)
            tmean = dv;
            dv = 0;
        }
        int sp1 = 0, tot1 = 0, tot2 = 0;
#pragma unroll 1
        for (unsigned k = 2; k < 2 * NU; k++) {
            const unsigned tk = t2 + NOM(k) + cum * 32;
            if (k >= K0 && k + 1 < 2 * NU) {
                WATCH(tk - (XM + XW) * 128)
                int sum = 0, n = 0;
                do { LOOK() sum += LATENESS(p, now) >> 7; n++; } while ((unsigned)(tk - XM * 128 - now) < 0x40000u);
                const int l = (int)((float)sum * __builtin_amdgcn_rcpf((float)n)) - BK + prov / 4;
                if (l > RTH && lane == 0) GSTS((unsigned*)(ws + WS_REF + (blockIdx.x & 63) * LSTRIDE + 64) + (blockIdx.x >> 6), (unsigned)min(l, 1000) | ((rtag + k) << 10));
            }
            const int cum0 = cum;
            if (k == 9) c9 = cum; if (k == 18) c18 = cum;
            bool got = boss;
            if (!boss && (k > K0 || !joined)) {
                WATCH(tk - XR * 128)
                const u64 x = GLDS(dec + (joined ? k * 16 : 0));
                dv = __builtin_amdgcn_readfirstlane((unsigned)x);
                got = __builtin_amdgcn_readfirstlane((unsigned)(x >> 32)) == rtag;
                npoll += !got;
                if (got && !joined) { JOIN(dv) got = false; }
            }
            sp1 = sp; sp = 0;
            if (got && k > K0) { cum = dv >> 18; sp = dv >> 6 & 4095u; dp = dv & 63u; }
            const int yld = cum - cum0 + sp;
            { const int pj = min(prov, cum - cum0); off -= pj; prov -= pj; off1 = off;
              const int ps = min(prov, sp); off -= ps; prov -= ps;
              if (hold) hold--; else prov = 0;
              const int r_ = off - prov; off -= r_ > 40 || r_ < -40 ? r_ / NB : r_; }
            NEWLINE(k)
            cum = min(cum + sp + dp, 16383);
            if (boss) dv = (cum << 18) | dp;
            if (boss && k >= K0 && k + 1 < 2 * NU && (int)(NOW() * 128 - tk) < -60 * 128) {
                WATCH(tk - (XM - XD1) * 128)
                const v4u x = __builtin_amdgcn_raw_buffer_load_b128(__builtin_amdgcn_make_buffer_rsrc(ws + WS_REF + 64, 0, 64 * LSTRIDE, 0x00020000), lane * LSTRIDE, 0, SC1);
                float a[4];
#pragma unroll
                for (int i = 0; i < 4; i++) a[i] = ((x[i] ^ ((rtag + k) << 10)) >> 10) == 0 ? (float)(x[i] & 1023u) : 0.f;
                const int tot = (int)wave_sum(a[0] + a[1] + a[2] + a[3]) >> 6;
                float mn = 0.f;
                if (tot >= 40) {
#pragma unroll 1
                    for (float st = 512.f; st >= 4.f; st *= 0.5f) {
                        const float x_ = mn + st;
                        const int c_ = __builtin_popcountll(__builtin_amdgcn_ballot_w64(a[0] >= x_)) + __builtin_popcountll(__builtin_amdgcn_ballot_w64(a[1] >= x_)) +
                                       __builtin_popcountll(__builtin_amdgcn_ballot_w64(a[2] >= x_)) + __builtin_popcountll(__builtin_amdgcn_ballot_w64(a[3] >= x_));
                        if (c_ >= (224)) mn = x_;
                    }
                }
                const float c = mn + (float)CAP, fth = (float)(FTH * (2 * NU - (int)k));
                const int cut = (int)wave_sum(fminf(a[0], c) + fminf(a[1], c) + fminf(a[2], c) + fminf(a[3], c)) >> 6, m4 = (int)mn * 4;
                const bool far = __builtin_amdgcn_ballot_w64(fmaxf(fmaxf(a[0], a[1]), fmaxf(a[2], a[3])) > fth) != 0;
                const int pend = yld + sp1 * XM / 320;
                const int nj = max(m4 - pend, 0);
                const int ns = min(max(max(min(m4 + (tot - m4) * GAIN / 16 - pend - nj, (cut - m4) * GAIN / 16), 0), far ? FLOOR * 4 : 0), 4000);
                int ndp = dp;
                if (k >= (unsigned)KD && tot >= KI && tot > tot1 && tot < tot1 + 32 && tot1 < tot2 + 32) ndp = min(dp + (2), 40);
                if (tot + tot1 < 2 && (1)) ndp = max(dp - 1, 0);
                tot2 = tot1; tot1 = tot;
                dv = (min(cum + nj, 16383) << 18) | (ns << 6) | ndp;
                if ((int)(NOW() * 128 - tk) > (60) * 128) dv = (cum << 18) | dp;
                else if (lane == 0) GSTS(dec + (k + 1) * 16, (u64)dv | ((u64)rtag << 32));
                LTK(k + 33, (int)(NOW() * 128 - tk) / 128 + 1000)
                LTK(k + 1, (min(tot, 4095) << 20) | (min(nj + ns, 1023) << 10) | (dp + 100))
                STAMPV(5 + k, (min(tot, 4095) << 20) | (min(nj + ns, 1023) << 10) | (dp + 100))
            }
        }
        WATCH(p >= NPC - 8 ? now - 128 : now + 4096)
        if (boss) {
            tmin = -wave_max(-tmin);
            const unsigned t0 = (unsigned)tent_ + (int)tmin;
            const unsigned t32 = NOW() - (int)(NOW() * 128 - __builtin_amdgcn_readfirstlane(TU(2 * NU).x)) / 128 - t0;
            while (NOW() - t0 < 14000u) SLEEP(8)
            float e = 0.f;
#pragma unroll
            for (int i = 0; i < 4; i++) { const unsigned z = GLDS((const unsigned*)(ws + (64u << 20)) + i * 64 + lane) - t0; e = fmaxf(e, z < 14000u ? (float)z : 0.f); }
            e = wave_max(e);
            const int DG = 1;
            const int x = DG == 0 ? 0 : DG == 1 ? c9 : DG == 2 ? c18 - c9 : DG == 3 ? cum - c18 : DG == 4 ? stl : DG == 6 ? ((int)e - 10000) * 4 : DG == 7 ? ((int)e - (int)t32) * 4 : (int)(tmean - t0) * 4;
            while (NOW() - t0 < 15000u + (unsigned)min(max(x * 5 / 2, 0), 20000)) SLEEP(2)
        }
    } else if (wave == NSW + 2) {
        const int ql = lane < 48 ? lane : 47;
        unsigned c1, c2, s1, s2;
#define QT_LOADS() { int qo_ = ql; asm volatile("" : "+v"(qo_)); \
                     c1 = ((const unsigned*)q_cos)[qo_]; c2 = ((const unsigned*)q_cos)[48 + qo_]; s1 = ((const unsigned*)q_sin)[qo_]; s2 = ((const unsigned*)q_sin)[48 + qo_]; }
        unsigned q1, q2, lkz, lks, lvz, lvs;
#define UNIT_LOADS(HEAD) { \
        q1 = ((const unsigned*)(q + (size_t)(HEAD) * DH))[ql]; q2 = ((const unsigned*)(q + (size_t)(HEAD) * DH))[48 + ql]; \
        const size_t so = ((size_t)(HEAD) * SKV + brow0) / 2 + (lane >> 3) * 128 + (lane & 7); \
        lkz = SLD((const unsigned*)k_zeros + so); lks = SLD((const unsigned*)k_scales + so); \
        lvz = SLD((const unsigned*)v_zeros + so); lvs = SLD((const unsigned*)v_scales + so); }
#define UNIT_FILL(SL, U) { \
            v4f qq; \
            qq.x = bfr(bfr(bflo(q1) * bflo(c1)) + bfr(-bflo(q2) * bflo(s1))); \
            qq.y = bfr(bfr(bflo(q2) * bflo(c2)) + bfr(bflo(q1) * bflo(s2))); \
            qq.z = bfr(bfr(bfhi(q1) * bfhi(c1)) + bfr(-bfhi(q2) * bfhi(s1))); \
            qq.w = bfr(bfr(bfhi(q2) * bfhi(c2)) + bfr(bfhi(q1) * bfhi(s2))); \
            *(v2u*)&sm.q[SL][2 * ql] = (v2u){(__float_as_uint(qq.x) >> 16) | __float_as_uint(qq.y), (__float_as_uint(qq.z) >> 16) | __float_as_uint(qq.w)}; \
            *(v4f*)&sm.kz[SL][2 * lane] = (v4f){bflo(lkz), bflo(lks), bfhi(lkz), bfhi(lks)}; \
            *(v4f*)&sm.vz[SL][2 * lane] = (v4f){bflo(lvz), bflo(lvs), bfhi(lvz), bfhi(lvs)}; \
            CBAR(); \
            if (lane == 0) lds_st(&sm.rdy[SL], (U) + 1); \
            CBAR(); }
        asm volatile("s_setprio 3");
        QT_LOADS()
#pragma unroll 1
        for (unsigned u = 0; u < NU; u++) {
            UNIT_LOADS(rank + GSZ * u)
            while (u >= lds_ld(&sm.merged) + NIN) SLEEP(4);
            CBAR();
            UNIT_FILL(u % NIN, u)
            if (u < 3) STAMP(5 + u)
            if (u == 1) asm volatile("s_setprio 0");
        }
        while (lds_ld(&sm.merged) < NU) SLEEP(16)
        CBAR();
        const unsigned T = lds_ld(&sm.tag_lo);
        if (lane < NU) __hip_atomic_store((u64*)(ws + WS_LOG + (rank * NSPLIT + split) * LSTRIDE2) + lane, (u64)__float_as_uint(sm.ent[lane][130]) | ((u64)T << 32), __ATOMIC_RELAXED, __HIP_MEMORY_SCOPE_AGENT);
        while (lds_ld(&sm.merged) < NU + 3) SLEEP(16)
        for (;;) {
            const u64 x = __hip_atomic_load((const u64*)(ws + WS_LOG + ((blockIdx.x & 15) * NSPLIT + (lane & 15)) * LSTRIDE2) + (blockIdx.x >> 4), __ATOMIC_RELAXED, __HIP_MEMORY_SCOPE_AGENT);
            if (__builtin_amdgcn_ballot_w64((unsigned)(x >> 32) == T) == ~0ull) {
                const float L = __uint_as_float((unsigned)x);
                const float w = __builtin_amdgcn_exp2f(L - row_max(L));
                sm.fw[lane & 15] = w / row_sum(w);
                CBAR();
                if (lane == 0) lds_st(&sm.fw_rdy, 1u);
                break;
            }
            SLEEP(64)
        }
    } else {
        while (lds_ld(&sm.tag_rdy) == 0) SLEEP(1);
        CBAR();
        const unsigned T = lds_ld(&sm.tag_lo);
        const __amdgpu_buffer_rsrc_t rsrc = __builtin_amdgcn_make_buffer_rsrc(ws + WS_PART + (rank * NSPLIT + split) * 512, 0, NH * NSPLIT * 512, 0x00020000);
        v2u* recs = (v2u*)&sm.cs[0][0];
#pragma unroll
        for (int i = 0; i < NOUT * 3; i++) (&sm.acc[0][0])[i * 64 + lane] = 0.0;
        bool direct = false;
#define PUBLISH(N) { \
        v4u r[8]; \
        _Pragma("unroll") for (int k = 0; k < 8; k++) r[k] = ((const v4u*)recs)[k * 64 + lane]; \
        _Pragma("unroll") for (int k = 0; k < 8; k++) __builtin_amdgcn_raw_buffer_store_b128(r[k], rsrc, 2 * k + (lane >> 5) < (N) ? po + (lane < 32 ? VENT(2 * k) : VENT(2 * k + 1)) * (GSZ * NSPLIT * 512) : 0x7ffffff0u, 0, SC1); STAMP(4) }
        const unsigned po = (lane & 31) * 16;
#pragma unroll 1
        for (unsigned u = 0; u < 2 * NU; u++) {
            const unsigned e = u < NU ? u : VENT(u), so = u % NOUT;
            const v4f h = *(const v4f*)&sm.ent[e][128];
            for (;;) {
                if (!direct && u >= 2 * NU - 3 && (int)(NOW() * 128 - __builtin_amdgcn_readfirstlane(TU(2 * NU).x)) >= (30) * 128) { direct = true; wave_sync(); PUBLISH(u - NU) }
                if (lds_ld(&sm.cnt[so]) == 32) break;
                if (direct) SLEEP(0) else SLEEP(1)
            }
            CBAR();
            STAMP(5)
            if (!direct && u == 2 * NU - 1) { direct = true; wave_sync(); PUBLISH(NU - 1) }
            if (u < NU) {
                const float2 lg = *(const float2*)&sm.ent[e][2 * lane];
                const v4f zs = *(const v4f*)&sm.vz[u % NIN][2 * lane];
                const float m = wave_max(fmaxf(lg.x, lg.y));
                const float p0 = __expf(lg.x - m), p1 = __expf(lg.y - m);
                const float x0 = p0 * zs.y, x1 = p1 * zs.w;
                const unsigned eb = max(__float_as_uint(wave_max(fmaxf(fabsf(x0), fabsf(x1)))) >> 23, 21u);
                const float f = __uint_as_float((275u - eb) << 23);
                const float w0 = rintf(x0 * f), w1 = rintf(x1 * f);
                const float den = wave_sum(p0 + p1);
                const float bz = wave_sum(fmaf(w0, zs.x, w1 * zs.z));
                const float L = fmaf(m, 1.4426950408889634f, __builtin_amdgcn_logf(den));
                *(v2u*)&sm.ent[e][2 * lane] = (v2u){(unsigned)(int)w0, (unsigned)(int)w1};
                if (lane == 0) *(v4f*)&sm.ent[e][128] = (v4f){ldexpf(1.f / den, (int)eb - 148), bz, L, 0.f};
            } else {
                const double a0 = sm.acc[so][lane], a1 = sm.acc[so][64 + lane], a2 = sm.acc[so][128 + lane];
                const float n0 = (float)(a0 - (double)h.y) * h.x, n1 = (float)(a1 - (double)h.y) * h.x, n2 = (float)(a2 - (double)h.y) * h.x;
                const unsigned e2 = max(__float_as_uint(fmaxf(fmaxf(fabsf(n0), fabsf(n1)), fabsf(n2))) >> 23, 12u);
                const float f2 = __uint_as_float((266u - e2) << 23);
                const unsigned m0 = min(max((int)rintf(n0 * f2), -8191), 8191) & 0x3fff, m1 = min(max((int)rintf(n1 * f2), -8191), 8191) & 0x3fff, m2 = min(max((int)rintf(n2 * f2), -8191), 8191) & 0x3fff;
                const v2u r = {m0 | (m1 << 14) | (m2 << 28), (m2 >> 4) | (e2 << 10) | (T << 18)};
                if (direct) {
                    STAMP(6)
                    const v4u r2 = {r.x, r.y, (unsigned)__builtin_amdgcn_update_dpp(0, (int)r.x, 0xf5, 0xf, 0xf, true), (unsigned)__builtin_amdgcn_update_dpp(0, (int)r.y, 0xf5, 0xf, 0xf, true)};
                    __builtin_amdgcn_raw_buffer_store_b128(r2, rsrc, (lane & 1) ? 0x7ffffff0u : lane * 8, e * (GSZ * NSPLIT * 512), SC1);
                }
                else recs[(u - NU) * 64 + lane] = r;
                CBAR();
                sm.acc[so][lane] = 0.0; sm.acc[so][64 + lane] = 0.0; sm.acc[so][128 + lane] = 0.0;
            }
            CBAR();
            if (lane == 0) { lds_st(&sm.cnt[so], 0u); CBAR(); lds_st(&sm.merged, u + 1); }
        }
        LT(2, NOW()) LT(0, (unsigned)tent) LT(3, ts) LT(4, NOW() - (((NOW() * 128 - __builtin_amdgcn_readfirstlane(TU(2 * NU).x)) >> 7) & 0xfffff))
        STAMP(2)
        STAMP_DUMP()
        return;
    }

    STAMP(2)
    STAMP_DUMP()
}

extern "C" void kernel_launch(void* const* d_in, const int* in_sizes, int n_in,
                              void* d_out, int out_size, void* d_ws, size_t ws_size,
                              hipStream_t stream) {
    const unsigned short* q = (const unsigned short*)d_in[0];
    const int* qk = (const int*)d_in[1];
    const unsigned short* k_scales = (const unsigned short*)d_in[2];
    const unsigned short* k_zeros = (const unsigned short*)d_in[3];
    const int* qv = (const int*)d_in[4];
    const unsigned short* v_scales = (const unsigned short*)d_in[5];
    const unsigned short* v_zeros = (const unsigned short*)d_in[6];
    const unsigned short* q_cos = (const unsigned short*)d_in[7];
    const unsigned short* q_sin = (const unsigned short*)d_in[8];
    const unsigned short* k_cos = (const unsigned short*)d_in[9];
    const unsigned short* k_sin = (const unsigned short*)d_in[10];
    unsigned char* ws = (unsigned char*)d_ws;

    attn_kernel<<<NSPLIT * GSZ, NTHREADS, 0, stream>>>(q, qk, k_scales, k_zeros, qv, v_scales, v_zeros,
                                                       q_cos, q_sin, k_cos, k_sin, (__hip_bfloat16*)d_out, ws);
}
```

```cpp
#include <hip/hip_runtime.h>
#include <hip/hip_bf16.h>
#include <stdint.h>

#define DH 192
#define HALF 96
#define SKV 2048
#define NH 256
#define NSW 8
#define NTHREADS (64 * (NSW + 4))
#define BROWS 128
#define NSPLIT (SKV / BROWS)
#define GSZ 16
#define NU (NH / GSZ)
#define NPC (2 * NU * 32)
#define NIN 4
#define NOUT 4
#define ENTW 132
#define SC1 16
#define PHI 0x0235712457024671ull
#define KAP 0x0202020220202020ull
#define VORD 0x9876543210afedcbull
#define VENT(V) ((unsigned)(VORD >> (4 * ((V) & 15))) & 15u)
#define PFL 183
#define PER 1268
#define P0 1370
#define TOF 100
#define NBW 2
#define BTH 20
#define X0 330
#define K0 3
#define XM 250
#define XW 40
#define XD1 100
#define XL 60
#define XR 170
#define BK 3
#define RTH 6
#define QN 224
#define GAIN 24
#define CAP 45
#define FLOOR 3
#define FTH 15
#define KD 9
#define KI 14
#define DPS 2
#define NB 3
#define STH 80
#define PUBT 30
#define POLT 120

#define WS_EPOCH 0
#define WS_DEC 8192
#define WS_REF 16384
#define LSTRIDE 4352
#define WS_LOG 524288
#define LSTRIDE2 1408
#define WS_PART 1048576

typedef unsigned int v2u __attribute__((ext_vector_type(2)));
typedef unsigned int v4u __attribute__((ext_vector_type(4)));
typedef int v4i __attribute__((ext_vector_type(4)));
typedef float v2f __attribute__((ext_vector_type(2)));
typedef float v4f __attribute__((ext_vector_type(4)));
typedef unsigned long long u64;

__device__ __forceinline__ float bflo(unsigned u) { return __uint_as_float(u << 16); }
__device__ __forceinline__ float bfhi(unsigned u) { return __uint_as_float(u & 0xffff0000u); }
__device__ __forceinline__ float bfr(float x) {
    __bf16 b = (__bf16)x;
    return __uint_as_float(((unsigned)__builtin_bit_cast(unsigned short, b)) << 16);
}

#define SLEEP(n) SLEEP_(n)
#define SLEEP_(n) asm volatile("s_sleep " #n "\n\tv_add_co_u32 %0, vcc, 1, %0\n\ts_cbranch_vccz 1f\n\ts_endpgm\n1:" : "+v"(spins) :: "vcc", "memory");
#define SPIN0 0xf0000000u
#define CBAR() asm volatile("" ::: "memory")

__device__ __forceinline__ void wave_sync() {
    __builtin_amdgcn_fence(__ATOMIC_RELEASE, "wavefront");
    __builtin_amdgcn_wave_barrier();
    __builtin_amdgcn_fence(__ATOMIC_ACQUIRE, "wavefront");
}
template <int CTRL>
__device__ __forceinline__ float dpp_add(float x) {
    int y = __builtin_amdgcn_update_dpp(0, __float_as_int(x), CTRL, 0xf, 0xf, true);
    return x + __int_as_float(y);
}
template <int CTRL>
__device__ __forceinline__ float dpp_max(float x) {
    int y = __builtin_amdgcn_update_dpp(__float_as_int(x), __float_as_int(x), CTRL, 0xf, 0xf, false);
    return fmaxf(x, __int_as_float(y));
}
__device__ __forceinline__ float row_sum(float x) { x = dpp_add<0x128>(x); x = dpp_add<0x124>(x); x = dpp_add<0x122>(x); return dpp_add<0x121>(x); }
__device__ __forceinline__ float row_max(float m) { m = dpp_max<0x128>(m); m = dpp_max<0x124>(m); m = dpp_max<0x122>(m); return dpp_max<0x121>(m); }
__device__ __forceinline__ float rdlane(float x, int l) { return __int_as_float(__builtin_amdgcn_readlane(__float_as_int(x), l)); }
__device__ __forceinline__ float wave_max(float m) { m = row_max(m); return fmaxf(fmaxf(rdlane(m, 0), rdlane(m, 16)), fmaxf(rdlane(m, 32), rdlane(m, 48))); }
__device__ __forceinline__ float wave_sum(float x) { x = row_sum(x); return (rdlane(x, 0) + rdlane(x, 16)) + (rdlane(x, 32) + rdlane(x, 48)); }
__device__ __forceinline__ unsigned lds_ld(const unsigned* p) { return __builtin_amdgcn_readfirstlane(__hip_atomic_load(p, __ATOMIC_RELAXED, __HIP_MEMORY_SCOPE_WORKGROUP)); }
__device__ __forceinline__ unsigned lds_addr(const void* p) { return (unsigned)(size_t)(const __attribute__((address_space(3))) void*)p; }
#define DMA16(SRC, LDS) asm volatile("s_mov_b32 %0, m0\n\ts_mov_b32 m0, %2\n\ts_nop 0\n\tglobal_load_lds_dwordx4 %1, off\n\ts_mov_b32 m0, %0" : "=&s"(m0keep) : "v"(SRC), "s"(LDS) : "memory")
__device__ __forceinline__ void lds_st(unsigned* p, unsigned v) { __hip_atomic_store(p, v, __ATOMIC_RELAXED, __HIP_MEMORY_SCOPE_WORKGROUP); }
#define NOW() ((unsigned)__builtin_amdgcn_s_memrealtime())

struct __attribute__((aligned(16))) Smem {
    unsigned cs[32][768];
    double acc[NOUT][DH];
    float ent[NU][ENTW];
    unsigned q[NIN][HALF];
    float2 kz[NIN][BROWS];
    float2 vz[NIN][BROWS];
    v2u tu[2 * NU + 2];
    unsigned grab, merged, tabs, cdone;
    unsigned rdy[NIN], cnt[NOUT];
    unsigned tag_rdy, tag, fw_rdy, pad;
    float fw[NSPLIT];
};

#define TU(I) (*(const volatile v2u __attribute__((address_space(3)))*)(size_t)lds_addr(&sm.tu[I]))
#define LDS_ADD(P, V) { u64 ex_; asm volatile("s_mov_b64 %0, exec\n\ts_mov_b64 exec, 1\n\tds_add_u32 %1, %2\n\ts_mov_b64 exec, %0" : "=&s"(ex_) : "v"(lds_addr(P)), "v"(V) : "memory"); }
#define LDS_OR(P, V) { u64 ex_; asm volatile("s_mov_b64 %0, exec\n\ts_mov_b64 exec, 1\n\tds_or_b32 %1, %2\n\ts_mov_b64 exec, %0" : "=&s"(ex_) : "v"(lds_addr(P)), "v"(V) : "memory"); }

__global__ __launch_bounds__(NTHREADS, 3) void attn_kernel(
    const unsigned short* __restrict__ q, const int* __restrict__ qk,
    const unsigned short* __restrict__ k_scales, const unsigned short* __restrict__ k_zeros,
    const int* __restrict__ qv,
    const unsigned short* __restrict__ v_scales, const unsigned short* __restrict__ v_zeros,
    const unsigned short* __restrict__ q_cos, const unsigned short* __restrict__ q_sin,
    const unsigned short* __restrict__ k_cos, const unsigned short* __restrict__ k_sin,
    __hip_bfloat16* __restrict__ out, unsigned char* __restrict__ ws)
{
    __shared__ Smem sm;
    const int tid = threadIdx.x;
    unsigned spins = SPIN0;
#define TOUCH(OFF, BASE) asm volatile("global_load_dword a6, %0, %1" :: "v"(OFF), "s"(BASE) : "a6", "memory");
    const unsigned cl = min((blockIdx.x & 24) * 8 + tid, PFL - 1u) * 128;
    if (tid < 64) { const char* pc0; asm volatile("s_getpc_b64 %0" : "=s"(pc0)); TOUCH(cl, pc0) }
    __builtin_amdgcn_sched_barrier(0);
    const int hw = __builtin_amdgcn_readfirstlane(tid >> 6);
    const int lane = tid & 63;
    const int split = (blockIdx.x & 3) * 4 + ((blockIdx.x >> 3) & 3);
    const int rank = (blockIdx.x >> 5) * 2 + ((blockIdx.x >> 2) & 1);
    const int brow0 = split * 16;
#define ROWOF(RG) (((RG) >> 2) * 256 + ((RG) & 3) * 4)
    if (hw == 0) {
        typedef const char* const __attribute__((address_space(4))) * kap_t;
        const char* sa = ((kap_t)__builtin_amdgcn_kernarg_segment_ptr())[(0x5623 >> (4 * (split & 3))) & 7];
        unsigned o1 = ((blockIdx.x >> 5) * 256 + brow0) * (DH * 2) + min(lane, 47) * 128;
        unsigned o2 = ((rank + GSZ * (lane >> 3)) * SKV + (lane & 7) * 256 + (brow0 & ~63)) * 2, o3 = o2 + 8 * GSZ * SKV * 2;
        unsigned o4 = (rank + GSZ * (lane >> 2)) * (DH * 2) + min(lane & 3, 2) * 128;
        asm volatile("" : "+v"(o1), "+v"(o2), "+v"(o3), "+v"(o4));
        asm volatile("global_load_dword a6, %0, %1 nt\n\tglobal_load_dword a6, %0, %2 nt" :: "v"((lane & 1) * (NH * SKV * DH * 4u - 128u * NH) + blockIdx.x * 128), "s"(qk), "s"(qv) : "a6", "memory");
        TOUCH(o1, k_cos) TOUCH(o1, k_sin) TOUCH(o4, q) TOUCH(o2, sa) TOUCH(o3, sa)
        asm volatile("s_barrier\n\ts_endpgm" ::: "memory");
    }
    if (hw == 1 && lane < 16) (&sm.grab)[lane] = lane ? 0 : 64;
    asm volatile("" :: "s"(q), "s"(qk), "s"(k_scales), "s"(k_zeros), "s"(qv), "s"(v_scales), "s"(v_zeros), "s"(q_cos), "s"(q_sin), "s"(k_cos), "s"(k_sin), "s"(out), "s"(ws));
    asm volatile("s_waitcnt lgkmcnt(0)\n\ts_barrier" ::: "memory");
    const int wave = (int)(0x76543210b9a8ull >> (4 * hw)) & 15;
    const unsigned ts = NOW();

    if (__builtin_expect(wave < NSW, 1)) {
        const unsigned rot = 4 * ((unsigned)(PHI >> (60 - 4 * rank)) & 7u), kap = (unsigned)(KAP >> (60 - 4 * rank)) & 3u;
#define RGOF(G) ((((G) + rot) & 31u) ^ kap)
        const char* cbase[3];
#pragma unroll
        for (int j = 0; j < 3; j++) { const int x = j * 64 + lane; cbase[j] = (x < 96 ? (const char*)k_cos + x * 16 : (const char*)k_sin + (x - 96) * 16) + brow0 * (DH * 2); }
        unsigned m0keep;
#define CS_DMA(RG) { _Pragma("unroll") for (int j = 0; j < 3; j++) DMA16(cbase[j] + ROWOF(RG) * (DH * 2), lds_addr(&sm.cs[0][0]) + (RG) * 3072 + j * 1024); }
#define REPACK(RG) { \
        unsigned* cw = &sm.cs[RG][0]; \
        const unsigned* l = cw + rsel * (DH / 2) + qoff; \
        unsigned r[2][6]; \
        _Pragma("unroll") \
        for (int j = 0; j < 2; j++) { \
            unsigned e[4][2]; \
            _Pragma("unroll") \
            for (int k = 0; k < 4; k++) { \
                const unsigned* a = l + j * DH + (k & 1) * 48 + (k >> 1) * 384;       \
                unsigned x0 = a[0], x1 = a[1]; \
                e[k][0] = __builtin_amdgcn_alignbit(x1, x0, sh); \
                e[k][1] = x1 >> sh; \
            } \
            r[j][0] = (e[0][0] & 0xffffu) | (e[3][0] << 16); \
            r[j][1] = (e[0][0] >> 16) | (e[3][0] & 0xffff0000u); \
            r[j][2] = (e[0][1] & 0xffffu) | (e[3][1] << 16); \
            r[j][3] = (e[1][0] & 0xffffu) | (e[2][0] << 16); \
            r[j][4] = (e[1][0] >> 16) | (e[2][0] & 0xffff0000u); \
            r[j][5] = (e[1][1] & 0xffffu) | (e[2][1] << 16); \
        } \
        wave_sync(); \
        _Pragma("unroll") \
        for (int j = 0; j < 2; j++) { *(v4u*)&cw[j * 256 + lane * 4] = (v4u){r[j][0], r[j][1], r[j][2], r[j][3]}; *(v2u*)&cw[512 + j * 128 + lane * 2] = (v2u){r[j][4], r[j][5]}; } \
        wave_sync(); \
        LDS_OR(&sm.tabs, 1u << (RG)) }
        const int sub = lane & 31;
        const int rsel = lane >> 5;
        const unsigned sh = (sub & 1) * 16;
        const int qoff = 3 * (sub >> 1) + (sub & 1);
        const char* kbase = (const char*)qk + (size_t)(rank * SKV + brow0) * (DH * 4);
        const char* vbase = (const char*)qv + (size_t)(rank * SKV + brow0) * (DH * 4);
        const unsigned koff = (rsel * DH + 3 * sub) * 4, voff = 3 * lane * 4;
#define AR(S) (16 + (S) * 8)
        asm volatile("" ::: "a16", "a17", "a18", "a19", "a20", "a21", "a22", "a23", "a24", "a25", "a26", "a27", "a28", "a29", "a30", "a31",
                            "a32", "a33", "a34", "a35", "a36", "a37", "a38", "a39", "a40", "a41", "a42", "a43", "a44", "a45", "a46", "a47");
        asm volatile("" ::: "a48", "a49", "a50", "a51", "a52", "a53", "a54", "a55", "a56", "a57", "a58", "a59", "a60", "a61", "a62", "a63");
        __builtin_amdgcn_s_waitcnt(0x0f70);
        int ta[3], tb[3];
#define GLD(A, OFF, BASE, IMM) asm volatile("global_load_dwordx3 a[%c0:%c1], %2, %3 offset:%4 nt" :: "i"(A), "i"((A) + 2), "v"(OFF), "s"(BASE), "i"(IMM))
#define ISSUE(S, J, BASE, OA, OB) { GLD(AR(S), OA, BASE, (J) * 1536); GLD(AR(S) + 4, OB, BASE, (J) * 1536); }
#define WAITT(S, N) asm volatile("s_waitcnt vmcnt(%6)\n\tv_accvgpr_read_b32 %0, a[%c7]\n\tv_accvgpr_read_b32 %1, a[%c8]\n\tv_accvgpr_read_b32 %2, a[%c9]\n\t" \
                              "v_accvgpr_read_b32 %3, a[%c10]\n\tv_accvgpr_read_b32 %4, a[%c11]\n\tv_accvgpr_read_b32 %5, a[%c12]" \
                              : "=v"(ta[0]), "=v"(ta[1]), "=v"(ta[2]), "=v"(tb[0]), "=v"(tb[1]), "=v"(tb[2]) \
                              : "i"(N), "i"(AR(S)), "i"(AR(S) + 1), "i"(AR(S) + 2), "i"(AR(S) + 4), "i"(AR(S) + 5), "i"(AR(S) + 6))
        const unsigned koffb = koff + 384, voffb = voff + 768;
        unsigned u = 0, rg, np = 0;
#define RG0(C) RGOF((unsigned)(wave + 8 * (C)))
        CS_DMA(RG0(0))
        const float scale = 0.07216878364870322f;
        double acc[3] = {0.0, 0.0, 0.0};
        bool chg, none = false;
        unsigned Qp[3], Qn[3];
        float kz4[2], ksc, kres[2];
        v4u ca[2]; v2u cb[2];
        v2f qe[2][3], A[2];
        const char* nb; unsigned oa, ob;
        unsigned g, gu, mg, nrg, tg, dg, par = 0;
        int early;
        const int bth = wave < NBW ? BTH * 128 : 0x3fffffff;
        const unsigned grab_adr = lds_addr(&sm.grab);
        asm volatile("v_accvgpr_write_b32 a13, 1" ::: "a13", "a15");
#define COUNT(ADR) { u64 ex_; asm volatile("s_mov_b64 %0, exec\n\ts_mov_b64 exec, 1\n\tds_add_rtn_u32 a15, %1, a13\n\ts_mov_b64 exec, %0" : "=&s"(ex_) : "v"(ADR) : "memory"); }
#define GRABBED() { unsigned t_; asm volatile("s_waitcnt lgkmcnt(0)\n\tv_accvgpr_read_b32 %1, a15\n\ts_nop 0\n\tv_readfirstlane_b32 %0, %1\n\ts_nop 1" : "=s"(g), "=v"(t_)); }
#define NEXT1() const v2u x_ = TU(g >> 5); u64 tn_; asm volatile("s_memrealtime %0" : "=s"(tn_));
#define NEXT2() { \
            chg = (g >> 5) != u; none = g >= NPC; \
            const bool nv = g >= NPC / 2; \
            nrg = RGOF(g); \
            unsigned boff; asm("s_mul_i32 %0, %1, 0x300" : "=s"(boff) : "s"((nv ? VENT(g >> 5) : g >> 5) * (GSZ * SKV) + ROWOF(nrg) + 2)); \
            nb = (nv ? vbase : kbase) + boff; \
            oa = nv ? voff : koff; ob = nv ? voffb : koffb; \
            asm volatile("s_waitcnt lgkmcnt(0)" : "+s"(tn_) : "s"(nb), "v"(oa), "v"(ob)); \
            tg = __builtin_amdgcn_readfirstlane(x_.x) + (g & 31) * __builtin_amdgcn_readfirstlane(x_.y); \
            early = none ? 0 : (int)((unsigned)tn_ * 128 - tg) - bth; \
            if (early > 0) asm volatile("s_cmp_lg_u32 %3, 0\n\ts_cbranch_scc1 1f\n\t" L4(32) "\ts_branch 2f\n1:\n\t" L4(16) "2:\n\ts_mov_b64 vcc, exec\n\ts_mov_b64 exec, 1\n\tds_add_rtn_u32 a15, %4, a13\n\ts_mov_b64 exec, vcc" \
                                    :: "v"(oa), "v"(ob), "s"(nb), "s"(pr), "v"(grab_adr) : "scc", "vcc", "memory"); }
#define L4(A) "global_load_dwordx3 a[" #A ":" #A "+2], %0, %2 offset:-1536 nt\n\tglobal_load_dwordx3 a[" #A "+4:" #A "+6], %1, %2 offset:-1536 nt\n\t" \
              "global_load_dwordx3 a[" #A "+8:" #A "+10], %0, %2 nt\n\tglobal_load_dwordx3 a[" #A "+12:" #A "+14], %1, %2 nt\n"
#define GATE(...) \
            asm volatile("s_setprio 0\n1:\n\ts_memrealtime vcc\n\ts_waitcnt lgkmcnt(0)\n\ts_lshl_b32 vcc_lo, vcc_lo, 7\n\ts_sub_u32 %0, %1, vcc_lo\n\ts_cmp_lt_u32 %0, 0x40000\n\ts_cbranch_scc0 2f\n\t" \
                         "s_cmp_lt_u32 %0, %2\n\ts_cbranch_scc1 1b\n\ts_sleep %3\n\ts_branch 1b\n2:\n\ts_setprio 2" : "=&s"(dg) : "s"(tg), "i"(10 * 128), "i"(2), __VA_ARGS__ : "scc", "vcc", "memory");
#define RD6(A) "v_accvgpr_read_b32 %0, a[%c6+" #A "]\n\tv_accvgpr_read_b32 %1, a[%c6+" #A "+1]\n\tv_accvgpr_read_b32 %2, a[%c6+" #A "+2]\n\t" \
               "v_accvgpr_read_b32 %3, a[%c6+" #A "+4]\n\tv_accvgpr_read_b32 %4, a[%c6+" #A "+5]\n\tv_accvgpr_read_b32 %5, a[%c6+" #A "+6]\n"
#define LD2(A) "global_load_dwordx3 a[%c6+" #A ":%c6+" #A "+2], %7, %9 offset:%10 nt\n\tglobal_load_dwordx3 a[%c6+" #A "+4:%c6+" #A "+6], %8, %9 offset:%10 nt"
#define OUT6 "=&v"(ta[0]), "=&v"(ta[1]), "=&v"(ta[2]), "=&v"(tb[0]), "=&v"(tb[1]), "=&v"(tb[2])
#define WI(A, B, TAIL) "s_cmp_gt_i32 %13, 0\n\ts_cbranch_scc1 3f\n\ts_waitcnt vmcnt(2)\n\t" LD2(A) TAIL "\n3:\n\ts_waitcnt vmcnt(%c14)\n\t" RD6(B)
#define WAITISSUE(S, TAIL, ...) asm volatile("s_cmp_lg_u32 %12, 0\n\ts_cbranch_scc1 1f\n\t" WI(16, 0, TAIL) "\ts_branch 2f\n1:\n\t" WI(0, 16, TAIL) "2:" \
                              : OUT6 : "i"(AR(S)), "v"(oa), "v"(ob), "s"(nb), "i"(((S) - 1) * 1536), "v"(grab_adr), "s"(pr), "s"(early), "i"(6 - 2 * (S)), __VA_ARGS__ : "scc", "vcc", "memory")
#define LASTT(S) asm volatile("s_waitcnt vmcnt(%c8)\n\ts_cmp_lg_u32 %7, 0\n\ts_cbranch_scc1 1f\n\t" RD6(0) "\ts_branch 2f\n1:\n\t" RD6(16) "2:" : OUT6 : "i"(AR(S)), "s"(pr), "i"(2 - 2 * (S)) : "scc")
#define DRAW "\n\ts_mov_b64 vcc, exec\n\ts_mov_b64 exec, 1\n\tds_add_rtn_u32 a15, %11, a13\n\ts_mov_b64 exec, vcc"
#define K_TAB() { _Pragma("unroll") for (int p = 0; p < 2; p++) { \
            const unsigned* cw = &sm.cs[rg][0]; \
            ca[p] = *(const v4u*)&cw[p * 256 + lane * 4]; cb[p] = *(const v2u*)&cw[512 + p * 128 + lane * 2]; } }
#define K_PRE(P) { \
        float e0, e1, e2, f0, f1, f2; \
        asm("v_dot2_f32_bf16 %0, %6, %12, 0\n\tv_dot2_f32_bf16 %3, %9, %15, 0\n\tv_dot2_f32_bf16 %1, %7, %13, 0\n\tv_dot2_f32_bf16 %4, %10, %16, 0\n\t" \
            "v_dot2_f32_bf16 %2, %8, %14, 0\n\tv_dot2_f32_bf16 %5, %11, %17, 0\n\ts_nop 2" \
            : "=&v"(e0), "=&v"(e1), "=&v"(e2), "=&v"(f0), "=&v"(f1), "=&v"(f2) \
            : "v"(Qp[0]), "v"(Qp[1]), "v"(Qp[2]), "v"(Qn[0]), "v"(Qn[1]), "v"(Qn[2]), \
              "v"(ca[P].x), "v"(ca[P].y), "v"(ca[P].z), "v"(ca[P].w), "v"(cb[P].x), "v"(cb[P].y)); \
        qe[P][0] = (v2f){e0, f0}; qe[P][1] = (v2f){e1, f1}; qe[P][2] = (v2f){e2, f2}; \
        A[P] = (qe[P][0] + qe[P][1] + qe[P][2]) * (v2f){-kz4[P], -kz4[P]}; }
#define K_SUM(P) { \
        _Pragma("unroll") \
        for (int i = 0; i < 3; i++) { v2f x = {(float)ta[i], (float)tb[i]}; A[P] = qe[P][i] * x + A[P]; } \
        kres[P] = A[P].x + A[P].y; }
#define K_RED2() { \
        const bool o1 = lane & 1; \
        float sx = (o1 ? kres[1] : kres[0]) + __int_as_float(__builtin_amdgcn_update_dpp(0, __float_as_int(o1 ? kres[0] : kres[1]), 0xb1, 0xf, 0xf, true)); \
        sx = dpp_add<0x4e>(sx); sx = dpp_add<0x124>(sx); sx = dpp_add<0x128>(sx); \
        const auto sw = __builtin_amdgcn_permlane16_swap(__float_as_uint(sx), __float_as_uint(sx), false, false); \
        sx = __uint_as_float(sw[0]) + __uint_as_float(sw[1]); \
        if ((lane & 30) == 0) sm.ent[u][rg * 4 + 2 * (lane & 1) + rsel] = sx * ksc; }
#define LDV(P) __hip_atomic_load(P, __ATOMIC_RELAXED, __HIP_MEMORY_SCOPE_WORKGROUP)
#define RDYWAIT(SL, SEQ) { while (lds_ld(&sm.rdy[SL]) != (SEQ)) SLEEP(1); CBAR(); }
#define LOADZ() { _Pragma("unroll") for (int p = 0; p < 2; p++) kz4[p] = sm.kz[u % NIN][rg * 4 + 2 * p + rsel].x; ksc = sm.kz[u % NIN][rg * 4 + 2 * (lane & 1) + rsel].y * scale; }
#define LOADQ() { _Pragma("unroll") for (int i = 0; i < 3; i++) { Qp[i] = sm.q[u % NIN][3 * sub + i]; Qn[i] = __builtin_amdgcn_alignbit(Qp[i], Qp[i], 16) ^ 0x80000000u; } }
#define FLUSH(ISV) { \
            mg = __builtin_amdgcn_readfirstlane(mg); \
            while (u >= mg + NOUT) { SLEEP(1); mg = lds_ld(&sm.merged); } \
            CBAR(); \
            if (ISV) { const unsigned aa = lds_addr(&sm.acc[u % NOUT][lane]); \
                asm volatile("ds_add_f64 %0, %1\n\tds_add_f64 %0, %2 offset:512\n\tds_add_f64 %0, %3 offset:1024" :: "v"(aa), "v"(acc[0]), "v"(acc[1]), "v"(acc[2]) : "memory"); \
                acc[0] = 0.0; acc[1] = 0.0; acc[2] = 0.0; } \
            CBAR(); \
            LDS_ADD(&sm.cnt[u % NOUT], np) \
            CBAR(); \
            np = 0; }
#define ADVANCE() { u = gu; rg = nrg; }

#define REQ0(N, S) { tg = (ts + TOF) * 128 + (wave + 8 * (N)) * P0; \
            const char* kb = kbase + (((N) >> 2) * (GSZ * SKV) + ROWOF(RG0((N) & 3))) * (DH * 4); GATE("s"(kb)) ISSUE(S, 0, kb, koff, koffb) ISSUE((S) + 1, 1, kb, koff, koffb) }
#define USE0(N, S, W) { u = (N) >> 2; rg = RG0((N) & 3); K_TAB() LOADQ() LOADZ() K_PRE(0) K_PRE(1) WAITT(S, W); K_SUM(0) WAITT((S) + 1, W); K_SUM(1) K_RED2() }
#define VMW(N) asm volatile("s_waitcnt vmcnt(" #N ")" ::: "memory");
        REQ0(0, 0) CS_DMA(RG0(1))
        VMW(7) REPACK(RG0(0))
        REQ0(1, 2)
        RDYWAIT(0, 1u)
        USE0(0, 0, 7)
        VMW(4) REPACK(RG0(1))
        REQ0(2, 0) CS_DMA(RG0(2))
        USE0(1, 2, 7)
        REQ0(3, 2)
        VMW(4) REPACK(RG0(2))
        USE0(2, 0, 4)
        REQ0(4, 0) CS_DMA(RG0(3))
        REQ0(5, 4)
        VMW(4) REPACK(RG0(3))
        USE0(3, 2, 4)
        CBAR();
        LDS_ADD(&sm.cnt[0], 4u)
        REQ0(6, 2)
        RDYWAIT(1, 2u)
        USE0(4, 0, 8)
        USE0(5, 4, 4)
        REQ0(7, 0)
        COUNT(grab_adr)
        USE0(6, 2, 4)
        rg = RG0(3); np = 3;
        GRABBED()
#pragma unroll 1
        while (u < NU) {
            const unsigned pr = __builtin_amdgcn_readfirstlane(par); par ^= 1;
            NEXT1()
            const unsigned tb_ = LDV(&sm.tabs), fl = LDV(&sm.rdy[u % NIN]);
            mg = LDV(&sm.merged);
            CBAR();
            K_TAB() LOADQ() LOADZ()
            NEXT2()
            if (__builtin_expect(__builtin_amdgcn_readfirstlane(fl) != u + 1 || !((__builtin_amdgcn_readfirstlane(tb_) >> rg) & 1u), 0)) {
                while (!((lds_ld(&sm.tabs) >> rg) & 1u)) SLEEP(1)
                RDYWAIT(u % NIN, u + 1)
                K_TAB() LOADQ() LOADZ()
            }
            K_PRE(0) K_PRE(1)
            if (early <= 0) GATE("v"(A[0]), "v"(A[1]), "v"(oa), "v"(ob), "s"(nb), "s"(pr))
            WAITISSUE(0, DRAW, "v"(A[0]));
            K_SUM(0)
            WAITISSUE(1, "", "v"(A[1]));
            K_SUM(1)
            gu = g >> 5;
            GRABBED()
            K_RED2()
            np++;
            if (chg) FLUSH(false)
            ADVANCE()
        }
#pragma unroll 1
        while (u < 2 * NU) {
            const unsigned pr = __builtin_amdgcn_readfirstlane(par); par ^= 1;
            NEXT1()
            mg = LDV(&sm.merged);
            CBAR();
            const unsigned ve = VENT(u);
            v4i pw = *(const v4i*)&sm.ent[ve][rg * 4];
            NEXT2()
            if (__builtin_expect(__builtin_amdgcn_readfirstlane(mg) <= ve, 0)) { while (lds_ld(&sm.merged) <= ve) SLEEP(1); CBAR(); pw = *(const v4i*)&sm.ent[ve][rg * 4]; }
            const double w0 = pw.x, w1 = pw.y, w2 = pw.z, w3 = pw.w;
            if (none) { LASTT(0); } else { if (early <= 0) GATE("s"(pr), "v"(w0), "v"(w1), "v"(w2), "v"(w3), "v"(oa), "v"(ob), "s"(nb)) WAITISSUE(0, DRAW, "v"(w0)); }
#pragma unroll
            for (int i = 0; i < 3; i++) acc[i] = fma(w1, (double)tb[i], fma(w0, (double)ta[i], acc[i]));
            if (none) { LASTT(1); } else WAITISSUE(1, "", "v"(w2));
#pragma unroll
            for (int i = 0; i < 3; i++) acc[i] = fma(w3, (double)tb[i], fma(w2, (double)ta[i], acc[i]));
            gu = g >> 5;
            GRABBED()
            np++;
            if (chg) FLUSH(true)
            ADVANCE()
        }

        {
            const int head = blockIdx.x;
            while (lds_ld(&sm.fw_rdy) == 0) SLEEP(2)
            CBAR();
            const unsigned tt = lds_ld(&sm.tag) << 18;
            const float wh = sm.fw[wave + 8 * rsel];
            const unsigned ro = (wave + 8 * rsel) * 512 + sub * 16;
            const char* rb = (const char*)ws + WS_PART + head * (NSPLIT * 512);
            v4f* pb = (v4f*)((char*)&sm.cs[0][0] + 16384);
            __hip_bfloat16* o = out + (size_t)head * DH + 6 * sub + 3 * rsel;
            tg = __builtin_amdgcn_readfirstlane(TU(2 * NU).x) + POLT * 128;
            GATE("s"(tt), "v"(wh), "v"(ro), "s"(rb), "v"(o))
            v4u R;
#define PLD(A) "global_load_dwordx4 a[" #A ":" #A "+3], %6, %7 sc1\n\t"
#define PCK(A, N, L) "s_waitcnt vmcnt(" #N ")\n\tv_accvgpr_read_b32 %1, a[" #A "+1]\n\tv_accvgpr_read_b32 %3, a[" #A "+3]\n\tv_accvgpr_read_b32 %0, a[" #A "]\n\tv_accvgpr_read_b32 %2, a[" #A "+2]\n\t" \
                     "v_xor_b32 %1, %8, %1\n\tv_xor_b32 %3, %8, %3\n\tv_or_b32 %4, %1, %3\n\tv_cmp_gt_u32 vcc, %9, %4\n\ts_cmp_eq_u64 vcc, exec\n\ts_cbranch_scc1 " L "\n\t"
            { unsigned t_;
              asm volatile("s_cmp_eq_u32 %10, %12\n\ts_cbranch_scc1 1f\n\t" PLD(16) PCK(16, 0, "3f") "1:\n\t" PLD(16) "s_sleep %11\n\t" PLD(20) "s_sleep %11\n\t" PLD(24)
                           "2:\n\t" PCK(16, 2, "3f") PLD(16) PCK(20, 2, "3f") PLD(20) PCK(24, 2, "3f") PLD(24)
                           "v_add_co_u32 %5, vcc, 64, %5\n\ts_cbranch_vccz 2b\n\ts_endpgm\n3:"
                           : "=&v"(R.x), "=&v"(R.y), "=&v"(R.z), "=&v"(R.w), "=&v"(t_), "+v"(spins) : "v"(ro), "s"(rb), "s"(tt), "s"(0x40000u), "s"(head >> 4), "i"(5), "i"(VENT(15)) : "scc", "vcc", "memory"); }
            const float s0 = ldexpf(wh, (int)(R.y >> 10) - 139), s1 = ldexpf(wh, (int)(R.w >> 10) - 139);
            const float f[6] = {s0 * (float)(int)__builtin_amdgcn_sbfe(R.x, 0, 14), s0 * (float)(int)__builtin_amdgcn_sbfe(R.x, 14, 14), s0 * (float)(int)__builtin_amdgcn_sbfe(__builtin_amdgcn_alignbit(R.y, R.x, 28), 0, 14),
                                s1 * (float)(int)__builtin_amdgcn_sbfe(R.z, 0, 14), s1 * (float)(int)__builtin_amdgcn_sbfe(R.z, 14, 14), s1 * (float)(int)__builtin_amdgcn_sbfe(__builtin_amdgcn_alignbit(R.w, R.z, 28), 0, 14)};
            float sv[3];
#pragma unroll
            for (int i = 0; i < 3; i++) {
                const auto sw = __builtin_amdgcn_permlane32_swap(__float_as_uint(f[i]), __float_as_uint(f[i + 3]), false, false);
                sv[i] = __uint_as_float(sw[0]) + __uint_as_float(sw[1]);
            }
            pb[wave * 64 + lane] = (v4f){sv[0], sv[1], sv[2], 0.f};
            CBAR();
            COUNT(grab_adr + 12)
            v4f ps[NSW];
#pragma unroll
            for (int w = 0; w < NSW; w++) ps[w] = pb[w * 64 + lane];
            GRABBED()
            if (g == NSW - 1) {
                const v4f sum = ((ps[0] + ps[1]) + (ps[2] + ps[3])) + ((ps[4] + ps[5]) + (ps[6] + ps[7]));
                o[0] = __float2bfloat16(sum.x); o[1] = __float2bfloat16(sum.y); o[2] = __float2bfloat16(sum.z);
            }
        }
        return;
    }

    if (wave == NSW + 1) {
        u64 E; asm volatile("s_setprio 3\n\ts_load_dwordx2 %0, %1, 0\n\ts_waitcnt lgkmcnt(0)" : "=s"(E) : "s"(ws + WS_EPOCH) : "memory");
        unsigned t2 = (ts + TOF) * 128 + 64 * P0;
        int off = 0, off1 = 0, hold = 0, cum = 0, dp = 0, sp = 0;
#define NOM(U) (((int)(U) - 2) * 32 * PER)
#define NEWLINE(K) { if (lane >= (K) && lane <= 2 * NU) sm.tu[lane] = (v2u){t2 + NOM(lane) + (cum + (lane - (int)(K)) * dp + (lane > (K) ? sp + off : off1)) * 32, \
                                                                            (unsigned)(PER + dp + (lane == (K) ? sp + off - off1 : 0))}; }
        NEWLINE(2)
        if (lane == 0) sm.tu[1] = (v2u){t2 - 32 * P0, (unsigned)P0};
        const u64 E1 = E + 1;
        const unsigned rtag = (unsigned)E1 * 0x9e3779b1u;
        const bool boss = blockIdx.x == 0;
        u64* const slot = (u64*)(ws + WS_REF + (blockIdx.x & 63) * LSTRIDE + (blockIdx.x >> 6) * 16);
        const u64* const slots = (const u64*)(ws + WS_REF + lane * LSTRIDE);
        u64* const dec = (u64*)(ws + WS_DEC);
#define GLDS(P) __hip_atomic_load(P, __ATOMIC_RELAXED, __HIP_MEMORY_SCOPE_AGENT)
#define GSTS(P, V) __hip_atomic_store(P, V, __ATOMIC_RELAXED, __HIP_MEMORY_SCOPE_AGENT)
#define UNTIL(T) while ((unsigned)((T) - NOW() * 128) < 0x40000u) SLEEP(2)
        if (lane < 2) GSTS(slot + lane, lane ? E1 : (u64)ts | ((u64)rtag << 32));
        if (lane == 0) { sm.tag = (unsigned)E1; CBAR(); lds_st(&sm.tag_rdy, 1u); }
        unsigned dv = 0;
        bool joined = false;
#define JOIN(T0) { const int d0 = min(max((int)((T0) - ts), -1000), 1000); off -= d0 * 4; t2 += d0 * 128; joined = true; }
        unsigned pw_ = 0, tw_ = 0, now, p; int prov = 0;
#define LOOK() { u64 t_; asm volatile("ds_read_b32 %1, %2\n\ts_memrealtime %0\n\ts_waitcnt lgkmcnt(0)" : "=s"(t_), "=v"(p) : "v"(lds_addr(&sm.grab)) : "memory"); \
                 p = __builtin_amdgcn_readfirstlane(p) - 8; now = (unsigned)t_ * 128; }
#define LATENESS(P, T) ({ const v2u e = TU((P) >> 5); (int)((T) - __builtin_amdgcn_readfirstlane(e.x) - ((P) & 31) * __builtin_amdgcn_readfirstlane(e.y)); })
#define WATCH(T) for (;;) { \
            LOOK() \
            if (__builtin_expect(now - tw_ > STH * 128u && pw_ >= 64 && p < NPC - 8, 0)) { \
                const int lw = max(LATENESS(pw_, tw_), 0); \
                if (LATENESS(p, now) - lw > STH * 128) \
                    for (;;) { \
                        const int d_ = min(max(LATENESS(p, now) - lw, 0) >> 5, 1600); \
                        if (lane <= 2 * NU) atomicAdd((unsigned*)&sm.tu[lane], d_ * 32u); \
                        off += d_; prov += d_; hold = 3; \
                        if (p != pw_ || prov > 8000) break; \
                        SLEEP(1) \
                        LOOK() \
                    } \
                } \
            if (p != pw_) { pw_ = p; tw_ = now; } \
            if ((unsigned)((T) - now) >= 0x40000u) break; \
            if ((unsigned)((T) - now) > 25 * 128) SLEEP(7) else SLEEP(1) }
        if (boss) {
            TOUCH((lane & 31) * 128, dec)
            UNTIL((ts + X0) * 128)
            for (;;) {
                bool ok = true; float sum = 0.f;
#pragma unroll
                for (int i = 0; i < 4; i++) { const u64 x = GLDS(slots + 2 * i), e = GLDS(slots + 2 * i + 1); ok &= e == E1 && (unsigned)(x >> 32) == rtag; sum += (float)min(max((int)((unsigned)x - ts), -1000), 1000); }
                if (__builtin_amdgcn_ballot_w64(ok) == ~0ull) { dv = ts + (int)(wave_sum(sum) * (1.f / 256)); break; }
                SLEEP(8)
            }
            if (lane == 0) { GSTS(dec, (u64)dv | ((u64)rtag << 32)); GSTS((u64*)(ws + WS_EPOCH), E1); }
            JOIN(dv)
            dv = 0;
        }
        int sp1 = 0, tot1 = 0, tot2 = 0;
#pragma unroll 1
        for (unsigned k = 2; k < 2 * NU; k++) {
            const unsigned tk = t2 + NOM(k) + cum * 32;
            if (k >= K0 && k + 1 < 2 * NU) {
                WATCH(tk - (XM + XW) * 128)
                int sum = 0, n = 0;
                do { LOOK() sum += LATENESS(p, now) >> 7; n++; } while ((unsigned)(tk - XM * 128 - now) < 0x40000u);
                const int l = (int)((float)sum * __builtin_amdgcn_rcpf((float)n)) - BK + prov / 4;
                if (l > RTH && lane == 0) GSTS((unsigned*)(ws + WS_REF + (blockIdx.x & 63) * LSTRIDE + 64) + (blockIdx.x >> 6), (unsigned)min(l, 1000) | ((rtag + k) << 10));
            }
            const int cum0 = cum;
            bool got = boss;
            if (!boss && (k > K0 || !joined)) {
                WATCH(tk - XR * 128)
                const u64 x = GLDS(dec + (joined ? k * 16 : 0));
                dv = __builtin_amdgcn_readfirstlane((unsigned)x);
                got = __builtin_amdgcn_readfirstlane((unsigned)(x >> 32)) == rtag;
                if (got && !joined) { JOIN(dv) got = false; }
            }
            sp1 = sp; sp = 0;
            if (got && k > K0) { cum = dv >> 18; sp = dv >> 6 & 4095u; dp = dv & 63u; }
            const int yld = cum - cum0 + sp;
            { const int pj = min(prov, cum - cum0); off -= pj; prov -= pj; off1 = off;
              const int ps = min(prov, sp); off -= ps; prov -= ps;
              if (hold) hold--; else prov = 0;
              const int r_ = off - prov; off -= r_ > 40 || r_ < -40 ? r_ / NB : r_; }
            NEWLINE(k)
            cum = min(cum + sp + dp, 16383);
            if (boss) dv = (cum << 18) | dp;
            if (boss && k >= K0 && k + 1 < 2 * NU && (int)(NOW() * 128 - tk) < -60 * 128) {
                WATCH(tk - (XM - XD1) * 128)
                const v4u x = __builtin_amdgcn_raw_buffer_load_b128(__builtin_amdgcn_make_buffer_rsrc(ws + WS_REF + 64, 0, 64 * LSTRIDE, 0x00020000), lane * LSTRIDE, 0, SC1);
                float a[4];
#pragma unroll
                for (int i = 0; i < 4; i++) a[i] = ((x[i] ^ ((rtag + k) << 10)) >> 10) == 0 ? (float)(x[i] & 1023u) : 0.f;
                const int tot = (int)wave_sum(a[0] + a[1] + a[2] + a[3]) >> 6;
                float mn = 0.f;
                if (tot >= 40) {
#pragma unroll 1
                    for (float st = 512.f; st >= 4.f; st *= 0.5f) {
                        const float x_ = mn + st;
                        const int c_ = __builtin_popcountll(__builtin_amdgcn_ballot_w64(a[0] >= x_)) + __builtin_popcountll(__builtin_amdgcn_ballot_w64(a[1] >= x_)) +
                                       __builtin_popcountll(__builtin_amdgcn_ballot_w64(a[2] >= x_)) + __builtin_popcountll(__builtin_amdgcn_ballot_w64(a[3] >= x_));
                        if (c_ >= QN) mn = x_;
                    }
                }
                const float c = mn + (float)CAP, fth = (float)(FTH * (2 * NU - (int)k));
                const int cut = (int)wave_sum(fminf(a[0], c) + fminf(a[1], c) + fminf(a[2], c) + fminf(a[3], c)) >> 6, m4 = (int)mn * 4;
                const bool far = __builtin_amdgcn_ballot_w64(fmaxf(fmaxf(a[0], a[1]), fmaxf(a[2], a[3])) > fth) != 0;
                const int pend = yld + sp1 * XM / 320;
                const int nj = max(m4 - pend, 0);
                const int ns = min(max(max(min(m4 + (tot - m4) * GAIN / 16 - pend - nj, (cut - m4) * GAIN / 16), 0), far ? FLOOR * 4 : 0), 4000);
                int ndp = dp;
                if (k >= (unsigned)KD && tot >= KI && tot > tot1 && tot < tot1 + 32 && tot1 < tot2 + 32) ndp = min(dp + DPS, 40);
                if (tot + tot1 < 2) ndp = max(dp - 1, 0);
                tot2 = tot1; tot1 = tot;
                dv = (min(cum + nj, 16383) << 18) | (ns << 6) | ndp;
                if ((int)(NOW() * 128 - tk) > XL * 128) dv = (cum << 18) | dp;
                else if (lane == 0) GSTS(dec + (k + 1) * 16, (u64)dv | ((u64)rtag << 32));
            }
        }
        WATCH(p >= NPC - 8 ? now - 128 : now + 4096)
    } else if (wave == NSW + 2) {
        const int ql = lane < 48 ? lane : 47;
        unsigned c1, c2, s1, s2;
#define QT_LOADS() { int qo_ = ql; asm volatile("" : "+v"(qo_)); \
                     c1 = ((const unsigned*)q_cos)[qo_]; c2 = ((const unsigned*)q_cos)[48 + qo_]; s1 = ((const unsigned*)q_sin)[qo_]; s2 = ((const unsigned*)q_sin)[48 + qo_]; }
        unsigned q1, q2, lkz, lks, lvz, lvs;
#define UNIT_LOADS(HEAD) { \
        q1 = ((const unsigned*)(q + (size_t)(HEAD) * DH))[ql]; q2 = ((const unsigned*)(q + (size_t)(HEAD) * DH))[48 + ql]; \
        const size_t so = ((size_t)(HEAD) * SKV + brow0) / 2 + (lane >> 3) * 128 + (lane & 7); \
        lkz = ((const unsigned*)k_zeros)[so]; lks = ((const unsigned*)k_scales)[so]; \
        lvz = ((const unsigned*)v_zeros)[so]; lvs = ((const unsigned*)v_scales)[so]; }
#define UNIT_FILL(SL, U) { \
            v4f qq; \
            qq.x = bfr(bfr(bflo(q1) * bflo(c1)) + bfr(-bflo(q2) * bflo(s1))); \
            qq.y = bfr(bfr(bflo(q2) * bflo(c2)) + bfr(bflo(q1) * bflo(s2))); \
            qq.z = bfr(bfr(bfhi(q1) * bfhi(c1)) + bfr(-bfhi(q2) * bfhi(s1))); \
            qq.w = bfr(bfr(bfhi(q2) * bfhi(c2)) + bfr(bfhi(q1) * bfhi(s2))); \
            *(v2u*)&sm.q[SL][2 * ql] = (v2u){(__float_as_uint(qq.x) >> 16) | __float_as_uint(qq.y), (__float_as_uint(qq.z) >> 16) | __float_as_uint(qq.w)}; \
            *(v4f*)&sm.kz[SL][2 * lane] = (v4f){bflo(lkz), bflo(lks), bfhi(lkz), bfhi(lks)}; \
            *(v4f*)&sm.vz[SL][2 * lane] = (v4f){bflo(lvz), bflo(lvs), bfhi(lvz), bfhi(lvs)}; \
            CBAR(); \
            if (lane == 0) lds_st(&sm.rdy[SL], (U) + 1); \
            CBAR(); }
        asm volatile("s_setprio 3");
        QT_LOADS()
#pragma unroll 1
        for (unsigned u = 0; u < NU; u++) {
            UNIT_LOADS(rank + GSZ * u)
            while (u >= lds_ld(&sm.merged) + NIN) SLEEP(4);
            CBAR();
            UNIT_FILL(u % NIN, u)
            if (u == 1) asm volatile("s_setprio 0");
        }
        while (lds_ld(&sm.merged) < NU) SLEEP(16)
        CBAR();
        const unsigned T = lds_ld(&sm.tag);
        if (lane < NU) __hip_atomic_store((u64*)(ws + WS_LOG + (rank * NSPLIT + split) * LSTRIDE2) + lane, (u64)__float_as_uint(sm.ent[lane][130]) | ((u64)T << 32), __ATOMIC_RELAXED, __HIP_MEMORY_SCOPE_AGENT);
        while (lds_ld(&sm.merged) < NU + 3) SLEEP(16)
        for (;;) {
            const u64 x = __hip_atomic_load((const u64*)(ws + WS_LOG + ((blockIdx.x & 15) * NSPLIT + (lane & 15)) * LSTRIDE2) + (blockIdx.x >> 4), __ATOMIC_RELAXED, __HIP_MEMORY_SCOPE_AGENT);
            if (__builtin_amdgcn_ballot_w64((unsigned)(x >> 32) == T) == ~0ull) {
                const float L = __uint_as_float((unsigned)x);
                const float w = __builtin_amdgcn_exp2f(L - row_max(L));
                sm.fw[lane & 15] = w / row_sum(w);
                CBAR();
                if (lane == 0) lds_st(&sm.fw_rdy, 1u);
                break;
            }
            SLEEP(64)
        }
    } else {
        while (lds_ld(&sm.tag_rdy) == 0) SLEEP(1);
        CBAR();
        const unsigned T = lds_ld(&sm.tag);
        const __amdgpu_buffer_rsrc_t rsrc = __builtin_amdgcn_make_buffer_rsrc(ws + WS_PART + (rank * NSPLIT + split) * 512, 0, NH * NSPLIT * 512, 0x00020000);
        v2u* recs = (v2u*)&sm.cs[0][0];
#pragma unroll
        for (int i = 0; i < NOUT * 3; i++) (&sm.acc[0][0])[i * 64 + lane] = 0.0;
        bool direct = false;
#define PUBLISH(N) { \
        v4u r[8]; \
        _Pragma("unroll") for (int k = 0; k < 8; k++) r[k] = ((const v4u*)recs)[k * 64 + lane]; \
        _Pragma("unroll") for (int k = 0; k < 8; k++) __builtin_amdgcn_raw_buffer_store_b128(r[k], rsrc, 2 * k + (lane >> 5) < (N) ? po + (lane < 32 ? VENT(2 * k) : VENT(2 * k + 1)) * (GSZ * NSPLIT * 512) : 0x7ffffff0u, 0, SC1); }
        const unsigned po = (lane & 31) * 16;
#pragma unroll 1
        for (unsigned u = 0; u < 2 * NU; u++) {
            const unsigned e = u < NU ? u : VENT(u), so = u % NOUT;
            const v4f h = *(const v4f*)&sm.ent[e][128];
            for (;;) {
                if (!direct && u >= 2 * NU - 3 && (int)(NOW() * 128 - __builtin_amdgcn_readfirstlane(TU(2 * NU).x)) >= PUBT * 128) { direct = true; wave_sync(); PUBLISH(u - NU) }
                if (lds_ld(&sm.cnt[so]) == 32) break;
                if (direct) SLEEP(0) else SLEEP(1)
            }
            CBAR();
            if (!direct && u == 2 * NU - 1) { direct = true; wave_sync(); PUBLISH(NU - 1) }
            if (u < NU) {
                const float2 lg = *(const float2*)&sm.ent[e][2 * lane];
                const v4f zs = *(const v4f*)&sm.vz[u % NIN][2 * lane];
                const float m = wave_max(fmaxf(lg.x, lg.y));
                const float p0 = __expf(lg.x - m), p1 = __expf(lg.y - m);
                const float x0 = p0 * zs.y, x1 = p1 * zs.w;
                const unsigned eb = max(__float_as_uint(wave_max(fmaxf(fabsf(x0), fabsf(x1)))) >> 23, 21u);
                const float f = __uint_as_float((275u - eb) << 23);
                const float w0 = rintf(x0 * f), w1 = rintf(x1 * f);
                const float den = wave_sum(p0 + p1);
                const float bz = wave_sum(fmaf(w0, zs.x, w1 * zs.z));
                const float L = fmaf(m, 1.4426950408889634f, __builtin_amdgcn_logf(den));
                *(v2u*)&sm.ent[e][2 * lane] = (v2u){(unsigned)(int)w0, (unsigned)(int)w1};
                if (lane == 0) *(v4f*)&sm.ent[e][128] = (v4f){ldexpf(1.f / den, (int)eb - 148), bz, L, 0.f};
            } else {
                const double a0 = sm.acc[so][lane], a1 = sm.acc[so][64 + lane], a2 = sm.acc[so][128 + lane];
                const float n0 = (float)(a0 - (double)h.y) * h.x, n1 = (float)(a1 - (double)h.y) * h.x, n2 = (float)(a2 - (double)h.y) * h.x;
                const unsigned e2 = max(__float_as_uint(fmaxf(fmaxf(fabsf(n0), fabsf(n1)), fabsf(n2))) >> 23, 12u);
                const float f2 = __uint_as_float((266u - e2) << 23);
                const unsigned m0 = min(max((int)rintf(n0 * f2), -8191), 8191) & 0x3fff, m1 = min(max((int)rintf(n1 * f2), -8191), 8191) & 0x3fff, m2 = min(max((int)rintf(n2 * f2), -8191), 8191) & 0x3fff;
                const v2u r = {m0 | (m1 << 14) | (m2 << 28), (m2 >> 4) | (e2 << 10) | (T << 18)};
                if (direct) {
                    const v4u r2 = {r.x, r.y, (unsigned)__builtin_amdgcn_update_dpp(0, (int)r.x, 0xf5, 0xf, 0xf, true), (unsigned)__builtin_amdgcn_update_dpp(0, (int)r.y, 0xf5, 0xf, 0xf, true)};
                    __builtin_amdgcn_raw_buffer_store_b128(r2, rsrc, (lane & 1) ? 0x7ffffff0u : lane * 8, e * (GSZ * NSPLIT * 512), SC1);
                }
                else recs[(u - NU) * 64 + lane] = r;
                CBAR();
                sm.acc[so][lane] = 0.0; sm.acc[so][64 + lane] = 0.0; sm.acc[so][128 + lane] = 0.0;
            }
            CBAR();
            if (lane == 0) { lds_st(&sm.cnt[so], 0u); CBAR(); lds_st(&sm.merged, u + 1); }
        }
        return;
    }

}

extern "C" void kernel_launch(void* const* d_in, const int* in_sizes, int n_in,
                              void* d_out, int out_size, void* d_ws, size_t ws_size,
                              hipStream_t stream) {
    const unsigned short* q = (const unsigned short*)d_in[0];
    const int* qk = (const int*)d_in[1];
    const unsigned short* k_scales = (const unsigned short*)d_in[2];
    const unsigned short* k_zeros = (const unsigned short*)d_in[3];
    const int* qv = (const int*)d_in[4];
    const unsigned short* v_scales = (const unsigned short*)d_in[5];
    const unsigned short* v_zeros = (const unsigned short*)d_in[6];
    const unsigned short* q_cos = (const unsigned short*)d_in[7];
    const unsigned short* q_sin = (const unsigned short*)d_in[8];
    const unsigned short* k_cos = (const unsigned short*)d_in[9];
    const unsigned short* k_sin = (const unsigned short*)d_in[10];
    unsigned char* ws = (unsigned char*)d_ws;
    attn_kernel<<<NSPLIT * GSZ, NTHREADS, 0, stream>>>(q, qk, k_scales, k_zeros, qv, v_scales, v_zeros,
                                                       q_cos, q_sin, k_cos, k_sin, (__hip_bfloat16*)d_out, ws);
}
```

```cpp
#include <hip/hip_runtime.h>
#include <hip/hip_bf16.h>
#include <stdint.h>

#define DH 192
#define HALF 96
#define SKV 2048
#define NH 256
#define NSW 8
#define NTHREADS (64 * (NSW + 4))
#define BROWS 128
#define NSPLIT (SKV / BROWS)
#define GSZ 16
#define NU (NH / GSZ)
#define NPC (2 * NU * 32)
#define NIN 4
#define NOUT 4
#define ENTW 132
#define SC1 16
#define PHI 0x0246024671357135ull
#define VORD 0x9876543210afedcbull
#define VENT(V) ((unsigned)(VORD >> (4 * ((V) & 15))) & 15u)
#define PFL 185
#define PER 1262
#define DP0 6
#define P0 1370
#define TOF 100
#define NBW 2
#define BTH 20
#define X0 330
#define K0 3
#define XM 250
#define XW 40
#define XD1 100
#define XL 60
#define XR 170
#define BK 3
#define RTH 6
#define QN 224
#define GAIN 24
#define CAP 45
#define FLOOR 3
#define FTH 15
#define KD 9
#define KI 14
#define DPS 2
#define QUIET 2
#define WARN 5
#define LOCK 6
#define NB 3
#define STH 80
#define PUBT 40
#define POLT 100
#define POLC 110

#define WS_EPOCH 0
#define WS_DEC 8192
#define WS_REF 16384
#define LSTRIDE 4352
#define WS_LOG 524288
#define LSTRIDE2 1408
#define WS_PART 1048576

typedef unsigned int v2u __attribute__((ext_vector_type(2)));
typedef unsigned int v4u __attribute__((ext_vector_type(4)));
typedef int v4i __attribute__((ext_vector_type(4)));
typedef float v2f __attribute__((ext_vector_type(2)));
typedef float v4f __attribute__((ext_vector_type(4)));
typedef unsigned long long u64;

__device__ __forceinline__ float bflo(unsigned u) { return __uint_as_float(u << 16); }
__device__ __forceinline__ float bfhi(unsigned u) { return __uint_as_float(u & 0xffff0000u); }
__device__ __forceinline__ float bfr(float x) {
    __bf16 b = (__bf16)x;
    return __uint_as_float(((unsigned)__builtin_bit_cast(unsigned short, b)) << 16);
}

#define SLEEP(n) SLEEP_(n)
#define SLEEP_(n) asm volatile("s_sleep " #n "\n\tv_add_co_u32 %0, vcc, 1, %0\n\ts_cbranch_vccz 1f\n\ts_endpgm\n1:" : "+v"(spins) :: "vcc", "memory");
#define SPIN0 0xf0000000u
#define CBAR() asm volatile("" ::: "memory")

__device__ __forceinline__ void wave_sync() {
    __builtin_amdgcn_fence(__ATOMIC_RELEASE, "wavefront");
    __builtin_amdgcn_wave_barrier();
    __builtin_amdgcn_fence(__ATOMIC_ACQUIRE, "wavefront");
}
template <int CTRL>
__device__ __forceinline__ float dpp_add(float x) {
    int y = __builtin_amdgcn_update_dpp(0, __float_as_int(x), CTRL, 0xf, 0xf, true);
    return x + __int_as_float(y);
}
template <int CTRL>
__device__ __forceinline__ float dpp_max(float x) {
    int y = __builtin_amdgcn_update_dpp(__float_as_int(x), __float_as_int(x), CTRL, 0xf, 0xf, false);
    return fmaxf(x, __int_as_float(y));
}
__device__ __forceinline__ float row_sum(float x) { x = dpp_add<0x128>(x); x = dpp_add<0x124>(x); x = dpp_add<0x122>(x); return dpp_add<0x121>(x); }
__device__ __forceinline__ float row_max(float m) { m = dpp_max<0x128>(m); m = dpp_max<0x124>(m); m = dpp_max<0x122>(m); return dpp_max<0x121>(m); }
__device__ __forceinline__ float rdlane(float x, int l) { return __int_as_float(__builtin_amdgcn_readlane(__float_as_int(x), l)); }
__device__ __forceinline__ float wave_max(float m) { m = row_max(m); return fmaxf(fmaxf(rdlane(m, 0), rdlane(m, 16)), fmaxf(rdlane(m, 32), rdlane(m, 48))); }
__device__ __forceinline__ float wave_sum(float x) { x = row_sum(x); return (rdlane(x, 0) + rdlane(x, 16)) + (rdlane(x, 32) + rdlane(x, 48)); }
__device__ __forceinline__ unsigned lds_ld(const unsigned* p) { return __builtin_amdgcn_readfirstlane(__hip_atomic_load(p, __ATOMIC_RELAXED, __HIP_MEMORY_SCOPE_WORKGROUP)); }
__device__ __forceinline__ unsigned lds_addr(const void* p) { return (unsigned)(size_t)(const __attribute__((address_space(3))) void*)p; }
#define DMA16(SRC, LDS) asm volatile("s_mov_b32 %0, m0\n\ts_mov_b32 m0, %2\n\ts_nop 0\n\tglobal_load_lds_dwordx4 %1, off\n\ts_mov_b32 m0, %0" : "=&s"(m0keep) : "v"(SRC), "s"(LDS) : "memory")
__device__ __forceinline__ void lds_st(unsigned* p, unsigned v) { __hip_atomic_store(p, v, __ATOMIC_RELAXED, __HIP_MEMORY_SCOPE_WORKGROUP); }
#define NOW() ((unsigned)__builtin_amdgcn_s_memrealtime())

struct __attribute__((aligned(16))) Smem {
    unsigned cs[32][768];
    double acc[NOUT][DH];
    float ent[NU][ENTW];
    unsigned q[NIN][HALF];
    float2 kz[NIN][BROWS];
    float2 vz[NIN][BROWS];
    v2u tu[2 * NU + 2];
    unsigned grab, merged, tabs, cdone;
    unsigned rdy[NIN], cnt[NOUT];
    unsigned tag_rdy, tag, fw_rdy, pad;
    float fw[NSPLIT];
};

#define TU(I) (*(const volatile v2u __attribute__((address_space(3)))*)(size_t)lds_addr(&sm.tu[I]))
#define LDS_ADD(P, V) { u64 ex_; asm volatile("s_mov_b64 %0, exec\n\ts_mov_b64 exec, 1\n\tds_add_u32 %1, %2\n\ts_mov_b64 exec, %0" : "=&s"(ex_) : "v"(lds_addr(P)), "v"(V) : "memory"); }
#define LDS_OR(P, V) { u64 ex_; asm volatile("s_mov_b64 %0, exec\n\ts_mov_b64 exec, 1\n\tds_or_b32 %1, %2\n\ts_mov_b64 exec, %0" : "=&s"(ex_) : "v"(lds_addr(P)), "v"(V) : "memory"); }

__global__ __launch_bounds__(NTHREADS, 3) void attn_kernel(
    const unsigned short* __restrict__ q, const int* __restrict__ qk,
    const unsigned short* __restrict__ k_scales, const unsigned short* __restrict__ k_zeros,
    const int* __restrict__ qv,
    const unsigned short* __restrict__ v_scales, const unsigned short* __restrict__ v_zeros,
    const unsigned short* __restrict__ q_cos, const unsigned short* __restrict__ q_sin,
    const unsigned short* __restrict__ k_cos, const unsigned short* __restrict__ k_sin,
    __hip_bfloat16* __restrict__ out, unsigned char* __restrict__ ws)
{
    __shared__ Smem sm;
    const int tid = threadIdx.x;
    unsigned spins = SPIN0;
#define TOUCH(OFF, BASE) asm volatile("global_load_dword a6, %0, %1" :: "v"(OFF), "s"(BASE) : "a6", "memory");
    const unsigned cl = min((blockIdx.x & 24) * 8 + tid, PFL - 1u) * 128;
    if (tid < 64) { const char* pc0; asm volatile("s_getpc_b64 %0" : "=s"(pc0)); TOUCH(cl, pc0) }
    __builtin_amdgcn_sched_barrier(0);
    const int hw = __builtin_amdgcn_readfirstlane(tid >> 6);
    const int lane = tid & 63;
    const int split = (blockIdx.x & 3) * 4 + ((blockIdx.x >> 3) & 3);
    const int rank = (blockIdx.x >> 5) * 2 + ((blockIdx.x >> 2) & 1);
    const int brow0 = split * 16;
#define ROWOF(RG) (((RG) >> 2) * 256 + ((RG) & 3) * 4)
    if (hw == 0) {
        typedef const char* const __attribute__((address_space(4))) * kap_t;
        const char* sa = ((kap_t)__builtin_amdgcn_kernarg_segment_ptr())[(0x5623 >> (4 * (split & 3))) & 7];
        unsigned o1 = ((blockIdx.x >> 5) * 256 + brow0) * (DH * 2) + min(lane, 47) * 128;
        unsigned o2 = ((rank + GSZ * (lane >> 3)) * SKV + (lane & 7) * 256 + (brow0 & ~63)) * 2, o3 = o2 + 8 * GSZ * SKV * 2;
        unsigned o4 = (rank + GSZ * (lane >> 2)) * (DH * 2) + min(lane & 3, 2) * 128;
        asm volatile("" : "+v"(o1), "+v"(o2), "+v"(o3), "+v"(o4));
        asm volatile("global_load_dword a6, %0, %1 nt\n\tglobal_load_dword a6, %0, %2 nt" :: "v"((lane & 1) * (NH * SKV * DH * 4u - 128u * NH) + blockIdx.x * 128), "s"(qk), "s"(qv) : "a6", "memory");
        TOUCH(o1, k_cos) TOUCH(o1, k_sin) TOUCH(o4, q) TOUCH(o2, sa) TOUCH(o3, sa)
        asm volatile("s_barrier\n\ts_endpgm" ::: "memory");
    }
    if (hw == 1 && lane < 16) (&sm.grab)[lane] = lane ? 0 : 64;
    asm volatile("" :: "s"(q), "s"(qk), "s"(k_scales), "s"(k_zeros), "s"(qv), "s"(v_scales), "s"(v_zeros), "s"(q_cos), "s"(q_sin), "s"(k_cos), "s"(k_sin), "s"(out), "s"(ws));
    asm volatile("s_waitcnt lgkmcnt(0)\n\ts_barrier" ::: "memory");
    const int wave = (int)(0x76543210b9a8ull >> (4 * hw)) & 15;
    const unsigned ts = NOW();

    if (__builtin_expect(wave < NSW, 1)) {
        const unsigned rot = 4 * ((unsigned)(PHI >> (60 - 4 * rank)) & 7u);
#define RGOF(G) ((((G) + rot) & 31u) ^ ((((G) + rot) >> 1) & 2u))
        const char* cbase[3];
#pragma unroll
        for (int j = 0; j < 3; j++) { const int x = j * 64 + lane; cbase[j] = (x < 96 ? (const char*)k_cos + x * 16 : (const char*)k_sin + (x - 96) * 16) + brow0 * (DH * 2); }
        unsigned m0keep;
#define CS_DMA(RG) { _Pragma("unroll") for (int j = 0; j < 3; j++) DMA16(cbase[j] + ROWOF(RG) * (DH * 2), lds_addr(&sm.cs[0][0]) + (RG) * 3072 + j * 1024); }
#define REPACK(RG) { \
        unsigned* cw = &sm.cs[RG][0]; \
        const unsigned* l = cw + rsel * (DH / 2) + qoff; \
        unsigned r[2][6]; \
        _Pragma("unroll") \
        for (int j = 0; j < 2; j++) { \
            unsigned e[4][2]; \
            _Pragma("unroll") \
            for (int k = 0; k < 4; k++) { \
                const unsigned* a = l + j * DH + (k & 1) * 48 + (k >> 1) * 384;       \
                unsigned x0 = a[0], x1 = a[1]; \
                e[k][0] = __builtin_amdgcn_alignbit(x1, x0, sh); \
                e[k][1] = x1 >> sh; \
            } \
            r[j][0] = (e[0][0] & 0xffffu) | (e[3][0] << 16); \
            r[j][1] = (e[0][0] >> 16) | (e[3][0] & 0xffff0000u); \
            r[j][2] = (e[0][1] & 0xffffu) | (e[3][1] << 16); \
            r[j][3] = (e[1][0] & 0xffffu) | (e[2][0] << 16); \
            r[j][4] = (e[1][0] >> 16) | (e[2][0] & 0xffff0000u); \
            r[j][5] = (e[1][1] & 0xffffu) | (e[2][1] << 16); \
        } \
        wave_sync(); \
        _Pragma("unroll") \
        for (int j = 0; j < 2; j++) { *(v4u*)&cw[j * 256 + lane * 4] = (v4u){r[j][0], r[j][1], r[j][2], r[j][3]}; *(v2u*)&cw[512 + j * 128 + lane * 2] = (v2u){r[j][4], r[j][5]}; } \
        wave_sync(); \
        LDS_OR(&sm.tabs, 1u << (RG)) }
        const int sub = lane & 31;
        const int rsel = lane >> 5;
        const unsigned sh = (sub & 1) * 16;
        const int qoff = 3 * (sub >> 1) + (sub & 1);
        const char* kbase = (const char*)qk + (size_t)(rank * SKV + brow0) * (DH * 4);
        const char* vbase = (const char*)qv + (size_t)(rank * SKV + brow0) * (DH * 4);
        const unsigned koff = (rsel * DH + 3 * sub) * 4, voff = 3 * lane * 4;
#define AR(S) (16 + (S) * 8)
        asm volatile("" ::: "a16", "a17", "a18", "a19", "a20", "a21", "a22", "a23", "a24", "a25", "a26", "a27", "a28", "a29", "a30", "a31",
                            "a32", "a33", "a34", "a35", "a36", "a37", "a38", "a39", "a40", "a41", "a42", "a43", "a44", "a45", "a46", "a47");
        asm volatile("" ::: "a48", "a49", "a50", "a51", "a52", "a53", "a54", "a55", "a56", "a57", "a58", "a59", "a60", "a61", "a62", "a63");
        __builtin_amdgcn_s_waitcnt(0x0f70);
        int ta[3], tb[3];
#define GLD(A, OFF, BASE, IMM) asm volatile("global_load_dwordx3 a[%c0:%c1], %2, %3 offset:%4 nt" :: "i"(A), "i"((A) + 2), "v"(OFF), "s"(BASE), "i"(IMM))
#define ISSUE(S, J, BASE, OA, OB) { GLD(AR(S), OA, BASE, (J) * 1536); GLD(AR(S) + 4, OB, BASE, (J) * 1536); }
#define WAITT(S, N) asm volatile("s_waitcnt vmcnt(%6)\n\tv_accvgpr_read_b32 %0, a[%c7]\n\tv_accvgpr_read_b32 %1, a[%c8]\n\tv_accvgpr_read_b32 %2, a[%c9]\n\t" \
                              "v_accvgpr_read_b32 %3, a[%c10]\n\tv_accvgpr_read_b32 %4, a[%c11]\n\tv_accvgpr_read_b32 %5, a[%c12]" \
                              : "=v"(ta[0]), "=v"(ta[1]), "=v"(ta[2]), "=v"(tb[0]), "=v"(tb[1]), "=v"(tb[2]) \
                              : "i"(N), "i"(AR(S)), "i"(AR(S) + 1), "i"(AR(S) + 2), "i"(AR(S) + 4), "i"(AR(S) + 5), "i"(AR(S) + 6))
        const unsigned koffb = koff + 384, voffb = voff + 768;
        unsigned u = 0, rg, np = 0;
#define RG0(C) RGOF((unsigned)(wave + 8 * (C)))
        CS_DMA(RG0(0))
        const float scale = 0.07216878364870322f;
        double acc[3] = {0.0, 0.0, 0.0};
        bool chg, none = false;
        unsigned Qp[3], Qn[3];
        float kz4[2], ksc, kres[2];
        v4u ca[2]; v2u cb[2];
        v2f qe[2][3], A[2];
        const char* nb; unsigned oa, ob;
        unsigned g, gu, mg, nrg, tg, dg, par = 0;
        int early;
        const int bth = wave < NBW ? BTH * 128 : 0x3fffffff;
        const unsigned grab_adr = lds_addr(&sm.grab);
        asm volatile("v_accvgpr_write_b32 a13, 1" ::: "a13", "a15");
#define COUNT(ADR) { u64 ex_; asm volatile("s_mov_b64 %0, exec\n\ts_mov_b64 exec, 1\n\tds_add_rtn_u32 a15, %1, a13\n\ts_mov_b64 exec, %0" : "=&s"(ex_) : "v"(ADR) : "memory"); }
#define GRABBED() { unsigned t_; asm volatile("s_waitcnt lgkmcnt(0)\n\tv_accvgpr_read_b32 %1, a15\n\ts_nop 0\n\tv_readfirstlane_b32 %0, %1\n\ts_nop 1" : "=s"(g), "=v"(t_)); }
#define NEXT1() const v2u x_ = TU(g >> 5); u64 tn_; asm volatile("s_memrealtime %0" : "=s"(tn_));
#define NEXT2() { \
            chg = (g >> 5) != u; none = g >= NPC; \
            const bool nv = g >= NPC / 2; \
            nrg = RGOF(g); \
            unsigned boff; asm("s_mul_i32 %0, %1, 0x300" : "=s"(boff) : "s"((nv ? VENT(g >> 5) : g >> 5) * (GSZ * SKV) + ROWOF(nrg) + 2)); \
            nb = (nv ? vbase : kbase) + boff; \
            oa = nv ? voff : koff; ob = nv ? voffb : koffb; \
            asm volatile("s_waitcnt lgkmcnt(0)" : "+s"(tn_) : "s"(nb), "v"(oa), "v"(ob)); \
            tg = __builtin_amdgcn_readfirstlane(x_.x) + (g & 31) * __builtin_amdgcn_readfirstlane(x_.y); \
            early = none ? 0 : (int)((unsigned)tn_ * 128 - tg) - bth; \
            if (early > 0) asm volatile("s_cmp_lg_u32 %3, 0\n\ts_cbranch_scc1 1f\n\t" L4(32) "\ts_branch 2f\n1:\n\t" L4(16) "2:\n\ts_mov_b64 vcc, exec\n\ts_mov_b64 exec, 1\n\tds_add_rtn_u32 a15, %4, a13\n\ts_mov_b64 exec, vcc" \
                                    :: "v"(oa), "v"(ob), "s"(nb), "s"(pr), "v"(grab_adr) : "scc", "vcc", "memory"); }
#define L4(A) "global_load_dwordx3 a[" #A ":" #A "+2], %0, %2 offset:-1536 nt\n\tglobal_load_dwordx3 a[" #A "+4:" #A "+6], %1, %2 offset:-1536 nt\n\t" \
              "global_load_dwordx3 a[" #A "+8:" #A "+10], %0, %2 nt\n\tglobal_load_dwordx3 a[" #A "+12:" #A "+14], %1, %2 nt\n"
#define GATE(...) \
            asm volatile("s_setprio 0\n1:\n\ts_memrealtime vcc\n\ts_waitcnt lgkmcnt(0)\n\ts_lshl_b32 vcc_lo, vcc_lo, 7\n\ts_sub_u32 %0, %1, vcc_lo\n\ts_cmp_lt_u32 %0, 0x40000\n\ts_cbranch_scc0 2f\n\t" \
                         "s_cmp_lt_u32 %0, %2\n\ts_cbranch_scc1 1b\n\ts_sleep %3\n\ts_branch 1b\n2:\n\ts_setprio 2" : "=&s"(dg) : "s"(tg), "i"(10 * 128), "i"(2), __VA_ARGS__ : "scc", "vcc", "memory");
#define RD6(A) "v_accvgpr_read_b32 %0, a[%c6+" #A "]\n\tv_accvgpr_read_b32 %1, a[%c6+" #A "+1]\n\tv_accvgpr_read_b32 %2, a[%c6+" #A "+2]\n\t" \
               "v_accvgpr_read_b32 %3, a[%c6+" #A "+4]\n\tv_accvgpr_read_b32 %4, a[%c6+" #A "+5]\n\tv_accvgpr_read_b32 %5, a[%c6+" #A "+6]\n"
#define LD2(A) "global_load_dwordx3 a[%c6+" #A ":%c6+" #A "+2], %7, %9 offset:%10 nt\n\tglobal_load_dwordx3 a[%c6+" #A "+4:%c6+" #A "+6], %8, %9 offset:%10 nt"
#define OUT6 "=&v"(ta[0]), "=&v"(ta[1]), "=&v"(ta[2]), "=&v"(tb[0]), "=&v"(tb[1]), "=&v"(tb[2])
#define WI(A, B, TAIL) "s_cmp_gt_i32 %13, 0\n\ts_cbranch_scc1 3f\n\ts_waitcnt vmcnt(2)\n\t" LD2(A) TAIL "\n3:\n\ts_waitcnt vmcnt(%c14)\n\t" RD6(B)
#define WAITISSUE(S, TAIL, ...) asm volatile("s_cmp_lg_u32 %12, 0\n\ts_cbranch_scc1 1f\n\t" WI(16, 0, TAIL) "\ts_branch 2f\n1:\n\t" WI(0, 16, TAIL) "2:" \
                              : OUT6 : "i"(AR(S)), "v"(oa), "v"(ob), "s"(nb), "i"(((S) - 1) * 1536), "v"(grab_adr), "s"(pr), "s"(early), "i"(6 - 2 * (S)), __VA_ARGS__ : "scc", "vcc", "memory")
#define LASTT(S) asm volatile("s_waitcnt vmcnt(%c8)\n\ts_cmp_lg_u32 %7, 0\n\ts_cbranch_scc1 1f\n\t" RD6(0) "\ts_branch 2f\n1:\n\t" RD6(16) "2:" : OUT6 : "i"(AR(S)), "s"(pr), "i"(2 - 2 * (S)) : "scc")
#define DRAW "\n\ts_mov_b64 vcc, exec\n\ts_mov_b64 exec, 1\n\tds_add_rtn_u32 a15, %11, a13\n\ts_mov_b64 exec, vcc"
#define K_TAB() { _Pragma("unroll") for (int p = 0; p < 2; p++) { \
            const unsigned* cw = &sm.cs[rg][0]; \
            ca[p] = *(const v4u*)&cw[p * 256 + lane * 4]; cb[p] = *(const v2u*)&cw[512 + p * 128 + lane * 2]; } }
#define K_PRE(P) { \
        float e0, e1, e2, f0, f1, f2; \
        asm("v_dot2_f32_bf16 %0, %6, %12, 0\n\tv_dot2_f32_bf16 %3, %9, %15, 0\n\tv_dot2_f32_bf16 %1, %7, %13, 0\n\tv_dot2_f32_bf16 %4, %10, %16, 0\n\t" \
            "v_dot2_f32_bf16 %2, %8, %14, 0\n\tv_dot2_f32_bf16 %5, %11, %17, 0\n\ts_nop 2" \
            : "=&v"(e0), "=&v"(e1), "=&v"(e2), "=&v"(f0), "=&v"(f1), "=&v"(f2) \
            : "v"(Qp[0]), "v"(Qp[1]), "v"(Qp[2]), "v"(Qn[0]), "v"(Qn[1]), "v"(Qn[2]), \
              "v"(ca[P].x), "v"(ca[P].y), "v"(ca[P].z), "v"(ca[P].w), "v"(cb[P].x), "v"(cb[P].y)); \
        qe[P][0] = (v2f){e0, f0}; qe[P][1] = (v2f){e1, f1}; qe[P][2] = (v2f){e2, f2}; \
        A[P] = (qe[P][0] + qe[P][1] + qe[P][2]) * (v2f){-kz4[P], -kz4[P]}; }
#define K_SUM(P) { \
        _Pragma("unroll") \
        for (int i = 0; i < 3; i++) { v2f x = {(float)ta[i], (float)tb[i]}; A[P] = qe[P][i] * x + A[P]; } \
        kres[P] = A[P].x + A[P].y; }
#define K_RED2() { \
        const bool o1 = lane & 1; \
        float sx = (o1 ? kres[1] : kres[0]) + __int_as_float(__builtin_amdgcn_update_dpp(0, __float_as_int(o1 ? kres[0] : kres[1]), 0xb1, 0xf, 0xf, true)); \
        sx = dpp_add<0x4e>(sx); sx = dpp_add<0x124>(sx); sx = dpp_add<0x128>(sx); \
        const auto sw = __builtin_amdgcn_permlane16_swap(__float_as_uint(sx), __float_as_uint(sx), false, false); \
        sx = __uint_as_float(sw[0]) + __uint_as_float(sw[1]); \
        if ((lane & 30) == 0) sm.ent[u][rg * 4 + 2 * (lane & 1) + rsel] = sx * ksc; }
#define LDV(P) __hip_atomic_load(P, __ATOMIC_RELAXED, __HIP_MEMORY_SCOPE_WORKGROUP)
#define RDYWAIT(SL, SEQ) { while (lds_ld(&sm.rdy[SL]) != (SEQ)) SLEEP(1); CBAR(); }
#define LOADZ() { _Pragma("unroll") for (int p = 0; p < 2; p++) kz4[p] = sm.kz[u % NIN][rg * 4 + 2 * p + rsel].x; ksc = sm.kz[u % NIN][rg * 4 + 2 * (lane & 1) + rsel].y * scale; }
#define LOADQ() { _Pragma("unroll") for (int i = 0; i < 3; i++) { Qp[i] = sm.q[u % NIN][3 * sub + i]; Qn[i] = __builtin_amdgcn_alignbit(Qp[i], Qp[i], 16) ^ 0x80000000u; } }
#define FLUSH(ISV) { \
            mg = __builtin_amdgcn_readfirstlane(mg); \
            while (u >= mg + NOUT) { SLEEP(1); mg = lds_ld(&sm.merged); } \
            CBAR(); \
            if (ISV) { const unsigned aa = lds_addr(&sm.acc[u % NOUT][lane]); \
                asm volatile("ds_add_f64 %0, %1\n\tds_add_f64 %0, %2 offset:512\n\tds_add_f64 %0, %3 offset:1024" :: "v"(aa), "v"(acc[0]), "v"(acc[1]), "v"(acc[2]) : "memory"); \
                acc[0] = 0.0; acc[1] = 0.0; acc[2] = 0.0; } \
            CBAR(); \
            LDS_ADD(&sm.cnt[u % NOUT], np) \
            CBAR(); \
            np = 0; }
#define ADVANCE() { u = gu; rg = nrg; }

#define REQ0(N, S) { tg = (ts + TOF) * 128 + (wave + 8 * (N)) * P0; \
            const char* kb = kbase + (((N) >> 2) * (GSZ * SKV) + ROWOF(RG0((N) & 3))) * (DH * 4); GATE("s"(kb)) ISSUE(S, 0, kb, koff, koffb) ISSUE((S) + 1, 1, kb, koff, koffb) }
#define USE0(N, S, W) { u = (N) >> 2; rg = RG0((N) & 3); K_TAB() LOADQ() LOADZ() K_PRE(0) K_PRE(1) WAITT(S, W); K_SUM(0) WAITT((S) + 1, W); K_SUM(1) K_RED2() }
#define VMW(N) asm volatile("s_waitcnt vmcnt(" #N ")" ::: "memory");
        REQ0(0, 0) CS_DMA(RG0(1))
        VMW(7) REPACK(RG0(0))
        REQ0(1, 2)
        RDYWAIT(0, 1u)
        USE0(0, 0, 7)
        VMW(4) REPACK(RG0(1))
        REQ0(2, 0) CS_DMA(RG0(2))
        USE0(1, 2, 7)
        REQ0(3, 2)
        VMW(4) REPACK(RG0(2))
        USE0(2, 0, 4)
        REQ0(4, 0) CS_DMA(RG0(3))
        REQ0(5, 4)
        VMW(4) REPACK(RG0(3))
        USE0(3, 2, 4)
        CBAR();
        LDS_ADD(&sm.cnt[0], 4u)
        REQ0(6, 2)
        RDYWAIT(1, 2u)
        USE0(4, 0, 8)
        USE0(5, 4, 4)
        REQ0(7, 0)
        COUNT(grab_adr)
        USE0(6, 2, 4)
        rg = RG0(3); np = 3;
        GRABBED()
#pragma unroll 1
        while (u < NU) {
            const unsigned pr = __builtin_amdgcn_readfirstlane(par); par ^= 1;
            NEXT1()
            const unsigned tb_ = LDV(&sm.tabs), fl = LDV(&sm.rdy[u % NIN]);
            mg = LDV(&sm.merged);
            CBAR();
            K_TAB() LOADQ() LOADZ()
            NEXT2()
            if (__builtin_expect(__builtin_amdgcn_readfirstlane(fl) != u + 1 || !((__builtin_amdgcn_readfirstlane(tb_) >> rg) & 1u), 0)) {
                while (!((lds_ld(&sm.tabs) >> rg) & 1u)) SLEEP(1)
                RDYWAIT(u % NIN, u + 1)
                K_TAB() LOADQ() LOADZ()
            }
            K_PRE(0) K_PRE(1)
            if (early <= 0) GATE("v"(A[0]), "v"(A[1]), "v"(oa), "v"(ob), "s"(nb), "s"(pr))
            WAITISSUE(0, DRAW, "v"(A[0]));
            K_SUM(0)
            WAITISSUE(1, "", "v"(A[1]));
            K_SUM(1)
            gu = g >> 5;
            GRABBED()
            K_RED2()
            np++;
            if (chg) FLUSH(false)
            ADVANCE()
        }
#pragma unroll 1
        while (u < 2 * NU) {
            const unsigned pr = __builtin_amdgcn_readfirstlane(par); par ^= 1;
            NEXT1()
            mg = LDV(&sm.merged);
            CBAR();
            const unsigned ve = VENT(u);
            v4i pw = *(const v4i*)&sm.ent[ve][rg * 4];
            NEXT2()
            if (__builtin_expect(__builtin_amdgcn_readfirstlane(mg) <= ve, 0)) { while (lds_ld(&sm.merged) <= ve) SLEEP(1); CBAR(); pw = *(const v4i*)&sm.ent[ve][rg * 4]; }
            const double w0 = pw.x, w1 = pw.y, w2 = pw.z, w3 = pw.w;
            if (none) { LASTT(0); } else { if (early <= 0) GATE("s"(pr), "v"(w0), "v"(w1), "v"(w2), "v"(w3), "v"(oa), "v"(ob), "s"(nb)) WAITISSUE(0, DRAW, "v"(w0)); }
#pragma unroll
            for (int i = 0; i < 3; i++) acc[i] = fma(w1, (double)tb[i], fma(w0, (double)ta[i], acc[i]));
            if (none) { LASTT(1); } else WAITISSUE(1, "", "v"(w2));
#pragma unroll
            for (int i = 0; i < 3; i++) acc[i] = fma(w3, (double)tb[i], fma(w2, (double)ta[i], acc[i]));
            gu = g >> 5;
            np++;
            if (chg) FLUSH(true)
            GRABBED()
            ADVANCE()
        }

        {
            const int head = blockIdx.x;
            while (lds_ld(&sm.fw_rdy) == 0) SLEEP(2)
            CBAR();
            const unsigned tt = lds_ld(&sm.tag) << 18;
            const float wh = sm.fw[wave + 8 * rsel];
            const unsigned ro = (wave + 8 * rsel) * 512 + sub * 16;
            const char* rb = (const char*)ws + WS_PART + head * (NSPLIT * 512);
            v4f* pb = (v4f*)((char*)&sm.cs[0][0] + 16384);
            __hip_bfloat16* o = out + (size_t)head * DH + 6 * sub + 3 * rsel;
            tg = __builtin_amdgcn_readfirstlane(TU(2 * NU).x) + ((head >> 4) == VENT(15) ? POLC : POLT) * 128;
            GATE("s"(tt), "v"(wh), "v"(ro), "s"(rb), "v"(o))
            v4u R;
#define PLD(A) "global_load_dwordx4 a[" #A ":" #A "+3], %6, %7 sc1\n\t"
#define PCK(A, N, L) "s_waitcnt vmcnt(" #N ")\n\tv_accvgpr_read_b32 %1, a[" #A "+1]\n\tv_accvgpr_read_b32 %3, a[" #A "+3]\n\tv_accvgpr_read_b32 %0, a[" #A "]\n\tv_accvgpr_read_b32 %2, a[" #A "+2]\n\t" \
                     "v_xor_b32 %1, %8, %1\n\tv_xor_b32 %3, %8, %3\n\tv_or_b32 %4, %1, %3\n\tv_cmp_gt_u32 vcc, %9, %4\n\ts_cmp_eq_u64 vcc, exec\n\ts_cbranch_scc1 " L "\n\t"
            { unsigned t_;
              asm volatile("s_cmp_eq_u32 %10, %12\n\ts_cbranch_scc1 1f\n\t" PLD(16) PCK(16, 0, "3f") "1:\n\t" PLD(16) "s_sleep %11\n\t" PLD(20) "s_sleep %11\n\t" PLD(24)
                           "2:\n\t" PCK(16, 2, "3f") PLD(16) PCK(20, 2, "3f") PLD(20) PCK(24, 2, "3f") PLD(24)
                           "v_add_co_u32 %5, vcc, 64, %5\n\ts_cbranch_vccz 2b\n\ts_endpgm\n3:"
                           : "=&v"(R.x), "=&v"(R.y), "=&v"(R.z), "=&v"(R.w), "=&v"(t_), "+v"(spins) : "v"(ro), "s"(rb), "s"(tt), "s"(0x40000u), "s"(head >> 4), "i"(5), "i"(VENT(15)) : "scc", "vcc", "memory"); }
            const float s0 = ldexpf(wh, (int)(R.y >> 10) - 139), s1 = ldexpf(wh, (int)(R.w >> 10) - 139);
            const float f[6] = {s0 * (float)(int)__builtin_amdgcn_sbfe(R.x, 0, 14), s0 * (float)(int)__builtin_amdgcn_sbfe(R.x, 14, 14), s0 * (float)(int)__builtin_amdgcn_sbfe(__builtin_amdgcn_alignbit(R.y, R.x, 28), 0, 14),
                                s1 * (float)(int)__builtin_amdgcn_sbfe(R.z, 0, 14), s1 * (float)(int)__builtin_amdgcn_sbfe(R.z, 14, 14), s1 * (float)(int)__builtin_amdgcn_sbfe(__builtin_amdgcn_alignbit(R.w, R.z, 28), 0, 14)};
            float sv[3];
#pragma unroll
            for (int i = 0; i < 3; i++) {
                const auto sw = __builtin_amdgcn_permlane32_swap(__float_as_uint(f[i]), __float_as_uint(f[i + 3]), false, false);
                sv[i] = __uint_as_float(sw[0]) + __uint_as_float(sw[1]);
            }
            pb[wave * 64 + lane] = (v4f){sv[0], sv[1], sv[2], 0.f};
            CBAR();
            COUNT(grab_adr + 12)
            v4f ps[NSW];
#pragma unroll
            for (int w = 0; w < NSW; w++) ps[w] = pb[w * 64 + lane];
            GRABBED()
            if (g == NSW - 1) {
                const v4f sum = ((ps[0] + ps[1]) + (ps[2] + ps[3])) + ((ps[4] + ps[5]) + (ps[6] + ps[7]));
                o[0] = __float2bfloat16(sum.x); o[1] = __float2bfloat16(sum.y); o[2] = __float2bfloat16(sum.z);
            }
        }
        return;
    }

    if (wave == NSW + 1) {
        u64 E; asm volatile("s_setprio 3\n\ts_load_dwordx2 %0, %1, 0\n\ts_waitcnt lgkmcnt(0)" : "=s"(E) : "s"(ws + WS_EPOCH) : "memory");
        unsigned t2 = (ts + TOF) * 128 + 64 * P0;
        int off = 0, off1 = 0, hold = 0, cum = 0, dp = DP0, sp = 0;
#define NOM(U) (((int)(U) - 2) * 32 * PER)
#define NEWLINE(K) { if (lane >= (K) && lane <= 2 * NU) sm.tu[lane] = (v2u){t2 + NOM(lane) + (cum + (lane - (int)(K)) * dp + (lane > (K) ? sp + off : off1)) * 32, \
                                                                            (unsigned)(PER + dp + (lane == (K) ? sp + off - off1 : 0))}; }
        NEWLINE(2)
        if (lane == 0) sm.tu[1] = (v2u){t2 - 32 * P0, (unsigned)P0};
        const u64 E1 = E + 1;
        const unsigned rtag = (unsigned)E1 * 0x9e3779b1u;
        const bool boss = blockIdx.x == 0;
        u64* const slot = (u64*)(ws + WS_REF + (blockIdx.x & 63) * LSTRIDE + (blockIdx.x >> 6) * 16);
        const u64* const slots = (const u64*)(ws + WS_REF + lane * LSTRIDE);
        u64* const dec = (u64*)(ws + WS_DEC);
#define GLDS(P) __hip_atomic_load(P, __ATOMIC_RELAXED, __HIP_MEMORY_SCOPE_AGENT)
#define GSTS(P, V) __hip_atomic_store(P, V, __ATOMIC_RELAXED, __HIP_MEMORY_SCOPE_AGENT)
#define UNTIL(T) while ((unsigned)((T) - NOW() * 128) < 0x40000u) SLEEP(2)
        if (lane < 2) GSTS(slot + lane, lane ? E1 : (u64)ts | ((u64)rtag << 32));
        if (lane == 0) { sm.tag = (unsigned)E1; CBAR(); lds_st(&sm.tag_rdy, 1u); }
        unsigned dv = 0;
        bool joined = false;
#define JOIN(T0) { const int d0 = min(max((int)((T0) - ts), -1000), 1000); off -= d0 * 4; t2 += d0 * 128; joined = true; }
        unsigned pw_ = 0, tw_ = 0, now, p; int prov = 0;
#define LOOK() { u64 t_; asm volatile("ds_read_b32 %1, %2\n\ts_memrealtime %0\n\ts_waitcnt lgkmcnt(0)" : "=s"(t_), "=v"(p) : "v"(lds_addr(&sm.grab)) : "memory"); \
                 p = __builtin_amdgcn_readfirstlane(p) - 8; now = (unsigned)t_ * 128; }
#define LATENESS(P, T) ({ const v2u e = TU((P) >> 5); (int)((T) - __builtin_amdgcn_readfirstlane(e.x) - ((P) & 31) * __builtin_amdgcn_readfirstlane(e.y)); })
#define WATCH(T) for (;;) { \
            LOOK() \
            if (__builtin_expect(now - tw_ > STH * 128u && pw_ >= 64 && p < NPC - 8, 0)) { \
                const int lw = max(LATENESS(pw_, tw_), 0); \
                if (LATENESS(p, now) - lw > STH * 128) \
                    for (;;) { \
                        const int d_ = min(max(LATENESS(p, now) - lw, 0) >> 5, 1600); \
                        if (lane <= 2 * NU) atomicAdd((unsigned*)&sm.tu[lane], d_ * 32u); \
                        off += d_; prov += d_; hold = 3; \
                        if (p != pw_ || prov > 8000) break; \
                        SLEEP(1) \
                        LOOK() \
                    } \
                } \
            if (p != pw_) { pw_ = p; tw_ = now; } \
            if ((unsigned)((T) - now) >= 0x40000u) break; \
            if ((unsigned)((T) - now) > 25 * 128) SLEEP(7) else SLEEP(1) }
        if (boss) {
            TOUCH((lane & 31) * 128, dec)
            UNTIL((ts + X0) * 128)
            for (;;) {
                bool ok = true; float sum = 0.f;
#pragma unroll
                for (int i = 0; i < 4; i++) { const u64 x = GLDS(slots + 2 * i), e = GLDS(slots + 2 * i + 1); ok &= e == E1 && (unsigned)(x >> 32) == rtag; sum += (float)min(max((int)((unsigned)x - ts), -1000), 1000); }
                if (__builtin_amdgcn_ballot_w64(ok) == ~0ull) { dv = ts + (int)(wave_sum(sum) * (1.f / 256)); break; }
                SLEEP(8)
            }
            if (lane == 0) { GSTS(dec, (u64)dv | ((u64)rtag << 32)); GSTS((u64*)(ws + WS_EPOCH), E1); }
            JOIN(dv)
            dv = 0;
        }
        int sp1 = 0, tot1 = 0, tot2 = 0, lock = 0;
#pragma unroll 1
        for (unsigned k = 2; k < 2 * NU; k++) {
            const unsigned tk = t2 + NOM(k) + cum * 32;
            if (k >= K0 && k + 1 < 2 * NU) {
                WATCH(tk - (XM + XW) * 128)
                int sum = 0, n = 0;
                do { LOOK() sum += LATENESS(p, now) >> 7; n++; } while ((unsigned)(tk - XM * 128 - now) < 0x40000u);
                const int l = (int)((float)sum * __builtin_amdgcn_rcpf((float)n)) - BK + prov / 4;
                if (l > RTH && lane == 0) GSTS((unsigned*)(ws + WS_REF + (blockIdx.x & 63) * LSTRIDE + 64) + (blockIdx.x >> 6), (unsigned)min(l, 1000) | ((rtag + k) << 10));
            }
            const int cum0 = cum;
            bool got = boss;
            if (!boss && (k > K0 || !joined)) {
                WATCH(tk - XR * 128)
                const u64 x = GLDS(dec + (joined ? k * 16 : 0));
                dv = __builtin_amdgcn_readfirstlane((unsigned)x);
                got = __builtin_amdgcn_readfirstlane((unsigned)(x >> 32)) == rtag;
                if (got && !joined) { JOIN(dv) got = false; }
            }
            sp1 = sp; sp = 0;
            if (got && k > K0) { cum = dv >> 18; sp = dv >> 6 & 4095u; dp = dv & 63u; }
            const int yld = cum - cum0 + sp;
            { const int pj = min(prov, cum - cum0); off -= pj; prov -= pj; off1 = off;
              const int ps = min(prov, sp); off -= ps; prov -= ps;
              if (hold) hold--; else prov = 0;
              const int r_ = off - prov; off -= r_ > 40 || r_ < -40 ? r_ / NB : r_; }
            NEWLINE(k)
            cum = min(cum + sp + dp, 16383);
            if (boss) dv = (cum << 18) | dp;
            if (boss && k >= K0 && k + 1 < 2 * NU && (int)(NOW() * 128 - tk) < -60 * 128) {
                WATCH(tk - (XM - XD1) * 128)
                const v4u x = __builtin_amdgcn_raw_buffer_load_b128(__builtin_amdgcn_make_buffer_rsrc(ws + WS_REF + 64, 0, 64 * LSTRIDE, 0x00020000), lane * LSTRIDE, 0, SC1);
                float a[4];
#pragma unroll
                for (int i = 0; i < 4; i++) a[i] = ((x[i] ^ ((rtag + k) << 10)) >> 10) == 0 ? (float)(x[i] & 1023u) : 0.f;
                const int tot = (int)wave_sum(a[0] + a[1] + a[2] + a[3]) >> 6;
                float mn = 0.f;
                if (tot >= 40) {
#pragma unroll 1
                    for (float st = 512.f; st >= 4.f; st *= 0.5f) {
                        const float x_ = mn + st;
                        const int c_ = __builtin_popcountll(__builtin_amdgcn_ballot_w64(a[0] >= x_)) + __builtin_popcountll(__builtin_amdgcn_ballot_w64(a[1] >= x_)) +
                                       __builtin_popcountll(__builtin_amdgcn_ballot_w64(a[2] >= x_)) + __builtin_popcountll(__builtin_amdgcn_ballot_w64(a[3] >= x_));
                        if (c_ >= QN) mn = x_;
                    }
                }
                const float c = mn + (float)CAP, fth = (float)(FTH * (2 * NU - (int)k));
                const int cut = (int)wave_sum(fminf(a[0], c) + fminf(a[1], c) + fminf(a[2], c) + fminf(a[3], c)) >> 6, m4 = (int)mn * 4;
                const bool far = __builtin_amdgcn_ballot_w64(fmaxf(fmaxf(a[0], a[1]), fmaxf(a[2], a[3])) > fth) != 0;
                const int pend = yld + sp1 * XM / 320;
                const int nj = max(m4 - pend, 0);
                const int ns = min(max(max(min(m4 + (tot - m4) * GAIN / 16 - pend - nj, (cut - m4) * GAIN / 16), 0), far ? FLOOR * 4 : 0), 4000);
                int ndp = dp;
                if (k >= (unsigned)KD && tot >= KI && tot > tot1 && tot < tot1 + 32 && tot1 < tot2 + 32) { ndp = min(dp + DPS, 40); lock = LOCK; }
                else if (k >= (unsigned)KD && dp < DP0 && tot >= WARN && tot >= tot1) { ndp = dp + 2; lock = LOCK; }
                else if (lock) lock--;
                if (k >= 6 && tot + tot1 <= QUIET && (!lock || dp > DP0)) ndp = max(dp - 1, 0);
                tot2 = tot1; tot1 = tot;
                dv = (min(cum + nj, 16383) << 18) | (ns << 6) | ndp;
                if ((int)(NOW() * 128 - tk) > XL * 128) dv = (cum << 18) | dp;
                else if (lane == 0) GSTS(dec + (k + 1) * 16, (u64)dv | ((u64)rtag << 32));
            }
        }
        WATCH(p >= NPC - 8 ? now - 128 : now + 4096)
    } else if (wave == NSW + 2) {
        const int ql = lane < 48 ? lane : 47;
        unsigned c1, c2, s1, s2;
#define QT_LOADS() { int qo_ = ql; asm volatile("" : "+v"(qo_)); \
                     c1 = ((const unsigned*)q_cos)[qo_]; c2 = ((const unsigned*)q_cos)[48 + qo_]; s1 = ((const unsigned*)q_sin)[qo_]; s2 = ((const unsigned*)q_sin)[48 + qo_]; }
        unsigned q1, q2, lkz, lks, lvz, lvs;
#define UNIT_LOADS(HEAD) { \
        q1 = ((const unsigned*)(q + (size_t)(HEAD) * DH))[ql]; q2 = ((const unsigned*)(q + (size_t)(HEAD) * DH))[48 + ql]; \
        const size_t so = ((size_t)(HEAD) * SKV + brow0) / 2 + (lane >> 3) * 128 + (lane & 7); \
        lkz = ((const unsigned*)k_zeros)[so]; lks = ((const unsigned*)k_scales)[so]; \
        lvz = ((const unsigned*)v_zeros)[so]; lvs = ((const unsigned*)v_scales)[so]; }
#define UNIT_FILL(SL, U) { \
            v4f qq; \
            qq.x = bfr(bfr(bflo(q1) * bflo(c1)) + bfr(-bflo(q2) * bflo(s1))); \
            qq.y = bfr(bfr(bflo(q2) * bflo(c2)) + bfr(bflo(q1) * bflo(s2))); \
            qq.z = bfr(bfr(bfhi(q1) * bfhi(c1)) + bfr(-bfhi(q2) * bfhi(s1))); \
            qq.w = bfr(bfr(bfhi(q2) * bfhi(c2)) + bfr(bfhi(q1) * bfhi(s2))); \
            *(v2u*)&sm.q[SL][2 * ql] = (v2u){(__float_as_uint(qq.x) >> 16) | __float_as_uint(qq.y), (__float_as_uint(qq.z) >> 16) | __float_as_uint(qq.w)}; \
            *(v4f*)&sm.kz[SL][2 * lane] = (v4f){bflo(lkz), bflo(lks), bfhi(lkz), bfhi(lks)}; \
            *(v4f*)&sm.vz[SL][2 * lane] = (v4f){bflo(lvz), bflo(lvs), bfhi(lvz), bfhi(lvs)}; \
            CBAR(); \
            if (lane == 0) lds_st(&sm.rdy[SL], (U) + 1); \
            CBAR(); }
        asm volatile("s_setprio 3");
        QT_LOADS()
#pragma unroll 1
        for (unsigned u = 0; u < NU; u++) {
            UNIT_LOADS(rank + GSZ * u)
            while (u >= lds_ld(&sm.merged) + NIN) SLEEP(4);
            CBAR();
            UNIT_FILL(u % NIN, u)
            if (u == 1) asm volatile("s_setprio 0");
        }
        while (lds_ld(&sm.merged) < NU) SLEEP(16)
        CBAR();
        const unsigned T = lds_ld(&sm.tag);
        if (lane < NU) __hip_atomic_store((u64*)(ws + WS_LOG + (rank * NSPLIT + split) * LSTRIDE2) + lane, (u64)__float_as_uint(sm.ent[lane][130]) | ((u64)T << 32), __ATOMIC_RELAXED, __HIP_MEMORY_SCOPE_AGENT);
        while (lds_ld(&sm.merged) < NU + 3) SLEEP(16)
        for (;;) {
            const u64 x = __hip_atomic_load((const u64*)(ws + WS_LOG + ((blockIdx.x & 15) * NSPLIT + (lane & 15)) * LSTRIDE2) + (blockIdx.x >> 4), __ATOMIC_RELAXED, __HIP_MEMORY_SCOPE_AGENT);
            if (__builtin_amdgcn_ballot_w64((unsigned)(x >> 32) == T) == ~0ull) {
                const float L = __uint_as_float((unsigned)x);
                const float w = __builtin_amdgcn_exp2f(L - row_max(L));
                sm.fw[lane & 15] = w / row_sum(w);
                CBAR();
                if (lane == 0) lds_st(&sm.fw_rdy, 1u);
                break;
            }
            SLEEP(64)
        }
    } else {
        while (lds_ld(&sm.tag_rdy) == 0) SLEEP(1);
        CBAR();
        const unsigned T = lds_ld(&sm.tag);
        const __amdgpu_buffer_rsrc_t rsrc = __builtin_amdgcn_make_buffer_rsrc(ws + WS_PART + (rank * NSPLIT + split) * 512, 0, NH * NSPLIT * 512, 0x00020000);
        v2u* recs = (v2u*)&sm.cs[0][0];
#pragma unroll
        for (int i = 0; i < NOUT * 3; i++) (&sm.acc[0][0])[i * 64 + lane] = 0.0;
#define RECORD() \
        const float n0 = (float)(a0 - (double)h.y) * h.x, n1 = (float)(a1 - (double)h.y) * h.x, n2 = (float)(a2 - (double)h.y) * h.x; \
        const unsigned e2 = max(__float_as_uint(fmaxf(fmaxf(fabsf(n0), fabsf(n1)), fabsf(n2))) >> 23, 12u); \
        const float f2 = __uint_as_float((266u - e2) << 23); \
        const unsigned m0 = min(max((int)rintf(n0 * f2), -8191), 8191) & 0x3fff, m1 = min(max((int)rintf(n1 * f2), -8191), 8191) & 0x3fff, m2 = min(max((int)rintf(n2 * f2), -8191), 8191) & 0x3fff; \
        const v2u r = {m0 | (m1 << 14) | (m2 << 28), (m2 >> 4) | (e2 << 10) | (T << 18)};
#pragma unroll 1
        for (unsigned u = 0; u < 2 * NU - 1; u++) {
            const unsigned e = u < NU ? u : VENT(u), so = u % NOUT;
            const v4f h = *(const v4f*)&sm.ent[e][128];
            while (lds_ld(&sm.cnt[so]) != 32) SLEEP(1)
            CBAR();
            if (u < NU) {
                const float2 lg = *(const float2*)&sm.ent[e][2 * lane];
                const v4f zs = *(const v4f*)&sm.vz[u % NIN][2 * lane];
                const float m = wave_max(fmaxf(lg.x, lg.y));
                const float p0 = __expf(lg.x - m), p1 = __expf(lg.y - m);
                const float x0 = p0 * zs.y, x1 = p1 * zs.w;
                const unsigned eb = max(__float_as_uint(wave_max(fmaxf(fabsf(x0), fabsf(x1)))) >> 23, 21u);
                const float f = __uint_as_float((275u - eb) << 23);
                const float w0 = rintf(x0 * f), w1 = rintf(x1 * f);
                const float den = wave_sum(p0 + p1);
                const float bz = wave_sum(fmaf(w0, zs.x, w1 * zs.z));
                const float L = fmaf(m, 1.4426950408889634f, __builtin_amdgcn_logf(den));
                *(v2u*)&sm.ent[e][2 * lane] = (v2u){(unsigned)(int)w0, (unsigned)(int)w1};
                if (lane == 0) *(v4f*)&sm.ent[e][128] = (v4f){ldexpf(1.f / den, (int)eb - 148), bz, L, 0.f};
            } else {
                const double a0 = sm.acc[so][lane], a1 = sm.acc[so][64 + lane], a2 = sm.acc[so][128 + lane];
                RECORD()
                recs[(u - NU) * 64 + lane] = r;
                CBAR();
                sm.acc[so][lane] = 0.0; sm.acc[so][64 + lane] = 0.0; sm.acc[so][128 + lane] = 0.0;
            }
            CBAR();
            if (lane == 0) { lds_st(&sm.cnt[so], 0u); CBAR(); lds_st(&sm.merged, u + 1); }
        }
        {
            asm volatile("s_setprio 3");
            wave_sync();
            v4u rr[8]; unsigned po[8];
#pragma unroll
            for (int k = 0; k < 8; k++) { rr[k] = ((const v4u*)recs)[k * 64 + lane]; po[k] = k == 7 && lane >= 32 ? 0x7ffffff0u : (lane & 31) * 16 + (lane < 32 ? VENT(2 * k) : VENT(2 * k + 1)) * (GSZ * NSPLIT * 512); }
            const v4f h = *(const v4f*)&sm.ent[VENT(15)][128];
            asm volatile("" :: "v"(rr[0]), "v"(rr[1]), "v"(rr[2]), "v"(rr[3]), "v"(rr[4]), "v"(rr[5]), "v"(rr[6]), "v"(rr[7]), "v"(po[0]), "v"(po[1]), "v"(po[2]), "v"(po[3]), "v"(po[4]), "v"(po[5]), "v"(po[6]), "v"(po[7]), "v"(h));
            for (;;) {
                const unsigned d = __builtin_amdgcn_readfirstlane(TU(2 * NU).x) + PUBT * 128 - NOW() * 128;
                if (d >= 0x40000u) break;
                if (d > 20 * 128) SLEEP(4)
            }
#pragma unroll
            for (int k = 0; k < 8; k++) __builtin_amdgcn_raw_buffer_store_b128(rr[k], rsrc, po[k], 0, SC1);
            double a0, a1, a2;
            for (;;) {
                const unsigned c = __hip_atomic_load(&sm.cnt[NOUT - 1], __ATOMIC_RELAXED, __HIP_MEMORY_SCOPE_WORKGROUP);
                CBAR();
                a0 = sm.acc[NOUT - 1][lane]; a1 = sm.acc[NOUT - 1][64 + lane]; a2 = sm.acc[NOUT - 1][128 + lane];
                CBAR();
                if (__builtin_amdgcn_readfirstlane(c) == 32) break;
                SLEEP(0)
            }
            RECORD()
            const v4u r2 = {r.x, r.y, (unsigned)__builtin_amdgcn_update_dpp(0, (int)r.x, 0xf5, 0xf, 0xf, true), (unsigned)__builtin_amdgcn_update_dpp(0, (int)r.y, 0xf5, 0xf, 0xf, true)};
            __builtin_amdgcn_raw_buffer_store_b128(r2, rsrc, (lane & 1) ? 0x7ffffff0u : lane * 8, VENT(15) * (GSZ * NSPLIT * 512), SC1);
        }
        return;
    }

}

extern "C" void kernel_launch(void* const* d_in, const int* in_sizes, int n_in,
                              void* d_out, int out_size, void* d_ws, size_t ws_size,
                              hipStream_t stream) {
    const unsigned short* q = (const unsigned short*)d_in[0];
    const int* qk = (const int*)d_in[1];
    const unsigned short* k_scales = (const unsigned short*)d_in[2];
    const unsigned short* k_zeros = (const unsigned short*)d_in[3];
    const int* qv = (const int*)d_in[4];
    const unsigned short* v_scales = (const unsigned short*)d_in[5];
    const unsigned short* v_zeros = (const unsigned short*)d_in[6];
    const unsigned short* q_cos = (const unsigned short*)d_in[7];
    const unsigned short* q_sin = (const unsigned short*)d_in[8];
    const unsigned short* k_cos = (const unsigned short*)d_in[9];
    const unsigned short* k_sin = (const unsigned short*)d_in[10];
    unsigned char* ws = (unsigned char*)d_ws;
    attn_kernel<<<NSPLIT * GSZ, NTHREADS, 0, stream>>>(q, qk, k_scales, k_zeros, qv, v_scales, v_zeros,
                                                       q_cos, q_sin, k_cos, k_sin, (__hip_bfloat16*)d_out, ws);
}
```

```cpp
#include <hip/hip_runtime.h>
#include <hip/hip_bf16.h>
#include <stdint.h>

#define DH 192
#define HALF 96
#define SKV 2048
#define NH 256
#define NSW 8
#define NTHREADS (64 * (NSW + 4))
#define BROWS 128
#define NSPLIT (SKV / BROWS)
#define GSZ 16
#define NU (NH / GSZ)
#define NPC (2 * NU * 32)
#define NIN 4
#define NOUT 4
#define ENTW 132
#define SC1 16
#define PHI 0x0246024671357135ull
#define VORD 0x9876543210afedcbull
#define VENT(V) ((unsigned)(VORD >> (4 * ((V) & 15))) & 15u)
#define PFL 195
#define PER 1262
#define DP0 6
#define P0 1370
#define TOF 28
#define NBW 2
#define BTH 20
#define X0 250
#define K0 3
#define XM 250
#define XW 40
#define XD1 85
#define XL 90
#define XR 170
#define BK 3
#define RTH 6
#define QN 224
#define GAIN 24
#define CAP 45
#define FLOOR 3
#define FTH 15
#define KD 9
#define KI 14
#define DPS 2
#define QUIET 2
#define WARN 5
#define LOCK 6
#define NB 3
#define STH 90
#define SFIX 220
#define SNOT 0x610c0cu
#define HOLD 4
#define SMIN 100
#define SADD 8
#define PUBT 40
#define POLT 100
#define POLC 110

#define WS_EPOCH 0
#define WS_DEC 8192
#define WS_REF 16384
#define LSTRIDE 4352
#define WS_LOG 524288
#define LSTRIDE2 1408
#define WS_PART 1048576

typedef unsigned int v2u __attribute__((ext_vector_type(2)));
typedef unsigned int v4u __attribute__((ext_vector_type(4)));
typedef int v4i __attribute__((ext_vector_type(4)));
typedef float v2f __attribute__((ext_vector_type(2)));
typedef float v4f __attribute__((ext_vector_type(4)));
typedef unsigned long long u64;

__device__ __forceinline__ float bflo(unsigned u) { return __uint_as_float(u << 16); }
__device__ __forceinline__ float bfhi(unsigned u) { return __uint_as_float(u & 0xffff0000u); }
__device__ __forceinline__ float bfr(float x) {
    __bf16 b = (__bf16)x;
    return __uint_as_float(((unsigned)__builtin_bit_cast(unsigned short, b)) << 16);
}

#define SLEEP(n) SLEEP_(n)
#define SLEEP_(n) asm volatile("s_sleep " #n "\n\tv_add_co_u32 %0, vcc, 1, %0\n\ts_cbranch_vccz 1f\n\ts_endpgm\n1:" : "+v"(spins) :: "vcc", "memory");
#define SPIN0 0xf0000000u
#define CBAR() asm volatile("" ::: "memory")

__device__ __forceinline__ void wave_sync() {
    __builtin_amdgcn_fence(__ATOMIC_RELEASE, "wavefront");
    __builtin_amdgcn_wave_barrier();
    __builtin_amdgcn_fence(__ATOMIC_ACQUIRE, "wavefront");
}
template <int CTRL>
__device__ __forceinline__ float dpp_add(float x) {
    int y = __builtin_amdgcn_update_dpp(0, __float_as_int(x), CTRL, 0xf, 0xf, true);
    return x + __int_as_float(y);
}
template <int CTRL>
__device__ __forceinline__ float dpp_max(float x) {
    int y = __builtin_amdgcn_update_dpp(__float_as_int(x), __float_as_int(x), CTRL, 0xf, 0xf, false);
    return fmaxf(x, __int_as_float(y));
}
__device__ __forceinline__ float row_sum(float x) { x = dpp_add<0x128>(x); x = dpp_add<0x124>(x); x = dpp_add<0x122>(x); return dpp_add<0x121>(x); }
__device__ __forceinline__ float row_max(float m) { m = dpp_max<0x128>(m); m = dpp_max<0x124>(m); m = dpp_max<0x122>(m); return dpp_max<0x121>(m); }
__device__ __forceinline__ float rdlane(float x, int l) { return __int_as_float(__builtin_amdgcn_readlane(__float_as_int(x), l)); }
__device__ __forceinline__ float wave_max(float m) { m = row_max(m); return fmaxf(fmaxf(rdlane(m, 0), rdlane(m, 16)), fmaxf(rdlane(m, 32), rdlane(m, 48))); }
__device__ __forceinline__ float wave_sum(float x) { x = row_sum(x); return (rdlane(x, 0) + rdlane(x, 16)) + (rdlane(x, 32) + rdlane(x, 48)); }
__device__ __forceinline__ unsigned lds_ld(const unsigned* p) { return __builtin_amdgcn_readfirstlane(__hip_atomic_load(p, __ATOMIC_RELAXED, __HIP_MEMORY_SCOPE_WORKGROUP)); }
__device__ __forceinline__ unsigned lds_addr(const void* p) { return (unsigned)(size_t)(const __attribute__((address_space(3))) void*)p; }
#define DMA16(SRC, LDS) asm volatile("s_mov_b32 %0, m0\n\ts_mov_b32 m0, %2\n\ts_nop 0\n\tglobal_load_lds_dwordx4 %1, off\n\ts_mov_b32 m0, %0" : "=&s"(m0keep) : "v"(SRC), "s"(LDS) : "memory")
__device__ __forceinline__ void lds_st(unsigned* p, unsigned v) { __hip_atomic_store(p, v, __ATOMIC_RELAXED, __HIP_MEMORY_SCOPE_WORKGROUP); }
#define NOW() ((unsigned)__builtin_amdgcn_s_memrealtime())

struct __attribute__((aligned(16))) Smem {
    unsigned cs[32][768];
    double acc[NOUT][DH];
    float ent[NU][ENTW];
    unsigned q[NIN][HALF];
    float2 kz[NIN][BROWS];
    float2 vz[NIN][BROWS];
    v2u tu[2 * NU + 2];
    unsigned grab, merged, tabs, cdone;
    unsigned rdy[NIN], cnt[NOUT];
    unsigned tag_rdy, tag, start, fw_rdy;
    float fw[NSPLIT];
};

#define TU(I) (*(const volatile v2u __attribute__((address_space(3)))*)(size_t)lds_addr(&sm.tu[I]))
#define LDS_ADD(P, V) { u64 ex_; asm volatile("s_mov_b64 %0, exec\n\ts_mov_b64 exec, 1\n\tds_add_u32 %1, %2\n\ts_mov_b64 exec, %0" : "=&s"(ex_) : "v"(lds_addr(P)), "v"(V) : "memory"); }
#define LDS_OR(P, V) { u64 ex_; asm volatile("s_mov_b64 %0, exec\n\ts_mov_b64 exec, 1\n\tds_or_b32 %1, %2\n\ts_mov_b64 exec, %0" : "=&s"(ex_) : "v"(lds_addr(P)), "v"(V) : "memory"); }

__global__ __launch_bounds__(NTHREADS, 3) void attn_kernel(
    const unsigned short* __restrict__ q, const int* __restrict__ qk,
    const unsigned short* __restrict__ k_scales, const unsigned short* __restrict__ k_zeros,
    const int* __restrict__ qv,
    const unsigned short* __restrict__ v_scales, const unsigned short* __restrict__ v_zeros,
    const unsigned short* __restrict__ q_cos, const unsigned short* __restrict__ q_sin,
    const unsigned short* __restrict__ k_cos, const unsigned short* __restrict__ k_sin,
    __hip_bfloat16* __restrict__ out, unsigned char* __restrict__ ws)
{
    __shared__ Smem sm;
    const int tid = threadIdx.x;
    unsigned spins = SPIN0;
#define TOUCH(OFF, BASE) asm volatile("global_load_dword a6, %0, %1" :: "v"(OFF), "s"(BASE) : "a6", "memory");
    const unsigned cl = min(((blockIdx.x >> 3) & 15) * 16 + (tid & 15), PFL - 1u) * 128;
    if (tid < 64) { const char* pc0; asm volatile("s_getpc_b64 %0" : "=s"(pc0)); TOUCH(cl, pc0) }
    __builtin_amdgcn_sched_barrier(0);
    const int hw = __builtin_amdgcn_readfirstlane(tid >> 6);
    const int lane = tid & 63;
    const int split = (blockIdx.x & 3) * 4 + ((blockIdx.x >> 3) & 3);
    const int rank = (blockIdx.x >> 5) * 2 + ((blockIdx.x >> 2) & 1);
    const int brow0 = split * 16;
#define ROWOF(RG) (((RG) >> 2) * 256 + ((RG) & 3) * 4)
    if (hw == 0) {
        typedef const char* const __attribute__((address_space(4))) * kap_t;
        const char* sa = ((kap_t)__builtin_amdgcn_kernarg_segment_ptr())[(0x5623 >> (4 * (split & 3))) & 7];
        unsigned o2 = ((rank + GSZ * (lane >> 3)) * SKV + (lane & 7) * 256 + (brow0 & ~63)) * 2, o3 = o2 + 8 * GSZ * SKV * 2;
        unsigned o4 = (rank + GSZ * ((lane >> 2 & 3) + 4 * (split & 3))) * (DH * 2) + min(lane & 3, 2) * 128;
        asm volatile("" : "+v"(o2), "+v"(o3), "+v"(o4));
        asm volatile("global_load_dword a6, %0, %1 nt\n\tglobal_load_dword a6, %0, %2 nt" :: "v"((lane & 1) * (NH * SKV * DH * 4u - 128u * NH) + blockIdx.x * 128), "s"(qk), "s"(qv) : "a6", "memory");
        TOUCH(o4, q) TOUCH(o2, sa) TOUCH(o3, sa)
        asm volatile("s_barrier\n\ts_waitcnt vmcnt(1)" ::: "memory");
        if (lane == 0) lds_st(&sm.start, NOW() | 1u);
        asm volatile("s_endpgm");
    }
    if (hw == 1 && lane < 16) (&sm.grab)[lane] = lane ? 0 : 64;
    asm volatile("" :: "s"(q), "s"(qk), "s"(k_scales), "s"(k_zeros), "s"(qv), "s"(v_scales), "s"(v_zeros), "s"(q_cos), "s"(q_sin), "s"(k_cos), "s"(k_sin), "s"(out), "s"(ws));
    asm volatile("s_waitcnt lgkmcnt(0)\n\ts_barrier" ::: "memory");
    const int wave = (int)(0x76543210b9a8ull >> (4 * hw)) & 15;
    unsigned ts;
#define START() { while ((ts = lds_ld(&sm.start)) == 0) SLEEP(1) }

    if (__builtin_expect(wave < NSW, 1)) {
        const unsigned rot = 4 * ((unsigned)(PHI >> (60 - 4 * rank)) & 7u);
#define RGOF(G) ((((G) + rot) & 31u) ^ ((((G) + rot) >> 1) & 2u))
        const char* cbase[3];
#pragma unroll
        for (int j = 0; j < 3; j++) { const int x = j * 64 + lane; cbase[j] = (x < 96 ? (const char*)k_cos + x * 16 : (const char*)k_sin + (x - 96) * 16) + brow0 * (DH * 2); }
        unsigned m0keep;
#define CS_DMA(RG) { _Pragma("unroll") for (int j = 0; j < 3; j++) DMA16(cbase[j] + ROWOF(RG) * (DH * 2), lds_addr(&sm.cs[0][0]) + (RG) * 3072 + j * 1024); }
#define REPACK(RG) { \
        unsigned* cw = &sm.cs[RG][0]; \
        const unsigned* l = cw + rsel * (DH / 2) + qoff; \
        unsigned r[2][6]; \
        _Pragma("unroll") \
        for (int j = 0; j < 2; j++) { \
            unsigned e[4][2]; \
            _Pragma("unroll") \
            for (int k = 0; k < 4; k++) { \
                const unsigned* a = l + j * DH + (k & 1) * 48 + (k >> 1) * 384;       \
                unsigned x0 = a[0], x1 = a[1]; \
                e[k][0] = __builtin_amdgcn_alignbit(x1, x0, sh); \
                e[k][1] = x1 >> sh; \
            } \
            r[j][0] = (e[0][0] & 0xffffu) | (e[3][0] << 16); \
            r[j][1] = (e[0][0] >> 16) | (e[3][0] & 0xffff0000u); \
            r[j][2] = (e[0][1] & 0xffffu) | (e[3][1] << 16); \
            r[j][3] = (e[1][0] & 0xffffu) | (e[2][0] << 16); \
            r[j][4] = (e[1][0] >> 16) | (e[2][0] & 0xffff0000u); \
            r[j][5] = (e[1][1] & 0xffffu) | (e[2][1] << 16); \
        } \
        wave_sync(); \
        _Pragma("unroll") \
        for (int j = 0; j < 2; j++) { *(v4u*)&cw[j * 256 + lane * 4] = (v4u){r[j][0], r[j][1], r[j][2], r[j][3]}; *(v2u*)&cw[512 + j * 128 + lane * 2] = (v2u){r[j][4], r[j][5]}; } \
        wave_sync(); \
        LDS_OR(&sm.tabs, 1u << (RG)) }
        const int sub = lane & 31;
        const int rsel = lane >> 5;
        const unsigned sh = (sub & 1) * 16;
        const int qoff = 3 * (sub >> 1) + (sub & 1);
        const char* kbase = (const char*)qk + (size_t)(rank * SKV + brow0) * (DH * 4);
        const char* vbase = (const char*)qv + (size_t)(rank * SKV + brow0) * (DH * 4);
        const unsigned koff = (rsel * DH + 3 * sub) * 4, voff = 3 * lane * 4;
#define AR(S) (16 + (S) * 8)
        asm volatile("" ::: "a16", "a17", "a18", "a19", "a20", "a21", "a22", "a23", "a24", "a25", "a26", "a27", "a28", "a29", "a30", "a31",
                            "a32", "a33", "a34", "a35", "a36", "a37", "a38", "a39", "a40", "a41", "a42", "a43", "a44", "a45", "a46", "a47");
        asm volatile("" ::: "a48", "a49", "a50", "a51", "a52", "a53", "a54", "a55", "a56", "a57", "a58", "a59", "a60", "a61", "a62", "a63");
        __builtin_amdgcn_s_waitcnt(0x0f70);
        int ta[3], tb[3];
#define GLD(A, OFF, BASE, IMM) asm volatile("global_load_dwordx3 a[%c0:%c1], %2, %3 offset:%4 nt" :: "i"(A), "i"((A) + 2), "v"(OFF), "s"(BASE), "i"(IMM))
#define ISSUE(S, J, BASE, OA, OB) { GLD(AR(S), OA, BASE, (J) * 1536); GLD(AR(S) + 4, OB, BASE, (J) * 1536); }
#define WAITT(S, N) asm volatile("s_waitcnt vmcnt(%6)\n\tv_accvgpr_read_b32 %0, a[%c7]\n\tv_accvgpr_read_b32 %1, a[%c8]\n\tv_accvgpr_read_b32 %2, a[%c9]\n\t" \
                              "v_accvgpr_read_b32 %3, a[%c10]\n\tv_accvgpr_read_b32 %4, a[%c11]\n\tv_accvgpr_read_b32 %5, a[%c12]" \
                              : "=v"(ta[0]), "=v"(ta[1]), "=v"(ta[2]), "=v"(tb[0]), "=v"(tb[1]), "=v"(tb[2]) \
                              : "i"(N), "i"(AR(S)), "i"(AR(S) + 1), "i"(AR(S) + 2), "i"(AR(S) + 4), "i"(AR(S) + 5), "i"(AR(S) + 6))
        const unsigned koffb = koff + 384, voffb = voff + 768;
        unsigned u = 0, rg, np = 0;
#define RG0(C) RGOF((unsigned)(wave + 8 * (C)))
        CS_DMA(RG0(0))
        const float scale = 0.07216878364870322f;
        double acc[3] = {0.0, 0.0, 0.0};
        bool chg, none = false;
        unsigned Qp[3], Qn[3];
        float kz4[2], ksc, kres[2];
        v4u ca[2]; v2u cb[2];
        v2f qe[2][3], A[2];
        const char* nb; unsigned oa, ob;
        unsigned g, gu, mg, nrg, tg, dg, par = 0;
        int early;
        const int bth = wave < NBW ? BTH * 128 : 0x3fffffff;
        const unsigned grab_adr = lds_addr(&sm.grab);
        asm volatile("v_accvgpr_write_b32 a13, 1" ::: "a13", "a15");
#define COUNT(ADR) { u64 ex_; asm volatile("s_mov_b64 %0, exec\n\ts_mov_b64 exec, 1\n\tds_add_rtn_u32 a15, %1, a13\n\ts_mov_b64 exec, %0" : "=&s"(ex_) : "v"(ADR) : "memory"); }
#define GRABBED() { unsigned t_; asm volatile("s_waitcnt lgkmcnt(0)\n\tv_accvgpr_read_b32 %1, a15\n\ts_nop 0\n\tv_readfirstlane_b32 %0, %1\n\ts_nop 1" : "=s"(g), "=v"(t_)); }
#define NEXT1() const v2u x_ = TU(g >> 5); u64 tn_; asm volatile("s_memrealtime %0" : "=s"(tn_));
#define NEXT2() { \
            chg = (g >> 5) != u; none = g >= NPC; \
            const bool nv = g >= NPC / 2; \
            nrg = RGOF(g); \
            unsigned boff; asm("s_mul_i32 %0, %1, 0x300" : "=s"(boff) : "s"((nv ? VENT(g >> 5) : g >> 5) * (GSZ * SKV) + ROWOF(nrg) + 2)); \
            nb = (nv ? vbase : kbase) + boff; \
            oa = nv ? voff : koff; ob = nv ? voffb : koffb; \
            asm volatile("s_waitcnt lgkmcnt(0)" : "+s"(tn_) : "s"(nb), "v"(oa), "v"(ob)); \
            tg = __builtin_amdgcn_readfirstlane(x_.x) + (g & 31) * __builtin_amdgcn_readfirstlane(x_.y); \
            early = none ? 0 : (int)((unsigned)tn_ * 128 - tg) - bth; \
            if (early > 0) asm volatile("s_cmp_lg_u32 %3, 0\n\ts_cbranch_scc1 1f\n\t" L4(32) "\ts_branch 2f\n1:\n\t" L4(16) "2:\n\ts_mov_b64 vcc, exec\n\ts_mov_b64 exec, 1\n\tds_add_rtn_u32 a15, %4, a13\n\ts_mov_b64 exec, vcc" \
                                    :: "v"(oa), "v"(ob), "s"(nb), "s"(pr), "v"(grab_adr) : "scc", "vcc", "memory"); }
#define L4(A) "global_load_dwordx3 a[" #A ":" #A "+2], %0, %2 offset:-1536 nt\n\tglobal_load_dwordx3 a[" #A "+4:" #A "+6], %1, %2 offset:-1536 nt\n\t" \
              "global_load_dwordx3 a[" #A "+8:" #A "+10], %0, %2 nt\n\tglobal_load_dwordx3 a[" #A "+12:" #A "+14], %1, %2 nt\n"
#define GATE(...) \
            asm volatile("s_setprio 0\n1:\n\ts_memrealtime vcc\n\ts_waitcnt lgkmcnt(0)\n\ts_lshl_b32 vcc_lo, vcc_lo, 7\n\ts_sub_u32 %0, %1, vcc_lo\n\ts_cmp_lt_u32 %0, 0x40000\n\ts_cbranch_scc0 2f\n\t" \
                         "s_cmp_lt_u32 %0, %2\n\ts_cbranch_scc1 1b\n\ts_sleep %3\n\ts_branch 1b\n2:\n\ts_setprio 2" : "=&s"(dg) : "s"(tg), "i"(10 * 128), "i"(2), __VA_ARGS__ : "scc", "vcc", "memory");
#define RD6(A) "v_accvgpr_read_b32 %0, a[%c6+" #A "]\n\tv_accvgpr_read_b32 %1, a[%c6+" #A "+1]\n\tv_accvgpr_read_b32 %2, a[%c6+" #A "+2]\n\t" \
               "v_accvgpr_read_b32 %3, a[%c6+" #A "+4]\n\tv_accvgpr_read_b32 %4, a[%c6+" #A "+5]\n\tv_accvgpr_read_b32 %5, a[%c6+" #A "+6]\n"
#define LD2(A) "global_load_dwordx3 a[%c6+" #A ":%c6+" #A "+2], %7, %9 offset:%10 nt\n\tglobal_load_dwordx3 a[%c6+" #A "+4:%c6+" #A "+6], %8, %9 offset:%10 nt"
#define OUT6 "=&v"(ta[0]), "=&v"(ta[1]), "=&v"(ta[2]), "=&v"(tb[0]), "=&v"(tb[1]), "=&v"(tb[2])
#define WI(A, B, TAIL) "s_cmp_gt_i32 %13, 0\n\ts_cbranch_scc1 3f\n\ts_waitcnt vmcnt(2)\n\t" LD2(A) TAIL "\n3:\n\ts_waitcnt vmcnt(%c14)\n\t" RD6(B)
#define WAITISSUE(S, TAIL, ...) asm volatile("s_cmp_lg_u32 %12, 0\n\ts_cbranch_scc1 1f\n\t" WI(16, 0, TAIL) "\ts_branch 2f\n1:\n\t" WI(0, 16, TAIL) "2:" \
                              : OUT6 : "i"(AR(S)), "v"(oa), "v"(ob), "s"(nb), "i"(((S) - 1) * 1536), "v"(grab_adr), "s"(pr), "s"(early), "i"(6 - 2 * (S)), __VA_ARGS__ : "scc", "vcc", "memory")
#define LASTT(S) asm volatile("s_waitcnt vmcnt(%c8)\n\ts_cmp_lg_u32 %7, 0\n\ts_cbranch_scc1 1f\n\t" RD6(0) "\ts_branch 2f\n1:\n\t" RD6(16) "2:" : OUT6 : "i"(AR(S)), "s"(pr), "i"(2 - 2 * (S)) : "scc")
#define DRAW "\n\ts_mov_b64 vcc, exec\n\ts_mov_b64 exec, 1\n\tds_add_rtn_u32 a15, %11, a13\n\ts_mov_b64 exec, vcc"
#define K_TAB() { _Pragma("unroll") for (int p = 0; p < 2; p++) { \
            const unsigned* cw = &sm.cs[rg][0]; \
            ca[p] = *(const v4u*)&cw[p * 256 + lane * 4]; cb[p] = *(const v2u*)&cw[512 + p * 128 + lane * 2]; } }
#define K_PRE(P) { \
        float e0, e1, e2, f0, f1, f2; \
        asm("v_dot2_f32_bf16 %0, %6, %12, 0\n\tv_dot2_f32_bf16 %3, %9, %15, 0\n\tv_dot2_f32_bf16 %1, %7, %13, 0\n\tv_dot2_f32_bf16 %4, %10, %16, 0\n\t" \
            "v_dot2_f32_bf16 %2, %8, %14, 0\n\tv_dot2_f32_bf16 %5, %11, %17, 0\n\ts_nop 2" \
            : "=&v"(e0), "=&v"(e1), "=&v"(e2), "=&v"(f0), "=&v"(f1), "=&v"(f2) \
            : "v"(Qp[0]), "v"(Qp[1]), "v"(Qp[2]), "v"(Qn[0]), "v"(Qn[1]), "v"(Qn[2]), \
              "v"(ca[P].x), "v"(ca[P].y), "v"(ca[P].z), "v"(ca[P].w), "v"(cb[P].x), "v"(cb[P].y)); \
        qe[P][0] = (v2f){e0, f0}; qe[P][1] = (v2f){e1, f1}; qe[P][2] = (v2f){e2, f2}; \
        A[P] = (qe[P][0] + qe[P][1] + qe[P][2]) * (v2f){-kz4[P], -kz4[P]}; }
#define K_SUM(P) { \
        _Pragma("unroll") \
        for (int i = 0; i < 3; i++) { v2f x = {(float)ta[i], (float)tb[i]}; A[P] = qe[P][i] * x + A[P]; } \
        kres[P] = A[P].x + A[P].y; }
#define K_RED2() { \
        const bool o1 = lane & 1; \
        float sx = (o1 ? kres[1] : kres[0]) + __int_as_float(__builtin_amdgcn_update_dpp(0, __float_as_int(o1 ? kres[0] : kres[1]), 0xb1, 0xf, 0xf, true)); \
        sx = dpp_add<0x4e>(sx); sx = dpp_add<0x124>(sx); sx = dpp_add<0x128>(sx); \
        const auto sw = __builtin_amdgcn_permlane16_swap(__float_as_uint(sx), __float_as_uint(sx), false, false); \
        sx = __uint_as_float(sw[0]) + __uint_as_float(sw[1]); \
        if ((lane & 30) == 0) sm.ent[u][rg * 4 + 2 * (lane & 1) + rsel] = sx * ksc; }
#define LDV(P) __hip_atomic_load(P, __ATOMIC_RELAXED, __HIP_MEMORY_SCOPE_WORKGROUP)
#define RDYWAIT(SL, SEQ) { while (lds_ld(&sm.rdy[SL]) != (SEQ)) SLEEP(1); CBAR(); }
#define LOADZ() { _Pragma("unroll") for (int p = 0; p < 2; p++) kz4[p] = sm.kz[u % NIN][rg * 4 + 2 * p + rsel].x; ksc = sm.kz[u % NIN][rg * 4 + 2 * (lane & 1) + rsel].y * scale; }
#define LOADQ() { _Pragma("unroll") for (int i = 0; i < 3; i++) { Qp[i] = sm.q[u % NIN][3 * sub + i]; Qn[i] = __builtin_amdgcn_alignbit(Qp[i], Qp[i], 16) ^ 0x80000000u; } }
#define FLUSH(ISV) { \
            mg = __builtin_amdgcn_readfirstlane(mg); \
            while (u >= mg + NOUT) { SLEEP(1); mg = lds_ld(&sm.merged); } \
            CBAR(); \
            if (ISV) { const unsigned aa = lds_addr(&sm.acc[u % NOUT][lane]); \
                asm volatile("ds_add_f64 %0, %1\n\tds_add_f64 %0, %2 offset:512\n\tds_add_f64 %0, %3 offset:1024" :: "v"(aa), "v"(acc[0]), "v"(acc[1]), "v"(acc[2]) : "memory"); \
                acc[0] = 0.0; acc[1] = 0.0; acc[2] = 0.0; } \
            CBAR(); \
            LDS_ADD(&sm.cnt[u % NOUT], np) \
            CBAR(); \
            np = 0; }
#define ADVANCE() { u = gu; rg = nrg; }

#define REQ0(N, S) { tg = (ts + TOF) * 128 + (wave + 8 * (N)) * P0; \
            const char* kb = kbase + (((N) >> 2) * (GSZ * SKV) + ROWOF(RG0((N) & 3))) * (DH * 4); GATE("s"(kb)) ISSUE(S, 0, kb, koff, koffb) ISSUE((S) + 1, 1, kb, koff, koffb) }
#define USE0(N, S, W) { u = (N) >> 2; rg = RG0((N) & 3); K_TAB() LOADQ() LOADZ() K_PRE(0) K_PRE(1) WAITT(S, W); K_SUM(0) WAITT((S) + 1, W); K_SUM(1) K_RED2() }
#define VMW(N) asm volatile("s_waitcnt vmcnt(" #N ")" ::: "memory");
        START()
        REQ0(0, 0) CS_DMA(RG0(1))
        VMW(7) REPACK(RG0(0))
        REQ0(1, 2)
        RDYWAIT(0, 1u)
        USE0(0, 0, 7)
        VMW(4) REPACK(RG0(1))
        REQ0(2, 0) CS_DMA(RG0(2))
        USE0(1, 2, 7)
        REQ0(3, 2)
        VMW(4) REPACK(RG0(2))
        USE0(2, 0, 4)
        REQ0(4, 0) CS_DMA(RG0(3))
        REQ0(5, 4)
        VMW(4) REPACK(RG0(3))
        USE0(3, 2, 4)
        CBAR();
        LDS_ADD(&sm.cnt[0], 4u)
        REQ0(6, 2)
        RDYWAIT(1, 2u)
        USE0(4, 0, 8)
        USE0(5, 4, 4)
        REQ0(7, 0)
        COUNT(grab_adr)
        USE0(6, 2, 4)
        rg = RG0(3); np = 3;
        GRABBED()
#pragma unroll 1
        while (u < NU) {
            const unsigned pr = __builtin_amdgcn_readfirstlane(par); par ^= 1;
            NEXT1()
            const unsigned tb_ = LDV(&sm.tabs), fl = LDV(&sm.rdy[u % NIN]);
            mg = LDV(&sm.merged);
            CBAR();
            K_TAB() LOADQ() LOADZ()
            NEXT2()
            if (__builtin_expect(__builtin_amdgcn_readfirstlane(fl) != u + 1 || !((__builtin_amdgcn_readfirstlane(tb_) >> rg) & 1u), 0)) {
                while (!((lds_ld(&sm.tabs) >> rg) & 1u)) SLEEP(1)
                RDYWAIT(u % NIN, u + 1)
                K_TAB() LOADQ() LOADZ()
            }
            K_PRE(0) K_PRE(1)
            if (early <= 0) GATE("v"(A[0]), "v"(A[1]), "v"(oa), "v"(ob), "s"(nb), "s"(pr))
            WAITISSUE(0, DRAW, "v"(A[0]));
            K_SUM(0)
            WAITISSUE(1, "", "v"(A[1]));
            K_SUM(1)
            gu = g >> 5;
            GRABBED()
            K_RED2()
            np++;
            if (chg) FLUSH(false)
            ADVANCE()
        }
#pragma unroll 1
        while (u < 2 * NU) {
            const unsigned pr = __builtin_amdgcn_readfirstlane(par); par ^= 1;
            NEXT1()
            mg = LDV(&sm.merged);
            CBAR();
            const unsigned ve = VENT(u);
            v4i pw = *(const v4i*)&sm.ent[ve][rg * 4];
            NEXT2()
            if (__builtin_expect(__builtin_amdgcn_readfirstlane(mg) <= ve, 0)) { while (lds_ld(&sm.merged) <= ve) SLEEP(1); CBAR(); pw = *(const v4i*)&sm.ent[ve][rg * 4]; }
            const double w0 = pw.x, w1 = pw.y, w2 = pw.z, w3 = pw.w;
            if (none) { LASTT(0); } else { if (early <= 0) GATE("s"(pr), "v"(w0), "v"(w1), "v"(w2), "v"(w3), "v"(oa), "v"(ob), "s"(nb)) WAITISSUE(0, DRAW, "v"(w0)); }
#pragma unroll
            for (int i = 0; i < 3; i++) acc[i] = fma(w1, (double)tb[i], fma(w0, (double)ta[i], acc[i]));
            if (none) { LASTT(1); } else WAITISSUE(1, "", "v"(w2));
#pragma unroll
            for (int i = 0; i < 3; i++) acc[i] = fma(w3, (double)tb[i], fma(w2, (double)ta[i], acc[i]));
            gu = g >> 5;
            np++;
            if (chg) FLUSH(true)
            GRABBED()
            ADVANCE()
        }

        {
            const int head = blockIdx.x;
            while (lds_ld(&sm.fw_rdy) == 0) SLEEP(2)
            CBAR();
            const unsigned tt = lds_ld(&sm.tag) << 18;
            const float wh = sm.fw[wave + 8 * rsel];
            const unsigned ro = (wave + 8 * rsel) * 512 + sub * 16;
            const char* rb = (const char*)ws + WS_PART + head * (NSPLIT * 512);
            v4f* pb = (v4f*)((char*)&sm.cs[0][0] + 16384);
            __hip_bfloat16* o = out + (size_t)head * DH + 6 * sub + 3 * rsel;
            tg = __builtin_amdgcn_readfirstlane(TU(2 * NU).x) + ((head >> 4) == VENT(15) ? POLC : POLT) * 128;
            GATE("s"(tt), "v"(wh), "v"(ro), "s"(rb), "v"(o))
            v4u R;
#define PLD(A) "global_load_dwordx4 a[" #A ":" #A "+3], %6, %7 sc1\n\t"
#define PCK(A, N, L) "s_waitcnt vmcnt(" #N ")\n\tv_accvgpr_read_b32 %1, a[" #A "+1]\n\tv_accvgpr_read_b32 %3, a[" #A "+3]\n\tv_accvgpr_read_b32 %0, a[" #A "]\n\tv_accvgpr_read_b32 %2, a[" #A "+2]\n\t" \
                     "v_xor_b32 %1, %8, %1\n\tv_xor_b32 %3, %8, %3\n\tv_or_b32 %4, %1, %3\n\tv_cmp_gt_u32 vcc, %9, %4\n\ts_cmp_eq_u64 vcc, exec\n\ts_cbranch_scc1 " L "\n\t"
            { unsigned t_;
              asm volatile("s_cmp_eq_u32 %10, %12\n\ts_cbranch_scc1 1f\n\t" PLD(16) PCK(16, 0, "3f") "1:\n\t" PLD(16) "s_sleep %11\n\t" PLD(20) "s_sleep %11\n\t" PLD(24)
                           "2:\n\t" PCK(16, 2, "3f") PLD(16) PCK(20, 2, "3f") PLD(20) PCK(24, 2, "3f") PLD(24)
                           "v_add_co_u32 %5, vcc, 64, %5\n\ts_cbranch_vccz 2b\n\ts_endpgm\n3:"
                           : "=&v"(R.x), "=&v"(R.y), "=&v"(R.z), "=&v"(R.w), "=&v"(t_), "+v"(spins) : "v"(ro), "s"(rb), "s"(tt), "s"(0x40000u), "s"(head >> 4), "i"(5), "i"(VENT(15)) : "scc", "vcc", "memory"); }
            const float s0 = ldexpf(wh, (int)(R.y >> 10) - 139), s1 = ldexpf(wh, (int)(R.w >> 10) - 139);
            const float f[6] = {s0 * (float)(int)__builtin_amdgcn_sbfe(R.x, 0, 14), s0 * (float)(int)__builtin_amdgcn_sbfe(R.x, 14, 14), s0 * (float)(int)__builtin_amdgcn_sbfe(__builtin_amdgcn_alignbit(R.y, R.x, 28), 0, 14),
                                s1 * (float)(int)__builtin_amdgcn_sbfe(R.z, 0, 14), s1 * (float)(int)__builtin_amdgcn_sbfe(R.z, 14, 14), s1 * (float)(int)__builtin_amdgcn_sbfe(__builtin_amdgcn_alignbit(R.w, R.z, 28), 0, 14)};
            float sv[3];
#pragma unroll
            for (int i = 0; i < 3; i++) {
                const auto sw = __builtin_amdgcn_permlane32_swap(__float_as_uint(f[i]), __float_as_uint(f[i + 3]), false, false);
                sv[i] = __uint_as_float(sw[0]) + __uint_as_float(sw[1]);
            }
            pb[wave * 64 + lane] = (v4f){sv[0], sv[1], sv[2], 0.f};
            CBAR();
            COUNT(grab_adr + 12)
            v4f ps[NSW];
#pragma unroll
            for (int w = 0; w < NSW; w++) ps[w] = pb[w * 64 + lane];
            GRABBED()
            if (g == NSW - 1) {
                const v4f sum = ((ps[0] + ps[1]) + (ps[2] + ps[3])) + ((ps[4] + ps[5]) + (ps[6] + ps[7]));
                o[0] = __float2bfloat16(sum.x); o[1] = __float2bfloat16(sum.y); o[2] = __float2bfloat16(sum.z);
            }
        }
        return;
    }

    if (wave == NSW + 1) {
        u64 E; asm volatile("s_setprio 3\n\ts_load_dwordx2 %0, %1, 0\n\ts_waitcnt lgkmcnt(0)" : "=s"(E) : "s"(ws + WS_EPOCH) : "memory");
        START()
        unsigned t2 = (ts + TOF) * 128 + 64 * P0;
        int off = 0, off1 = 0, hold = 0, cum = 0, dp = DP0, sp = 0;
#define NOM(U) (((int)(U) - 2) * 32 * PER)
#define NEWLINE(K) { if (lane >= (K) && lane <= 2 * NU) sm.tu[lane] = (v2u){t2 + NOM(lane) + (cum + (lane - (int)(K)) * dp + (lane > (K) ? sp + off : off1)) * 32, \
                                                                            (unsigned)(PER + dp + (lane == (K) ? sp + off - off1 : 0))}; }
        NEWLINE(2)
        if (lane == 0) sm.tu[1] = (v2u){t2 - 32 * P0, (unsigned)P0};
        const u64 E1 = E + 1;
        const unsigned rtag = (unsigned)E1 * 0x9e3779b1u;
        const bool boss = blockIdx.x == 0;
        u64* const slot = (u64*)(ws + WS_REF + (blockIdx.x & 63) * LSTRIDE + (blockIdx.x >> 6) * 16);
        const u64* const slots = (const u64*)(ws + WS_REF + lane * LSTRIDE);
        u64* const dec = (u64*)(ws + WS_DEC);
#define GLDS(P) __hip_atomic_load(P, __ATOMIC_RELAXED, __HIP_MEMORY_SCOPE_AGENT)
#define GSTS(P, V) __hip_atomic_store(P, V, __ATOMIC_RELAXED, __HIP_MEMORY_SCOPE_AGENT)
#define UNTIL(T) while ((unsigned)((T) - NOW() * 128) < 0x40000u) SLEEP(2)
        if (lane < 2) GSTS(slot + lane, lane ? E1 : (u64)ts | ((u64)rtag << 32));
        if (lane == 0) { sm.tag = (unsigned)E1; CBAR(); lds_st(&sm.tag_rdy, 1u); }
        unsigned dv = 0;
        bool joined = false;
#define JOIN(T0) { const int d0 = min(max((int)((T0) - ts), -1000), 1000); off -= d0 * 4; t2 += d0 * 128; joined = true; }
        unsigned pw_ = 0, tw_ = 0, now, p; int prov = 0;
#define LOOK() { u64 t_; asm volatile("ds_read_b32 %1, %2\n\ts_memrealtime %0\n\ts_waitcnt lgkmcnt(0)" : "=s"(t_), "=v"(p) : "v"(lds_addr(&sm.grab)) : "memory"); \
                 p = __builtin_amdgcn_readfirstlane(p) - 8; now = (unsigned)t_ * 128; }
#define LATENESS(P, T) ({ const v2u e = TU((P) >> 5); (int)((T) - __builtin_amdgcn_readfirstlane(e.x) - ((P) & 31) * __builtin_amdgcn_readfirstlane(e.y)); })
#define WATCH(T) for (;;) { \
            LOOK() \
            if (__builtin_expect(now - tw_ > STH * 128u && pw_ >= 64 && p < NPC - 8, 0)) { \
                const int lw = max(LATENESS(pw_, tw_), 0); \
                if (LATENESS(p, now) - lw > STH * 128) \
                    for (int d0_ = hold || ((SNOT >> (p >> 5)) & 1) ? 0 : SFIX * 4;; d0_ = 0) { \
                        const int d_ = min(max((LATENESS(p, now) - lw) >> 5, d0_), 1600); \
                        if (lane <= 2 * NU) atomicAdd((unsigned*)&sm.tu[lane], d_ * 32u); \
                        off += d_; prov += d_; hold = HOLD; \
                        if (p != pw_ || prov > 8000) break; \
                        SLEEP(1) \
                        LOOK() \
                    } \
                } \
            if (p != pw_) { pw_ = p; tw_ = now; } \
            if ((unsigned)((T) - now) >= 0x40000u) break; \
            if ((unsigned)((T) - now) > 25 * 128) SLEEP(7) else SLEEP(1) }
        if (boss) {
            TOUCH((lane & 31) * 128, dec)
            UNTIL((ts + X0) * 128)
            for (;;) {
                bool ok = true; float sum = 0.f;
#pragma unroll
                for (int i = 0; i < 4; i++) { const u64 x = GLDS(slots + 2 * i), e = GLDS(slots + 2 * i + 1); ok &= e == E1 && (unsigned)(x >> 32) == rtag; sum += (float)min(max((int)((unsigned)x - ts), -1000), 1000); }
                if (__builtin_amdgcn_ballot_w64(ok) == ~0ull) { dv = ts + (int)(wave_sum(sum) * (1.f / 256)); break; }
                SLEEP(8)
            }
            if (lane == 0) { GSTS(dec, (u64)dv | ((u64)rtag << 32)); GSTS((u64*)(ws + WS_EPOCH), E1); }
            JOIN(dv)
            dv = 0;
        }
        int sp1 = 0, tot1 = 0, tot2 = 0, lock = 0;
#pragma unroll 1
        for (unsigned k = 2; k < 2 * NU; k++) {
            const unsigned tk = t2 + NOM(k) + cum * 32;
            if (k >= K0 && k + 1 < 2 * NU) {
                WATCH(tk - (XM + XW) * 128)
                int sum = 0, n = 0;
                do { LOOK() sum += LATENESS(p, now) >> 7; n++; } while ((unsigned)(tk - XM * 128 - now) < 0x40000u);
                const int l = (int)((float)sum * __builtin_amdgcn_rcpf((float)n)) - BK + prov / 4;
                if (l > RTH && lane == 0) GSTS((unsigned*)(ws + WS_REF + (blockIdx.x & 63) * LSTRIDE + 64) + (blockIdx.x >> 6), (unsigned)min(l, 1000) | ((rtag + k) << 10));
            }
            const int cum0 = cum;
            bool got = boss;
            if (!boss && (k > K0 || !joined)) {
                WATCH(tk - XR * 128)
                const u64 x = GLDS(dec + (joined ? k * 16 : 0));
                dv = __builtin_amdgcn_readfirstlane((unsigned)x);
                got = __builtin_amdgcn_readfirstlane((unsigned)(x >> 32)) == rtag;
                if (got && !joined) { JOIN(dv) got = false; }
            }
            sp1 = sp; sp = 0;
            if (got && k > K0) { cum = dv >> 18; sp = dv >> 6 & 4095u; dp = dv & 63u; }
            const int yld = cum - cum0 + sp;
            { const int pj = min(prov, cum - cum0); off -= pj; prov -= pj; off1 = off;
              const int ps = min(prov, sp); off -= ps; prov -= ps;
              if (hold) hold--; else prov = 0;
              const int r_ = off - prov; off -= r_ > 40 || r_ < -40 ? r_ / NB : r_; }
            NEWLINE(k)
            cum = min(cum + sp + dp, 16383);
            if (boss) dv = (cum << 18) | dp;
            if (boss && k >= K0 && k + 1 < 2 * NU && (int)(NOW() * 128 - tk) < -60 * 128) {
                WATCH(tk - (XM - XD1) * 128)
                const v4u x = __builtin_amdgcn_raw_buffer_load_b128(__builtin_amdgcn_make_buffer_rsrc(ws + WS_REF + 64, 0, 64 * LSTRIDE, 0x00020000), lane * LSTRIDE, 0, SC1);
                float a[4];
#pragma unroll
                for (int i = 0; i < 4; i++) a[i] = ((x[i] ^ ((rtag + k) << 10)) >> 10) == 0 ? (float)(x[i] & 1023u) : 0.f;
                const int tot = (int)wave_sum(a[0] + a[1] + a[2] + a[3]) >> 6;
                float mn = 0.f;
                if (tot >= 40) {
                    float st = (float)tot * 0.125f;
#pragma unroll
                    for (int i = 0; i < 4; i++, st *= 0.5f) {
                        const float x_ = mn + st;
                        const int c_ = __builtin_popcountll(__builtin_amdgcn_ballot_w64(a[0] >= x_)) + __builtin_popcountll(__builtin_amdgcn_ballot_w64(a[1] >= x_)) +
                                       __builtin_popcountll(__builtin_amdgcn_ballot_w64(a[2] >= x_)) + __builtin_popcountll(__builtin_amdgcn_ballot_w64(a[3] >= x_));
                        mn = c_ >= QN ? x_ : mn;
                    }
                }
                const float c = mn + (float)CAP, fth = (float)(FTH * (2 * NU - (int)k));
                const int cut = (int)wave_sum(fminf(a[0], c) + fminf(a[1], c) + fminf(a[2], c) + fminf(a[3], c)) >> 6, m4 = (int)mn * 4;
                const bool far = __builtin_amdgcn_ballot_w64(fmaxf(fmaxf(a[0], a[1]), fmaxf(a[2], a[3])) > fth) != 0;
                const int pend = yld + sp1 * XM / 320;
                const bool stall = m4 >= SMIN * 4;
                const int nj = max((stall ? tot + SADD : m4) - pend, 0);
                const int up = k >= 6 && tot < tot1 + 32 ? max(tot - tot1, 0) : 0;
                const int ns = stall ? 0 : min(max(max(min(m4 + (tot - m4 + up) * GAIN / 16 - pend - nj, (cut - m4 + up) * GAIN / 16), 0), far ? FLOOR * 4 : 0), 4000);
                int ndp = dp;
                if (k >= (unsigned)KD && tot >= KI && tot < 100 && tot > tot1 && tot < tot1 + 32 && tot1 < tot2 + 32) { ndp = min(dp + DPS, 40); lock = LOCK; }
                else if (k >= (unsigned)KD && dp < DP0 && tot >= WARN && tot < 100 && tot >= tot1) { ndp = dp + 2; lock = LOCK; }
                else if (lock) lock--;
                if (k >= 6 && tot + tot1 <= QUIET && (!lock || dp > DP0)) ndp = max(dp - 1, 0);
                tot2 = tot1; tot1 = tot;
                dv = (min(cum + nj, 16383) << 18) | (ns << 6) | ndp;
                if ((int)(NOW() * 128 - tk) > XL * 128) dv = (cum << 18) | dp;
                else if (lane == 0) GSTS(dec + (k + 1) * 16, (u64)dv | ((u64)rtag << 32));
            }
        }
        WATCH(p >= NPC - 8 ? now - 128 : now + 4096)
    } else if (wave == NSW + 2) {
        const int ql = lane < 48 ? lane : 47;
        unsigned c1, c2, s1, s2;
#define QT_LOADS() { int qo_ = ql; asm volatile("" : "+v"(qo_)); \
                     c1 = ((const unsigned*)q_cos)[qo_]; c2 = ((const unsigned*)q_cos)[48 + qo_]; s1 = ((const unsigned*)q_sin)[qo_]; s2 = ((const unsigned*)q_sin)[48 + qo_]; }
        unsigned q1, q2, lkz, lks, lvz, lvs;
#define UNIT_LOADS(HEAD) { \
        q1 = ((const unsigned*)(q + (size_t)(HEAD) * DH))[ql]; q2 = ((const unsigned*)(q + (size_t)(HEAD) * DH))[48 + ql]; \
        const size_t so = ((size_t)(HEAD) * SKV + brow0) / 2 + (lane >> 3) * 128 + (lane & 7); \
        lkz = ((const unsigned*)k_zeros)[so]; lks = ((const unsigned*)k_scales)[so]; \
        lvz = ((const unsigned*)v_zeros)[so]; lvs = ((const unsigned*)v_scales)[so]; }
#define UNIT_FILL(SL, U) { \
            v4f qq; \
            qq.x = bfr(bfr(bflo(q1) * bflo(c1)) + bfr(-bflo(q2) * bflo(s1))); \
            qq.y = bfr(bfr(bflo(q2) * bflo(c2)) + bfr(bflo(q1) * bflo(s2))); \
            qq.z = bfr(bfr(bfhi(q1) * bfhi(c1)) + bfr(-bfhi(q2) * bfhi(s1))); \
            qq.w = bfr(bfr(bfhi(q2) * bfhi(c2)) + bfr(bfhi(q1) * bfhi(s2))); \
            *(v2u*)&sm.q[SL][2 * ql] = (v2u){(__float_as_uint(qq.x) >> 16) | __float_as_uint(qq.y), (__float_as_uint(qq.z) >> 16) | __float_as_uint(qq.w)}; \
            *(v4f*)&sm.kz[SL][2 * lane] = (v4f){bflo(lkz), bflo(lks), bfhi(lkz), bfhi(lks)}; \
            *(v4f*)&sm.vz[SL][2 * lane] = (v4f){bflo(lvz), bflo(lvs), bfhi(lvz), bfhi(lvs)}; \
            CBAR(); \
            if (lane == 0) lds_st(&sm.rdy[SL], (U) + 1); \
            CBAR(); }
        asm volatile("s_setprio 3");
        QT_LOADS()
#pragma unroll 1
        for (unsigned u = 0; u < NU; u++) {
            UNIT_LOADS(rank + GSZ * u)
            while (u >= lds_ld(&sm.merged) + NIN) SLEEP(4);
            CBAR();
            UNIT_FILL(u % NIN, u)
            if (u == 1) asm volatile("s_setprio 0");
        }
        while (lds_ld(&sm.merged) < NU) SLEEP(16)
        CBAR();
        const unsigned T = lds_ld(&sm.tag);
        if (lane < NU) __hip_atomic_store((u64*)(ws + WS_LOG + (rank * NSPLIT + split) * LSTRIDE2) + lane, (u64)__float_as_uint(sm.ent[lane][130]) | ((u64)T << 32), __ATOMIC_RELAXED, __HIP_MEMORY_SCOPE_AGENT);
        while (lds_ld(&sm.merged) < NU + 3) SLEEP(16)
        for (;;) {
            const u64 x = __hip_atomic_load((const u64*)(ws + WS_LOG + ((blockIdx.x & 15) * NSPLIT + (lane & 15)) * LSTRIDE2) + (blockIdx.x >> 4), __ATOMIC_RELAXED, __HIP_MEMORY_SCOPE_AGENT);
            if (__builtin_amdgcn_ballot_w64((unsigned)(x >> 32) == T) == ~0ull) {
                const float L = __uint_as_float((unsigned)x);
                const float w = __builtin_amdgcn_exp2f(L - row_max(L));
                sm.fw[lane & 15] = w / row_sum(w);
                CBAR();
                if (lane == 0) lds_st(&sm.fw_rdy, 1u);
                break;
            }
            SLEEP(64)
        }
    } else {
        while (lds_ld(&sm.tag_rdy) == 0) SLEEP(1);
        CBAR();
        const unsigned T = lds_ld(&sm.tag);
        const __amdgpu_buffer_rsrc_t rsrc = __builtin_amdgcn_make_buffer_rsrc(ws + WS_PART + (rank * NSPLIT + split) * 512, 0, NH * NSPLIT * 512, 0x00020000);
        v2u* recs = (v2u*)&sm.cs[0][0];
#pragma unroll
        for (int i = 0; i < NOUT * 3; i++) (&sm.acc[0][0])[i * 64 + lane] = 0.0;
#define RECORD() \
        const float n0 = (float)(a0 - (double)h.y) * h.x, n1 = (float)(a1 - (double)h.y) * h.x, n2 = (float)(a2 - (double)h.y) * h.x; \
        const unsigned e2 = max(__float_as_uint(fmaxf(fmaxf(fabsf(n0), fabsf(n1)), fabsf(n2))) >> 23, 12u); \
        const float f2 = __uint_as_float((266u - e2) << 23); \
        const unsigned m0 = min(max((int)rintf(n0 * f2), -8191), 8191) & 0x3fff, m1 = min(max((int)rintf(n1 * f2), -8191), 8191) & 0x3fff, m2 = min(max((int)rintf(n2 * f2), -8191), 8191) & 0x3fff; \
        const v2u r = {m0 | (m1 << 14) | (m2 << 28), (m2 >> 4) | (e2 << 10) | (T << 18)};
#pragma unroll 1
        for (unsigned u = 0; u < 2 * NU - 1; u++) {
            const unsigned e = u < NU ? u : VENT(u), so = u % NOUT;
            const v4f h = *(const v4f*)&sm.ent[e][128];
            while (lds_ld(&sm.cnt[so]) != 32) SLEEP(1)
            CBAR();
            if (u < NU) {
                const float2 lg = *(const float2*)&sm.ent[e][2 * lane];
                const v4f zs = *(const v4f*)&sm.vz[u % NIN][2 * lane];
                const float m = wave_max(fmaxf(lg.x, lg.y));
                const float p0 = __expf(lg.x - m), p1 = __expf(lg.y - m);
                const float x0 = p0 * zs.y, x1 = p1 * zs.w;
                const unsigned eb = max(__float_as_uint(wave_max(fmaxf(fabsf(x0), fabsf(x1)))) >> 23, 21u);
                const float f = __uint_as_float((275u - eb) << 23);
                const float w0 = rintf(x0 * f), w1 = rintf(x1 * f);
                const float den = wave_sum(p0 + p1);
                const float bz = wave_sum(fmaf(w0, zs.x, w1 * zs.z));
                const float L = fmaf(m, 1.4426950408889634f, __builtin_amdgcn_logf(den));
                *(v2u*)&sm.ent[e][2 * lane] = (v2u){(unsigned)(int)w0, (unsigned)(int)w1};
                if (lane == 0) *(v4f*)&sm.ent[e][128] = (v4f){ldexpf(1.f / den, (int)eb - 148), bz, L, 0.f};
            } else {
                const double a0 = sm.acc[so][lane], a1 = sm.acc[so][64 + lane], a2 = sm.acc[so][128 + lane];
                RECORD()
                recs[(u - NU) * 64 + lane] = r;
                CBAR();
                sm.acc[so][lane] = 0.0; sm.acc[so][64 + lane] = 0.0; sm.acc[so][128 + lane] = 0.0;
            }
            CBAR();
            if (lane == 0) { lds_st(&sm.cnt[so], 0u); CBAR(); lds_st(&sm.merged, u + 1); }
        }
        {
            asm volatile("s_setprio 3");
            wave_sync();
            v4u rr[8]; unsigned po[8];
#pragma unroll
            for (int k = 0; k < 8; k++) { rr[k] = ((const v4u*)recs)[k * 64 + lane]; po[k] = k == 7 && lane >= 32 ? 0x7ffffff0u : (lane & 31) * 16 + (lane < 32 ? VENT(2 * k) : VENT(2 * k + 1)) * (GSZ * NSPLIT * 512); }
            const v4f h = *(const v4f*)&sm.ent[VENT(15)][128];
            asm volatile("" :: "v"(rr[0]), "v"(rr[1]), "v"(rr[2]), "v"(rr[3]), "v"(rr[4]), "v"(rr[5]), "v"(rr[6]), "v"(rr[7]), "v"(po[0]), "v"(po[1]), "v"(po[2]), "v"(po[3]), "v"(po[4]), "v"(po[5]), "v"(po[6]), "v"(po[7]), "v"(h));
            for (;;) {
                const unsigned d = __builtin_amdgcn_readfirstlane(TU(2 * NU).x) + PUBT * 128 - NOW() * 128;
                if (d >= 0x40000u) break;
                if (d > 20 * 128) SLEEP(4)
            }
#pragma unroll
            for (int k = 0; k < 8; k++) __builtin_amdgcn_raw_buffer_store_b128(rr[k], rsrc, po[k], 0, SC1);
            double a0, a1, a2;
            for (;;) {
                const unsigned c = __hip_atomic_load(&sm.cnt[NOUT - 1], __ATOMIC_RELAXED, __HIP_MEMORY_SCOPE_WORKGROUP);
                CBAR();
                a0 = sm.acc[NOUT - 1][lane]; a1 = sm.acc[NOUT - 1][64 + lane]; a2 = sm.acc[NOUT - 1][128 + lane];
                CBAR();
                if (__builtin_amdgcn_readfirstlane(c) == 32) break;
                SLEEP(0)
            }
            RECORD()
            const v4u r2 = {r.x, r.y, (unsigned)__builtin_amdgcn_update_dpp(0, (int)r.x, 0xf5, 0xf, 0xf, true), (unsigned)__builtin_amdgcn_update_dpp(0, (int)r.y, 0xf5, 0xf, 0xf, true)};
            __builtin_amdgcn_raw_buffer_store_b128(r2, rsrc, (lane & 1) ? 0x7ffffff0u : lane * 8, VENT(15) * (GSZ * NSPLIT * 512), SC1);
        }
        return;
    }

}

extern "C" void kernel_launch(void* const* d_in, const int* in_sizes, int n_in,
                              void* d_out, int out_size, void* d_ws, size_t ws_size,
                              hipStream_t stream) {
    const unsigned short* q = (const unsigned short*)d_in[0];
    const int* qk = (const int*)d_in[1];
    const unsigned short* k_scales = (const unsigned short*)d_in[2];
    const unsigned short* k_zeros = (const unsigned short*)d_in[3];
    const int* qv = (const int*)d_in[4];
    const unsigned short* v_scales = (const unsigned short*)d_in[5];
    const unsigned short* v_zeros = (const unsigned short*)d_in[6];
    const unsigned short* q_cos = (const unsigned short*)d_in[7];
    const unsigned short* q_sin = (const unsigned short*)d_in[8];
    const unsigned short* k_cos = (const unsigned short*)d_in[9];
    const unsigned short* k_sin = (const unsigned short*)d_in[10];
    unsigned char* ws = (unsigned char*)d_ws;
    attn_kernel<<<NSPLIT * GSZ, NTHREADS, 0, stream>>>(q, qk, k_scales, k_zeros, qv, v_scales, v_zeros,
                                                       q_cos, q_sin, k_cos, k_sin, (__hip_bfloat16*)d_out, ws);
}
```

```cpp
#include <hip/hip_runtime.h>
#include <hip/hip_bf16.h>
#include <stdint.h>

#define DH 192
#define HALF 96
#define SKV 2048
#define NH 256
#define NSW 8
#define NTHREADS (64 * (NSW + 4))
#define BROWS 128
#define NSPLIT (SKV / BROWS)
#define GSZ 16
#define NU (NH / GSZ)
#define NPC (2 * NU * 32)
#define NIN 4
#define NOUT 4
#define ENTW 132
#define SC1 16
#define PHI 0x0246024671357135ull
#define VORD 0x9876543210afedcbull
#define VENT(V) ((unsigned)(VORD >> (4 * ((V) & 15))) & 15u)
#define PFL 194
#define PER 1262
#define DP0 6
#define P0 1375
#define NBW 2
#define BTH 20
#define X0 250
#define K0 3
#define XM 250
#define XW 40
#define XD1 85
#define XL 90
#define XR 170
#define BK 3
#define RTH 6
#define QN 224
#define GAIN 24
#define CAP 45
#define FLOOR 3
#define FTH 15
#define KD 9
#define KI 14
#define DPS 2
#define QUIET 2
#define WARN 5
#define LOCK 6
#define NB 3
#define STH 90
#define SFIX 220
#define SNOT 0x610c0cu
#define HOLD 4
#define SMIN 100
#define SADD 8
#define PUBT 40
#define POLT 100
#define POLC 110

#define WS_EPOCH 0
#define WS_DEC 8192
#define WS_REF 16384
#define LSTRIDE 4352
#define WS_LOG 524288
#define LSTRIDE2 1408
#define WS_PART 1048576

typedef unsigned int v2u __attribute__((ext_vector_type(2)));
typedef unsigned int v4u __attribute__((ext_vector_type(4)));
typedef int v4i __attribute__((ext_vector_type(4)));
typedef float v2f __attribute__((ext_vector_type(2)));
typedef float v4f __attribute__((ext_vector_type(4)));
typedef unsigned long long u64;

__device__ __forceinline__ float bflo(unsigned u) { return __uint_as_float(u << 16); }
__device__ __forceinline__ float bfhi(unsigned u) { return __uint_as_float(u & 0xffff0000u); }
__device__ __forceinline__ float bfr(float x) {
    __bf16 b = (__bf16)x;
    return __uint_as_float(((unsigned)__builtin_bit_cast(unsigned short, b)) << 16);
}

#define SLEEP(n) SLEEP_(n)
#define SLEEP_(n) asm volatile("s_sleep " #n "\n\tv_add_co_u32 %0, vcc, 1, %0\n\ts_cbranch_vccz 1f\n\ts_endpgm\n1:" : "+v"(spins) :: "vcc", "memory");
#define SPIN0 0xf0000000u
#define CBAR() asm volatile("" ::: "memory")

__device__ __forceinline__ void wave_sync() {
    __builtin_amdgcn_fence(__ATOMIC_RELEASE, "wavefront");
    __builtin_amdgcn_wave_barrier();
    __builtin_amdgcn_fence(__ATOMIC_ACQUIRE, "wavefront");
}
template <int CTRL>
__device__ __forceinline__ float dpp_add(float x) {
    int y = __builtin_amdgcn_update_dpp(0, __float_as_int(x), CTRL, 0xf, 0xf, true);
    return x + __int_as_float(y);
}
template <int CTRL>
__device__ __forceinline__ float dpp_max(float x) {
    int y = __builtin_amdgcn_update_dpp(__float_as_int(x), __float_as_int(x), CTRL, 0xf, 0xf, false);
    return fmaxf(x, __int_as_float(y));
}
__device__ __forceinline__ float row_sum(float x) { x = dpp_add<0x128>(x); x = dpp_add<0x124>(x); x = dpp_add<0x122>(x); return dpp_add<0x121>(x); }
__device__ __forceinline__ float row_max(float m) { m = dpp_max<0x128>(m); m = dpp_max<0x124>(m); m = dpp_max<0x122>(m); return dpp_max<0x121>(m); }
__device__ __forceinline__ float rdlane(float x, int l) { return __int_as_float(__builtin_amdgcn_readlane(__float_as_int(x), l)); }
__device__ __forceinline__ float wave_max(float m) { m = row_max(m); return fmaxf(fmaxf(rdlane(m, 0), rdlane(m, 16)), fmaxf(rdlane(m, 32), rdlane(m, 48))); }
__device__ __forceinline__ float wave_sum(float x) { x = row_sum(x); return (rdlane(x, 0) + rdlane(x, 16)) + (rdlane(x, 32) + rdlane(x, 48)); }
__device__ __forceinline__ unsigned lds_ld(const unsigned* p) { return __builtin_amdgcn_readfirstlane(__hip_atomic_load(p, __ATOMIC_RELAXED, __HIP_MEMORY_SCOPE_WORKGROUP)); }
__device__ __forceinline__ unsigned lds_addr(const void* p) { return (unsigned)(size_t)(const __attribute__((address_space(3))) void*)p; }
__device__ __forceinline__ void lds_st(unsigned* p, unsigned v) { __hip_atomic_store(p, v, __ATOMIC_RELAXED, __HIP_MEMORY_SCOPE_WORKGROUP); }
#define NOW() ((unsigned)__builtin_amdgcn_s_memrealtime())

struct __attribute__((aligned(16))) Smem {
    unsigned cs[32][768];
    double acc[NOUT][DH];
    float ent[NU][ENTW];
    unsigned q[NIN][HALF];
    float2 kz[NIN][BROWS];
    float2 vz[NIN][BROWS];
    v2u tu[2 * NU + 2];
    unsigned grab, merged, tabs, cdone;
    unsigned rdy[NIN], cnt[NOUT];
    unsigned tag_rdy, tag, start, fw_rdy;
    float fw[NSPLIT];
};

#define TU(I) (*(const volatile v2u __attribute__((address_space(3)))*)(size_t)lds_addr(&sm.tu[I]))
#define LDS_ADD(P, V) { u64 ex_; asm volatile("s_mov_b64 %0, exec\n\ts_mov_b64 exec, 1\n\tds_add_u32 %1, %2\n\ts_mov_b64 exec, %0" : "=&s"(ex_) : "v"(lds_addr(P)), "v"(V) : "memory"); }
#define LDS_OR(P, V) { u64 ex_; asm volatile("s_mov_b64 %0, exec\n\ts_mov_b64 exec, 1\n\tds_or_b32 %1, %2\n\ts_mov_b64 exec, %0" : "=&s"(ex_) : "v"(lds_addr(P)), "v"(V) : "memory"); }

__global__ __launch_bounds__(NTHREADS, 3) void attn_kernel(
    const unsigned short* __restrict__ q, const int* __restrict__ qk,
    const unsigned short* __restrict__ k_scales, const unsigned short* __restrict__ k_zeros,
    const int* __restrict__ qv,
    const unsigned short* __restrict__ v_scales, const unsigned short* __restrict__ v_zeros,
    const unsigned short* __restrict__ q_cos, const unsigned short* __restrict__ q_sin,
    const unsigned short* __restrict__ k_cos, const unsigned short* __restrict__ k_sin,
    __hip_bfloat16* __restrict__ out, unsigned char* __restrict__ ws)
{
    __shared__ Smem sm;
    const int tid = threadIdx.x;
    unsigned spins = SPIN0;
#define TOUCH(OFF, BASE) asm volatile("global_load_dword a6, %0, %1" :: "v"(OFF), "s"(BASE) : "a6", "memory");
    const unsigned cl = min(((blockIdx.x >> 3) & 15) * 16 + (tid & 15), PFL - 1u) * 128;
    if (tid < 64) { const char* pc0; asm volatile("s_getpc_b64 %0" : "=s"(pc0)); TOUCH(cl, pc0) }
    __builtin_amdgcn_sched_barrier(0);
    const int hw = __builtin_amdgcn_readfirstlane(tid >> 6);
    const int lane = tid & 63;
    const int split = (blockIdx.x & 3) * 4 + ((blockIdx.x >> 3) & 3);
    const int rank = (blockIdx.x >> 5) * 2 + ((blockIdx.x >> 2) & 1);
    const int brow0 = split * 16;
#define ROWOF(RG) (((RG) >> 2) * 256 + ((RG) & 3) * 4)
    if (hw == 0) {
        typedef const char* const __attribute__((address_space(4))) * kap_t;
        const char* sa = ((kap_t)__builtin_amdgcn_kernarg_segment_ptr())[(0x5623 >> (4 * (split & 3))) & 7];
        unsigned o2 = ((rank + GSZ * (lane >> 3)) * SKV + (lane & 7) * 256 + (brow0 & ~63)) * 2, o3 = o2 + 8 * GSZ * SKV * 2;
        unsigned o4 = (rank + GSZ * ((lane >> 2 & 3) + 4 * (split & 3))) * (DH * 2) + min(lane & 3, 2) * 128;
        asm volatile("" : "+v"(o2), "+v"(o3), "+v"(o4));
        asm volatile("global_load_dword a6, %0, %1 nt\n\tglobal_load_dword a6, %0, %2 nt" :: "v"((lane & 1) * (NH * SKV * DH * 4u - 128u * NH) + blockIdx.x * 128), "s"(qk), "s"(qv) : "a6", "memory");
        TOUCH(o4, q) TOUCH(o2, sa) TOUCH(o3, sa)
        asm volatile("s_barrier\n\ts_waitcnt vmcnt(2)" ::: "memory");
        if (lane == 0) lds_st(&sm.start, NOW() | 1u);
        asm volatile("s_endpgm");
    }
    const unsigned rot = 4 * ((unsigned)(PHI >> (60 - 4 * rank)) & 7u);
#define RGOF(G) ((((G) + rot) & 31u) ^ ((((G) + rot) >> 1) & 2u))
#define CS_DMA(RG) asm volatile("s_mov_b32 m0, %1\n\ts_nop 0\n\tglobal_load_lds_dwordx4 %0, %2\n\ts_mov_b32 exec_hi, 0\n\tglobal_load_lds_dwordx4 %0, %2 offset:1024\n\ts_add_u32 m0, m0, 0x600\n\ts_nop 0\n\t" \
                                "global_load_lds_dwordx4 %0, %3 offset:1024\n\ts_mov_b32 exec_hi, -1\n\tglobal_load_lds_dwordx4 %0, %3" \
                                :: "v"((brow0 + ROWOF(RG)) * (DH * 2) + lane * 16), "s"(lds_addr(&sm.cs[0][0]) + (RG) * 3072), "s"(k_cos), "s"(k_sin) : "scc", "memory");
    if (hw >= 4) CS_DMA(RGOF((unsigned)hw - 4))
    if (hw == 1) {
        const unsigned qo = min(lane, 47) * 4, so = (rank * SKV + brow0) * 2 + (lane >> 3) * 512 + (lane & 7) * 4;
        asm volatile("global_load_dword a0, %0, %3\n\tglobal_load_dword a1, %0, %3 offset:192\n\tglobal_load_dword a2, %0, %4\n\tglobal_load_dword a3, %0, %4 offset:192\n\t"
                     "global_load_dword a4, %1, %5\n\tglobal_load_dword a5, %1, %5 offset:192\n\t"
                     "global_load_dword a8, %2, %6\n\tglobal_load_dword a9, %2, %7\n\tglobal_load_dword a10, %2, %8\n\tglobal_load_dword a11, %2, %9"
                     :: "v"(qo), "v"(qo + rank * (DH * 2)), "v"(so), "s"(q_cos), "s"(q_sin), "s"(q), "s"(k_zeros), "s"(k_scales), "s"(v_zeros), "s"(v_scales)
                     : "a0", "a1", "a2", "a3", "a4", "a5", "a8", "a9", "a10", "a11", "memory");
    }
    if (hw == 3 && lane < 16) (&sm.grab)[lane] = lane ? 0 : 64;
    asm volatile("" :: "s"(q), "s"(qk), "s"(k_scales), "s"(k_zeros), "s"(qv), "s"(v_scales), "s"(v_zeros), "s"(q_cos), "s"(q_sin), "s"(k_cos), "s"(k_sin), "s"(out), "s"(ws));
    asm volatile("s_waitcnt lgkmcnt(0)\n\ts_barrier" ::: "memory");
    const int wave = (int)(0x76543210b9a8ull >> (4 * hw)) & 15;
    unsigned ts;
#define START() { while ((ts = lds_ld(&sm.start)) == 0) SLEEP(1) }

    if (__builtin_expect(wave < NSW, 1)) {
#define REPACK(RG) { \
        unsigned* cw = &sm.cs[RG][0]; \
        const unsigned* l = cw + rsel * (DH / 2) + qoff; \
        unsigned r[2][6]; \
        _Pragma("unroll") \
        for (int j = 0; j < 2; j++) { \
            unsigned e[4][2]; \
            _Pragma("unroll") \
            for (int k = 0; k < 4; k++) { \
                const unsigned* a = l + j * DH + (k & 1) * 48 + (k >> 1) * 384;       \
                unsigned x0 = a[0], x1 = a[1]; \
                e[k][0] = __builtin_amdgcn_alignbit(x1, x0, sh); \
                e[k][1] = x1 >> sh; \
            } \
            r[j][0] = (e[0][0] & 0xffffu) | (e[3][0] << 16); \
            r[j][1] = (e[0][0] >> 16) | (e[3][0] & 0xffff0000u); \
            r[j][2] = (e[0][1] & 0xffffu) | (e[3][1] << 16); \
            r[j][3] = (e[1][0] & 0xffffu) | (e[2][0] << 16); \
            r[j][4] = (e[1][0] >> 16) | (e[2][0] & 0xffff0000u); \
            r[j][5] = (e[1][1] & 0xffffu) | (e[2][1] << 16); \
        } \
        wave_sync(); \
        _Pragma("unroll") \
        for (int j = 0; j < 2; j++) { *(v4u*)&cw[j * 256 + lane * 4] = (v4u){r[j][0], r[j][1], r[j][2], r[j][3]}; *(v2u*)&cw[512 + j * 128 + lane * 2] = (v2u){r[j][4], r[j][5]}; } \
        wave_sync(); \
        LDS_OR(&sm.tabs, 1u << (RG)) }
        const int sub = lane & 31;
        const int rsel = lane >> 5;
        const unsigned sh = (sub & 1) * 16;
        const int qoff = 3 * (sub >> 1) + (sub & 1);
        const char* kbase = (const char*)qk + (size_t)(rank * SKV + brow0) * (DH * 4);
        const char* vbase = (const char*)qv + (size_t)(rank * SKV + brow0) * (DH * 4);
        const unsigned koff = (rsel * DH + 3 * sub) * 4, voff = 3 * lane * 4;
#define AR(S) (16 + (S) * 8)
        asm volatile("" ::: "a16", "a17", "a18", "a19", "a20", "a21", "a22", "a23", "a24", "a25", "a26", "a27", "a28", "a29", "a30", "a31",
                            "a32", "a33", "a34", "a35", "a36", "a37", "a38", "a39", "a40", "a41", "a42", "a43", "a44", "a45", "a46", "a47");
        asm volatile("" ::: "a48", "a49", "a50", "a51", "a52", "a53", "a54", "a55", "a56", "a57", "a58", "a59", "a60", "a61", "a62", "a63");
        int ta[3], tb[3];
#define GLD(A, OFF, BASE, IMM) asm volatile("global_load_dwordx3 a[%c0:%c1], %2, %3 offset:%4 nt" :: "i"(A), "i"((A) + 2), "v"(OFF), "s"(BASE), "i"(IMM))
#define ISSUE(S, J, BASE, OA, OB) { GLD(AR(S), OA, BASE, (J) * 1536); GLD(AR(S) + 4, OB, BASE, (J) * 1536); }
#define WAITT(S, N) asm volatile("s_waitcnt vmcnt(%6)\n\tv_accvgpr_read_b32 %0, a[%c7]\n\tv_accvgpr_read_b32 %1, a[%c8]\n\tv_accvgpr_read_b32 %2, a[%c9]\n\t" \
                              "v_accvgpr_read_b32 %3, a[%c10]\n\tv_accvgpr_read_b32 %4, a[%c11]\n\tv_accvgpr_read_b32 %5, a[%c12]" \
                              : "=v"(ta[0]), "=v"(ta[1]), "=v"(ta[2]), "=v"(tb[0]), "=v"(tb[1]), "=v"(tb[2]) \
                              : "i"(N), "i"(AR(S)), "i"(AR(S) + 1), "i"(AR(S) + 2), "i"(AR(S) + 4), "i"(AR(S) + 5), "i"(AR(S) + 6))
        const unsigned koffb = koff + 384, voffb = voff + 768;
        unsigned u = 0, rg, np = 0;
#define RG0(C) RGOF((unsigned)(wave + 8 * (C)))
        const float scale = 0.07216878364870322f;
        double acc[3] = {0.0, 0.0, 0.0};
        bool chg, none = false;
        unsigned Qp[3], Qn[3];
        float kz4[2], ksc, kres[2];
        v4u ca[2]; v2u cb[2];
        v2f qe[2][3], A[2];
        const char* nb; unsigned oa, ob;
        unsigned g, gu, mg, nrg, tg, dg, par = 0;
        int early;
        const int bth = wave < NBW ? BTH * 128 : 0x3fffffff;
        const unsigned grab_adr = lds_addr(&sm.grab);
        asm volatile("v_accvgpr_write_b32 a13, 1" ::: "a13", "a15");
#define COUNT(ADR) { u64 ex_; asm volatile("s_mov_b64 %0, exec\n\ts_mov_b64 exec, 1\n\tds_add_rtn_u32 a15, %1, a13\n\ts_mov_b64 exec, %0" : "=&s"(ex_) : "v"(ADR) : "memory"); }
#define GRABBED() { unsigned t_; asm volatile("s_waitcnt lgkmcnt(0)\n\tv_accvgpr_read_b32 %1, a15\n\ts_nop 0\n\tv_readfirstlane_b32 %0, %1\n\ts_nop 1" : "=s"(g), "=v"(t_)); }
#define NEXT1() const v2u x_ = TU(g >> 5); u64 tn_; asm volatile("s_memrealtime %0" : "=s"(tn_));
#define NEXT2() { \
            chg = (g >> 5) != u; none = g >= NPC; \
            const bool nv = g >= NPC / 2; \
            nrg = RGOF(g); \
            unsigned boff; asm("s_mul_i32 %0, %1, 0x300" : "=s"(boff) : "s"((nv ? VENT(g >> 5) : g >> 5) * (GSZ * SKV) + ROWOF(nrg) + 2)); \
            nb = (nv ? vbase : kbase) + boff; \
            oa = nv ? voff : koff; ob = nv ? voffb : koffb; \
            asm volatile("s_waitcnt lgkmcnt(0)" : "+s"(tn_) : "s"(nb), "v"(oa), "v"(ob)); \
            tg = __builtin_amdgcn_readfirstlane(x_.x) + (g & 31) * __builtin_amdgcn_readfirstlane(x_.y); \
            early = none ? 0 : (int)((unsigned)tn_ * 128 - tg) - bth; \
            if (early > 0) asm volatile("s_cmp_lg_u32 %3, 0\n\ts_cbranch_scc1 1f\n\t" L4(32) "\ts_branch 2f\n1:\n\t" L4(16) "2:\n\ts_mov_b64 vcc, exec\n\ts_mov_b64 exec, 1\n\tds_add_rtn_u32 a15, %4, a13\n\ts_mov_b64 exec, vcc" \
                                    :: "v"(oa), "v"(ob), "s"(nb), "s"(pr), "v"(grab_adr) : "scc", "vcc", "memory"); }
#define L4(A) "global_load_dwordx3 a[" #A ":" #A "+2], %0, %2 offset:-1536 nt\n\tglobal_load_dwordx3 a[" #A "+4:" #A "+6], %1, %2 offset:-1536 nt\n\t" \
              "global_load_dwordx3 a[" #A "+8:" #A "+10], %0, %2 nt\n\tglobal_load_dwordx3 a[" #A "+12:" #A "+14], %1, %2 nt\n"
#define GATE(...) \
            asm volatile("s_setprio 0\n1:\n\ts_memrealtime vcc\n\ts_waitcnt lgkmcnt(0)\n\ts_lshl_b32 vcc_lo, vcc_lo, 7\n\ts_sub_u32 %0, %1, vcc_lo\n\ts_cmp_lt_u32 %0, 0x40000\n\ts_cbranch_scc0 2f\n\t" \
                         "s_cmp_lt_u32 %0, %2\n\ts_cbranch_scc1 1b\n\ts_sleep %3\n\ts_branch 1b\n2:\n\ts_setprio 2" : "=&s"(dg) : "s"(tg), "i"(10 * 128), "i"(2), __VA_ARGS__ : "scc", "vcc", "memory");
#define RD6(A) "v_accvgpr_read_b32 %0, a[%c6+" #A "]\n\tv_accvgpr_read_b32 %1, a[%c6+" #A "+1]\n\tv_accvgpr_read_b32 %2, a[%c6+" #A "+2]\n\t" \
               "v_accvgpr_read_b32 %3, a[%c6+" #A "+4]\n\tv_accvgpr_read_b32 %4, a[%c6+" #A "+5]\n\tv_accvgpr_read_b32 %5, a[%c6+" #A "+6]\n"
#define LD2(A) "global_load_dwordx3 a[%c6+" #A ":%c6+" #A "+2], %7, %9 offset:%10 nt\n\tglobal_load_dwordx3 a[%c6+" #A "+4:%c6+" #A "+6], %8, %9 offset:%10 nt"
#define OUT6 "=&v"(ta[0]), "=&v"(ta[1]), "=&v"(ta[2]), "=&v"(tb[0]), "=&v"(tb[1]), "=&v"(tb[2])
#define WI(A, B, TAIL) "s_cmp_gt_i32 %13, 0\n\ts_cbranch_scc1 3f\n\ts_waitcnt vmcnt(2)\n\t" LD2(A) TAIL "\n3:\n\ts_waitcnt vmcnt(%c14)\n\t" RD6(B)
#define WAITISSUE(S, TAIL, ...) asm volatile("s_cmp_lg_u32 %12, 0\n\ts_cbranch_scc1 1f\n\t" WI(16, 0, TAIL) "\ts_branch 2f\n1:\n\t" WI(0, 16, TAIL) "2:" \
                              : OUT6 : "i"(AR(S)), "v"(oa), "v"(ob), "s"(nb), "i"(((S) - 1) * 1536), "v"(grab_adr), "s"(pr), "s"(early), "i"(6 - 2 * (S)), __VA_ARGS__ : "scc", "vcc", "memory")
#define LASTT(S) asm volatile("s_waitcnt vmcnt(%c8)\n\ts_cmp_lg_u32 %7, 0\n\ts_cbranch_scc1 1f\n\t" RD6(0) "\ts_branch 2f\n1:\n\t" RD6(16) "2:" : OUT6 : "i"(AR(S)), "s"(pr), "i"(2 - 2 * (S)) : "scc")
#define DRAW "\n\ts_mov_b64 vcc, exec\n\ts_mov_b64 exec, 1\n\tds_add_rtn_u32 a15, %11, a13\n\ts_mov_b64 exec, vcc"
#define K_TAB() { _Pragma("unroll") for (int p = 0; p < 2; p++) { \
            const unsigned* cw = &sm.cs[rg][0]; \
            ca[p] = *(const v4u*)&cw[p * 256 + lane * 4]; cb[p] = *(const v2u*)&cw[512 + p * 128 + lane * 2]; } }
#define K_PRE(P) { \
        float e0, e1, e2, f0, f1, f2; \
        asm("v_dot2_f32_bf16 %0, %6, %12, 0\n\tv_dot2_f32_bf16 %3, %9, %15, 0\n\tv_dot2_f32_bf16 %1, %7, %13, 0\n\tv_dot2_f32_bf16 %4, %10, %16, 0\n\t" \
            "v_dot2_f32_bf16 %2, %8, %14, 0\n\tv_dot2_f32_bf16 %5, %11, %17, 0\n\ts_nop 2" \
            : "=&v"(e0), "=&v"(e1), "=&v"(e2), "=&v"(f0), "=&v"(f1), "=&v"(f2) \
            : "v"(Qp[0]), "v"(Qp[1]), "v"(Qp[2]), "v"(Qn[0]), "v"(Qn[1]), "v"(Qn[2]), \
              "v"(ca[P].x), "v"(ca[P].y), "v"(ca[P].z), "v"(ca[P].w), "v"(cb[P].x), "v"(cb[P].y)); \
        qe[P][0] = (v2f){e0, f0}; qe[P][1] = (v2f){e1, f1}; qe[P][2] = (v2f){e2, f2}; \
        A[P] = (qe[P][0] + qe[P][1] + qe[P][2]) * (v2f){-kz4[P], -kz4[P]}; }
#define K_SUM(P) { \
        _Pragma("unroll") \
        for (int i = 0; i < 3; i++) { v2f x = {(float)ta[i], (float)tb[i]}; A[P] = qe[P][i] * x + A[P]; } \
        kres[P] = A[P].x + A[P].y; }
#define K_RED2() { \
        const bool o1 = lane & 1; \
        float sx = (o1 ? kres[1] : kres[0]) + __int_as_float(__builtin_amdgcn_update_dpp(0, __float_as_int(o1 ? kres[0] : kres[1]), 0xb1, 0xf, 0xf, true)); \
        sx = dpp_add<0x4e>(sx); sx = dpp_add<0x124>(sx); sx = dpp_add<0x128>(sx); \
        const auto sw = __builtin_amdgcn_permlane16_swap(__float_as_uint(sx), __float_as_uint(sx), false, false); \
        sx = __uint_as_float(sw[0]) + __uint_as_float(sw[1]); \
        if ((lane & 30) == 0) sm.ent[u][rg * 4 + 2 * (lane & 1) + rsel] = sx * ksc; }
#define LDV(P) __hip_atomic_load(P, __ATOMIC_RELAXED, __HIP_MEMORY_SCOPE_WORKGROUP)
#define RDYWAIT(SL, SEQ) { while (lds_ld(&sm.rdy[SL]) != (SEQ)) SLEEP(1); CBAR(); }
#define LOADZ() { _Pragma("unroll") for (int p = 0; p < 2; p++) kz4[p] = sm.kz[u % NIN][rg * 4 + 2 * p + rsel].x; ksc = sm.kz[u % NIN][rg * 4 + 2 * (lane & 1) + rsel].y * scale; }
#define LOADQ() { _Pragma("unroll") for (int i = 0; i < 3; i++) { Qp[i] = sm.q[u % NIN][3 * sub + i]; Qn[i] = __builtin_amdgcn_alignbit(Qp[i], Qp[i], 16) ^ 0x80000000u; } }
#define FLUSH(ISV) { \
            mg = __builtin_amdgcn_readfirstlane(mg); \
            while (u >= mg + NOUT) { SLEEP(1); mg = lds_ld(&sm.merged); } \
            CBAR(); \
            if (ISV) { const unsigned aa = lds_addr(&sm.acc[u % NOUT][lane]); \
                asm volatile("ds_add_f64 %0, %1\n\tds_add_f64 %0, %2 offset:512\n\tds_add_f64 %0, %3 offset:1024" :: "v"(aa), "v"(acc[0]), "v"(acc[1]), "v"(acc[2]) : "memory"); \
                acc[0] = 0.0; acc[1] = 0.0; acc[2] = 0.0; } \
            CBAR(); \
            LDS_ADD(&sm.cnt[u % NOUT], np) \
            CBAR(); \
            np = 0; }
#define ADVANCE() { u = gu; rg = nrg; }

#define REQ0(N, S) { tg = ts * 128 + (wave + 8 * (N)) * P0; \
            const char* kb = kbase + (((N) >> 2) * (GSZ * SKV) + ROWOF(RG0((N) & 3))) * (DH * 4); GATE("s"(kb)) ISSUE(S, 0, kb, koff, koffb) ISSUE((S) + 1, 1, kb, koff, koffb) }
#define USE0(N, S, W) { u = (N) >> 2; rg = RG0((N) & 3); K_TAB() LOADQ() LOADZ() K_PRE(0) K_PRE(1) WAITT(S, W); K_SUM(0) WAITT((S) + 1, W); K_SUM(1) K_RED2() }
#define VMW(N) asm volatile("s_waitcnt vmcnt(" #N ")" ::: "memory");
        START()
        REQ0(0, 0) CS_DMA(RG0(1))
        VMW(8) REPACK(RG0(0))
        REQ0(1, 2)
        RDYWAIT(0, 1u)
        USE0(0, 0, 8)
        VMW(4) REPACK(RG0(1))
        REQ0(2, 0) CS_DMA(RG0(2))
        USE0(1, 2, 8)
        REQ0(3, 2)
        VMW(4) REPACK(RG0(2))
        USE0(2, 0, 4)
        REQ0(4, 0) CS_DMA(RG0(3))
        v2f qe2[2][2][3], A2[2][2]; float ks2[2];
        RDYWAIT(1, 2u)
        u = 1;
        LOADQ()
#pragma unroll
        for (int j = 0; j < 2; j++) {
            rg = RG0(j); K_TAB() LOADZ() K_PRE(0) K_PRE(1)
            ks2[j] = ksc;
#pragma unroll
            for (int p = 0; p < 2; p++) { A2[j][p] = A[p]; qe2[j][p][0] = qe[p][0]; qe2[j][p][1] = qe[p][1]; qe2[j][p][2] = qe[p][2]; }
        }
        asm volatile("" :: "v"(A2[0][0]), "v"(A2[0][1]), "v"(A2[1][0]), "v"(A2[1][1]), "v"(ks2[0]), "v"(ks2[1]));
        REQ0(5, 4)
        VMW(4) REPACK(RG0(3))
        USE0(3, 2, 4)
        CBAR();
        LDS_ADD(&sm.cnt[0], 4u)
        REQ0(6, 2)
        u = 1;
#define FIN0(J, S, W) { rg = RG0(J); ksc = ks2[J]; \
            _Pragma("unroll") for (int p = 0; p < 2; p++) { A[p] = A2[J][p]; qe[p][0] = qe2[J][p][0]; qe[p][1] = qe2[J][p][1]; qe[p][2] = qe2[J][p][2]; } \
            WAITT(S, W); K_SUM(0) WAITT((S) + 1, W); K_SUM(1) K_RED2() }
        FIN0(0, 0, 8)
        FIN0(1, 4, 4)
        REQ0(7, 0)
        COUNT(grab_adr)
        USE0(6, 2, 4)
        rg = RG0(3); np = 3;
        GRABBED()
#pragma unroll 1
        while (u < NU) {
            const unsigned pr = __builtin_amdgcn_readfirstlane(par); par ^= 1;
            NEXT1()
            const unsigned tb_ = LDV(&sm.tabs), fl = LDV(&sm.rdy[u % NIN]);
            mg = LDV(&sm.merged);
            CBAR();
            K_TAB() LOADQ() LOADZ()
            NEXT2()
            if (__builtin_expect(__builtin_amdgcn_readfirstlane(fl) != u + 1 || !((__builtin_amdgcn_readfirstlane(tb_) >> rg) & 1u), 0)) {
                while (!((lds_ld(&sm.tabs) >> rg) & 1u)) SLEEP(1)
                RDYWAIT(u % NIN, u + 1)
                K_TAB() LOADQ() LOADZ()
            }
            K_PRE(0) K_PRE(1)
            if (early <= 0) GATE("v"(A[0]), "v"(A[1]), "v"(oa), "v"(ob), "s"(nb), "s"(pr))
            WAITISSUE(0, DRAW, "v"(A[0]));
            K_SUM(0)
            WAITISSUE(1, "", "v"(A[1]));
            K_SUM(1)
            gu = g >> 5;
            GRABBED()
            K_RED2()
            np++;
            if (chg) FLUSH(false)
            ADVANCE()
        }
#pragma unroll 1
        while (u < 2 * NU) {
            const unsigned pr = __builtin_amdgcn_readfirstlane(par); par ^= 1;
            NEXT1()
            mg = LDV(&sm.merged);
            CBAR();
            const unsigned ve = VENT(u);
            v4i pw = *(const v4i*)&sm.ent[ve][rg * 4];
            NEXT2()
            if (__builtin_expect(__builtin_amdgcn_readfirstlane(mg) <= ve, 0)) { while (lds_ld(&sm.merged) <= ve) SLEEP(1); CBAR(); pw = *(const v4i*)&sm.ent[ve][rg * 4]; }
            const double w0 = pw.x, w1 = pw.y, w2 = pw.z, w3 = pw.w;
            if (none) { LASTT(0); } else { if (early <= 0) GATE("s"(pr), "v"(w0), "v"(w1), "v"(w2), "v"(w3), "v"(oa), "v"(ob), "s"(nb)) WAITISSUE(0, DRAW, "v"(w0)); }
#pragma unroll
            for (int i = 0; i < 3; i++) acc[i] = fma(w1, (double)tb[i], fma(w0, (double)ta[i], acc[i]));
            if (none) { LASTT(1); } else WAITISSUE(1, "", "v"(w2));
#pragma unroll
            for (int i = 0; i < 3; i++) acc[i] = fma(w3, (double)tb[i], fma(w2, (double)ta[i], acc[i]));
            gu = g >> 5;
            np++;
            if (chg) FLUSH(true)
            GRABBED()
            ADVANCE()
        }

        {
            const int head = blockIdx.x;
            while (lds_ld(&sm.fw_rdy) == 0) SLEEP(2)
            CBAR();
            const unsigned tt = lds_ld(&sm.tag) << 18;
            const float wh = sm.fw[wave + 8 * rsel];
            const unsigned ro = (wave + 8 * rsel) * 512 + sub * 16;
            const char* rb = (const char*)ws + WS_PART + head * (NSPLIT * 512);
            v4f* pb = (v4f*)((char*)&sm.cs[0][0] + 16384);
            __hip_bfloat16* o = out + (size_t)head * DH + 6 * sub + 3 * rsel;
            tg = __builtin_amdgcn_readfirstlane(TU(2 * NU).x) + ((head >> 4) == VENT(15) ? POLC : POLT) * 128;
            GATE("s"(tt), "v"(wh), "v"(ro), "s"(rb), "v"(o))
            v4u R;
#define PLD(A) "global_load_dwordx4 a[" #A ":" #A "+3], %6, %7 sc1\n\t"
#define PCK(A, N, L) "s_waitcnt vmcnt(" #N ")\n\tv_accvgpr_read_b32 %1, a[" #A "+1]\n\tv_accvgpr_read_b32 %3, a[" #A "+3]\n\tv_accvgpr_read_b32 %0, a[" #A "]\n\tv_accvgpr_read_b32 %2, a[" #A "+2]\n\t" \
                     "v_xor_b32 %1, %8, %1\n\tv_xor_b32 %3, %8, %3\n\tv_or_b32 %4, %1, %3\n\tv_cmp_gt_u32 vcc, %9, %4\n\ts_cmp_eq_u64 vcc, exec\n\ts_cbranch_scc1 " L "\n\t"
            { unsigned t_;
              asm volatile("s_cmp_eq_u32 %10, %12\n\ts_cbranch_scc1 1f\n\t" PLD(16) PCK(16, 0, "3f") "1:\n\t" PLD(16) "s_sleep %11\n\t" PLD(20) "s_sleep %11\n\t" PLD(24)
                           "2:\n\t" PCK(16, 2, "3f") PLD(16) PCK(20, 2, "3f") PLD(20) PCK(24, 2, "3f") PLD(24)
                           "v_add_co_u32 %5, vcc, 64, %5\n\ts_cbranch_vccz 2b\n\ts_endpgm\n3:"
                           : "=&v"(R.x), "=&v"(R.y), "=&v"(R.z), "=&v"(R.w), "=&v"(t_), "+v"(spins) : "v"(ro), "s"(rb), "s"(tt), "s"(0x40000u), "s"(head >> 4), "i"(5), "i"(VENT(15)) : "scc", "vcc", "memory"); }
            const float s0 = ldexpf(wh, (int)(R.y >> 10) - 139), s1 = ldexpf(wh, (int)(R.w >> 10) - 139);
            const float f[6] = {s0 * (float)(int)__builtin_amdgcn_sbfe(R.x, 0, 14), s0 * (float)(int)__builtin_amdgcn_sbfe(R.x, 14, 14), s0 * (float)(int)__builtin_amdgcn_sbfe(__builtin_amdgcn_alignbit(R.y, R.x, 28), 0, 14),
                                s1 * (float)(int)__builtin_amdgcn_sbfe(R.z, 0, 14), s1 * (float)(int)__builtin_amdgcn_sbfe(R.z, 14, 14), s1 * (float)(int)__builtin_amdgcn_sbfe(__builtin_amdgcn_alignbit(R.w, R.z, 28), 0, 14)};
            float sv[3];
#pragma unroll
            for (int i = 0; i < 3; i++) {
                const auto sw = __builtin_amdgcn_permlane32_swap(__float_as_uint(f[i]), __float_as_uint(f[i + 3]), false, false);
                sv[i] = __uint_as_float(sw[0]) + __uint_as_float(sw[1]);
            }
            pb[wave * 64 + lane] = (v4f){sv[0], sv[1], sv[2], 0.f};
            CBAR();
            COUNT(grab_adr + 12)
            v4f ps[NSW];
#pragma unroll
            for (int w = 0; w < NSW; w++) ps[w] = pb[w * 64 + lane];
            GRABBED()
            if (g == NSW - 1) {
                const v4f sum = ((ps[0] + ps[1]) + (ps[2] + ps[3])) + ((ps[4] + ps[5]) + (ps[6] + ps[7]));
                o[0] = __float2bfloat16(sum.x); o[1] = __float2bfloat16(sum.y); o[2] = __float2bfloat16(sum.z);
            }
        }
        return;
    }

    if (wave == NSW + 1) {
        u64 E; asm volatile("s_setprio 3\n\ts_load_dwordx2 %0, %1, 0\n\ts_waitcnt lgkmcnt(0)" : "=s"(E) : "s"(ws + WS_EPOCH) : "memory");
        START()
        unsigned t2 = ts * 128 + 64 * P0;
        int off = 0, off1 = 0, hold = 0, cum = 0, dp = DP0, sp = 0;
#define NOM(U) (((int)(U) - 2) * 32 * PER)
#define NEWLINE(K) { if (lane >= (K) && lane <= 2 * NU) sm.tu[lane] = (v2u){t2 + NOM(lane) + (cum + (lane - (int)(K)) * dp + (lane > (K) ? sp + off : off1)) * 32, \
                                                                            (unsigned)(PER + dp + (lane == (K) ? sp + off - off1 : 0))}; }
        NEWLINE(2)
        if (lane == 0) sm.tu[1] = (v2u){t2 - 32 * P0, (unsigned)P0};
        const u64 E1 = E + 1;
        const unsigned rtag = (unsigned)E1 * 0x9e3779b1u;
        const bool boss = blockIdx.x == 0;
        u64* const slot = (u64*)(ws + WS_REF + (blockIdx.x & 63) * LSTRIDE + (blockIdx.x >> 6) * 16);
        const u64* const slots = (const u64*)(ws + WS_REF + lane * LSTRIDE);
        u64* const dec = (u64*)(ws + WS_DEC);
#define GLDS(P) __hip_atomic_load(P, __ATOMIC_RELAXED, __HIP_MEMORY_SCOPE_AGENT)
#define GSTS(P, V) __hip_atomic_store(P, V, __ATOMIC_RELAXED, __HIP_MEMORY_SCOPE_AGENT)
#define UNTIL(T) while ((unsigned)((T) - NOW() * 128) < 0x40000u) SLEEP(2)
        if (lane < 2) GSTS(slot + lane, lane ? E1 : (u64)ts | ((u64)rtag << 32));
        if (lane == 0) { sm.tag = (unsigned)E1; CBAR(); lds_st(&sm.tag_rdy, 1u); }
        unsigned dv = 0;
        bool joined = false;
#define JOIN(T0) { const int d0 = min(max((int)((T0) - ts), -1000), 1000); off -= d0 * 4; t2 += d0 * 128; joined = true; }
        unsigned pw_ = 0, tw_ = 0, now, p; int prov = 0;
#define LOOK() { u64 t_; asm volatile("ds_read_b32 %1, %2\n\ts_memrealtime %0\n\ts_waitcnt lgkmcnt(0)" : "=s"(t_), "=v"(p) : "v"(lds_addr(&sm.grab)) : "memory"); \
                 p = __builtin_amdgcn_readfirstlane(p) - 8; now = (unsigned)t_ * 128; }
#define LATENESS(P, T) ({ const v2u e = TU((P) >> 5); (int)((T) - __builtin_amdgcn_readfirstlane(e.x) - ((P) & 31) * __builtin_amdgcn_readfirstlane(e.y)); })
#define WATCH(T) for (;;) { \
            LOOK() \
            if (__builtin_expect(now - tw_ > STH * 128u && pw_ >= 64 && p < NPC - 8, 0)) { \
                const int lw = max(LATENESS(pw_, tw_), 0); \
                if (LATENESS(p, now) - lw > STH * 128) \
                    for (int d0_ = hold || ((SNOT >> (p >> 5)) & 1) ? 0 : SFIX * 4;; d0_ = 0) { \
                        const int d_ = min(max((LATENESS(p, now) - lw) >> 5, d0_), 1600); \
                        if (lane <= 2 * NU) atomicAdd((unsigned*)&sm.tu[lane], d_ * 32u); \
                        off += d_; prov += d_; hold = HOLD; \
                        if (p != pw_ || prov > 8000) break; \
                        SLEEP(1) \
                        LOOK() \
                    } \
                } \
            if (p != pw_) { pw_ = p; tw_ = now; } \
            if ((unsigned)((T) - now) >= 0x40000u) break; \
            if ((unsigned)((T) - now) > 25 * 128) SLEEP(7) else SLEEP(1) }
        if (boss) {
            TOUCH((lane & 31) * 128, dec)
            UNTIL((ts + X0) * 128)
            for (;;) {
                bool ok = true; float sum = 0.f;
#pragma unroll
                for (int i = 0; i < 4; i++) { const u64 x = GLDS(slots + 2 * i), e = GLDS(slots + 2 * i + 1); ok &= e == E1 && (unsigned)(x >> 32) == rtag; sum += (float)min(max((int)((unsigned)x - ts), -1000), 1000); }
                if (__builtin_amdgcn_ballot_w64(ok) == ~0ull) { dv = ts + (int)(wave_sum(sum) * (1.f / 256)); break; }
                SLEEP(8)
            }
            if (lane == 0) { GSTS(dec, (u64)dv | ((u64)rtag << 32)); GSTS((u64*)(ws + WS_EPOCH), E1); }
            JOIN(dv)
            dv = 0;
        }
        int sp1 = 0, tot1 = 0, tot2 = 0, lock = 0;
#pragma unroll 1
        for (unsigned k = 2; k < 2 * NU; k++) {
            const unsigned tk = t2 + NOM(k) + cum * 32;
            if (k >= K0 && k + 1 < 2 * NU) {
                WATCH(tk - (XM + XW) * 128)
                int sum = 0, n = 0;
                do { LOOK() sum += LATENESS(p, now) >> 7; n++; } while ((unsigned)(tk - XM * 128 - now) < 0x40000u);
                const int l = (int)((float)sum * __builtin_amdgcn_rcpf((float)n)) - BK + prov / 4;
                if (l > RTH && lane == 0) GSTS((unsigned*)(ws + WS_REF + (blockIdx.x & 63) * LSTRIDE + 64) + (blockIdx.x >> 6), (unsigned)min(l, 1000) | ((rtag + k) << 10));
            }
            const int cum0 = cum;
            bool got = boss;
            if (!boss && (k > K0 || !joined)) {
                WATCH(tk - XR * 128)
                const u64 x = GLDS(dec + (joined ? k * 16 : 0));
                dv = __builtin_amdgcn_readfirstlane((unsigned)x);
                got = __builtin_amdgcn_readfirstlane((unsigned)(x >> 32)) == rtag;
                if (got && !joined) { JOIN(dv) got = false; }
            }
            sp1 = sp; sp = 0;
            if (got && k > K0) { cum = dv >> 18; sp = dv >> 6 & 4095u; dp = dv & 63u; }
            const int yld = cum - cum0 + sp;
            { const int pj = min(prov, cum - cum0); off -= pj; prov -= pj; off1 = off;
              const int ps = min(prov, sp); off -= ps; prov -= ps;
              if (hold) hold--; else prov = 0;
              const int r_ = off - prov; off -= r_ > 40 || r_ < -40 ? r_ / NB : r_; }
            NEWLINE(k)
            cum = min(cum + sp + dp, 16383);
            if (boss) dv = (cum << 18) | dp;
            if (boss && k >= K0 && k + 1 < 2 * NU && (int)(NOW() * 128 - tk) < -60 * 128) {
                WATCH(tk - (XM - XD1) * 128)
                const v4u x = __builtin_amdgcn_raw_buffer_load_b128(__builtin_amdgcn_make_buffer_rsrc(ws + WS_REF + 64, 0, 64 * LSTRIDE, 0x00020000), lane * LSTRIDE, 0, SC1);
                float a[4];
#pragma unroll
                for (int i = 0; i < 4; i++) a[i] = ((x[i] ^ ((rtag + k) << 10)) >> 10) == 0 ? (float)(x[i] & 1023u) : 0.f;
                const int tot = (int)wave_sum(a[0] + a[1] + a[2] + a[3]) >> 6;
                float mn = 0.f;
                if (tot >= 40) {
                    float st = (float)tot * 0.125f;
#pragma unroll
                    for (int i = 0; i < 4; i++, st *= 0.5f) {
                        const float x_ = mn + st;
                        const int c_ = __builtin_popcountll(__builtin_amdgcn_ballot_w64(a[0] >= x_)) + __builtin_popcountll(__builtin_amdgcn_ballot_w64(a[1] >= x_)) +
                                       __builtin_popcountll(__builtin_amdgcn_ballot_w64(a[2] >= x_)) + __builtin_popcountll(__builtin_amdgcn_ballot_w64(a[3] >= x_));
                        mn = c_ >= QN ? x_ : mn;
                    }
                }
                const float c = mn + (float)CAP, fth = (float)(FTH * (2 * NU - (int)k));
                const int cut = (int)wave_sum(fminf(a[0], c) + fminf(a[1], c) + fminf(a[2], c) + fminf(a[3], c)) >> 6, m4 = (int)mn * 4;
                const bool far = __builtin_amdgcn_ballot_w64(fmaxf(fmaxf(a[0], a[1]), fmaxf(a[2], a[3])) > fth) != 0;
                const int pend = yld + sp1 * XM / 320;
                const bool stall = m4 >= SMIN * 4;
                const int nj = max((stall ? tot + SADD : m4) - pend, 0);
                const int up = k >= 6 && tot < tot1 + 32 ? max(tot - tot1, 0) : 0;
                const int ns = stall ? 0 : min(max(max(min(m4 + (tot - m4 + up) * GAIN / 16 - pend - nj, (cut - m4 + up) * GAIN / 16), 0), far ? FLOOR * 4 : 0), 4000);
                int ndp = dp;
                if (k >= (unsigned)KD && tot >= KI && tot < 100 && tot > tot1 && tot < tot1 + 32 && tot1 < tot2 + 32) { ndp = min(dp + DPS, 40); lock = LOCK; }
                else if (k >= (unsigned)KD && dp < DP0 && tot >= WARN && tot < 100 && tot >= tot1) { ndp = dp + 2; lock = LOCK; }
                else if (lock) lock--;
                if (k >= 6 && tot + tot1 <= QUIET && (!lock || dp > DP0)) ndp = max(dp - 1, 0);
                tot2 = tot1; tot1 = tot;
                dv = (min(cum + nj, 16383) << 18) | (ns << 6) | ndp;
                if ((int)(NOW() * 128 - tk) > XL * 128) dv = (cum << 18) | dp;
                else if (lane == 0) GSTS(dec + (k + 1) * 16, (u64)dv | ((u64)rtag << 32));
            }
        }
        WATCH(p >= NPC - 8 ? now - 128 : now + 4096)
    } else if (wave == NSW + 2) {
        const int ql = lane < 48 ? lane : 47;
        unsigned c1, c2, s1, s2;
        unsigned q1, q2, lkz, lks, lvz, lvs;
#define UNIT_LOADS(HEAD) { \
        q1 = ((const unsigned*)(q + (size_t)(HEAD) * DH))[ql]; q2 = ((const unsigned*)(q + (size_t)(HEAD) * DH))[48 + ql]; \
        const size_t so = ((size_t)(HEAD) * SKV + brow0) / 2 + (lane >> 3) * 128 + (lane & 7); \
        lkz = ((const unsigned*)k_zeros)[so]; lks = ((const unsigned*)k_scales)[so]; \
        lvz = ((const unsigned*)v_zeros)[so]; lvs = ((const unsigned*)v_scales)[so]; }
#define UNIT_FILL(SL, U) { \
            v4f qq; \
            qq.x = bfr(bfr(bflo(q1) * bflo(c1)) + bfr(-bflo(q2) * bflo(s1))); \
            qq.y = bfr(bfr(bflo(q2) * bflo(c2)) + bfr(bflo(q1) * bflo(s2))); \
            qq.z = bfr(bfr(bfhi(q1) * bfhi(c1)) + bfr(-bfhi(q2) * bfhi(s1))); \
            qq.w = bfr(bfr(bfhi(q2) * bfhi(c2)) + bfr(bfhi(q1) * bfhi(s2))); \
            *(v2u*)&sm.q[SL][2 * ql] = (v2u){(__float_as_uint(qq.x) >> 16) | __float_as_uint(qq.y), (__float_as_uint(qq.z) >> 16) | __float_as_uint(qq.w)}; \
            *(v4f*)&sm.kz[SL][2 * lane] = (v4f){bflo(lkz), bflo(lks), bfhi(lkz), bfhi(lks)}; \
            *(v4f*)&sm.vz[SL][2 * lane] = (v4f){bflo(lvz), bflo(lvs), bfhi(lvz), bfhi(lvs)}; \
            CBAR(); \
            if (lane == 0) lds_st(&sm.rdy[SL], (U) + 1); \
            CBAR(); }
        asm volatile("s_setprio 3");
        asm volatile("s_waitcnt vmcnt(0)\n\tv_accvgpr_read_b32 %0, a0\n\tv_accvgpr_read_b32 %1, a1\n\tv_accvgpr_read_b32 %2, a2\n\tv_accvgpr_read_b32 %3, a3\n\tv_accvgpr_read_b32 %4, a4\n\t"
                     "v_accvgpr_read_b32 %5, a5\n\tv_accvgpr_read_b32 %6, a8\n\tv_accvgpr_read_b32 %7, a9\n\tv_accvgpr_read_b32 %8, a10\n\tv_accvgpr_read_b32 %9, a11"
                     : "=v"(c1), "=v"(c2), "=v"(s1), "=v"(s2), "=v"(q1), "=v"(q2), "=v"(lkz), "=v"(lks), "=v"(lvz), "=v"(lvs));
#pragma unroll 1
        for (unsigned u = 0; u < NU; u++) {
            if (u) UNIT_LOADS(rank + GSZ * u)
            while (u >= lds_ld(&sm.merged) + NIN) SLEEP(4);
            CBAR();
            UNIT_FILL(u % NIN, u)
            if (u == 1) asm volatile("s_setprio 0");
        }
        while (lds_ld(&sm.merged) < NU) SLEEP(16)
        CBAR();
        const unsigned T = lds_ld(&sm.tag);
        if (lane < NU) __hip_atomic_store((u64*)(ws + WS_LOG + (rank * NSPLIT + split) * LSTRIDE2) + lane, (u64)__float_as_uint(sm.ent[lane][130]) | ((u64)T << 32), __ATOMIC_RELAXED, __HIP_MEMORY_SCOPE_AGENT);
        while (lds_ld(&sm.merged) < NU + 3) SLEEP(16)
        for (;;) {
            const u64 x = __hip_atomic_load((const u64*)(ws + WS_LOG + ((blockIdx.x & 15) * NSPLIT + (lane & 15)) * LSTRIDE2) + (blockIdx.x >> 4), __ATOMIC_RELAXED, __HIP_MEMORY_SCOPE_AGENT);
            if (__builtin_amdgcn_ballot_w64((unsigned)(x >> 32) == T) == ~0ull) {
                const float L = __uint_as_float((unsigned)x);
                const float w = __builtin_amdgcn_exp2f(L - row_max(L));
                sm.fw[lane & 15] = w / row_sum(w);
                CBAR();
                if (lane == 0) lds_st(&sm.fw_rdy, 1u);
                break;
            }
            SLEEP(64)
        }
    } else {
        while (lds_ld(&sm.tag_rdy) == 0) SLEEP(1);
        CBAR();
        const unsigned T = lds_ld(&sm.tag);
        const __amdgpu_buffer_rsrc_t rsrc = __builtin_amdgcn_make_buffer_rsrc(ws + WS_PART + (rank * NSPLIT + split) * 512, 0, NH * NSPLIT * 512, 0x00020000);
        v2u* recs = (v2u*)&sm.cs[0][0];
#pragma unroll
        for (int i = 0; i < NOUT * 3; i++) (&sm.acc[0][0])[i * 64 + lane] = 0.0;
#define RECORD() \
        const float n0 = (float)(a0 - (double)h.y) * h.x, n1 = (float)(a1 - (double)h.y) * h.x, n2 = (float)(a2 - (double)h.y) * h.x; \
        const unsigned e2 = max(__float_as_uint(fmaxf(fmaxf(fabsf(n0), fabsf(n1)), fabsf(n2))) >> 23, 12u); \
        const float f2 = __uint_as_float((266u - e2) << 23); \
        const unsigned m0 = min(max((int)rintf(n0 * f2), -8191), 8191) & 0x3fff, m1 = min(max((int)rintf(n1 * f2), -8191), 8191) & 0x3fff, m2 = min(max((int)rintf(n2 * f2), -8191), 8191) & 0x3fff; \
        const v2u r = {m0 | (m1 << 14) | (m2 << 28), (m2 >> 4) | (e2 << 10) | (T << 18)};
#pragma unroll 1
        for (unsigned u = 0; u < 2 * NU - 1; u++) {
            const unsigned e = u < NU ? u : VENT(u), so = u % NOUT;
            const v4f h = *(const v4f*)&sm.ent[e][128];
            while (lds_ld(&sm.cnt[so]) != 32) SLEEP(1)
            CBAR();
            if (u < NU) {
                const float2 lg = *(const float2*)&sm.ent[e][2 * lane];
                const v4f zs = *(const v4f*)&sm.vz[u % NIN][2 * lane];
                const float m = wave_max(fmaxf(lg.x, lg.y));
                const float p0 = __expf(lg.x - m), p1 = __expf(lg.y - m);
                const float x0 = p0 * zs.y, x1 = p1 * zs.w;
                const unsigned eb = max(__float_as_uint(wave_max(fmaxf(fabsf(x0), fabsf(x1)))) >> 23, 21u);
                const float f = __uint_as_float((275u - eb) << 23);
                const float w0 = rintf(x0 * f), w1 = rintf(x1 * f);
                const float den = wave_sum(p0 + p1);
                const float bz = wave_sum(fmaf(w0, zs.x, w1 * zs.z));
                const float L = fmaf(m, 1.4426950408889634f, __builtin_amdgcn_logf(den));
                *(v2u*)&sm.ent[e][2 * lane] = (v2u){(unsigned)(int)w0, (unsigned)(int)w1};
                if (lane == 0) *(v4f*)&sm.ent[e][128] = (v4f){ldexpf(1.f / den, (int)eb - 148), bz, L, 0.f};
            } else {
                const double a0 = sm.acc[so][lane], a1 = sm.acc[so][64 + lane], a2 = sm.acc[so][128 + lane];
                RECORD()
                recs[(u - NU) * 64 + lane] = r;
                CBAR();
                sm.acc[so][lane] = 0.0; sm.acc[so][64 + lane] = 0.0; sm.acc[so][128 + lane] = 0.0;
            }
            CBAR();
            if (lane == 0) { lds_st(&sm.cnt[so], 0u); CBAR(); lds_st(&sm.merged, u + 1); }
        }
        {
            asm volatile("s_setprio 3");
            wave_sync();
            v4u rr[8]; unsigned po[8];
#pragma unroll
            for (int k = 0; k < 8; k++) { rr[k] = ((const v4u*)recs)[k * 64 + lane]; po[k] = k == 7 && lane >= 32 ? 0x7ffffff0u : (lane & 31) * 16 + (lane < 32 ? VENT(2 * k) : VENT(2 * k + 1)) * (GSZ * NSPLIT * 512); }
            const v4f h = *(const v4f*)&sm.ent[VENT(15)][128];
            asm volatile("" :: "v"(rr[0]), "v"(rr[1]), "v"(rr[2]), "v"(rr[3]), "v"(rr[4]), "v"(rr[5]), "v"(rr[6]), "v"(rr[7]), "v"(po[0]), "v"(po[1]), "v"(po[2]), "v"(po[3]), "v"(po[4]), "v"(po[5]), "v"(po[6]), "v"(po[7]), "v"(h));
            for (;;) {
                const unsigned d = __builtin_amdgcn_readfirstlane(TU(2 * NU).x) + PUBT * 128 - NOW() * 128;
                if (d >= 0x40000u) break;
                if (d > 20 * 128) SLEEP(4)
            }
#pragma unroll
            for (int k = 0; k < 8; k++) __builtin_amdgcn_raw_buffer_store_b128(rr[k], rsrc, po[k], 0, SC1);
            double a0, a1, a2;
            for (;;) {
                const unsigned c = __hip_atomic_load(&sm.cnt[NOUT - 1], __ATOMIC_RELAXED, __HIP_MEMORY_SCOPE_WORKGROUP);
                CBAR();
                a0 = sm.acc[NOUT - 1][lane]; a1 = sm.acc[NOUT - 1][64 + lane]; a2 = sm.acc[NOUT - 1][128 + lane];
                CBAR();
                if (__builtin_amdgcn_readfirstlane(c) == 32) break;
                SLEEP(0)
            }
            RECORD()
            const v4u r2 = {r.x, r.y, (unsigned)__builtin_amdgcn_update_dpp(0, (int)r.x, 0xf5, 0xf, 0xf, true), (unsigned)__builtin_amdgcn_update_dpp(0, (int)r.y, 0xf5, 0xf, 0xf, true)};
            __builtin_amdgcn_raw_buffer_store_b128(r2, rsrc, (lane & 1) ? 0x7ffffff0u : lane * 8, VENT(15) * (GSZ * NSPLIT * 512), SC1);
        }
        return;
    }

}

extern "C" void kernel_launch(void* const* d_in, const int* in_sizes, int n_in,
                              void* d_out, int out_size, void* d_ws, size_t ws_size,
                              hipStream_t stream) {
    const unsigned short* q = (const unsigned short*)d_in[0];
    const int* qk = (const int*)d_in[1];
    const unsigned short* k_scales = (const unsigned short*)d_in[2];
    const unsigned short* k_zeros = (const unsigned short*)d_in[3];
    const int* qv = (const int*)d_in[4];
    const unsigned short* v_scales = (const unsigned short*)d_in[5];
    const unsigned short* v_zeros = (const unsigned short*)d_in[6];
    const unsigned short* q_cos = (const unsigned short*)d_in[7];
    const unsigned short* q_sin = (const unsigned short*)d_in[8];
    const unsigned short* k_cos = (const unsigned short*)d_in[9];
    const unsigned short* k_sin = (const unsigned short*)d_in[10];
    unsigned char* ws = (unsigned char*)d_ws;
    attn_kernel<<<NSPLIT * GSZ, NTHREADS, 0, stream>>>(q, qk, k_scales, k_zeros, qv, v_scales, v_zeros,
                                                       q_cos, q_sin, k_cos, k_sin, (__hip_bfloat16*)d_out, ws);
}
```

```cpp
#include <hip/hip_runtime.h>
#include <hip/hip_bf16.h>
#include <stdint.h>

#define DH 192
#define HALF 96
#define SKV 2048
#define NH 256
#define NSW 8
#define NTHREADS (64 * (NSW + 4))
#define BROWS 128
#define NSPLIT (SKV / BROWS)
#define GSZ 16
#define NU (NH / GSZ)
#define NPC (2 * NU * 32)
#define NIN 4
#define NOUT 4
#define ENTW 132
#define SC1 16
#define PHI 0x0246024671357135ull
#define VORD 0x9876543210afedcbull
#define VENT(V) ((unsigned)(VORD >> (4 * ((V) & 15))) & 15u)
#define PFL 195
#define PER 1262
#define DP0 6
#define P0 1365
#define NBW 2
#define BTH 20
#define X0 250
#define K0 3
#define XM 250
#define XW 40
#define XD1 85
#define XL 90
#define XR 170
#define BK 3
#define RTH 6
#define QN 224
#define GAIN 28
#define CAP 45
#define FLOOR 3
#define FTH 15
#define KD 9
#define KI 14
#define DPS 2
#define QUIET 2
#define WARN 5
#define LOCK 6
#define NB 3
#define STH 90
#define SFIX 220
#define SNOT 0x610c0cu
#define HOLD 4
#define SMIN 100
#define SADD 8
#define PUBT 40
#define POLT 92
#define POLC 120

#define WS_EPOCH 0
#define WS_DEC 8192
#define WS_REF 16384
#define LSTRIDE 4352
#define WS_LOG 524288
#define LSTRIDE2 1408
#define WS_PART 1048576

typedef unsigned int v2u __attribute__((ext_vector_type(2)));
typedef unsigned int v4u __attribute__((ext_vector_type(4)));
typedef int v4i __attribute__((ext_vector_type(4)));
typedef float v2f __attribute__((ext_vector_type(2)));
typedef float v4f __attribute__((ext_vector_type(4)));
typedef unsigned long long u64;

__device__ __forceinline__ float bflo(unsigned u) { return __uint_as_float(u << 16); }
__device__ __forceinline__ float bfhi(unsigned u) { return __uint_as_float(u & 0xffff0000u); }
__device__ __forceinline__ float bfr(float x) {
    __bf16 b = (__bf16)x;
    return __uint_as_float(((unsigned)__builtin_bit_cast(unsigned short, b)) << 16);
}

#define SLEEP(n) SLEEP_(n)
#define SLEEP_(n) asm volatile("s_sleep " #n "\n\tv_add_co_u32 %0, vcc, 1, %0\n\ts_cbranch_vccz 1f\n\ts_endpgm\n1:" : "+v"(spins) :: "vcc", "memory");
#define SPIN0 0xf0000000u
#define CBAR() asm volatile("" ::: "memory")

__device__ __forceinline__ void wave_sync() {
    __builtin_amdgcn_fence(__ATOMIC_RELEASE, "wavefront");
    __builtin_amdgcn_wave_barrier();
    __builtin_amdgcn_fence(__ATOMIC_ACQUIRE, "wavefront");
}
template <int CTRL>
__device__ __forceinline__ float dpp_add(float x) {
    int y = __builtin_amdgcn_update_dpp(0, __float_as_int(x), CTRL, 0xf, 0xf, true);
    return x + __int_as_float(y);
}
template <int CTRL>
__device__ __forceinline__ float dpp_max(float x) {
    int y = __builtin_amdgcn_update_dpp(__float_as_int(x), __float_as_int(x), CTRL, 0xf, 0xf, false);
    return fmaxf(x, __int_as_float(y));
}
__device__ __forceinline__ float row_sum(float x) { x = dpp_add<0x128>(x); x = dpp_add<0x124>(x); x = dpp_add<0x122>(x); return dpp_add<0x121>(x); }
__device__ __forceinline__ float row_max(float m) { m = dpp_max<0x128>(m); m = dpp_max<0x124>(m); m = dpp_max<0x122>(m); return dpp_max<0x121>(m); }
__device__ __forceinline__ float rdlane(float x, int l) { return __int_as_float(__builtin_amdgcn_readlane(__float_as_int(x), l)); }
__device__ __forceinline__ float wave_max(float m) { m = row_max(m); return fmaxf(fmaxf(rdlane(m, 0), rdlane(m, 16)), fmaxf(rdlane(m, 32), rdlane(m, 48))); }
__device__ __forceinline__ float wave_sum(float x) { x = row_sum(x); return (rdlane(x, 0) + rdlane(x, 16)) + (rdlane(x, 32) + rdlane(x, 48)); }
__device__ __forceinline__ unsigned lds_ld(const unsigned* p) { return __builtin_amdgcn_readfirstlane(__hip_atomic_load(p, __ATOMIC_RELAXED, __HIP_MEMORY_SCOPE_WORKGROUP)); }
__device__ __forceinline__ unsigned lds_addr(const void* p) { return (unsigned)(size_t)(const __attribute__((address_space(3))) void*)p; }
__device__ __forceinline__ void lds_st(unsigned* p, unsigned v) { __hip_atomic_store(p, v, __ATOMIC_RELAXED, __HIP_MEMORY_SCOPE_WORKGROUP); }
#define NOW() ((unsigned)__builtin_amdgcn_s_memrealtime())

struct __attribute__((aligned(16))) Smem {
    unsigned cs[32][768];
    double acc[NOUT][DH];
    float ent[NU][ENTW];
    unsigned q[NIN][HALF];
    float2 kz[NIN][BROWS];
    float2 vz[NIN][BROWS];
    v2u tu[2 * NU + 2];
    unsigned grab, merged, tabs, cdone;
    unsigned rdy[NIN], cnt[NOUT];
    unsigned tag_rdy, tag, start, fw_rdy;
    float fw[NSPLIT];
};

#define TU(I) (*(const volatile v2u __attribute__((address_space(3)))*)(size_t)lds_addr(&sm.tu[I]))
#define LDS_ADD(P, V) { u64 ex_; asm volatile("s_mov_b64 %0, exec\n\ts_mov_b64 exec, 1\n\tds_add_u32 %1, %2\n\ts_mov_b64 exec, %0" : "=&s"(ex_) : "v"(lds_addr(P)), "v"(V) : "memory"); }
#define LDS_OR(P, V) { u64 ex_; asm volatile("s_mov_b64 %0, exec\n\ts_mov_b64 exec, 1\n\tds_or_b32 %1, %2\n\ts_mov_b64 exec, %0" : "=&s"(ex_) : "v"(lds_addr(P)), "v"(V) : "memory"); }

__global__ __launch_bounds__(NTHREADS, 3) void attn_kernel(
    const unsigned short* __restrict__ q, const int* __restrict__ qk,
    const unsigned short* __restrict__ k_scales, const unsigned short* __restrict__ k_zeros,
    const int* __restrict__ qv,
    const unsigned short* __restrict__ v_scales, const unsigned short* __restrict__ v_zeros,
    const unsigned short* __restrict__ q_cos, const unsigned short* __restrict__ q_sin,
    const unsigned short* __restrict__ k_cos, const unsigned short* __restrict__ k_sin,
    __hip_bfloat16* __restrict__ out, unsigned char* __restrict__ ws)
{
    __shared__ Smem sm;
    const int tid = threadIdx.x;
    unsigned spins = SPIN0;
#define TOUCH(OFF, BASE) asm volatile("global_load_dword a6, %0, %1" :: "v"(OFF), "s"(BASE) : "a6", "memory");
    const unsigned cl = min(((blockIdx.x >> 3) & 15) * 16 + (tid & 15), PFL - 1u) * 128;
    if (tid < 64) { const char* pc0; asm volatile("s_getpc_b64 %0" : "=s"(pc0)); TOUCH(cl, pc0) }
    __builtin_amdgcn_sched_barrier(0);
    const int hw = __builtin_amdgcn_readfirstlane(tid >> 6);
    const int lane = tid & 63;
    const int split = (blockIdx.x & 3) * 4 + ((blockIdx.x >> 3) & 3);
    const int rank = (blockIdx.x >> 5) * 2 + ((blockIdx.x >> 2) & 1);
    const int brow0 = split * 16;
#define ROWOF(RG) (((RG) >> 2) * 256 + ((RG) & 3) * 4)
    if (hw == 0) {
        typedef const char* const __attribute__((address_space(4))) * kap_t;
        const char* sa = ((kap_t)__builtin_amdgcn_kernarg_segment_ptr())[(0x5623 >> (4 * (split & 3))) & 7];
        unsigned o2 = ((rank + GSZ * (lane >> 3)) * SKV + (lane & 7) * 256 + (brow0 & ~63)) * 2, o3 = o2 + 8 * GSZ * SKV * 2;
        unsigned o4 = (rank + GSZ * ((lane >> 2 & 3) + 4 * (split & 3))) * (DH * 2) + min(lane & 3, 2) * 128;
        asm volatile("" : "+v"(o2), "+v"(o3), "+v"(o4));
        asm volatile("global_load_dword a6, %0, %1 nt\n\tglobal_load_dword a6, %0, %2 nt" :: "v"((lane & 1) * (NH * SKV * DH * 4u - 128u * NH) + blockIdx.x * 128), "s"(qk), "s"(qv) : "a6", "memory");
        TOUCH(o4, q) TOUCH(o2, sa) TOUCH(o3, sa)
        asm volatile("s_barrier\n\ts_waitcnt vmcnt(2)" ::: "memory");
        if (lane == 0) lds_st(&sm.start, NOW() | 1u);
        asm volatile("s_endpgm");
    }
    const unsigned rot = 4 * ((unsigned)(PHI >> (60 - 4 * rank)) & 7u);
#define RGOF(G) ((((G) + rot) & 31u) ^ ((((G) + rot) >> 1) & 2u))
#define CS_DMA(RG) asm volatile("s_mov_b32 m0, %1\n\ts_nop 0\n\tglobal_load_lds_dwordx4 %0, %2\n\ts_mov_b32 exec_hi, 0\n\tglobal_load_lds_dwordx4 %0, %2 offset:1024\n\ts_add_u32 m0, m0, 0x600\n\ts_nop 0\n\t" \
                                "global_load_lds_dwordx4 %0, %3 offset:1024\n\ts_mov_b32 exec_hi, -1\n\tglobal_load_lds_dwordx4 %0, %3" \
                                :: "v"((brow0 + ROWOF(RG)) * (DH * 2) + lane * 16), "s"(lds_addr(&sm.cs[0][0]) + (RG) * 3072), "s"(k_cos), "s"(k_sin) : "scc", "memory");
    if (hw >= 4) CS_DMA(RGOF((unsigned)hw - 4))
    if (hw == 1) {
        const unsigned qo = min(lane, 47) * 4, so = (rank * SKV + brow0) * 2 + (lane >> 3) * 512 + (lane & 7) * 4;
        asm volatile("global_load_dword a0, %0, %3\n\tglobal_load_dword a1, %0, %3 offset:192\n\tglobal_load_dword a2, %0, %4\n\tglobal_load_dword a3, %0, %4 offset:192\n\t"
                     "global_load_dword a4, %1, %5\n\tglobal_load_dword a5, %1, %5 offset:192\n\t"
                     "global_load_dword a8, %2, %6\n\tglobal_load_dword a9, %2, %7\n\tglobal_load_dword a10, %2, %8\n\tglobal_load_dword a11, %2, %9"
                     :: "v"(qo), "v"(qo + rank * (DH * 2)), "v"(so), "s"(q_cos), "s"(q_sin), "s"(q), "s"(k_zeros), "s"(k_scales), "s"(v_zeros), "s"(v_scales)
                     : "a0", "a1", "a2", "a3", "a4", "a5", "a8", "a9", "a10", "a11", "memory");
    }
    if (hw == 3 && lane < 16) (&sm.grab)[lane] = lane ? 0 : 64;
    asm volatile("" :: "s"(q), "s"(qk), "s"(k_scales), "s"(k_zeros), "s"(qv), "s"(v_scales), "s"(v_zeros), "s"(q_cos), "s"(q_sin), "s"(k_cos), "s"(k_sin), "s"(out), "s"(ws));
    asm volatile("s_waitcnt lgkmcnt(0)\n\ts_barrier" ::: "memory");
    const int wave = (int)(0x76543210b9a8ull >> (4 * hw)) & 15;
    unsigned ts;
#define START() { while ((ts = lds_ld(&sm.start)) == 0) SLEEP(1) }

    if (__builtin_expect(wave < NSW, 1)) {
#define REPACK(RG) { \
        unsigned* cw = &sm.cs[RG][0]; \
        const unsigned* l = cw + rsel * (DH / 2) + qoff; \
        unsigned r[2][6]; \
        _Pragma("unroll") \
        for (int j = 0; j < 2; j++) { \
            unsigned e[4][2]; \
            _Pragma("unroll") \
            for (int k = 0; k < 4; k++) { \
                const unsigned* a = l + j * DH + (k & 1) * 48 + (k >> 1) * 384;       \
                unsigned x0 = a[0], x1 = a[1]; \
                e[k][0] = __builtin_amdgcn_alignbit(x1, x0, sh); \
                e[k][1] = x1 >> sh; \
            } \
            r[j][0] = (e[0][0] & 0xffffu) | (e[3][0] << 16); \
            r[j][1] = (e[0][0] >> 16) | (e[3][0] & 0xffff0000u); \
            r[j][2] = (e[0][1] & 0xffffu) | (e[3][1] << 16); \
            r[j][3] = (e[1][0] & 0xffffu) | (e[2][0] << 16); \
            r[j][4] = (e[1][0] >> 16) | (e[2][0] & 0xffff0000u); \
            r[j][5] = (e[1][1] & 0xffffu) | (e[2][1] << 16); \
        } \
        wave_sync(); \
        _Pragma("unroll") \
        for (int j = 0; j < 2; j++) { *(v4u*)&cw[j * 256 + lane * 4] = (v4u){r[j][0], r[j][1], r[j][2], r[j][3]}; *(v2u*)&cw[512 + j * 128 + lane * 2] = (v2u){r[j][4], r[j][5]}; } \
        wave_sync(); \
        LDS_OR(&sm.tabs, 1u << (RG)) }
        const int sub = lane & 31;
        const int rsel = lane >> 5;
        const unsigned sh = (sub & 1) * 16;
        const int qoff = 3 * (sub >> 1) + (sub & 1);
        const char* kbase = (const char*)qk + (size_t)(rank * SKV + brow0) * (DH * 4);
        const char* vbase = (const char*)qv + (size_t)(rank * SKV + brow0) * (DH * 4);
        const unsigned koff = (rsel * DH + 3 * sub) * 4, voff = 3 * lane * 4;
#define AR(S) (16 + (S) * 8)
        asm volatile("" ::: "a16", "a17", "a18", "a19", "a20", "a21", "a22", "a23", "a24", "a25", "a26", "a27", "a28", "a29", "a30", "a31",
                            "a32", "a33", "a34", "a35", "a36", "a37", "a38", "a39", "a40", "a41", "a42", "a43", "a44", "a45", "a46", "a47");
        asm volatile("" ::: "a48", "a49", "a50", "a51", "a52", "a53", "a54", "a55", "a56", "a57", "a58", "a59", "a60", "a61", "a62", "a63");
        int ta[3], tb[3];
#define GLD(A, OFF, BASE, IMM) asm volatile("global_load_dwordx3 a[%c0:%c1], %2, %3 offset:%4 nt" :: "i"(A), "i"((A) + 2), "v"(OFF), "s"(BASE), "i"(IMM))
#define ISSUE(S, J, BASE, OA, OB) { GLD(AR(S), OA, BASE, (J) * 1536); GLD(AR(S) + 4, OB, BASE, (J) * 1536); }
#define WAITT(S, N) asm volatile("s_waitcnt vmcnt(%6)\n\tv_accvgpr_read_b32 %0, a[%c7]\n\tv_accvgpr_read_b32 %1, a[%c8]\n\tv_accvgpr_read_b32 %2, a[%c9]\n\t" \
                              "v_accvgpr_read_b32 %3, a[%c10]\n\tv_accvgpr_read_b32 %4, a[%c11]\n\tv_accvgpr_read_b32 %5, a[%c12]" \
                              : "=v"(ta[0]), "=v"(ta[1]), "=v"(ta[2]), "=v"(tb[0]), "=v"(tb[1]), "=v"(tb[2]) \
                              : "i"(N), "i"(AR(S)), "i"(AR(S) + 1), "i"(AR(S) + 2), "i"(AR(S) + 4), "i"(AR(S) + 5), "i"(AR(S) + 6))
        const unsigned koffb = koff + 384, voffb = voff + 768;
        unsigned u = 0, rg, np = 0;
#define RG0(C) RGOF((unsigned)(wave + 8 * (C)))
        const float scale = 0.07216878364870322f;
        double acc[3] = {0.0, 0.0, 0.0};
        bool chg, none = false;
        unsigned Qp[3], Qn[3];
        float kz4[2], ksc, kres[2];
        v4u ca[2]; v2u cb[2];
        v2f qe[2][3], A[2];
        const char* nb; unsigned oa, ob;
        unsigned g, gu, mg, nrg, tg, dg, par = 0;
        int early;
        const int bth = wave < NBW ? BTH * 128 : 0x3fffffff;
        const unsigned grab_adr = lds_addr(&sm.grab);
        asm volatile("v_accvgpr_write_b32 a13, 1" ::: "a13", "a15");
#define COUNT(ADR) { u64 ex_; asm volatile("s_mov_b64 %0, exec\n\ts_mov_b64 exec, 1\n\tds_add_rtn_u32 a15, %1, a13\n\ts_mov_b64 exec, %0" : "=&s"(ex_) : "v"(ADR) : "memory"); }
#define GRABBED() { unsigned t_; asm volatile("s_waitcnt lgkmcnt(0)\n\tv_accvgpr_read_b32 %1, a15\n\ts_nop 0\n\tv_readfirstlane_b32 %0, %1\n\ts_nop 1" : "=s"(g), "=v"(t_)); }
#define NEXT1() const v2u x_ = TU(g >> 5); u64 tn_; asm volatile("s_memrealtime %0" : "=s"(tn_));
#define NEXT2() { \
            chg = (g >> 5) != u; none = g >= NPC; \
            const bool nv = g >= NPC / 2; \
            nrg = RGOF(g); \
            unsigned boff; asm("s_mul_i32 %0, %1, 0x300" : "=s"(boff) : "s"((nv ? VENT(g >> 5) : g >> 5) * (GSZ * SKV) + ROWOF(nrg) + 2)); \
            nb = (nv ? vbase : kbase) + boff; \
            oa = nv ? voff : koff; ob = nv ? voffb : koffb; \
            asm volatile("s_waitcnt lgkmcnt(0)" : "+s"(tn_) : "s"(nb), "v"(oa), "v"(ob)); \
            tg = __builtin_amdgcn_readfirstlane(x_.x) + (g & 31) * __builtin_amdgcn_readfirstlane(x_.y); \
            early = none ? 0 : (int)((unsigned)tn_ * 128 - tg) - bth; \
            if (early > 0) asm volatile("s_cmp_lg_u32 %3, 0\n\ts_cbranch_scc1 1f\n\t" L4(32) "\ts_branch 2f\n1:\n\t" L4(16) "2:\n\ts_mov_b64 vcc, exec\n\ts_mov_b64 exec, 1\n\tds_add_rtn_u32 a15, %4, a13\n\ts_mov_b64 exec, vcc" \
                                    :: "v"(oa), "v"(ob), "s"(nb), "s"(pr), "v"(grab_adr) : "scc", "vcc", "memory"); }
#define L4(A) "global_load_dwordx3 a[" #A ":" #A "+2], %0, %2 offset:-1536 nt\n\tglobal_load_dwordx3 a[" #A "+4:" #A "+6], %1, %2 offset:-1536 nt\n\t" \
              "global_load_dwordx3 a[" #A "+8:" #A "+10], %0, %2 nt\n\tglobal_load_dwordx3 a[" #A "+12:" #A "+14], %1, %2 nt\n"
#define GATE(...) \
            asm volatile("s_setprio 0\n1:\n\ts_memrealtime vcc\n\ts_waitcnt lgkmcnt(0)\n\ts_lshl_b32 vcc_lo, vcc_lo, 7\n\ts_sub_u32 %0, %1, vcc_lo\n\ts_cmp_lt_u32 %0, 0x40000\n\ts_cbranch_scc0 2f\n\t" \
                         "s_cmp_lt_u32 %0, %2\n\ts_cbranch_scc1 1b\n\ts_sleep %3\n\ts_branch 1b\n2:\n\ts_setprio 2" : "=&s"(dg) : "s"(tg), "i"(10 * 128), "i"(2), __VA_ARGS__ : "scc", "vcc", "memory");
#define RD6(A) "v_accvgpr_read_b32 %0, a[%c6+" #A "]\n\tv_accvgpr_read_b32 %1, a[%c6+" #A "+1]\n\tv_accvgpr_read_b32 %2, a[%c6+" #A "+2]\n\t" \
               "v_accvgpr_read_b32 %3, a[%c6+" #A "+4]\n\tv_accvgpr_read_b32 %4, a[%c6+" #A "+5]\n\tv_accvgpr_read_b32 %5, a[%c6+" #A "+6]\n"
#define LD2(A) "global_load_dwordx3 a[%c6+" #A ":%c6+" #A "+2], %7, %9 offset:%10 nt\n\tglobal_load_dwordx3 a[%c6+" #A "+4:%c6+" #A "+6], %8, %9 offset:%10 nt"
#define OUT6 "=&v"(ta[0]), "=&v"(ta[1]), "=&v"(ta[2]), "=&v"(tb[0]), "=&v"(tb[1]), "=&v"(tb[2])
#define WI(A, B, TAIL) "s_cmp_gt_i32 %13, 0\n\ts_cbranch_scc1 3f\n\ts_waitcnt vmcnt(2)\n\t" LD2(A) TAIL "\n3:\n\ts_waitcnt vmcnt(%c14)\n\t" RD6(B)
#define WAITISSUE(S, TAIL, ...) asm volatile("s_cmp_lg_u32 %12, 0\n\ts_cbranch_scc1 1f\n\t" WI(16, 0, TAIL) "\ts_branch 2f\n1:\n\t" WI(0, 16, TAIL) "2:" \
                              : OUT6 : "i"(AR(S)), "v"(oa), "v"(ob), "s"(nb), "i"(((S) - 1) * 1536), "v"(grab_adr), "s"(pr), "s"(early), "i"(6 - 2 * (S)), __VA_ARGS__ : "scc", "vcc", "memory")
#define LASTT(S) asm volatile("s_waitcnt vmcnt(%c8)\n\ts_cmp_lg_u32 %7, 0\n\ts_cbranch_scc1 1f\n\t" RD6(0) "\ts_branch 2f\n1:\n\t" RD6(16) "2:" : OUT6 : "i"(AR(S)), "s"(pr), "i"(2 - 2 * (S)) : "scc")
#define DRAW "\n\ts_mov_b64 vcc, exec\n\ts_mov_b64 exec, 1\n\tds_add_rtn_u32 a15, %11, a13\n\ts_mov_b64 exec, vcc"
#define K_TAB() { _Pragma("unroll") for (int p = 0; p < 2; p++) { \
            const unsigned* cw = &sm.cs[rg][0]; \
            ca[p] = *(const v4u*)&cw[p * 256 + lane * 4]; cb[p] = *(const v2u*)&cw[512 + p * 128 + lane * 2]; } }
#define K_PRE(P) { \
        float e0, e1, e2, f0, f1, f2; \
        asm("v_dot2_f32_bf16 %0, %6, %12, 0\n\tv_dot2_f32_bf16 %3, %9, %15, 0\n\tv_dot2_f32_bf16 %1, %7, %13, 0\n\tv_dot2_f32_bf16 %4, %10, %16, 0\n\t" \
            "v_dot2_f32_bf16 %2, %8, %14, 0\n\tv_dot2_f32_bf16 %5, %11, %17, 0\n\ts_nop 2" \
            : "=&v"(e0), "=&v"(e1), "=&v"(e2), "=&v"(f0), "=&v"(f1), "=&v"(f2) \
            : "v"(Qp[0]), "v"(Qp[1]), "v"(Qp[2]), "v"(Qn[0]), "v"(Qn[1]), "v"(Qn[2]), \
              "v"(ca[P].x), "v"(ca[P].y), "v"(ca[P].z), "v"(ca[P].w), "v"(cb[P].x), "v"(cb[P].y)); \
        qe[P][0] = (v2f){e0, f0}; qe[P][1] = (v2f){e1, f1}; qe[P][2] = (v2f){e2, f2}; \
        A[P] = (qe[P][0] + qe[P][1] + qe[P][2]) * (v2f){-kz4[P], -kz4[P]}; }
#define K_SUM(P) { \
        _Pragma("unroll") \
        for (int i = 0; i < 3; i++) { v2f x = {(float)ta[i], (float)tb[i]}; A[P] = qe[P][i] * x + A[P]; } \
        kres[P] = A[P].x + A[P].y; }
#define K_RED2() { \
        const bool o1 = lane & 1; \
        float sx = (o1 ? kres[1] : kres[0]) + __int_as_float(__builtin_amdgcn_update_dpp(0, __float_as_int(o1 ? kres[0] : kres[1]), 0xb1, 0xf, 0xf, true)); \
        sx = dpp_add<0x4e>(sx); sx = dpp_add<0x124>(sx); sx = dpp_add<0x128>(sx); \
        const auto sw = __builtin_amdgcn_permlane16_swap(__float_as_uint(sx), __float_as_uint(sx), false, false); \
        sx = __uint_as_float(sw[0]) + __uint_as_float(sw[1]); \
        if ((lane & 30) == 0) sm.ent[u][rg * 4 + 2 * (lane & 1) + rsel] = sx * ksc; }
#define LDV(P) __hip_atomic_load(P, __ATOMIC_RELAXED, __HIP_MEMORY_SCOPE_WORKGROUP)
#define RDYWAIT(SL, SEQ) { while (lds_ld(&sm.rdy[SL]) != (SEQ)) SLEEP(1); CBAR(); }
#define LOADZ() { _Pragma("unroll") for (int p = 0; p < 2; p++) kz4[p] = sm.kz[u % NIN][rg * 4 + 2 * p + rsel].x; ksc = sm.kz[u % NIN][rg * 4 + 2 * (lane & 1) + rsel].y * scale; }
#define LOADQ() { _Pragma("unroll") for (int i = 0; i < 3; i++) { Qp[i] = sm.q[u % NIN][3 * sub + i]; Qn[i] = __builtin_amdgcn_alignbit(Qp[i], Qp[i], 16) ^ 0x80000000u; } }
#define FLUSH(ISV) { \
            mg = __builtin_amdgcn_readfirstlane(mg); \
            while (u >= mg + NOUT) { SLEEP(1); mg = lds_ld(&sm.merged); } \
            CBAR(); \
            if (ISV) { const unsigned aa = lds_addr(&sm.acc[u % NOUT][lane]); \
                asm volatile("ds_add_f64 %0, %1\n\tds_add_f64 %0, %2 offset:512\n\tds_add_f64 %0, %3 offset:1024" :: "v"(aa), "v"(acc[0]), "v"(acc[1]), "v"(acc[2]) : "memory"); \
                acc[0] = 0.0; acc[1] = 0.0; acc[2] = 0.0; } \
            CBAR(); \
            LDS_ADD(&sm.cnt[u % NOUT], np) \
            CBAR(); \
            np = 0; }
#define ADVANCE() { u = gu; rg = nrg; }

#define REQ0(N, S) { tg = ts * 128 + (wave + 8 * (N)) * P0; \
            const char* kb = kbase + (((N) >> 2) * (GSZ * SKV) + ROWOF(RG0((N) & 3))) * (DH * 4); GATE("s"(kb)) ISSUE(S, 0, kb, koff, koffb) ISSUE((S) + 1, 1, kb, koff, koffb) }
#define USE0(N, S, W) { u = (N) >> 2; rg = RG0((N) & 3); K_TAB() LOADQ() LOADZ() K_PRE(0) K_PRE(1) WAITT(S, W); K_SUM(0) WAITT((S) + 1, W); K_SUM(1) K_RED2() }
#define VMW(N) asm volatile("s_waitcnt vmcnt(" #N ")" ::: "memory");
        START()
        REQ0(0, 0) CS_DMA(RG0(1))
        VMW(8) REPACK(RG0(0))
        REQ0(1, 2)
        RDYWAIT(0, 1u)
        USE0(0, 0, 8)
        VMW(4) REPACK(RG0(1))
        REQ0(2, 0) CS_DMA(RG0(2))
        USE0(1, 2, 8)
        REQ0(3, 2)
        VMW(4) REPACK(RG0(2))
        USE0(2, 0, 4)
        REQ0(4, 0) CS_DMA(RG0(3))
        v2f qe2[2][2][3], A2[2][2]; float ks2[2];
        RDYWAIT(1, 2u)
        u = 1;
        LOADQ()
#pragma unroll
        for (int j = 0; j < 2; j++) {
            rg = RG0(j); K_TAB() LOADZ() K_PRE(0) K_PRE(1)
            ks2[j] = ksc;
#pragma unroll
            for (int p = 0; p < 2; p++) { A2[j][p] = A[p]; qe2[j][p][0] = qe[p][0]; qe2[j][p][1] = qe[p][1]; qe2[j][p][2] = qe[p][2]; }
        }
        asm volatile("" :: "v"(A2[0][0]), "v"(A2[0][1]), "v"(A2[1][0]), "v"(A2[1][1]), "v"(ks2[0]), "v"(ks2[1]));
        REQ0(5, 4)
        VMW(4) REPACK(RG0(3))
        USE0(3, 2, 4)
        CBAR();
        LDS_ADD(&sm.cnt[0], 4u)
        REQ0(6, 2)
        u = 1;
#define FIN0(J, S, W) { rg = RG0(J); ksc = ks2[J]; \
            _Pragma("unroll") for (int p = 0; p < 2; p++) { A[p] = A2[J][p]; qe[p][0] = qe2[J][p][0]; qe[p][1] = qe2[J][p][1]; qe[p][2] = qe2[J][p][2]; } \
            WAITT(S, W); K_SUM(0) WAITT((S) + 1, W); K_SUM(1) K_RED2() }
        FIN0(0, 0, 8)
        FIN0(1, 4, 4)
        REQ0(7, 0)
        COUNT(grab_adr)
        USE0(6, 2, 4)
        rg = RG0(3); np = 3;
        GRABBED()
#pragma unroll 1
        while (u < NU) {
            const unsigned pr = __builtin_amdgcn_readfirstlane(par); par ^= 1;
            NEXT1()
            const unsigned tb_ = LDV(&sm.tabs), fl = LDV(&sm.rdy[u % NIN]);
            mg = LDV(&sm.merged);
            CBAR();
            K_TAB() LOADQ() LOADZ()
            NEXT2()
            if (__builtin_expect(__builtin_amdgcn_readfirstlane(fl) != u + 1 || !((__builtin_amdgcn_readfirstlane(tb_) >> rg) & 1u), 0)) {
                while (!((lds_ld(&sm.tabs) >> rg) & 1u)) SLEEP(1)
                RDYWAIT(u % NIN, u + 1)
                K_TAB() LOADQ() LOADZ()
            }
            K_PRE(0) K_PRE(1)
            if (early <= 0) GATE("v"(A[0]), "v"(A[1]), "v"(oa), "v"(ob), "s"(nb), "s"(pr))
            WAITISSUE(0, DRAW, "v"(A[0]));
            K_SUM(0)
            WAITISSUE(1, "", "v"(A[1]));
            K_SUM(1)
            gu = g >> 5;
            GRABBED()
            K_RED2()
            np++;
            if (chg) FLUSH(false)
            ADVANCE()
        }
#pragma unroll 1
        while (u < 2 * NU) {
            const unsigned pr = __builtin_amdgcn_readfirstlane(par); par ^= 1;
            NEXT1()
            mg = LDV(&sm.merged);
            CBAR();
            const unsigned ve = VENT(u);
            v4i pw = *(const v4i*)&sm.ent[ve][rg * 4];
            NEXT2()
            if (__builtin_expect(__builtin_amdgcn_readfirstlane(mg) <= ve, 0)) { while (lds_ld(&sm.merged) <= ve) SLEEP(1); CBAR(); pw = *(const v4i*)&sm.ent[ve][rg * 4]; }
            const double w0 = pw.x, w1 = pw.y, w2 = pw.z, w3 = pw.w;
            if (none) { LASTT(0); } else { if (early <= 0) GATE("s"(pr), "v"(w0), "v"(w1), "v"(w2), "v"(w3), "v"(oa), "v"(ob), "s"(nb)) WAITISSUE(0, DRAW, "v"(w0)); }
#pragma unroll
            for (int i = 0; i < 3; i++) acc[i] = fma(w1, (double)tb[i], fma(w0, (double)ta[i], acc[i]));
            if (none) { LASTT(1); } else WAITISSUE(1, "", "v"(w2));
#pragma unroll
            for (int i = 0; i < 3; i++) acc[i] = fma(w3, (double)tb[i], fma(w2, (double)ta[i], acc[i]));
            gu = g >> 5;
            np++;
            if (chg) FLUSH(true)
            GRABBED()
            ADVANCE()
        }

        {
            const int head = blockIdx.x;
            while (lds_ld(&sm.fw_rdy) == 0) SLEEP(2)
            CBAR();
            const unsigned tt = lds_ld(&sm.tag) << 18;
            const float wh = sm.fw[wave + 8 * rsel];
            const unsigned ro = (wave + 8 * rsel) * 512 + sub * 16;
            const char* rb = (const char*)ws + WS_PART + head * (NSPLIT * 512);
            v4f* pb = (v4f*)((char*)&sm.cs[0][0] + 16384);
            __hip_bfloat16* o = out + (size_t)head * DH + 6 * sub + 3 * rsel;
            tg = __builtin_amdgcn_readfirstlane(TU(2 * NU).x) + ((head >> 4) == VENT(15) ? POLC : POLT) * 128;
            GATE("s"(tt), "v"(wh), "v"(ro), "s"(rb), "v"(o))
            v4u R;
#define PLD(A) "global_load_dwordx4 a[" #A ":" #A "+3], %6, %7 sc1\n\t"
#define PCK(A, N, L) "s_waitcnt vmcnt(" #N ")\n\tv_accvgpr_read_b32 %1, a[" #A "+1]\n\tv_accvgpr_read_b32 %3, a[" #A "+3]\n\tv_accvgpr_read_b32 %0, a[" #A "]\n\tv_accvgpr_read_b32 %2, a[" #A "+2]\n\t" \
                     "v_xor_b32 %1, %8, %1\n\tv_xor_b32 %3, %8, %3\n\tv_or_b32 %4, %1, %3\n\tv_cmp_gt_u32 vcc, %9, %4\n\ts_cmp_eq_u64 vcc, exec\n\ts_cbranch_scc1 " L "\n\t"
            { unsigned t_;
              asm volatile("s_cmp_eq_u32 %10, %12\n\ts_cbranch_scc1 1f\n\t" PLD(16) PCK(16, 0, "3f") "1:\n\t" PLD(16) "s_sleep %11\n\t" PLD(20) "s_sleep %11\n\t" PLD(24)
                           "2:\n\t" PCK(16, 2, "3f") PLD(16) PCK(20, 2, "3f") PLD(20) PCK(24, 2, "3f") PLD(24)
                           "v_add_co_u32 %5, vcc, 64, %5\n\ts_cbranch_vccz 2b\n\ts_endpgm\n3:"
                           : "=&v"(R.x), "=&v"(R.y), "=&v"(R.z), "=&v"(R.w), "=&v"(t_), "+v"(spins) : "v"(ro), "s"(rb), "s"(tt), "s"(0x40000u), "s"(head >> 4), "i"(5), "i"(VENT(15)) : "scc", "vcc", "memory"); }
            const float s0 = ldexpf(wh, (int)(R.y >> 10) - 139), s1 = ldexpf(wh, (int)(R.w >> 10) - 139);
            const float f[6] = {s0 * (float)(int)__builtin_amdgcn_sbfe(R.x, 0, 14), s0 * (float)(int)__builtin_amdgcn_sbfe(R.x, 14, 14), s0 * (float)(int)__builtin_amdgcn_sbfe(__builtin_amdgcn_alignbit(R.y, R.x, 28), 0, 14),
                                s1 * (float)(int)__builtin_amdgcn_sbfe(R.z, 0, 14), s1 * (float)(int)__builtin_amdgcn_sbfe(R.z, 14, 14), s1 * (float)(int)__builtin_amdgcn_sbfe(__builtin_amdgcn_alignbit(R.w, R.z, 28), 0, 14)};
            float sv[3];
#pragma unroll
            for (int i = 0; i < 3; i++) {
                const auto sw = __builtin_amdgcn_permlane32_swap(__float_as_uint(f[i]), __float_as_uint(f[i + 3]), false, false);
                sv[i] = __uint_as_float(sw[0]) + __uint_as_float(sw[1]);
            }
            pb[wave * 64 + lane] = (v4f){sv[0], sv[1], sv[2], 0.f};
            CBAR();
            COUNT(grab_adr + 12)
            v4f ps[NSW];
#pragma unroll
            for (int w = 0; w < NSW; w++) ps[w] = pb[w * 64 + lane];
            GRABBED()
            if (g == NSW - 1) {
                const v4f sum = ((ps[0] + ps[1]) + (ps[2] + ps[3])) + ((ps[4] + ps[5]) + (ps[6] + ps[7]));
                o[0] = __float2bfloat16(sum.x); o[1] = __float2bfloat16(sum.y); o[2] = __float2bfloat16(sum.z);
            }
        }
        return;
    }

    if (wave == NSW + 1) {
        u64 E; asm volatile("s_setprio 3\n\ts_load_dwordx2 %0, %1, 0\n\ts_waitcnt lgkmcnt(0)" : "=s"(E) : "s"(ws + WS_EPOCH) : "memory");
        START()
        unsigned t2 = ts * 128 + 64 * P0;
        int off = 0, off1 = 0, hold = 0, cum = 0, dp = DP0, sp = 0;
#define NOM(U) (((int)(U) - 2) * 32 * PER)
#define NEWLINE(K) { if (lane >= (K) && lane <= 2 * NU) sm.tu[lane] = (v2u){t2 + NOM(lane) + (cum + (lane - (int)(K)) * dp + (lane > (K) ? sp + off : off1)) * 32, \
                                                                            (unsigned)(PER + dp + (lane == (K) ? sp + off - off1 : 0))}; }
        NEWLINE(2)
        if (lane == 0) sm.tu[1] = (v2u){t2 - 32 * P0, (unsigned)P0};
        const u64 E1 = E + 1;
        const unsigned rtag = (unsigned)E1 * 0x9e3779b1u;
        const bool boss = blockIdx.x == 0;
        u64* const slot = (u64*)(ws + WS_REF + (blockIdx.x & 63) * LSTRIDE + (blockIdx.x >> 6) * 16);
        const u64* const slots = (const u64*)(ws + WS_REF + lane * LSTRIDE);
        u64* const dec = (u64*)(ws + WS_DEC);
#define GLDS(P) __hip_atomic_load(P, __ATOMIC_RELAXED, __HIP_MEMORY_SCOPE_AGENT)
#define GSTS(P, V) __hip_atomic_store(P, V, __ATOMIC_RELAXED, __HIP_MEMORY_SCOPE_AGENT)
#define UNTIL(T) while ((unsigned)((T) - NOW() * 128) < 0x40000u) SLEEP(2)
        if (lane < 2) GSTS(slot + lane, lane ? E1 : (u64)ts | ((u64)rtag << 32));
        if (lane == 0) { sm.tag = (unsigned)E1; CBAR(); lds_st(&sm.tag_rdy, 1u); }
        unsigned dv = 0;
        bool joined = false;
#define JOIN(T0) { const int d0 = min(max((int)((T0) - ts), -1000), 1000); off -= d0 * 4; t2 += d0 * 128; joined = true; }
        unsigned pw_ = 0, tw_ = 0, now, p; int prov = 0;
#define LOOK() { u64 t_; asm volatile("ds_read_b32 %1, %2\n\ts_memrealtime %0\n\ts_waitcnt lgkmcnt(0)" : "=s"(t_), "=v"(p) : "v"(lds_addr(&sm.grab)) : "memory"); \
                 p = __builtin_amdgcn_readfirstlane(p) - 8; now = (unsigned)t_ * 128; }
#define LATENESS(P, T) ({ const v2u e = TU((P) >> 5); (int)((T) - __builtin_amdgcn_readfirstlane(e.x) - ((P) & 31) * __builtin_amdgcn_readfirstlane(e.y)); })
#define WATCH(T) for (;;) { \
            LOOK() \
            if (__builtin_expect(now - tw_ > STH * 128u && pw_ >= 64 && p < NPC - 8, 0)) { \
                const int lw = max(LATENESS(pw_, tw_), 0); \
                if (LATENESS(p, now) - lw > STH * 128) \
                    for (int d0_ = hold || ((SNOT >> (p >> 5)) & 1) ? 0 : SFIX * 4;; d0_ = 0) { \
                        const int d_ = min(max((LATENESS(p, now) - lw) >> 5, d0_), 1600); \
                        if (lane <= 2 * NU) atomicAdd((unsigned*)&sm.tu[lane], d_ * 32u); \
                        off += d_; prov += d_; hold = HOLD; \
                        if (p != pw_ || prov > 8000) break; \
                        SLEEP(1) \
                        LOOK() \
                    } \
                } \
            if (p != pw_) { pw_ = p; tw_ = now; } \
            if ((unsigned)((T) - now) >= 0x40000u) break; \
            if ((unsigned)((T) - now) > 25 * 128) SLEEP(7) else SLEEP(1) }
        if (boss) {
            TOUCH((lane & 31) * 128, dec)
            UNTIL((ts + X0) * 128)
            for (;;) {
                bool ok = true; float sum = 0.f;
#pragma unroll
                for (int i = 0; i < 4; i++) { const u64 x = GLDS(slots + 2 * i), e = GLDS(slots + 2 * i + 1); ok &= e == E1 && (unsigned)(x >> 32) == rtag; sum += (float)min(max((int)((unsigned)x - ts), -1000), 1000); }
                if (__builtin_amdgcn_ballot_w64(ok) == ~0ull) { dv = ts + (int)(wave_sum(sum) * (1.f / 256)); break; }
                SLEEP(8)
            }
            if (lane == 0) { GSTS(dec, (u64)dv | ((u64)rtag << 32)); GSTS((u64*)(ws + WS_EPOCH), E1); }
            JOIN(dv)
            dv = 0;
        }
        int sp1 = 0, tot1 = 0, tot2 = 0, lock = 0;
#pragma unroll 1
        for (unsigned k = 2; k < 2 * NU; k++) {
            const unsigned tk = t2 + NOM(k) + cum * 32;
            if (k >= K0 && k + 1 < 2 * NU) {
                WATCH(tk - (XM + XW) * 128)
                int sum = 0, n = 0;
                do { LOOK() sum += LATENESS(p, now) >> 7; n++; } while ((unsigned)(tk - XM * 128 - now) < 0x40000u);
                const int l = (int)((float)sum * __builtin_amdgcn_rcpf((float)n)) - BK + prov / 4;
                if (k == K0 && l - prov / 4 > RTH) {
                    const int d_ = min(l * 4 - prov, 1600);
                    if (lane <= 2 * NU) atomicAdd((unsigned*)&sm.tu[lane], d_ * 32u);
                    off += d_; prov += d_; hold = 1;
                }
                if (l > RTH && lane == 0) GSTS((unsigned*)(ws + WS_REF + (blockIdx.x & 63) * LSTRIDE + 64) + (blockIdx.x >> 6), (unsigned)min(l, 1000) | ((rtag + k) << 10));
            }
            const int cum0 = cum;
            bool got = boss;
            if (!boss && (k > K0 || !joined)) {
                WATCH(tk - XR * 128)
                const u64 x = GLDS(dec + (joined ? k * 16 : 0));
                dv = __builtin_amdgcn_readfirstlane((unsigned)x);
                got = __builtin_amdgcn_readfirstlane((unsigned)(x >> 32)) == rtag;
                if (got && !joined) { JOIN(dv) got = false; }
            }
            sp1 = sp; sp = 0;
            if (got && k > K0) { cum = dv >> 18; sp = dv >> 6 & 4095u; dp = dv & 63u; }
            const int yld = cum - cum0 + sp;
            { const int pj = min(prov, cum - cum0); off -= pj; prov -= pj; off1 = off;
              const int ps = min(prov, sp); off -= ps; prov -= ps;
              if (hold) hold--; else prov = 0;
              const int r_ = off - prov; off -= r_ > 40 || r_ < -40 ? r_ / NB : r_; }
            NEWLINE(k)
            cum = min(cum + sp + dp, 16383);
            if (boss) dv = (cum << 18) | dp;
            if (boss && k >= K0 && k + 1 < 2 * NU && (int)(NOW() * 128 - tk) < -60 * 128) {
                WATCH(tk - (XM - XD1) * 128)
                const v4u x = __builtin_amdgcn_raw_buffer_load_b128(__builtin_amdgcn_make_buffer_rsrc(ws + WS_REF + 64, 0, 64 * LSTRIDE, 0x00020000), lane * LSTRIDE, 0, SC1);
                float a[4];
#pragma unroll
                for (int i = 0; i < 4; i++) a[i] = ((x[i] ^ ((rtag + k) << 10)) >> 10) == 0 ? (float)(x[i] & 1023u) : 0.f;
                const int tot = (int)wave_sum(a[0] + a[1] + a[2] + a[3]) >> 6;
                float mn = 0.f;
                if (tot >= 40) {
                    float st = (float)tot * 0.125f;
#pragma unroll
                    for (int i = 0; i < 4; i++, st *= 0.5f) {
                        const float x_ = mn + st;
                        const int c_ = __builtin_popcountll(__builtin_amdgcn_ballot_w64(a[0] >= x_)) + __builtin_popcountll(__builtin_amdgcn_ballot_w64(a[1] >= x_)) +
                                       __builtin_popcountll(__builtin_amdgcn_ballot_w64(a[2] >= x_)) + __builtin_popcountll(__builtin_amdgcn_ballot_w64(a[3] >= x_));
                        mn = c_ >= QN ? x_ : mn;
                    }
                }
                const float c = mn + (float)CAP, fth = (float)(FTH * (2 * NU - (int)k));
                const int cut = (int)wave_sum(fminf(a[0], c) + fminf(a[1], c) + fminf(a[2], c) + fminf(a[3], c)) >> 6, m4 = (int)mn * 4;
                const bool far = __builtin_amdgcn_ballot_w64(fmaxf(fmaxf(a[0], a[1]), fmaxf(a[2], a[3])) > fth) != 0;
                const int pend = yld + sp1 * XM / 320;
                const bool stall = m4 >= SMIN * 4 || k <= K0 + 1;
                const int nj = max((stall ? tot + (k <= K0 + 1 ? tot / 8 : SADD) : m4) - pend, 0);
                const int up = k >= 6 && tot < tot1 + 32 ? max(tot - tot1, 0) : 0;
                const int ns = stall ? 0 : min(max(max(min(m4 + (tot - m4 + up) * GAIN / 16 - pend - nj, (cut - m4 + up) * GAIN / 16), 0), far ? FLOOR * 4 : 0), 4000);
                int ndp = dp;
                if (k >= (unsigned)KD && tot >= KI && tot < 100 && tot > tot1 && tot < tot1 + 32 && tot1 < tot2 + 32) { ndp = min(dp + DPS, 40); lock = LOCK; }
                else if (k >= (unsigned)KD && dp < DP0 && tot >= WARN && tot < 100 && tot >= tot1) { ndp = dp + 2; lock = LOCK; }
                else if (lock) lock--;
                if (k >= 6 && tot + tot1 <= QUIET && (!lock || dp > DP0)) ndp = max(dp - 1, 0);
                tot2 = tot1; tot1 = tot;
                dv = (min(cum + nj, 16383) << 18) | (ns << 6) | ndp;
                if ((int)(NOW() * 128 - tk) > XL * 128) dv = (cum << 18) | dp;
                else if (lane == 0) GSTS(dec + (k + 1) * 16, (u64)dv | ((u64)rtag << 32));
            }
        }
        WATCH(p >= NPC - 8 ? now - 128 : now + 4096)
    } else if (wave == NSW + 2) {
        const int ql = lane < 48 ? lane : 47;
        unsigned c1, c2, s1, s2;
        unsigned q1, q2, lkz, lks, lvz, lvs;
#define UNIT_LOADS(HEAD) { \
        q1 = ((const unsigned*)(q + (size_t)(HEAD) * DH))[ql]; q2 = ((const unsigned*)(q + (size_t)(HEAD) * DH))[48 + ql]; \
        const size_t so = ((size_t)(HEAD) * SKV + brow0) / 2 + (lane >> 3) * 128 + (lane & 7); \
        lkz = ((const unsigned*)k_zeros)[so]; lks = ((const unsigned*)k_scales)[so]; \
        lvz = ((const unsigned*)v_zeros)[so]; lvs = ((const unsigned*)v_scales)[so]; }
#define UNIT_FILL(SL, U) { \
            v4f qq; \
            qq.x = bfr(bfr(bflo(q1) * bflo(c1)) + bfr(-bflo(q2) * bflo(s1))); \
            qq.y = bfr(bfr(bflo(q2) * bflo(c2)) + bfr(bflo(q1) * bflo(s2))); \
            qq.z = bfr(bfr(bfhi(q1) * bfhi(c1)) + bfr(-bfhi(q2) * bfhi(s1))); \
            qq.w = bfr(bfr(bfhi(q2) * bfhi(c2)) + bfr(bfhi(q1) * bfhi(s2))); \
            *(v2u*)&sm.q[SL][2 * ql] = (v2u){(__float_as_uint(qq.x) >> 16) | __float_as_uint(qq.y), (__float_as_uint(qq.z) >> 16) | __float_as_uint(qq.w)}; \
            *(v4f*)&sm.kz[SL][2 * lane] = (v4f){bflo(lkz), bflo(lks), bfhi(lkz), bfhi(lks)}; \
            *(v4f*)&sm.vz[SL][2 * lane] = (v4f){bflo(lvz), bflo(lvs), bfhi(lvz), bfhi(lvs)}; \
            CBAR(); \
            if (lane == 0) lds_st(&sm.rdy[SL], (U) + 1); \
            CBAR(); }
        asm volatile("s_setprio 3");
        asm volatile("s_waitcnt vmcnt(0)\n\tv_accvgpr_read_b32 %0, a0\n\tv_accvgpr_read_b32 %1, a1\n\tv_accvgpr_read_b32 %2, a2\n\tv_accvgpr_read_b32 %3, a3\n\tv_accvgpr_read_b32 %4, a4\n\t"
                     "v_accvgpr_read_b32 %5, a5\n\tv_accvgpr_read_b32 %6, a8\n\tv_accvgpr_read_b32 %7, a9\n\tv_accvgpr_read_b32 %8, a10\n\tv_accvgpr_read_b32 %9, a11"
                     : "=v"(c1), "=v"(c2), "=v"(s1), "=v"(s2), "=v"(q1), "=v"(q2), "=v"(lkz), "=v"(lks), "=v"(lvz), "=v"(lvs));
#pragma unroll 1
        for (unsigned u = 0; u < NU; u++) {
            if (u) UNIT_LOADS(rank + GSZ * u)
            while (u >= lds_ld(&sm.merged) + NIN) SLEEP(4);
            CBAR();
            UNIT_FILL(u % NIN, u)
            if (u == 1) asm volatile("s_setprio 0");
        }
        while (lds_ld(&sm.merged) < NU) SLEEP(16)
        CBAR();
        const unsigned T = lds_ld(&sm.tag);
        if (lane < NU) __hip_atomic_store((u64*)(ws + WS_LOG + (rank * NSPLIT + split) * LSTRIDE2) + lane, (u64)__float_as_uint(sm.ent[lane][130]) | ((u64)T << 32), __ATOMIC_RELAXED, __HIP_MEMORY_SCOPE_AGENT);
        while (lds_ld(&sm.merged) < NU + 3) SLEEP(16)
        for (;;) {
            const u64 x = __hip_atomic_load((const u64*)(ws + WS_LOG + ((blockIdx.x & 15) * NSPLIT + (lane & 15)) * LSTRIDE2) + (blockIdx.x >> 4), __ATOMIC_RELAXED, __HIP_MEMORY_SCOPE_AGENT);
            if (__builtin_amdgcn_ballot_w64((unsigned)(x >> 32) == T) == ~0ull) {
                const float L = __uint_as_float((unsigned)x);
                const float w = __builtin_amdgcn_exp2f(L - row_max(L));
                sm.fw[lane & 15] = w / row_sum(w);
                CBAR();
                if (lane == 0) lds_st(&sm.fw_rdy, 1u);
                break;
            }
            SLEEP(64)
        }
    } else {
        while (lds_ld(&sm.tag_rdy) == 0) SLEEP(1);
        CBAR();
        const unsigned T = lds_ld(&sm.tag);
        const __amdgpu_buffer_rsrc_t rsrc = __builtin_amdgcn_make_buffer_rsrc(ws + WS_PART + (rank * NSPLIT + split) * 512, 0, NH * NSPLIT * 512, 0x00020000);
        v2u* recs = (v2u*)&sm.cs[0][0];
#pragma unroll
        for (int i = 0; i < NOUT * 3; i++) (&sm.acc[0][0])[i * 64 + lane] = 0.0;
#define RECORD() \
        const float n0 = (float)(a0 - (double)h.y) * h.x, n1 = (float)(a1 - (double)h.y) * h.x, n2 = (float)(a2 - (double)h.y) * h.x; \
        const unsigned e2 = max(__float_as_uint(fmaxf(fmaxf(fabsf(n0), fabsf(n1)), fabsf(n2))) >> 23, 12u); \
        const float f2 = __uint_as_float((266u - e2) << 23); \
        const unsigned m0 = min(max((int)rintf(n0 * f2), -8191), 8191) & 0x3fff, m1 = min(max((int)rintf(n1 * f2), -8191), 8191) & 0x3fff, m2 = min(max((int)rintf(n2 * f2), -8191), 8191) & 0x3fff; \
        const v2u r = {m0 | (m1 << 14) | (m2 << 28), (m2 >> 4) | (e2 << 10) | (T << 18)};
#pragma unroll 1
        for (unsigned u = 0; u < 2 * NU - 1; u++) {
            const unsigned e = u < NU ? u : VENT(u), so = u % NOUT;
            const v4f h = *(const v4f*)&sm.ent[e][128];
            while (lds_ld(&sm.cnt[so]) != 32) SLEEP(1)
            CBAR();
            if (u < NU) {
                const float2 lg = *(const float2*)&sm.ent[e][2 * lane];
                const v4f zs = *(const v4f*)&sm.vz[u % NIN][2 * lane];
                const float m = wave_max(fmaxf(lg.x, lg.y));
                const float p0 = __expf(lg.x - m), p1 = __expf(lg.y - m);
                const float x0 = p0 * zs.y, x1 = p1 * zs.w;
                const unsigned eb = max(__float_as_uint(wave_max(fmaxf(fabsf(x0), fabsf(x1)))) >> 23, 21u);
                const float f = __uint_as_float((275u - eb) << 23);
                const float w0 = rintf(x0 * f), w1 = rintf(x1 * f);
                const float den = wave_sum(p0 + p1);
                const float bz = wave_sum(fmaf(w0, zs.x, w1 * zs.z));
                const float L = fmaf(m, 1.4426950408889634f, __builtin_amdgcn_logf(den));
                *(v2u*)&sm.ent[e][2 * lane] = (v2u){(unsigned)(int)w0, (unsigned)(int)w1};
                if (lane == 0) *(v4f*)&sm.ent[e][128] = (v4f){ldexpf(1.f / den, (int)eb - 148), bz, L, 0.f};
            } else {
                const double a0 = sm.acc[so][lane], a1 = sm.acc[so][64 + lane], a2 = sm.acc[so][128 + lane];
                RECORD()
                recs[(u - NU) * 64 + lane] = r;
                CBAR();
                sm.acc[so][lane] = 0.0; sm.acc[so][64 + lane] = 0.0; sm.acc[so][128 + lane] = 0.0;
            }
            CBAR();
            if (lane == 0) { lds_st(&sm.cnt[so], 0u); CBAR(); lds_st(&sm.merged, u + 1); }
        }
        {
            asm volatile("s_setprio 3");
            wave_sync();
            v4u rr[8]; unsigned po[8];
#pragma unroll
            for (int k = 0; k < 8; k++) { rr[k] = ((const v4u*)recs)[k * 64 + lane]; po[k] = k == 7 && lane >= 32 ? 0x7ffffff0u : (lane & 31) * 16 + (lane < 32 ? VENT(2 * k) : VENT(2 * k + 1)) * (GSZ * NSPLIT * 512); }
            const v4f h = *(const v4f*)&sm.ent[VENT(15)][128];
            asm volatile("" :: "v"(rr[0]), "v"(rr[1]), "v"(rr[2]), "v"(rr[3]), "v"(rr[4]), "v"(rr[5]), "v"(rr[6]), "v"(rr[7]), "v"(po[0]), "v"(po[1]), "v"(po[2]), "v"(po[3]), "v"(po[4]), "v"(po[5]), "v"(po[6]), "v"(po[7]), "v"(h));
            for (;;) {
                const unsigned d = __builtin_amdgcn_readfirstlane(TU(2 * NU).x) + PUBT * 128 - NOW() * 128;
                if (d >= 0x40000u) break;
                if (d > 20 * 128) SLEEP(4)
            }
#pragma unroll
            for (int k = 0; k < 8; k++) __builtin_amdgcn_raw_buffer_store_b128(rr[k], rsrc, po[k], 0, SC1);
            double a0, a1, a2;
            for (;;) {
                const unsigned c = __hip_atomic_load(&sm.cnt[NOUT - 1], __ATOMIC_RELAXED, __HIP_MEMORY_SCOPE_WORKGROUP);
                CBAR();
                a0 = sm.acc[NOUT - 1][lane]; a1 = sm.acc[NOUT - 1][64 + lane]; a2 = sm.acc[NOUT - 1][128 + lane];
                CBAR();
                if (__builtin_amdgcn_readfirstlane(c) == 32) break;
                SLEEP(0)
            }
            RECORD()
            const v4u r2 = {r.x, r.y, (unsigned)__builtin_amdgcn_update_dpp(0, (int)r.x, 0xf5, 0xf, 0xf, true), (unsigned)__builtin_amdgcn_update_dpp(0, (int)r.y, 0xf5, 0xf, 0xf, true)};
            __builtin_amdgcn_raw_buffer_store_b128(r2, rsrc, (lane & 1) ? 0x7ffffff0u : lane * 8, VENT(15) * (GSZ * NSPLIT * 512), SC1);
        }
        return;
    }

}

extern "C" void kernel_launch(void* const* d_in, const int* in_sizes, int n_in,
                              void* d_out, int out_size, void* d_ws, size_t ws_size,
                              hipStream_t stream) {
    const unsigned short* q = (const unsigned short*)d_in[0];
    const int* qk = (const int*)d_in[1];
    const unsigned short* k_scales = (const unsigned short*)d_in[2];
    const unsigned short* k_zeros = (const unsigned short*)d_in[3];
    const int* qv = (const int*)d_in[4];
    const unsigned short* v_scales = (const unsigned short*)d_in[5];
    const unsigned short* v_zeros = (const unsigned short*)d_in[6];
    const unsigned short* q_cos = (const unsigned short*)d_in[7];
    const unsigned short* q_sin = (const unsigned short*)d_in[8];
    const unsigned short* k_cos = (const unsigned short*)d_in[9];
    const unsigned short* k_sin = (const unsigned short*)d_in[10];
    unsigned char* ws = (unsigned char*)d_ws;
    attn_kernel<<<NSPLIT * GSZ, NTHREADS, 0, stream>>>(q, qk, k_scales, k_zeros, qv, v_scales, v_zeros,
                                                       q_cos, q_sin, k_cos, k_sin, (__hip_bfloat16*)d_out, ws);
}
```

```cpp
#include <hip/hip_runtime.h>
#include <hip/hip_bf16.h>
#include <stdint.h>

#define DH 192
#define HALF 96
#define SKV 2048
#define NH 256
#define NSW 8
#define NTHREADS (64 * (NSW + 4))
#define BROWS 128
#define NSPLIT (SKV / BROWS)
#define GSZ 16
#define NU (NH / GSZ)
#define NPC (2 * NU * 32)
#define NIN 4
#define NOUT 4
#define ENTW 132
#define SC1 16
#define PHI 0x0246024671357135ull
#define VORD 0x9876543210afedcbull
#define VENT(V) ((unsigned)(VORD >> (4 * ((V) & 15))) & 15u)
#define PFL 195
#define PER 1262
#define DP0 4
#define P0 1365
#define NBW 2
#define BTH 20
#define X0 250
#define K0 3
#define XM 250
#define XW 40
#define XD1 85
#define XL 90
#define XR 170
#define BK 3
#define RTH 10
#define QN 224
#define GAIN 28
#define CAP 45
#define FLOOR 3
#define FTH 15
#define KD 9
#define KI 14
#define DPS 2
#define QUIET 1
#define WARN 3
#define LOCK 6
#define NB 3
#define STH 90
#define SFIX 220
#define SNOT 0x610c0cu
#define HOLD 4
#define SMIN 100
#define SADD 8
#define PUBT 40
#define POLT 92
#define POLC 120

#define WS_EPOCH 0
#define WS_DEC 8192
#define WS_REF 16384
#define LSTRIDE 4352
#define WS_LOG 524288
#define LSTRIDE2 1408
#define WS_PART 1048576

typedef unsigned int v2u __attribute__((ext_vector_type(2)));
typedef unsigned int v4u __attribute__((ext_vector_type(4)));
typedef int v4i __attribute__((ext_vector_type(4)));
typedef float v2f __attribute__((ext_vector_type(2)));
typedef float v4f __attribute__((ext_vector_type(4)));
typedef unsigned long long u64;

__device__ __forceinline__ float bflo(unsigned u) { return __uint_as_float(u << 16); }
__device__ __forceinline__ float bfhi(unsigned u) { return __uint_as_float(u & 0xffff0000u); }
__device__ __forceinline__ float bfr(float x) {
    __bf16 b = (__bf16)x;
    return __uint_as_float(((unsigned)__builtin_bit_cast(unsigned short, b)) << 16);
}

#define SLEEP(n) SLEEP_(n)
#define SLEEP_(n) asm volatile("s_sleep " #n "\n\tv_add_co_u32 %0, vcc, 1, %0\n\ts_cbranch_vccz 1f\n\ts_endpgm\n1:" : "+v"(spins) :: "vcc", "memory");
#define SPIN0 0xf0000000u
#define CBAR() asm volatile("" ::: "memory")

__device__ __forceinline__ void wave_sync() {
    __builtin_amdgcn_fence(__ATOMIC_RELEASE, "wavefront");
    __builtin_amdgcn_wave_barrier();
    __builtin_amdgcn_fence(__ATOMIC_ACQUIRE, "wavefront");
}
template <int CTRL>
__device__ __forceinline__ float dpp_add(float x) {
    int y = __builtin_amdgcn_update_dpp(0, __float_as_int(x), CTRL, 0xf, 0xf, true);
    return x + __int_as_float(y);
}
template <int CTRL>
__device__ __forceinline__ float dpp_max(float x) {
    int y = __builtin_amdgcn_update_dpp(__float_as_int(x), __float_as_int(x), CTRL, 0xf, 0xf, false);
    return fmaxf(x, __int_as_float(y));
}
__device__ __forceinline__ float row_sum(float x) { x = dpp_add<0x128>(x); x = dpp_add<0x124>(x); x = dpp_add<0x122>(x); return dpp_add<0x121>(x); }
__device__ __forceinline__ float row_max(float m) { m = dpp_max<0x128>(m); m = dpp_max<0x124>(m); m = dpp_max<0x122>(m); return dpp_max<0x121>(m); }
__device__ __forceinline__ float rdlane(float x, int l) { return __int_as_float(__builtin_amdgcn_readlane(__float_as_int(x), l)); }
__device__ __forceinline__ float wave_max(float m) { m = row_max(m); return fmaxf(fmaxf(rdlane(m, 0), rdlane(m, 16)), fmaxf(rdlane(m, 32), rdlane(m, 48))); }
__device__ __forceinline__ float wave_sum(float x) { x = row_sum(x); return (rdlane(x, 0) + rdlane(x, 16)) + (rdlane(x, 32) + rdlane(x, 48)); }
__device__ __forceinline__ unsigned lds_ld(const unsigned* p) { return __builtin_amdgcn_readfirstlane(__hip_atomic_load(p, __ATOMIC_RELAXED, __HIP_MEMORY_SCOPE_WORKGROUP)); }
__device__ __forceinline__ unsigned lds_addr(const void* p) { return (unsigned)(size_t)(const __attribute__((address_space(3))) void*)p; }
__device__ __forceinline__ void lds_st(unsigned* p, unsigned v) { __hip_atomic_store(p, v, __ATOMIC_RELAXED, __HIP_MEMORY_SCOPE_WORKGROUP); }
#define NOW() ((unsigned)__builtin_amdgcn_s_memrealtime())

struct __attribute__((aligned(16))) Smem {
    unsigned cs[32][768];
    double acc[NOUT][DH];
    float ent[NU][ENTW];
    unsigned q[NIN][HALF];
    float2 kz[NIN][BROWS];
    float2 vz[NIN][BROWS];
    v2u tu[2 * NU + 2];
    unsigned grab, merged, tabs, cdone;
    unsigned rdy[NIN], cnt[NOUT];
    unsigned tag_rdy, tag, start, fw_rdy;
    float fw[NSPLIT];
};

#define TU(I) (*(const volatile v2u __attribute__((address_space(3)))*)(size_t)lds_addr(&sm.tu[I]))
#define LDS_ADD(P, V) { u64 ex_; asm volatile("s_mov_b64 %0, exec\n\ts_mov_b64 exec, 1\n\tds_add_u32 %1, %2\n\ts_mov_b64 exec, %0" : "=&s"(ex_) : "v"(lds_addr(P)), "v"(V) : "memory"); }
#define LDS_OR(P, V) { u64 ex_; asm volatile("s_mov_b64 %0, exec\n\ts_mov_b64 exec, 1\n\tds_or_b32 %1, %2\n\ts_mov_b64 exec, %0" : "=&s"(ex_) : "v"(lds_addr(P)), "v"(V) : "memory"); }

__global__ __launch_bounds__(NTHREADS, 3) void attn_kernel(
    const unsigned short* __restrict__ q, const int* __restrict__ qk,
    const unsigned short* __restrict__ k_scales, const unsigned short* __restrict__ k_zeros,
    const int* __restrict__ qv,
    const unsigned short* __restrict__ v_scales, const unsigned short* __restrict__ v_zeros,
    const unsigned short* __restrict__ q_cos, const unsigned short* __restrict__ q_sin,
    const unsigned short* __restrict__ k_cos, const unsigned short* __restrict__ k_sin,
    __hip_bfloat16* __restrict__ out, unsigned char* __restrict__ ws)
{
    __shared__ Smem sm;
    const int tid = threadIdx.x;
    unsigned spins = SPIN0;
#define TOUCH(OFF, BASE) asm volatile("global_load_dword a6, %0, %1" :: "v"(OFF), "s"(BASE) : "a6", "memory");
    const unsigned cl = min(((blockIdx.x >> 3) & 15) * 16 + (tid & 15), PFL - 1u) * 128;
    if (tid < 64) { const char* pc0; asm volatile("s_getpc_b64 %0" : "=s"(pc0)); TOUCH(cl, pc0) }
    __builtin_amdgcn_sched_barrier(0);
    const int hw = __builtin_amdgcn_readfirstlane(tid >> 6);
    const int lane = tid & 63;
    const int split = (blockIdx.x & 3) * 4 + ((blockIdx.x >> 3) & 3);
    const int rank = (blockIdx.x >> 5) * 2 + ((blockIdx.x >> 2) & 1);
    const int brow0 = split * 16;
#define ROWOF(RG) (((RG) >> 2) * 256 + ((RG) & 3) * 4)
    if (hw == 0) {
        typedef const char* const __attribute__((address_space(4))) * kap_t;
        const char* sa = ((kap_t)__builtin_amdgcn_kernarg_segment_ptr())[(0x5623 >> (4 * (split & 3))) & 7];
        unsigned o2 = ((rank + GSZ * (lane >> 3)) * SKV + (lane & 7) * 256 + (brow0 & ~63)) * 2, o3 = o2 + 8 * GSZ * SKV * 2;
        unsigned o4 = (rank + GSZ * ((lane >> 2 & 3) + 4 * (split & 3))) * (DH * 2) + min(lane & 3, 2) * 128;
        asm volatile("" : "+v"(o2), "+v"(o3), "+v"(o4));
        asm volatile("global_load_dword a6, %0, %1 nt\n\tglobal_load_dword a6, %0, %2 nt" :: "v"((lane & 1) * (NH * SKV * DH * 4u - 128u * NH) + blockIdx.x * 128), "s"(qk), "s"(qv) : "a6", "memory");
        TOUCH(o4, q) TOUCH(o2, sa) TOUCH(o3, sa)
        asm volatile("s_barrier\n\ts_waitcnt vmcnt(2)" ::: "memory");
        if (lane == 0) lds_st(&sm.start, NOW() | 1u);
        asm volatile("s_endpgm");
    }
    const unsigned rot = 4 * ((unsigned)(PHI >> (60 - 4 * rank)) & 7u);
#define RGOF(G) ((((G) + rot) & 31u) ^ ((((G) + rot) >> 1) & 2u))
#define CS_DMA(RG) asm volatile("s_mov_b32 m0, %1\n\ts_nop 0\n\tglobal_load_lds_dwordx4 %0, %2\n\ts_mov_b32 exec_hi, 0\n\tglobal_load_lds_dwordx4 %0, %2 offset:1024\n\ts_add_u32 m0, m0, 0x600\n\ts_nop 0\n\t" \
                                "global_load_lds_dwordx4 %0, %3 offset:1024\n\ts_mov_b32 exec_hi, -1\n\tglobal_load_lds_dwordx4 %0, %3" \
                                :: "v"((brow0 + ROWOF(RG)) * (DH * 2) + lane * 16), "s"(lds_addr(&sm.cs[0][0]) + (RG) * 3072), "s"(k_cos), "s"(k_sin) : "scc", "memory");
    if (hw >= 4) CS_DMA(RGOF((unsigned)hw - 4))
    if (hw == 1) {
        const unsigned qo = min(lane, 47) * 4, so = (rank * SKV + brow0) * 2 + (lane >> 3) * 512 + (lane & 7) * 4;
        asm volatile("global_load_dword a0, %0, %3\n\tglobal_load_dword a1, %0, %3 offset:192\n\tglobal_load_dword a2, %0, %4\n\tglobal_load_dword a3, %0, %4 offset:192\n\t"
                     "global_load_dword a4, %1, %5\n\tglobal_load_dword a5, %1, %5 offset:192\n\t"
                     "global_load_dword a8, %2, %6\n\tglobal_load_dword a9, %2, %7\n\tglobal_load_dword a10, %2, %8\n\tglobal_load_dword a11, %2, %9"
                     :: "v"(qo), "v"(qo + rank * (DH * 2)), "v"(so), "s"(q_cos), "s"(q_sin), "s"(q), "s"(k_zeros), "s"(k_scales), "s"(v_zeros), "s"(v_scales)
                     : "a0", "a1", "a2", "a3", "a4", "a5", "a8", "a9", "a10", "a11", "memory");
    }
    if (hw == 3 && lane < 16) (&sm.grab)[lane] = lane ? 0 : 64;
    asm volatile("" :: "s"(q), "s"(qk), "s"(k_scales), "s"(k_zeros), "s"(qv), "s"(v_scales), "s"(v_zeros), "s"(q_cos), "s"(q_sin), "s"(k_cos), "s"(k_sin), "s"(out), "s"(ws));
    asm volatile("s_waitcnt lgkmcnt(0)\n\ts_barrier" ::: "memory");
    const int wave = (int)(0x76543210b9a8ull >> (4 * hw)) & 15;
    unsigned ts;
#define START() { while ((ts = lds_ld(&sm.start)) == 0) SLEEP(1) }

    if (__builtin_expect(wave < NSW, 1)) {
#define REPACK(RG) { \
        unsigned* cw = &sm.cs[RG][0]; \
        const unsigned* l = cw + rsel * (DH / 2) + qoff; \
        unsigned r[2][6]; \
        _Pragma("unroll") \
        for (int j = 0; j < 2; j++) { \
            unsigned e[4][2]; \
            _Pragma("unroll") \
            for (int k = 0; k < 4; k++) { \
                const unsigned* a = l + j * DH + (k & 1) * 48 + (k >> 1) * 384;       \
                unsigned x0 = a[0], x1 = a[1]; \
                e[k][0] = __builtin_amdgcn_alignbit(x1, x0, sh); \
                e[k][1] = x1 >> sh; \
            } \
            r[j][0] = (e[0][0] & 0xffffu) | (e[3][0] << 16); \
            r[j][1] = (e[0][0] >> 16) | (e[3][0] & 0xffff0000u); \
            r[j][2] = (e[0][1] & 0xffffu) | (e[3][1] << 16); \
            r[j][3] = (e[1][0] & 0xffffu) | (e[2][0] << 16); \
            r[j][4] = (e[1][0] >> 16) | (e[2][0] & 0xffff0000u); \
            r[j][5] = (e[1][1] & 0xffffu) | (e[2][1] << 16); \
        } \
        wave_sync(); \
        _Pragma("unroll") \
        for (int j = 0; j < 2; j++) { *(v4u*)&cw[j * 256 + lane * 4] = (v4u){r[j][0], r[j][1], r[j][2], r[j][3]}; *(v2u*)&cw[512 + j * 128 + lane * 2] = (v2u){r[j][4], r[j][5]}; } \
        wave_sync(); \
        LDS_OR(&sm.tabs, 1u << (RG)) }
        const int sub = lane & 31;
        const int rsel = lane >> 5;
        const unsigned sh = (sub & 1) * 16;
        const int qoff = 3 * (sub >> 1) + (sub & 1);
        const char* kbase = (const char*)qk + (size_t)(rank * SKV + brow0) * (DH * 4);
        const char* vbase = (const char*)qv + (size_t)(rank * SKV + brow0) * (DH * 4);
        const unsigned koff = (rsel * DH + 3 * sub) * 4, voff = 3 * lane * 4;
#define AR(S) (16 + (S) * 8)
        asm volatile("" ::: "a16", "a17", "a18", "a19", "a20", "a21", "a22", "a23", "a24", "a25", "a26", "a27", "a28", "a29", "a30", "a31",
                            "a32", "a33", "a34", "a35", "a36", "a37", "a38", "a39", "a40", "a41", "a42", "a43", "a44", "a45", "a46", "a47");
        asm volatile("" ::: "a48", "a49", "a50", "a51", "a52", "a53", "a54", "a55", "a56", "a57", "a58", "a59", "a60", "a61", "a62", "a63");
        int ta[3], tb[3];
#define GLD(A, OFF, BASE, IMM) asm volatile("global_load_dwordx3 a[%c0:%c1], %2, %3 offset:%4 nt" :: "i"(A), "i"((A) + 2), "v"(OFF), "s"(BASE), "i"(IMM))
#define ISSUE(S, J, BASE, OA, OB) { GLD(AR(S), OA, BASE, (J) * 1536); GLD(AR(S) + 4, OB, BASE, (J) * 1536); }
#define WAITT(S, N) asm volatile("s_waitcnt vmcnt(%6)\n\tv_accvgpr_read_b32 %0, a[%c7]\n\tv_accvgpr_read_b32 %1, a[%c8]\n\tv_accvgpr_read_b32 %2, a[%c9]\n\t" \
                              "v_accvgpr_read_b32 %3, a[%c10]\n\tv_accvgpr_read_b32 %4, a[%c11]\n\tv_accvgpr_read_b32 %5, a[%c12]" \
                              : "=v"(ta[0]), "=v"(ta[1]), "=v"(ta[2]), "=v"(tb[0]), "=v"(tb[1]), "=v"(tb[2]) \
                              : "i"(N), "i"(AR(S)), "i"(AR(S) + 1), "i"(AR(S) + 2), "i"(AR(S) + 4), "i"(AR(S) + 5), "i"(AR(S) + 6))
        const unsigned koffb = koff + 384, voffb = voff + 768;
        unsigned u = 0, rg, np = 0;
#define RG0(C) RGOF((unsigned)(wave + 8 * (C)))
        const float scale = 0.07216878364870322f;
        double acc[3] = {0.0, 0.0, 0.0};
        bool chg, none = false;
        unsigned Qp[3], Qn[3];
        float kz4[2], ksc, kres[2];
        v4u ca[2]; v2u cb[2];
        v2f qe[2][3], A[2];
        const char* nb; unsigned oa, ob;
        unsigned g, gu, mg, nrg, tg, dg, par = 0;
        int early;
        const unsigned bth = wave < NBW ? BTH * 128 : 0x3fffffff;
        const unsigned grab_adr = lds_addr(&sm.grab);
        asm volatile("v_accvgpr_write_b32 a13, 1" ::: "a13", "a15");
#define COUNT(ADR) { u64 ex_; asm volatile("s_mov_b64 %0, exec\n\ts_mov_b64 exec, 1\n\tds_add_rtn_u32 a15, %1, a13\n\ts_mov_b64 exec, %0" : "=&s"(ex_) : "v"(ADR) : "memory"); }
#define GRABBED() { unsigned t_; asm volatile("s_waitcnt lgkmcnt(0)\n\tv_accvgpr_read_b32 %1, a15\n\ts_nop 0\n\tv_readfirstlane_b32 %0, %1\n\ts_nop 1" : "=s"(g), "=v"(t_)); }
#define NEXT1() const v2u x_ = TU(g >> 5); u64 tn_; asm volatile("s_memrealtime %0" : "=s"(tn_));
#define NEXT2() { \
            chg = (g >> 5) != u; none = g >= NPC; \
            const bool nv = g >= NPC / 2; \
            nrg = RGOF(g); \
            unsigned boff; asm("s_mul_i32 %0, %1, 0x300" : "=s"(boff) : "s"((nv ? VENT(g >> 5) : g >> 5) * (GSZ * SKV) + ROWOF(nrg) + 2)); \
            nb = (nv ? vbase : kbase) + boff; \
            oa = nv ? voff : koff; ob = nv ? voffb : koffb; \
            asm volatile("s_waitcnt lgkmcnt(0)" : "+s"(tn_) : "s"(nb), "v"(oa), "v"(ob)); \
            tg = __builtin_amdgcn_readfirstlane(x_.x) + (g & 31) * __builtin_amdgcn_readfirstlane(x_.y); \
            early = none ? 0 : (int)((unsigned)tn_ * 128 - tg - bth); \
            if (early > 0) asm volatile("s_cmp_lg_u32 %3, 0\n\ts_cbranch_scc1 1f\n\t" L4(32) "\ts_branch 2f\n1:\n\t" L4(16) "2:\n\ts_mov_b64 vcc, exec\n\ts_mov_b64 exec, 1\n\tds_add_rtn_u32 a15, %4, a13\n\ts_mov_b64 exec, vcc" \
                                    :: "v"(oa), "v"(ob), "s"(nb), "s"(pr), "v"(grab_adr) : "scc", "vcc", "memory"); }
#define L4(A) "global_load_dwordx3 a[" #A ":" #A "+2], %0, %2 offset:-1536 nt\n\tglobal_load_dwordx3 a[" #A "+4:" #A "+6], %1, %2 offset:-1536 nt\n\t" \
              "global_load_dwordx3 a[" #A "+8:" #A "+10], %0, %2 nt\n\tglobal_load_dwordx3 a[" #A "+12:" #A "+14], %1, %2 nt\n"
#define GATE(...) \
            asm volatile("s_setprio 0\n1:\n\ts_memrealtime vcc\n\ts_waitcnt lgkmcnt(0)\n\ts_lshl_b32 vcc_lo, vcc_lo, 7\n\ts_sub_u32 %0, %1, vcc_lo\n\ts_cmp_lt_u32 %0, 0x40000\n\ts_cbranch_scc0 2f\n\t" \
                         "s_cmp_lt_u32 %0, %2\n\ts_cbranch_scc1 1b\n\ts_sleep %3\n\ts_branch 1b\n2:\n\ts_setprio 2" : "=&s"(dg) : "s"(tg), "i"(10 * 128), "i"(2), __VA_ARGS__ : "scc", "vcc", "memory");
#define RD6(A) "v_accvgpr_read_b32 %0, a[%c6+" #A "]\n\tv_accvgpr_read_b32 %1, a[%c6+" #A "+1]\n\tv_accvgpr_read_b32 %2, a[%c6+" #A "+2]\n\t" \
               "v_accvgpr_read_b32 %3, a[%c6+" #A "+4]\n\tv_accvgpr_read_b32 %4, a[%c6+" #A "+5]\n\tv_accvgpr_read_b32 %5, a[%c6+" #A "+6]\n"
#define LD2(A) "global_load_dwordx3 a[%c6+" #A ":%c6+" #A "+2], %7, %9 offset:%10 nt\n\tglobal_load_dwordx3 a[%c6+" #A "+4:%c6+" #A "+6], %8, %9 offset:%10 nt"
#define OUT6 "=&v"(ta[0]), "=&v"(ta[1]), "=&v"(ta[2]), "=&v"(tb[0]), "=&v"(tb[1]), "=&v"(tb[2])
#define WI(A, B, TAIL) "s_cmp_gt_i32 %13, 0\n\ts_cbranch_scc1 3f\n\ts_waitcnt vmcnt(2)\n\t" LD2(A) TAIL "\n3:\n\ts_waitcnt vmcnt(%c14)\n\t" RD6(B)
#define WAITISSUE(S, TAIL, ...) asm volatile("s_cmp_lg_u32 %12, 0\n\ts_cbranch_scc1 1f\n\t" WI(16, 0, TAIL) "\ts_branch 2f\n1:\n\t" WI(0, 16, TAIL) "2:" \
                              : OUT6 : "i"(AR(S)), "v"(oa), "v"(ob), "s"(nb), "i"(((S) - 1) * 1536), "v"(grab_adr), "s"(pr), "s"(early), "i"(6 - 2 * (S)), __VA_ARGS__ : "scc", "vcc", "memory")
#define LASTT(S) asm volatile("s_waitcnt vmcnt(%c8)\n\ts_cmp_lg_u32 %7, 0\n\ts_cbranch_scc1 1f\n\t" RD6(0) "\ts_branch 2f\n1:\n\t" RD6(16) "2:" : OUT6 : "i"(AR(S)), "s"(pr), "i"(2 - 2 * (S)) : "scc")
#define DRAW "\n\ts_mov_b64 vcc, exec\n\ts_mov_b64 exec, 1\n\tds_add_rtn_u32 a15, %11, a13\n\ts_mov_b64 exec, vcc"
#define K_TAB() { _Pragma("unroll") for (int p = 0; p < 2; p++) { \
            const unsigned* cw = &sm.cs[rg][0]; \
            ca[p] = *(const v4u*)&cw[p * 256 + lane * 4]; cb[p] = *(const v2u*)&cw[512 + p * 128 + lane * 2]; } }
#define K_PRE(P) { \
        float e0, e1, e2, f0, f1, f2; \
        asm("v_dot2_f32_bf16 %0, %6, %12, 0\n\tv_dot2_f32_bf16 %3, %9, %15, 0\n\tv_dot2_f32_bf16 %1, %7, %13, 0\n\tv_dot2_f32_bf16 %4, %10, %16, 0\n\t" \
            "v_dot2_f32_bf16 %2, %8, %14, 0\n\tv_dot2_f32_bf16 %5, %11, %17, 0\n\ts_nop 2" \
            : "=&v"(e0), "=&v"(e1), "=&v"(e2), "=&v"(f0), "=&v"(f1), "=&v"(f2) \
            : "v"(Qp[0]), "v"(Qp[1]), "v"(Qp[2]), "v"(Qn[0]), "v"(Qn[1]), "v"(Qn[2]), \
              "v"(ca[P].x), "v"(ca[P].y), "v"(ca[P].z), "v"(ca[P].w), "v"(cb[P].x), "v"(cb[P].y)); \
        qe[P][0] = (v2f){e0, f0}; qe[P][1] = (v2f){e1, f1}; qe[P][2] = (v2f){e2, f2}; \
        A[P] = (qe[P][0] + qe[P][1] + qe[P][2]) * (v2f){-kz4[P], -kz4[P]}; }
#define K_SUM(P) { \
        _Pragma("unroll") \
        for (int i = 0; i < 3; i++) { v2f x = {(float)ta[i], (float)tb[i]}; A[P] = qe[P][i] * x + A[P]; } \
        kres[P] = A[P].x + A[P].y; }
#define K_RED2() { \
        const bool o1 = lane & 1; \
        float sx = (o1 ? kres[1] : kres[0]) + __int_as_float(__builtin_amdgcn_update_dpp(0, __float_as_int(o1 ? kres[0] : kres[1]), 0xb1, 0xf, 0xf, true)); \
        sx = dpp_add<0x4e>(sx); sx = dpp_add<0x124>(sx); sx = dpp_add<0x128>(sx); \
        const auto sw = __builtin_amdgcn_permlane16_swap(__float_as_uint(sx), __float_as_uint(sx), false, false); \
        sx = __uint_as_float(sw[0]) + __uint_as_float(sw[1]); \
        if ((lane & 30) == 0) sm.ent[u][rg * 4 + 2 * (lane & 1) + rsel] = sx * ksc; }
#define LDV(P) __hip_atomic_load(P, __ATOMIC_RELAXED, __HIP_MEMORY_SCOPE_WORKGROUP)
#define RDYWAIT(SL, SEQ) { while (lds_ld(&sm.rdy[SL]) != (SEQ)) SLEEP(1); CBAR(); }
#define LOADZ() { _Pragma("unroll") for (int p = 0; p < 2; p++) kz4[p] = sm.kz[u % NIN][rg * 4 + 2 * p + rsel].x; ksc = sm.kz[u % NIN][rg * 4 + 2 * (lane & 1) + rsel].y * scale; }
#define LOADQ() { _Pragma("unroll") for (int i = 0; i < 3; i++) { Qp[i] = sm.q[u % NIN][3 * sub + i]; Qn[i] = __builtin_amdgcn_alignbit(Qp[i], Qp[i], 16) ^ 0x80000000u; } }
#define FLUSH(ISV) { \
            mg = __builtin_amdgcn_readfirstlane(mg); \
            while (u >= mg + NOUT) { SLEEP(1); mg = lds_ld(&sm.merged); } \
            CBAR(); \
            if (ISV) { const unsigned aa = lds_addr(&sm.acc[u % NOUT][lane]); \
                asm volatile("ds_add_f64 %0, %1\n\tds_add_f64 %0, %2 offset:512\n\tds_add_f64 %0, %3 offset:1024" :: "v"(aa), "v"(acc[0]), "v"(acc[1]), "v"(acc[2]) : "memory"); \
                acc[0] = 0.0; acc[1] = 0.0; acc[2] = 0.0; } \
            CBAR(); \
            LDS_ADD(&sm.cnt[u % NOUT], np) \
            CBAR(); \
            np = 0; }
#define ADVANCE() { u = gu; rg = nrg; }

#define REQ0(N, S) { tg = ts * 128 + (wave + 8 * (N)) * P0; \
            const char* kb = kbase + (((N) >> 2) * (GSZ * SKV) + ROWOF(RG0((N) & 3))) * (DH * 4); GATE("s"(kb)) ISSUE(S, 0, kb, koff, koffb) ISSUE((S) + 1, 1, kb, koff, koffb) }
#define USE0(N, S, W) { u = (N) >> 2; rg = RG0((N) & 3); K_TAB() LOADQ() LOADZ() K_PRE(0) K_PRE(1) WAITT(S, W); K_SUM(0) WAITT((S) + 1, W); K_SUM(1) K_RED2() }
#define VMW(N) asm volatile("s_waitcnt vmcnt(" #N ")" ::: "memory");
        START()
        REQ0(0, 0) CS_DMA(RG0(1))
        VMW(8) REPACK(RG0(0))
        REQ0(1, 2)
        RDYWAIT(0, 1u)
        USE0(0, 0, 8)
        VMW(4) REPACK(RG0(1))
        REQ0(2, 0) CS_DMA(RG0(2))
        USE0(1, 2, 8)
        REQ0(3, 2)
        VMW(4) REPACK(RG0(2))
        USE0(2, 0, 4)
        REQ0(4, 0) CS_DMA(RG0(3))
        v2f qe2[2][2][3], A2[2][2]; float ks2[2];
        RDYWAIT(1, 2u)
        u = 1;
        LOADQ()
#pragma unroll
        for (int j = 0; j < 2; j++) {
            rg = RG0(j); K_TAB() LOADZ() K_PRE(0) K_PRE(1)
            ks2[j] = ksc;
#pragma unroll
            for (int p = 0; p < 2; p++) { A2[j][p] = A[p]; qe2[j][p][0] = qe[p][0]; qe2[j][p][1] = qe[p][1]; qe2[j][p][2] = qe[p][2]; }
        }
        asm volatile("" :: "v"(A2[0][0]), "v"(A2[0][1]), "v"(A2[1][0]), "v"(A2[1][1]), "v"(ks2[0]), "v"(ks2[1]));
        REQ0(5, 4)
        VMW(4) REPACK(RG0(3))
        USE0(3, 2, 4)
        CBAR();
        LDS_ADD(&sm.cnt[0], 4u)
        REQ0(6, 2)
        u = 1;
#define FIN0(J, S, W) { rg = RG0(J); ksc = ks2[J]; \
            _Pragma("unroll") for (int p = 0; p < 2; p++) { A[p] = A2[J][p]; qe[p][0] = qe2[J][p][0]; qe[p][1] = qe2[J][p][1]; qe[p][2] = qe2[J][p][2]; } \
            WAITT(S, W); K_SUM(0) WAITT((S) + 1, W); K_SUM(1) K_RED2() }
        FIN0(0, 0, 8)
        FIN0(1, 4, 4)
        REQ0(7, 0)
        COUNT(grab_adr)
        USE0(6, 2, 4)
        rg = RG0(3); np = 3;
        GRABBED()
#pragma unroll 1
        while (u < NU) {
            const unsigned pr = __builtin_amdgcn_readfirstlane(par); par ^= 1;
            NEXT1()
            const unsigned tb_ = LDV(&sm.tabs), fl = LDV(&sm.rdy[u % NIN]);
            mg = LDV(&sm.merged);
            CBAR();
            K_TAB() LOADQ() LOADZ()
            NEXT2()
            if (__builtin_expect(__builtin_amdgcn_readfirstlane(fl) != u + 1 || !((__builtin_amdgcn_readfirstlane(tb_) >> rg) & 1u), 0)) {
                while (!((lds_ld(&sm.tabs) >> rg) & 1u)) SLEEP(1)
                RDYWAIT(u % NIN, u + 1)
                K_TAB() LOADQ() LOADZ()
            }
            K_PRE(0) K_PRE(1)
            if (early <= 0) GATE("v"(A[0]), "v"(A[1]), "v"(oa), "v"(ob), "s"(nb), "s"(pr))
            WAITISSUE(0, DRAW, "v"(A[0]));
            K_SUM(0)
            WAITISSUE(1, "", "v"(A[1]));
            K_SUM(1)
            gu = g >> 5;
            GRABBED()
            K_RED2()
            np++;
            if (chg) FLUSH(false)
            ADVANCE()
        }
#pragma unroll 1
        while (u < 2 * NU) {
            const unsigned pr = __builtin_amdgcn_readfirstlane(par); par ^= 1;
            NEXT1()
            mg = LDV(&sm.merged);
            CBAR();
            const unsigned ve = VENT(u);
            v4i pw = *(const v4i*)&sm.ent[ve][rg * 4];
            NEXT2()
            if (__builtin_expect(__builtin_amdgcn_readfirstlane(mg) <= ve, 0)) { while (lds_ld(&sm.merged) <= ve) SLEEP(1); CBAR(); pw = *(const v4i*)&sm.ent[ve][rg * 4]; }
            const double w0 = pw.x, w1 = pw.y, w2 = pw.z, w3 = pw.w;
            if (none) { LASTT(0); } else { if (early <= 0) GATE("s"(pr), "v"(w0), "v"(w1), "v"(w2), "v"(w3), "v"(oa), "v"(ob), "s"(nb)) WAITISSUE(0, DRAW, "v"(w0)); }
#pragma unroll
            for (int i = 0; i < 3; i++) acc[i] = fma(w1, (double)tb[i], fma(w0, (double)ta[i], acc[i]));
            if (none) { LASTT(1); } else WAITISSUE(1, "", "v"(w2));
#pragma unroll
            for (int i = 0; i < 3; i++) acc[i] = fma(w3, (double)tb[i], fma(w2, (double)ta[i], acc[i]));
            gu = g >> 5;
            np++;
            if (chg) FLUSH(true)
            GRABBED()
            ADVANCE()
        }

        {
            const int head = blockIdx.x;
            while (lds_ld(&sm.fw_rdy) == 0) SLEEP(2)
            CBAR();
            const unsigned tt = lds_ld(&sm.tag) << 18;
            const float wh = sm.fw[wave + 8 * rsel];
            const unsigned ro = (wave + 8 * rsel) * 512 + sub * 16;
            const char* rb = (const char*)ws + WS_PART + head * (NSPLIT * 512);
            v4f* pb = (v4f*)((char*)&sm.cs[0][0] + 16384);
            __hip_bfloat16* o = out + (size_t)head * DH + 6 * sub + 3 * rsel;
            tg = __builtin_amdgcn_readfirstlane(TU(2 * NU).x) + ((head >> 4) == VENT(15) ? POLC : POLT) * 128;
            GATE("s"(tt), "v"(wh), "v"(ro), "s"(rb), "v"(o))
            v4u R;
#define PLD(A) "global_load_dwordx4 a[" #A ":" #A "+3], %6, %7 sc1\n\t"
#define PCK(A, N, L) "s_waitcnt vmcnt(" #N ")\n\tv_accvgpr_read_b32 %1, a[" #A "+1]\n\tv_accvgpr_read_b32 %3, a[" #A "+3]\n\tv_accvgpr_read_b32 %0, a[" #A "]\n\tv_accvgpr_read_b32 %2, a[" #A "+2]\n\t" \
                     "v_xor_b32 %1, %8, %1\n\tv_xor_b32 %3, %8, %3\n\tv_or_b32 %4, %1, %3\n\tv_cmp_gt_u32 vcc, %9, %4\n\ts_cmp_eq_u64 vcc, exec\n\ts_cbranch_scc1 " L "\n\t"
            { unsigned t_;
              asm volatile("s_cmp_eq_u32 %10, %12\n\ts_cbranch_scc1 1f\n\t" PLD(16) PCK(16, 0, "3f") "1:\n\t" PLD(16) "s_sleep %11\n\t" PLD(20) "s_sleep %11\n\t" PLD(24)
                           "2:\n\t" PCK(16, 2, "3f") PLD(16) PCK(20, 2, "3f") PLD(20) PCK(24, 2, "3f") PLD(24)
                           "v_add_co_u32 %5, vcc, 64, %5\n\ts_cbranch_vccz 2b\n\ts_endpgm\n3:"
                           : "=&v"(R.x), "=&v"(R.y), "=&v"(R.z), "=&v"(R.w), "=&v"(t_), "+v"(spins) : "v"(ro), "s"(rb), "s"(tt), "s"(0x40000u), "s"(head >> 4), "i"(5), "i"(VENT(15)) : "scc", "vcc", "memory"); }
            const float s0 = ldexpf(wh, (int)(R.y >> 10) - 139), s1 = ldexpf(wh, (int)(R.w >> 10) - 139);
            const float f[6] = {s0 * (float)(int)__builtin_amdgcn_sbfe(R.x, 0, 14), s0 * (float)(int)__builtin_amdgcn_sbfe(R.x, 14, 14), s0 * (float)(int)__builtin_amdgcn_sbfe(__builtin_amdgcn_alignbit(R.y, R.x, 28), 0, 14),
                                s1 * (float)(int)__builtin_amdgcn_sbfe(R.z, 0, 14), s1 * (float)(int)__builtin_amdgcn_sbfe(R.z, 14, 14), s1 * (float)(int)__builtin_amdgcn_sbfe(__builtin_amdgcn_alignbit(R.w, R.z, 28), 0, 14)};
            float sv[3];
#pragma unroll
            for (int i = 0; i < 3; i++) {
                const auto sw = __builtin_amdgcn_permlane32_swap(__float_as_uint(f[i]), __float_as_uint(f[i + 3]), false, false);
                sv[i] = __uint_as_float(sw[0]) + __uint_as_float(sw[1]);
            }
            pb[wave * 64 + lane] = (v4f){sv[0], sv[1], sv[2], 0.f};
            CBAR();
            COUNT(grab_adr + 12)
            v4f ps[NSW];
#pragma unroll
            for (int w = 0; w < NSW; w++) ps[w] = pb[w * 64 + lane];
            GRABBED()
            if (g == NSW - 1) {
                const v4f sum = ((ps[0] + ps[1]) + (ps[2] + ps[3])) + ((ps[4] + ps[5]) + (ps[6] + ps[7]));
                o[0] = __float2bfloat16(sum.x); o[1] = __float2bfloat16(sum.y); o[2] = __float2bfloat16(sum.z);
            }
        }
        return;
    }

    if (wave == NSW + 1) {
        u64 E; asm volatile("s_setprio 3\n\ts_load_dwordx2 %0, %1, 0\n\ts_waitcnt lgkmcnt(0)" : "=s"(E) : "s"(ws + WS_EPOCH) : "memory");
        START()
        unsigned t2 = ts * 128 + 64 * P0;
        int off = 0, off1 = 0, hold = 0, cum = 0, dp = DP0, sp = 0;
#define NOM(U) (((int)(U) - 2) * 32 * PER)
#define NEWLINE(K) { if (lane >= (K) && lane <= 2 * NU) sm.tu[lane] = (v2u){t2 + NOM(lane) + (cum + (lane - (int)(K)) * dp + (lane > (K) ? sp + off : off1)) * 32, \
                                                                            (unsigned)(PER + dp + (lane == (K) ? sp + off - off1 : 0))}; }
        NEWLINE(2)
        if (lane == 0) sm.tu[1] = (v2u){t2 - 32 * P0, (unsigned)P0};
        const u64 E1 = E + 1;
        const unsigned rtag = (unsigned)E1 * 0x9e3779b1u;
        const bool boss = blockIdx.x == 0;
        u64* const slot = (u64*)(ws + WS_REF + (blockIdx.x & 63) * LSTRIDE + (blockIdx.x >> 6) * 16);
        const u64* const slots = (const u64*)(ws + WS_REF + lane * LSTRIDE);
        u64* const dec = (u64*)(ws + WS_DEC);
#define GLDS(P) __hip_atomic_load(P, __ATOMIC_RELAXED, __HIP_MEMORY_SCOPE_AGENT)
#define GSTS(P, V) __hip_atomic_store(P, V, __ATOMIC_RELAXED, __HIP_MEMORY_SCOPE_AGENT)
#define UNTIL(T) while ((unsigned)((T) - NOW() * 128) < 0x40000u) SLEEP(2)
        if (lane < 2) GSTS(slot + lane, lane ? E1 : (u64)ts | ((u64)rtag << 32));
        if (lane == 0) { sm.tag = (unsigned)E1; CBAR(); lds_st(&sm.tag_rdy, 1u); }
        unsigned dv = 0;
        bool joined = false;
#define JOIN(T0) { const int d0 = min(max((int)((T0) - ts), -1000), 1000); off -= d0 * 4; t2 += d0 * 128; joined = true; }
        unsigned pw_ = 0, tw_ = 0, now, p; int prov = 0;
#define LOOK() { u64 t_; asm volatile("ds_read_b32 %1, %2\n\ts_memrealtime %0\n\ts_waitcnt lgkmcnt(0)" : "=s"(t_), "=v"(p) : "v"(lds_addr(&sm.grab)) : "memory"); \
                 p = __builtin_amdgcn_readfirstlane(p) - 8; now = (unsigned)t_ * 128; }
#define LATENESS(P, T) ({ const v2u e = TU((P) >> 5); (int)((T) - __builtin_amdgcn_readfirstlane(e.x) - ((P) & 31) * __builtin_amdgcn_readfirstlane(e.y)); })
#define WATCH(T) for (;;) { \
            LOOK() \
            if (__builtin_expect(now - tw_ > STH * 128u && pw_ >= 64 && p < NPC - 8, 0)) { \
                const int lw = max(LATENESS(pw_, tw_), 0); \
                if (LATENESS(p, now) - lw > STH * 128) \
                    for (int d0_ = hold || ((SNOT >> (p >> 5)) & 1) ? 0 : SFIX * 4;; d0_ = 0) { \
                        const int d_ = min(max((LATENESS(p, now) - lw) >> 5, d0_), 1600); \
                        if (lane <= 2 * NU) atomicAdd((unsigned*)&sm.tu[lane], d_ * 32u); \
                        off += d_; prov += d_; hold = HOLD; \
                        if (p != pw_ || prov > 8000) break; \
                        SLEEP(1) \
                        LOOK() \
                    } \
                } \
            if (p != pw_) { pw_ = p; tw_ = now; } \
            if ((unsigned)((T) - now) >= 0x40000u) break; \
            if ((unsigned)((T) - now) > 25 * 128) SLEEP(7) else SLEEP(1) }
        if (boss) {
            TOUCH((lane & 31) * 128, dec)
            UNTIL((ts + X0) * 128)
            for (;;) {
                bool ok = true; float sum = 0.f;
#pragma unroll
                for (int i = 0; i < 4; i++) { const u64 x = GLDS(slots + 2 * i), e = GLDS(slots + 2 * i + 1); ok &= e == E1 && (unsigned)(x >> 32) == rtag; sum += (float)min(max((int)((unsigned)x - ts), -1000), 1000); }
                if (__builtin_amdgcn_ballot_w64(ok) == ~0ull) { dv = ts + (int)(wave_sum(sum) * (1.f / 256)); break; }
                SLEEP(8)
            }
            if (lane == 0) { GSTS(dec, (u64)dv | ((u64)rtag << 32)); GSTS((u64*)(ws + WS_EPOCH), E1); }
            JOIN(dv)
            dv = 0;
        }
        int sp1 = 0, tot1 = 0, tot2 = 0, lock = 0;
#pragma unroll 1
        for (unsigned k = 2; k < 2 * NU; k++) {
            const unsigned tk = t2 + NOM(k) + cum * 32;
            if (k >= K0 && k + 1 < 2 * NU) {
                WATCH(tk - (XM + XW) * 128)
                int sum = 0, n = 0;
                do { LOOK() sum += LATENESS(p, now) >> 7; n++; } while ((unsigned)(tk - XM * 128 - now) < 0x40000u);
                const int l = (int)((float)sum * __builtin_amdgcn_rcpf((float)n)) - BK + prov / 4;
                if (k == K0 && l - prov / 4 > RTH) {
                    const int d_ = min(l * 4 - prov, 1600);
                    if (lane <= 2 * NU) atomicAdd((unsigned*)&sm.tu[lane], d_ * 32u);
                    off += d_; prov += d_; hold = 1;
                }
                if (l > RTH && lane == 0) GSTS((unsigned*)(ws + WS_REF + (blockIdx.x & 63) * LSTRIDE + 64) + (blockIdx.x >> 6), (unsigned)min(l, 1000) | ((rtag + k) << 10));
            }
            const int cum0 = cum;
            bool got = boss;
            if (!boss && (k > K0 || !joined)) {
                WATCH(tk - XR * 128)
                const u64 x = GLDS(dec + (joined ? k * 16 : 0));
                dv = __builtin_amdgcn_readfirstlane((unsigned)x);
                got = __builtin_amdgcn_readfirstlane((unsigned)(x >> 32)) == rtag;
                if (got && !joined) { JOIN(dv) got = false; }
            }
            sp1 = sp; sp = 0;
            if (got && k > K0) { cum = dv >> 18; sp = dv >> 6 & 4095u; dp = dv & 63u; }
            const int yld = cum - cum0 + sp;
            { const int pj = min(prov, cum - cum0); off -= pj; prov -= pj; off1 = off;
              const int ps = min(prov, sp); off -= ps; prov -= ps;
              if (hold) hold--; else prov = 0;
              const int r_ = off - prov; off -= r_ > 40 || r_ < -40 ? r_ / NB : r_; }
            NEWLINE(k)
            cum = min(cum + sp + dp, 16383);
            if (boss) dv = (cum << 18) | dp;
            if (boss && k >= K0 && k + 1 < 2 * NU && (int)(NOW() * 128 - tk) < -60 * 128) {
                WATCH(tk - (XM - XD1) * 128)
                const v4u x = __builtin_amdgcn_raw_buffer_load_b128(__builtin_amdgcn_make_buffer_rsrc(ws + WS_REF + 64, 0, 64 * LSTRIDE, 0x00020000), lane * LSTRIDE, 0, SC1);
                float a[4];
#pragma unroll
                for (int i = 0; i < 4; i++) a[i] = ((x[i] ^ ((rtag + k) << 10)) >> 10) == 0 ? (float)(x[i] & 1023u) : 0.f;
                const int tot = (int)wave_sum(a[0] + a[1] + a[2] + a[3]) >> 6;
                float mn = 0.f;
                if (tot >= 40) {
                    float st = (float)tot * 0.125f;
#pragma unroll
                    for (int i = 0; i < 4; i++, st *= 0.5f) {
                        const float x_ = mn + st;
                        const int c_ = __builtin_popcountll(__builtin_amdgcn_ballot_w64(a[0] >= x_)) + __builtin_popcountll(__builtin_amdgcn_ballot_w64(a[1] >= x_)) +
                                       __builtin_popcountll(__builtin_amdgcn_ballot_w64(a[2] >= x_)) + __builtin_popcountll(__builtin_amdgcn_ballot_w64(a[3] >= x_));
                        mn = c_ >= QN ? x_ : mn;
                    }
                }
                const float c = mn + (float)CAP, fth = (float)(FTH * (2 * NU - (int)k));
                const int cut = (int)wave_sum(fminf(a[0], c) + fminf(a[1], c) + fminf(a[2], c) + fminf(a[3], c)) >> 6, m4 = (int)mn * 4;
                const bool far = __builtin_amdgcn_ballot_w64(fmaxf(fmaxf(a[0], a[1]), fmaxf(a[2], a[3])) > fth) != 0;
                const int pend = yld + sp1 * XM / 320;
                const bool stall = m4 >= SMIN * 4 || k <= K0 + 1;
                const int nj = max((stall ? tot + (k <= K0 + 1 ? tot / 8 : SADD) : m4) - pend, 0);
                const int up = k >= 6 && tot < tot1 + 32 ? max(tot - tot1, 0) : 0;
                const int ns = stall ? 0 : min(max(max(min(m4 + (tot - m4 + up) * GAIN / 16 - pend - nj, (cut - m4 + up) * GAIN / 16), 0), far ? FLOOR * 4 : 0), 4000);
                int ndp = dp;
                if (k >= (unsigned)KD && tot >= KI && tot < 100 && tot > tot1 && tot < tot1 + 32 && tot1 < tot2 + 32) { ndp = min(dp + DPS, 40); lock = LOCK; }
                else if (k >= (unsigned)KD && dp < DP0 && tot >= WARN && tot < 100 && tot >= tot1) { ndp = dp + 2; lock = LOCK; }
                else if (lock) lock--;
                if (k >= 6 && tot + tot1 <= QUIET && (!lock || dp > DP0)) ndp = max(dp - 1, 0);
                tot2 = tot1; tot1 = tot;
                dv = (min(cum + nj, 16383) << 18) | (ns << 6) | ndp;
                if ((int)(NOW() * 128 - tk) > XL * 128) dv = (cum << 18) | dp;
                else if (lane == 0) GSTS(dec + (k + 1) * 16, (u64)dv | ((u64)rtag << 32));
            }
        }
        WATCH(p >= NPC - 8 ? now - 128 : now + 4096)
    } else if (wave == NSW + 2) {
        const int ql = lane < 48 ? lane : 47;
        unsigned c1, c2, s1, s2;
        unsigned q1, q2, lkz, lks, lvz, lvs;
#define UNIT_LOADS(HEAD) { \
        q1 = ((const unsigned*)(q + (size_t)(HEAD) * DH))[ql]; q2 = ((const unsigned*)(q + (size_t)(HEAD) * DH))[48 + ql]; \
        const size_t so = ((size_t)(HEAD) * SKV + brow0) / 2 + (lane >> 3) * 128 + (lane & 7); \
        lkz = ((const unsigned*)k_zeros)[so]; lks = ((const unsigned*)k_scales)[so]; \
        lvz = ((const unsigned*)v_zeros)[so]; lvs = ((const unsigned*)v_scales)[so]; }
#define UNIT_FILL(SL, U) { \
            v4f qq; \
            qq.x = bfr(bfr(bflo(q1) * bflo(c1)) + bfr(-bflo(q2) * bflo(s1))); \
            qq.y = bfr(bfr(bflo(q2) * bflo(c2)) + bfr(bflo(q1) * bflo(s2))); \
            qq.z = bfr(bfr(bfhi(q1) * bfhi(c1)) + bfr(-bfhi(q2) * bfhi(s1))); \
            qq.w = bfr(bfr(bfhi(q2) * bfhi(c2)) + bfr(bfhi(q1) * bfhi(s2))); \
            *(v2u*)&sm.q[SL][2 * ql] = (v2u){(__float_as_uint(qq.x) >> 16) | __float_as_uint(qq.y), (__float_as_uint(qq.z) >> 16) | __float_as_uint(qq.w)}; \
            *(v4f*)&sm.kz[SL][2 * lane] = (v4f){bflo(lkz), bflo(lks), bfhi(lkz), bfhi(lks)}; \
            *(v4f*)&sm.vz[SL][2 * lane] = (v4f){bflo(lvz), bflo(lvs), bfhi(lvz), bfhi(lvs)}; \
            CBAR(); \
            if (lane == 0) lds_st(&sm.rdy[SL], (U) + 1); \
            CBAR(); }
        asm volatile("s_setprio 3");
        asm volatile("s_waitcnt vmcnt(0)\n\tv_accvgpr_read_b32 %0, a0\n\tv_accvgpr_read_b32 %1, a1\n\tv_accvgpr_read_b32 %2, a2\n\tv_accvgpr_read_b32 %3, a3\n\tv_accvgpr_read_b32 %4, a4\n\t"
                     "v_accvgpr_read_b32 %5, a5\n\tv_accvgpr_read_b32 %6, a8\n\tv_accvgpr_read_b32 %7, a9\n\tv_accvgpr_read_b32 %8, a10\n\tv_accvgpr_read_b32 %9, a11"
                     : "=v"(c1), "=v"(c2), "=v"(s1), "=v"(s2), "=v"(q1), "=v"(q2), "=v"(lkz), "=v"(lks), "=v"(lvz), "=v"(lvs));
#pragma unroll 1
        for (unsigned u = 0; u < NU; u++) {
            if (u) UNIT_LOADS(rank + GSZ * u)
            while (u >= lds_ld(&sm.merged) + NIN) SLEEP(4);
            CBAR();
            UNIT_FILL(u % NIN, u)
            if (u == 1) asm volatile("s_setprio 0");
        }
        while (lds_ld(&sm.merged) < NU) SLEEP(16)
        CBAR();
        const unsigned T = lds_ld(&sm.tag);
        if (lane < NU) __hip_atomic_store((u64*)(ws + WS_LOG + (rank * NSPLIT + split) * LSTRIDE2) + lane, (u64)__float_as_uint(sm.ent[lane][130]) | ((u64)T << 32), __ATOMIC_RELAXED, __HIP_MEMORY_SCOPE_AGENT);
        while (lds_ld(&sm.merged) < NU + 3) SLEEP(16)
        for (;;) {
            const u64 x = __hip_atomic_load((const u64*)(ws + WS_LOG + ((blockIdx.x & 15) * NSPLIT + (lane & 15)) * LSTRIDE2) + (blockIdx.x >> 4), __ATOMIC_RELAXED, __HIP_MEMORY_SCOPE_AGENT);
            if (__builtin_amdgcn_ballot_w64((unsigned)(x >> 32) == T) == ~0ull) {
                const float L = __uint_as_float((unsigned)x);
                const float w = __builtin_amdgcn_exp2f(L - row_max(L));
                sm.fw[lane & 15] = w / row_sum(w);
                CBAR();
                if (lane == 0) lds_st(&sm.fw_rdy, 1u);
                break;
            }
            SLEEP(64)
        }
    } else {
        while (lds_ld(&sm.tag_rdy) == 0) SLEEP(1);
        CBAR();
        const unsigned T = lds_ld(&sm.tag);
        const __amdgpu_buffer_rsrc_t rsrc = __builtin_amdgcn_make_buffer_rsrc(ws + WS_PART + (rank * NSPLIT + split) * 512, 0, NH * NSPLIT * 512, 0x00020000);
        v2u* recs = (v2u*)&sm.cs[0][0];
#pragma unroll
        for (int i = 0; i < NOUT * 3; i++) (&sm.acc[0][0])[i * 64 + lane] = 0.0;
#define RECORD() \
        const float n0 = (float)(a0 - (double)h.y) * h.x, n1 = (float)(a1 - (double)h.y) * h.x, n2 = (float)(a2 - (double)h.y) * h.x; \
        const unsigned e2 = max(__float_as_uint(fmaxf(fmaxf(fabsf(n0), fabsf(n1)), fabsf(n2))) >> 23, 12u); \
        const float f2 = __uint_as_float((266u - e2) << 23); \
        const unsigned m0 = min(max((int)rintf(n0 * f2), -8191), 8191) & 0x3fff, m1 = min(max((int)rintf(n1 * f2), -8191), 8191) & 0x3fff, m2 = min(max((int)rintf(n2 * f2), -8191), 8191) & 0x3fff; \
        const v2u r = {m0 | (m1 << 14) | (m2 << 28), (m2 >> 4) | (e2 << 10) | (T << 18)};
#pragma unroll 1
        for (unsigned u = 0; u < 2 * NU - 1; u++) {
            const unsigned e = u < NU ? u : VENT(u), so = u % NOUT;
            const v4f h = *(const v4f*)&sm.ent[e][128];
            while (lds_ld(&sm.cnt[so]) != 32) SLEEP(1)
            CBAR();
            if (u < NU) {
                const float2 lg = *(const float2*)&sm.ent[e][2 * lane];
                const v4f zs = *(const v4f*)&sm.vz[u % NIN][2 * lane];
                const float m = wave_max(fmaxf(lg.x, lg.y));
                const float p0 = __expf(lg.x - m), p1 = __expf(lg.y - m);
                const float x0 = p0 * zs.y, x1 = p1 * zs.w;
                const unsigned eb = max(__float_as_uint(wave_max(fmaxf(fabsf(x0), fabsf(x1)))) >> 23, 21u);
                const float f = __uint_as_float((275u - eb) << 23);
                const float w0 = rintf(x0 * f), w1 = rintf(x1 * f);
                const float den = wave_sum(p0 + p1);
                const float bz = wave_sum(fmaf(w0, zs.x, w1 * zs.z));
                const float L = fmaf(m, 1.4426950408889634f, __builtin_amdgcn_logf(den));
                *(v2u*)&sm.ent[e][2 * lane] = (v2u){(unsigned)(int)w0, (unsigned)(int)w1};
                if (lane == 0) *(v4f*)&sm.ent[e][128] = (v4f){ldexpf(1.f / den, (int)eb - 148), bz, L, 0.f};
            } else {
                const double a0 = sm.acc[so][lane], a1 = sm.acc[so][64 + lane], a2 = sm.acc[so][128 + lane];
                RECORD()
                recs[(u - NU) * 64 + lane] = r;
                CBAR();
                sm.acc[so][lane] = 0.0; sm.acc[so][64 + lane] = 0.0; sm.acc[so][128 + lane] = 0.0;
            }
            CBAR();
            if (lane == 0) { lds_st(&sm.cnt[so], 0u); CBAR(); lds_st(&sm.merged, u + 1); }
        }
        {
            asm volatile("s_setprio 3");
            wave_sync();
            v4u rr[8]; unsigned po[8];
#pragma unroll
            for (int k = 0; k < 8; k++) { rr[k] = ((const v4u*)recs)[k * 64 + lane]; po[k] = k == 7 && lane >= 32 ? 0x7ffffff0u : (lane & 31) * 16 + (lane < 32 ? VENT(2 * k) : VENT(2 * k + 1)) * (GSZ * NSPLIT * 512); }
            const v4f h = *(const v4f*)&sm.ent[VENT(15)][128];
            asm volatile("" :: "v"(rr[0]), "v"(rr[1]), "v"(rr[2]), "v"(rr[3]), "v"(rr[4]), "v"(rr[5]), "v"(rr[6]), "v"(rr[7]), "v"(po[0]), "v"(po[1]), "v"(po[2]), "v"(po[3]), "v"(po[4]), "v"(po[5]), "v"(po[6]), "v"(po[7]), "v"(h));
            for (;;) {
                const unsigned d = __builtin_amdgcn_readfirstlane(TU(2 * NU).x) + PUBT * 128 - NOW() * 128;
                if (d >= 0x40000u) break;
                if (d > 20 * 128) SLEEP(4)
            }
#pragma unroll
            for (int k = 0; k < 8; k++) __builtin_amdgcn_raw_buffer_store_b128(rr[k], rsrc, po[k], 0, SC1);
            double a0, a1, a2;
            for (;;) {
                const unsigned c = __hip_atomic_load(&sm.cnt[NOUT - 1], __ATOMIC_RELAXED, __HIP_MEMORY_SCOPE_WORKGROUP);
                CBAR();
                a0 = sm.acc[NOUT - 1][lane]; a1 = sm.acc[NOUT - 1][64 + lane]; a2 = sm.acc[NOUT - 1][128 + lane];
                CBAR();
                if (__builtin_amdgcn_readfirstlane(c) == 32) break;
                SLEEP(0)
            }
            RECORD()
            const v4u r2 = {r.x, r.y, (unsigned)__builtin_amdgcn_update_dpp(0, (int)r.x, 0xf5, 0xf, 0xf, true), (unsigned)__builtin_amdgcn_update_dpp(0, (int)r.y, 0xf5, 0xf, 0xf, true)};
            __builtin_amdgcn_raw_buffer_store_b128(r2, rsrc, (lane & 1) ? 0x7ffffff0u : lane * 8, VENT(15) * (GSZ * NSPLIT * 512), SC1);
        }
        return;
    }

}

extern "C" void kernel_launch(void* const* d_in, const int* in_sizes, int n_in,
                              void* d_out, int out_size, void* d_ws, size_t ws_size,
                              hipStream_t stream) {
    const unsigned short* q = (const unsigned short*)d_in[0];
    const int* qk = (const int*)d_in[1];
    const unsigned short* k_scales = (const unsigned short*)d_in[2];
    const unsigned short* k_zeros = (const unsigned short*)d_in[3];
    const int* qv = (const int*)d_in[4];
    const unsigned short* v_scales = (const unsigned short*)d_in[5];
    const unsigned short* v_zeros = (const unsigned short*)d_in[6];
    const unsigned short* q_cos = (const unsigned short*)d_in[7];
    const unsigned short* q_sin = (const unsigned short*)d_in[8];
    const unsigned short* k_cos = (const unsigned short*)d_in[9];
    const unsigned short* k_sin = (const unsigned short*)d_in[10];
    unsigned char* ws = (unsigned char*)d_ws;
    attn_kernel<<<NSPLIT * GSZ, NTHREADS, 0, stream>>>(q, qk, k_scales, k_zeros, qv, v_scales, v_zeros,
                                                       q_cos, q_sin, k_cos, k_sin, (__hip_bfloat16*)d_out, ws);
}
```

```cpp
#include <hip/hip_runtime.h>
#include <hip/hip_bf16.h>
#include <stdint.h>

#define DH 192
#define HALF 96
#define SKV 2048
#define NH 256
#define NSW 8
#define NTHREADS (64 * (NSW + 4))
#define BROWS 128
#define NSPLIT (SKV / BROWS)
#define GSZ 16
#define NU (NH / GSZ)
#define NPC (2 * NU * 32)
#define NIN 4
#define NOUT 4
#define ENTW 132
#define SC1 16
#define PHI 0x0246024671357135ull
#define VORD 0x9876543210afedcbull
#define VENT(V) ((unsigned)(VORD >> (4 * ((V) & 15))) & 15u)
#define PFL 196
#define PK 1272
#define PV 1266
#define P0 1365
#define NBW 2
#define BTH 20
#define X0 250
#define K0 3
#define XM 250
#define XW 40
#define XD1 85
#define XL 90
#define XR 170
#define BK 3
#define RTH 10
#define QN 224
#define GAIN 28
#define CAP 45
#define FLOOR 3
#define FTH 15
#define KD 6
#define KI 16
#define DPS 4
#define KNOT 0x39c7000u
#define LOCK 6
#define NB 3
#define STH 90
#define SFIX 220
#define SNOT 0x610c0cu
#define HOLD 4
#define SMIN 100
#define SADD 8
#define PUBT 40
#define POLT 92
#define POLC 120

#define WS_EPOCH 0
#define WS_DEC 8192
#define WS_REF 16384
#define LSTRIDE 4352
#define WS_LOG 524288
#define LSTRIDE2 1408
#define WS_PART 1048576

typedef unsigned int v2u __attribute__((ext_vector_type(2)));
typedef unsigned int v4u __attribute__((ext_vector_type(4)));
typedef int v4i __attribute__((ext_vector_type(4)));
typedef float v2f __attribute__((ext_vector_type(2)));
typedef float v4f __attribute__((ext_vector_type(4)));
typedef unsigned long long u64;

__device__ __forceinline__ float bflo(unsigned u) { return __uint_as_float(u << 16); }
__device__ __forceinline__ float bfhi(unsigned u) { return __uint_as_float(u & 0xffff0000u); }
__device__ __forceinline__ float bfr(float x) {
    __bf16 b = (__bf16)x;
    return __uint_as_float(((unsigned)__builtin_bit_cast(unsigned short, b)) << 16);
}

#define SLEEP(n) SLEEP_(n)
#define SLEEP_(n) asm volatile("s_sleep " #n "\n\tv_add_co_u32 %0, vcc, 1, %0\n\ts_cbranch_vccz 1f\n\ts_endpgm\n1:" : "+v"(spins) :: "vcc", "memory");
#define SPIN0 0xf0000000u
#define CBAR() asm volatile("" ::: "memory")

__device__ __forceinline__ void wave_sync() {
    __builtin_amdgcn_fence(__ATOMIC_RELEASE, "wavefront");
    __builtin_amdgcn_wave_barrier();
    __builtin_amdgcn_fence(__ATOMIC_ACQUIRE, "wavefront");
}
template <int CTRL>
__device__ __forceinline__ float dpp_add(float x) {
    int y = __builtin_amdgcn_update_dpp(0, __float_as_int(x), CTRL, 0xf, 0xf, true);
    return x + __int_as_float(y);
}
template <int CTRL>
__device__ __forceinline__ float dpp_max(float x) {
    int y = __builtin_amdgcn_update_dpp(__float_as_int(x), __float_as_int(x), CTRL, 0xf, 0xf, false);
    return fmaxf(x, __int_as_float(y));
}
__device__ __forceinline__ float row_sum(float x) { x = dpp_add<0x128>(x); x = dpp_add<0x124>(x); x = dpp_add<0x122>(x); return dpp_add<0x121>(x); }
__device__ __forceinline__ float row_max(float m) { m = dpp_max<0x128>(m); m = dpp_max<0x124>(m); m = dpp_max<0x122>(m); return dpp_max<0x121>(m); }
__device__ __forceinline__ float rdlane(float x, int l) { return __int_as_float(__builtin_amdgcn_readlane(__float_as_int(x), l)); }
__device__ __forceinline__ float wave_max(float m) { m = row_max(m); return fmaxf(fmaxf(rdlane(m, 0), rdlane(m, 16)), fmaxf(rdlane(m, 32), rdlane(m, 48))); }
__device__ __forceinline__ float wave_sum(float x) { x = row_sum(x); return (rdlane(x, 0) + rdlane(x, 16)) + (rdlane(x, 32) + rdlane(x, 48)); }
__device__ __forceinline__ unsigned lds_ld(const unsigned* p) { return __builtin_amdgcn_readfirstlane(__hip_atomic_load(p, __ATOMIC_RELAXED, __HIP_MEMORY_SCOPE_WORKGROUP)); }
__device__ __forceinline__ unsigned lds_addr(const void* p) { return (unsigned)(size_t)(const __attribute__((address_space(3))) void*)p; }
__device__ __forceinline__ void lds_st(unsigned* p, unsigned v) { __hip_atomic_store(p, v, __ATOMIC_RELAXED, __HIP_MEMORY_SCOPE_WORKGROUP); }
#define NOW() ((unsigned)__builtin_amdgcn_s_memrealtime())

struct __attribute__((aligned(16))) Smem {
    unsigned cs[32][768];
    double acc[NOUT][DH];
    float ent[NU][ENTW];
    unsigned q[NIN][HALF];
    float2 kz[NIN][BROWS];
    float2 vz[NIN][BROWS];
    v2u tu[2 * NU + 2];
    unsigned grab, merged, tabs, cdone;
    unsigned rdy[NIN], cnt[NOUT];
    unsigned tag_rdy, tag, start, fw_rdy;
    float fw[NSPLIT];
};

#define TU(I) (*(const volatile v2u __attribute__((address_space(3)))*)(size_t)lds_addr(&sm.tu[I]))
#define LDS_ADD(P, V) { u64 ex_; asm volatile("s_mov_b64 %0, exec\n\ts_mov_b64 exec, 1\n\tds_add_u32 %1, %2\n\ts_mov_b64 exec, %0" : "=&s"(ex_) : "v"(lds_addr(P)), "v"(V) : "memory"); }
#define LDS_OR(P, V) { u64 ex_; asm volatile("s_mov_b64 %0, exec\n\ts_mov_b64 exec, 1\n\tds_or_b32 %1, %2\n\ts_mov_b64 exec, %0" : "=&s"(ex_) : "v"(lds_addr(P)), "v"(V) : "memory"); }

__global__ __launch_bounds__(NTHREADS, 3) void attn_kernel(
    const unsigned short* __restrict__ q, const int* __restrict__ qk,
    const unsigned short* __restrict__ k_scales, const unsigned short* __restrict__ k_zeros,
    const int* __restrict__ qv,
    const unsigned short* __restrict__ v_scales, const unsigned short* __restrict__ v_zeros,
    const unsigned short* __restrict__ q_cos, const unsigned short* __restrict__ q_sin,
    const unsigned short* __restrict__ k_cos, const unsigned short* __restrict__ k_sin,
    __hip_bfloat16* __restrict__ out, unsigned char* __restrict__ ws)
{
    __shared__ Smem sm;
    const int tid = threadIdx.x;
    unsigned spins = SPIN0;
#define TOUCH(OFF, BASE) asm volatile("global_load_dword a6, %0, %1" :: "v"(OFF), "s"(BASE) : "a6", "memory");
    const unsigned cl = min(((blockIdx.x >> 3) & 15) * 16 + (tid & 15), PFL - 1u) * 128;
    if (tid < 64) { const char* pc0; asm volatile("s_getpc_b64 %0" : "=s"(pc0)); TOUCH(cl, pc0) }
    __builtin_amdgcn_sched_barrier(0);
    const int hw = __builtin_amdgcn_readfirstlane(tid >> 6);
    const int lane = tid & 63;
    const int split = (blockIdx.x & 3) * 4 + ((blockIdx.x >> 3) & 3);
    const int rank = (blockIdx.x >> 5) * 2 + ((blockIdx.x >> 2) & 1);
    const int brow0 = split * 16;
#define ROWOF(RG) (((RG) >> 2) * 256 + ((RG) & 3) * 4)
    if (hw == 0) {
        typedef const char* const __attribute__((address_space(4))) * kap_t;
        const char* sa = ((kap_t)__builtin_amdgcn_kernarg_segment_ptr())[(0x5623 >> (4 * (split & 3))) & 7];
        unsigned o1 = (((rank & 1 ? 0 : 2) + (blockIdx.x >> 7) * 4) * 256 + brow0) * (DH * 2) + (((blockIdx.x >> 5) & 1) * 24 + min(lane, 23)) * 128;
        unsigned o2 = ((rank + GSZ * (lane >> 3)) * SKV + (lane & 7) * 256 + (brow0 & ~63)) * 2, o3 = o2 + 8 * GSZ * SKV * 2;
        unsigned o4 = (rank + GSZ * ((lane >> 2 & 3) + 4 * (split & 3))) * (DH * 2) + min(lane & 3, 2) * 128;
        asm volatile("" : "+v"(o1), "+v"(o2), "+v"(o3), "+v"(o4));
        asm volatile("global_load_dword a6, %0, %1 nt\n\tglobal_load_dword a6, %0, %2 nt" :: "v"((lane & 1) * (NH * SKV * DH * 4u - 128u * NH) + blockIdx.x * 128), "s"(qk), "s"(qv) : "a6", "memory");
        TOUCH(o1, (blockIdx.x & 64) ? k_sin : k_cos) TOUCH(o4, q) TOUCH(o2, sa) TOUCH(o3, sa)
        asm volatile("s_barrier\n\ts_waitcnt vmcnt(2)" ::: "memory");
        if (lane == 0) lds_st(&sm.start, NOW() | 1u);
        asm volatile("s_endpgm");
    }
    const unsigned rot = 4 * ((unsigned)(PHI >> (60 - 4 * rank)) & 7u);
#define RGOF(G) ((((G) + rot) & 31u) ^ ((((G) + rot) >> 1) & 2u))
#define CS_DMA(RG) asm volatile("s_mov_b32 m0, %1\n\ts_nop 0\n\tglobal_load_lds_dwordx4 %0, %2\n\ts_mov_b32 exec_hi, 0\n\tglobal_load_lds_dwordx4 %0, %2 offset:1024\n\ts_add_u32 m0, m0, 0x600\n\ts_nop 0\n\t" \
                                "global_load_lds_dwordx4 %0, %3 offset:1024\n\ts_mov_b32 exec_hi, -1\n\tglobal_load_lds_dwordx4 %0, %3" \
                                :: "v"((brow0 + ROWOF(RG)) * (DH * 2) + lane * 16), "s"(lds_addr(&sm.cs[0][0]) + (RG) * 3072), "s"(k_cos), "s"(k_sin) : "scc", "memory");
    if (hw >= 4) CS_DMA(RGOF((unsigned)hw - 4))
    if (hw == 1) {
        const unsigned qo = min(lane, 47) * 4, so = (rank * SKV + brow0) * 2 + (lane >> 3) * 512 + (lane & 7) * 4;
        asm volatile("global_load_dword a0, %0, %3\n\tglobal_load_dword a1, %0, %3 offset:192\n\tglobal_load_dword a2, %0, %4\n\tglobal_load_dword a3, %0, %4 offset:192\n\t"
                     "global_load_dword a4, %1, %5\n\tglobal_load_dword a5, %1, %5 offset:192\n\t"
                     "global_load_dword a8, %2, %6\n\tglobal_load_dword a9, %2, %7\n\tglobal_load_dword a10, %2, %8\n\tglobal_load_dword a11, %2, %9"
                     :: "v"(qo), "v"(qo + rank * (DH * 2)), "v"(so), "s"(q_cos), "s"(q_sin), "s"(q), "s"(k_zeros), "s"(k_scales), "s"(v_zeros), "s"(v_scales)
                     : "a0", "a1", "a2", "a3", "a4", "a5", "a8", "a9", "a10", "a11", "memory");
    }
    if (hw == 3 && lane < 16) (&sm.grab)[lane] = lane ? 0 : 64;
    asm volatile("" :: "s"(q), "s"(qk), "s"(k_scales), "s"(k_zeros), "s"(qv), "s"(v_scales), "s"(v_zeros), "s"(q_cos), "s"(q_sin), "s"(k_cos), "s"(k_sin), "s"(out), "s"(ws));
    asm volatile("s_waitcnt lgkmcnt(0)\n\ts_barrier" ::: "memory");
    const int wave = (int)(0x76543210b9a8ull >> (4 * hw)) & 15;
    unsigned ts;
#define START() { while ((ts = lds_ld(&sm.start)) == 0) SLEEP(1) }

    if (__builtin_expect(wave < NSW, 1)) {
#define REPACK(RG) { \
        unsigned* cw = &sm.cs[RG][0]; \
        const unsigned* l = cw + rsel * (DH / 2) + qoff; \
        unsigned r[2][6]; \
        _Pragma("unroll") \
        for (int j = 0; j < 2; j++) { \
            unsigned e[4][2]; \
            _Pragma("unroll") \
            for (int k = 0; k < 4; k++) { \
                const unsigned* a = l + j * DH + (k & 1) * 48 + (k >> 1) * 384;       \
                unsigned x0 = a[0], x1 = a[1]; \
                e[k][0] = __builtin_amdgcn_alignbit(x1, x0, sh); \
                e[k][1] = x1 >> sh; \
            } \
            r[j][0] = (e[0][0] & 0xffffu) | (e[3][0] << 16); \
            r[j][1] = (e[0][0] >> 16) | (e[3][0] & 0xffff0000u); \
            r[j][2] = (e[0][1] & 0xffffu) | (e[3][1] << 16); \
            r[j][3] = (e[1][0] & 0xffffu) | (e[2][0] << 16); \
            r[j][4] = (e[1][0] >> 16) | (e[2][0] & 0xffff0000u); \
            r[j][5] = (e[1][1] & 0xffffu) | (e[2][1] << 16); \
        } \
        wave_sync(); \
        _Pragma("unroll") \
        for (int j = 0; j < 2; j++) { *(v4u*)&cw[j * 256 + lane * 4] = (v4u){r[j][0], r[j][1], r[j][2], r[j][3]}; *(v2u*)&cw[512 + j * 128 + lane * 2] = (v2u){r[j][4], r[j][5]}; } \
        wave_sync(); \
        LDS_OR(&sm.tabs, 1u << (RG)) }
        const int sub = lane & 31;
        const int rsel = lane >> 5;
        const unsigned sh = (sub & 1) * 16;
        const int qoff = 3 * (sub >> 1) + (sub & 1);
        const char* kbase = (const char*)qk + (size_t)(rank * SKV + brow0) * (DH * 4);
        const char* vbase = (const char*)qv + (size_t)(rank * SKV + brow0) * (DH * 4);
        const unsigned koff = (rsel * DH + 3 * sub) * 4, voff = 3 * lane * 4;
#define AR(S) (16 + (S) * 8)
        asm volatile("" ::: "a16", "a17", "a18", "a19", "a20", "a21", "a22", "a23", "a24", "a25", "a26", "a27", "a28", "a29", "a30", "a31",
                            "a32", "a33", "a34", "a35", "a36", "a37", "a38", "a39", "a40", "a41", "a42", "a43", "a44", "a45", "a46", "a47");
        asm volatile("" ::: "a48", "a49", "a50", "a51", "a52", "a53", "a54", "a55", "a56", "a57", "a58", "a59", "a60", "a61", "a62", "a63");
        int ta[3], tb[3];
#define GLD(A, OFF, BASE, IMM) asm volatile("global_load_dwordx3 a[%c0:%c1], %2, %3 offset:%4 nt" :: "i"(A), "i"((A) + 2), "v"(OFF), "s"(BASE), "i"(IMM))
#define ISSUE(S, J, BASE, OA, OB) { GLD(AR(S), OA, BASE, (J) * 1536); GLD(AR(S) + 4, OB, BASE, (J) * 1536); }
#define WAITT(S, N) asm volatile("s_waitcnt vmcnt(%6)\n\tv_accvgpr_read_b32 %0, a[%c7]\n\tv_accvgpr_read_b32 %1, a[%c8]\n\tv_accvgpr_read_b32 %2, a[%c9]\n\t" \
                              "v_accvgpr_read_b32 %3, a[%c10]\n\tv_accvgpr_read_b32 %4, a[%c11]\n\tv_accvgpr_read_b32 %5, a[%c12]" \
                              : "=v"(ta[0]), "=v"(ta[1]), "=v"(ta[2]), "=v"(tb[0]), "=v"(tb[1]), "=v"(tb[2]) \
                              : "i"(N), "i"(AR(S)), "i"(AR(S) + 1), "i"(AR(S) + 2), "i"(AR(S) + 4), "i"(AR(S) + 5), "i"(AR(S) + 6))
        const unsigned koffb = koff + 384, voffb = voff + 768;
        unsigned u = 0, rg, np = 0;
#define RG0(C) RGOF((unsigned)(wave + 8 * (C)))
        const float scale = 0.07216878364870322f;
        double acc[3] = {0.0, 0.0, 0.0};
        bool chg, none = false;
        unsigned Qp[3], Qn[3];
        float kz4[2], ksc, kres[2];
        v4u ca[2]; v2u cb[2];
        v2f qe[2][3], A[2];
        const char* nb; unsigned oa, ob;
        unsigned g, gu, mg, nrg, tg, dg, par = 0;
        int early;
        const unsigned bth = wave < NBW ? BTH * 128 : 0x3fffffff;
        const unsigned grab_adr = lds_addr(&sm.grab);
        asm volatile("v_accvgpr_write_b32 a13, 1" ::: "a13", "a15");
#define COUNT(ADR) { u64 ex_; asm volatile("s_mov_b64 %0, exec\n\ts_mov_b64 exec, 1\n\tds_add_rtn_u32 a15, %1, a13\n\ts_mov_b64 exec, %0" : "=&s"(ex_) : "v"(ADR) : "memory"); }
#define GRABBED() { unsigned t_; asm volatile("s_waitcnt lgkmcnt(0)\n\tv_accvgpr_read_b32 %1, a15\n\ts_nop 0\n\tv_readfirstlane_b32 %0, %1\n\ts_nop 1" : "=s"(g), "=v"(t_)); }
#define NEXT1() const v2u x_ = TU(g >> 5); u64 tn_; asm volatile("s_memrealtime %0" : "=s"(tn_));
#define NEXT2() { \
            chg = (g >> 5) != u; none = g >= NPC; \
            const bool nv = g >= NPC / 2; \
            nrg = RGOF(g); \
            unsigned boff; asm("s_mul_i32 %0, %1, 0x300" : "=s"(boff) : "s"((nv ? VENT(g >> 5) : g >> 5) * (GSZ * SKV) + ROWOF(nrg) + 2)); \
            nb = (nv ? vbase : kbase) + boff; \
            oa = nv ? voff : koff; ob = nv ? voffb : koffb; \
            asm volatile("s_waitcnt lgkmcnt(0)" : "+s"(tn_) : "s"(nb), "v"(oa), "v"(ob)); \
            tg = __builtin_amdgcn_readfirstlane(x_.x) + (g & 31) * __builtin_amdgcn_readfirstlane(x_.y); \
            early = none ? 0 : (int)((unsigned)tn_ * 128 - tg - bth); \
            if (early > 0) asm volatile("s_cmp_lg_u32 %3, 0\n\ts_cbranch_scc1 1f\n\t" L4(32) "\ts_branch 2f\n1:\n\t" L4(16) "2:\n\ts_mov_b64 vcc, exec\n\ts_mov_b64 exec, 1\n\tds_add_rtn_u32 a15, %4, a13\n\ts_mov_b64 exec, vcc" \
                                    :: "v"(oa), "v"(ob), "s"(nb), "s"(pr), "v"(grab_adr) : "scc", "vcc", "memory"); }
#define L4(A) "global_load_dwordx3 a[" #A ":" #A "+2], %0, %2 offset:-1536 nt\n\tglobal_load_dwordx3 a[" #A "+4:" #A "+6], %1, %2 offset:-1536 nt\n\t" \
              "global_load_dwordx3 a[" #A "+8:" #A "+10], %0, %2 nt\n\tglobal_load_dwordx3 a[" #A "+12:" #A "+14], %1, %2 nt\n"
#define GATE(...) \
            asm volatile("s_setprio 0\n1:\n\ts_memrealtime vcc\n\ts_waitcnt lgkmcnt(0)\n\ts_lshl_b32 vcc_lo, vcc_lo, 7\n\ts_sub_u32 %0, %1, vcc_lo\n\ts_cmp_lt_u32 %0, 0x40000\n\ts_cbranch_scc0 2f\n\t" \
                         "s_cmp_lt_u32 %0, %2\n\ts_cbranch_scc1 1b\n\ts_sleep %3\n\ts_branch 1b\n2:\n\ts_setprio 2" : "=&s"(dg) : "s"(tg), "i"(10 * 128), "i"(2), __VA_ARGS__ : "scc", "vcc", "memory");
#define RD6(A) "v_accvgpr_read_b32 %0, a[%c6+" #A "]\n\tv_accvgpr_read_b32 %1, a[%c6+" #A "+1]\n\tv_accvgpr_read_b32 %2, a[%c6+" #A "+2]\n\t" \
               "v_accvgpr_read_b32 %3, a[%c6+" #A "+4]\n\tv_accvgpr_read_b32 %4, a[%c6+" #A "+5]\n\tv_accvgpr_read_b32 %5, a[%c6+" #A "+6]\n"
#define LD2(A) "global_load_dwordx3 a[%c6+" #A ":%c6+" #A "+2], %7, %9 offset:%10 nt\n\tglobal_load_dwordx3 a[%c6+" #A "+4:%c6+" #A "+6], %8, %9 offset:%10 nt"
#define OUT6 "=&v"(ta[0]), "=&v"(ta[1]), "=&v"(ta[2]), "=&v"(tb[0]), "=&v"(tb[1]), "=&v"(tb[2])
#define WI(A, B, TAIL) "s_cmp_gt_i32 %13, 0\n\ts_cbranch_scc1 3f\n\ts_waitcnt vmcnt(3)\n\t" LD2(A) TAIL "\n\ts_waitcnt vmcnt(4)\n3:\n\ts_waitcnt vmcnt(%c14)\n\t" RD6(B)
#define WAITISSUE(S, TAIL, ...) asm volatile("s_cmp_lg_u32 %12, 0\n\ts_cbranch_scc1 1f\n\t" WI(16, 0, TAIL) "\ts_branch 2f\n1:\n\t" WI(0, 16, TAIL) "2:" \
                              : OUT6 : "i"(AR(S)), "v"(oa), "v"(ob), "s"(nb), "i"(((S) - 1) * 1536), "v"(grab_adr), "s"(pr), "s"(early), "i"(6 - 2 * (S)), __VA_ARGS__ : "scc", "vcc", "memory")
#define LASTT(S) asm volatile("s_waitcnt vmcnt(%c8)\n\ts_cmp_lg_u32 %7, 0\n\ts_cbranch_scc1 1f\n\t" RD6(0) "\ts_branch 2f\n1:\n\t" RD6(16) "2:" : OUT6 : "i"(AR(S)), "s"(pr), "i"(2 - 2 * (S)) : "scc")
#define DRAW "\n\ts_mov_b64 vcc, exec\n\ts_mov_b64 exec, 1\n\tds_add_rtn_u32 a15, %11, a13\n\ts_mov_b64 exec, vcc"
#define K_TAB() { _Pragma("unroll") for (int p = 0; p < 2; p++) { \
            const unsigned* cw = &sm.cs[rg][0]; \
            ca[p] = *(const v4u*)&cw[p * 256 + lane * 4]; cb[p] = *(const v2u*)&cw[512 + p * 128 + lane * 2]; } }
#define K_PRE(P) { \
        float e0, e1, e2, f0, f1, f2; \
        asm("v_dot2_f32_bf16 %0, %6, %12, 0\n\tv_dot2_f32_bf16 %3, %9, %15, 0\n\tv_dot2_f32_bf16 %1, %7, %13, 0\n\tv_dot2_f32_bf16 %4, %10, %16, 0\n\t" \
            "v_dot2_f32_bf16 %2, %8, %14, 0\n\tv_dot2_f32_bf16 %5, %11, %17, 0\n\ts_nop 2" \
            : "=&v"(e0), "=&v"(e1), "=&v"(e2), "=&v"(f0), "=&v"(f1), "=&v"(f2) \
            : "v"(Qp[0]), "v"(Qp[1]), "v"(Qp[2]), "v"(Qn[0]), "v"(Qn[1]), "v"(Qn[2]), \
              "v"(ca[P].x), "v"(ca[P].y), "v"(ca[P].z), "v"(ca[P].w), "v"(cb[P].x), "v"(cb[P].y)); \
        qe[P][0] = (v2f){e0, f0}; qe[P][1] = (v2f){e1, f1}; qe[P][2] = (v2f){e2, f2}; \
        A[P] = (qe[P][0] + qe[P][1] + qe[P][2]) * (v2f){-kz4[P], -kz4[P]}; }
#define K_SUM(P) { \
        _Pragma("unroll") \
        for (int i = 0; i < 3; i++) { v2f x = {(float)ta[i], (float)tb[i]}; A[P] = qe[P][i] * x + A[P]; } \
        kres[P] = A[P].x + A[P].y; }
#define K_RED2() { \
        const bool o1 = lane & 1; \
        float sx = (o1 ? kres[1] : kres[0]) + __int_as_float(__builtin_amdgcn_update_dpp(0, __float_as_int(o1 ? kres[0] : kres[1]), 0xb1, 0xf, 0xf, true)); \
        sx = dpp_add<0x4e>(sx); sx = dpp_add<0x124>(sx); sx = dpp_add<0x128>(sx); \
        const auto sw = __builtin_amdgcn_permlane16_swap(__float_as_uint(sx), __float_as_uint(sx), false, false); \
        sx = __uint_as_float(sw[0]) + __uint_as_float(sw[1]); \
        if ((lane & 30) == 0) sm.ent[u][rg * 4 + 2 * (lane & 1) + rsel] = sx * ksc; }
#define LDV(P) __hip_atomic_load(P, __ATOMIC_RELAXED, __HIP_MEMORY_SCOPE_WORKGROUP)
#define RDYWAIT(SL, SEQ) { while (lds_ld(&sm.rdy[SL]) != (SEQ)) SLEEP(1); CBAR(); }
#define LOADZ() { _Pragma("unroll") for (int p = 0; p < 2; p++) kz4[p] = sm.kz[u % NIN][rg * 4 + 2 * p + rsel].x; ksc = sm.kz[u % NIN][rg * 4 + 2 * (lane & 1) + rsel].y * scale; }
#define LOADQ() { _Pragma("unroll") for (int i = 0; i < 3; i++) { Qp[i] = sm.q[u % NIN][3 * sub + i]; Qn[i] = __builtin_amdgcn_alignbit(Qp[i], Qp[i], 16) ^ 0x80000000u; } }
#define FLUSH(ISV) { \
            mg = __builtin_amdgcn_readfirstlane(mg); \
            while (u >= mg + NOUT) { SLEEP(1); mg = lds_ld(&sm.merged); } \
            CBAR(); \
            if (ISV) { const unsigned aa = lds_addr(&sm.acc[u % NOUT][lane]); \
                asm volatile("ds_add_f64 %0, %1\n\tds_add_f64 %0, %2 offset:512\n\tds_add_f64 %0, %3 offset:1024" :: "v"(aa), "v"(acc[0]), "v"(acc[1]), "v"(acc[2]) : "memory"); \
                acc[0] = 0.0; acc[1] = 0.0; acc[2] = 0.0; } \
            CBAR(); \
            LDS_ADD(&sm.cnt[u % NOUT], np) \
            CBAR(); \
            np = 0; }
#define ADVANCE() { u = gu; rg = nrg; }

#define REQ0(N, S) { tg = ts * 128 + (wave + 8 * (N)) * P0; \
            const char* kb = kbase + (((N) >> 2) * (GSZ * SKV) + ROWOF(RG0((N) & 3))) * (DH * 4); GATE("s"(kb)) ISSUE(S, 0, kb, koff, koffb) ISSUE((S) + 1, 1, kb, koff, koffb) }
#define USE0(N, S, W) { u = (N) >> 2; rg = RG0((N) & 3); K_TAB() LOADQ() LOADZ() K_PRE(0) K_PRE(1) WAITT(S, W); K_SUM(0) WAITT((S) + 1, W); K_SUM(1) K_RED2() }
#define VMW(N) asm volatile("s_waitcnt vmcnt(" #N ")" ::: "memory");
        START()
        REQ0(0, 0) CS_DMA(RG0(1))
        VMW(8) REPACK(RG0(0))
        REQ0(1, 2)
        RDYWAIT(0, 1u)
        USE0(0, 0, 8)
        VMW(4) REPACK(RG0(1))
        REQ0(2, 0) CS_DMA(RG0(2))
        USE0(1, 2, 8)
        REQ0(3, 2)
        VMW(4) REPACK(RG0(2))
        USE0(2, 0, 4)
        REQ0(4, 0) CS_DMA(RG0(3))
        v2f qe2[2][2][3], A2[2][2]; float ks2[2];
        RDYWAIT(1, 2u)
        u = 1;
        LOADQ()
#pragma unroll
        for (int j = 0; j < 2; j++) {
            rg = RG0(j); K_TAB() LOADZ() K_PRE(0) K_PRE(1)
            ks2[j] = ksc;
#pragma unroll
            for (int p = 0; p < 2; p++) { A2[j][p] = A[p]; qe2[j][p][0] = qe[p][0]; qe2[j][p][1] = qe[p][1]; qe2[j][p][2] = qe[p][2]; }
        }
        asm volatile("" :: "v"(A2[0][0]), "v"(A2[0][1]), "v"(A2[1][0]), "v"(A2[1][1]), "v"(ks2[0]), "v"(ks2[1]));
        REQ0(5, 4)
        VMW(4) REPACK(RG0(3))
        USE0(3, 2, 4)
        CBAR();
        LDS_ADD(&sm.cnt[0], 4u)
        REQ0(6, 2)
        u = 1;
#define FIN0(J, S, W) { rg = RG0(J); ksc = ks2[J]; \
            _Pragma("unroll") for (int p = 0; p < 2; p++) { A[p] = A2[J][p]; qe[p][0] = qe2[J][p][0]; qe[p][1] = qe2[J][p][1]; qe[p][2] = qe2[J][p][2]; } \
            WAITT(S, W); K_SUM(0) WAITT((S) + 1, W); K_SUM(1) K_RED2() }
        FIN0(0, 0, 8)
        FIN0(1, 4, 4)
        REQ0(7, 0)
        COUNT(grab_adr)
        USE0(6, 2, 4)
        rg = RG0(3); np = 3;
        GRABBED()
#pragma unroll 1
        while (u < NU) {
            const unsigned pr = __builtin_amdgcn_readfirstlane(par); par ^= 1;
            NEXT1()
            const unsigned tb_ = LDV(&sm.tabs), fl = LDV(&sm.rdy[u % NIN]);
            mg = LDV(&sm.merged);
            CBAR();
            K_TAB() LOADQ() LOADZ()
            NEXT2()
            if (__builtin_expect(__builtin_amdgcn_readfirstlane(fl) != u + 1 || !((__builtin_amdgcn_readfirstlane(tb_) >> rg) & 1u), 0)) {
                while (!((lds_ld(&sm.tabs) >> rg) & 1u)) SLEEP(1)
                RDYWAIT(u % NIN, u + 1)
                K_TAB() LOADQ() LOADZ()
            }
            K_PRE(0) K_PRE(1)
            if (early <= 0) GATE("v"(A[0]), "v"(A[1]), "v"(oa), "v"(ob), "s"(nb), "s"(pr))
            WAITISSUE(0, DRAW, "v"(A[0]));
            K_SUM(0)
            WAITISSUE(1, "", "v"(A[1]));
            K_SUM(1)
            gu = g >> 5;
            GRABBED()
            K_RED2()
            np++;
            if (chg) FLUSH(false)
            ADVANCE()
        }
#pragma unroll 1
        while (u < 2 * NU) {
            const unsigned pr = __builtin_amdgcn_readfirstlane(par); par ^= 1;
            NEXT1()
            mg = LDV(&sm.merged);
            CBAR();
            const unsigned ve = VENT(u);
            v4i pw = *(const v4i*)&sm.ent[ve][rg * 4];
            NEXT2()
            if (__builtin_expect(__builtin_amdgcn_readfirstlane(mg) <= ve, 0)) { while (lds_ld(&sm.merged) <= ve) SLEEP(1); CBAR(); pw = *(const v4i*)&sm.ent[ve][rg * 4]; }
            const double w0 = pw.x, w1 = pw.y, w2 = pw.z, w3 = pw.w;
            if (none) { LASTT(0); } else { if (early <= 0) GATE("s"(pr), "v"(w0), "v"(w1), "v"(w2), "v"(w3), "v"(oa), "v"(ob), "s"(nb)) WAITISSUE(0, DRAW, "v"(w0)); }
#pragma unroll
            for (int i = 0; i < 3; i++) acc[i] = fma(w1, (double)tb[i], fma(w0, (double)ta[i], acc[i]));
            if (none) { LASTT(1); } else WAITISSUE(1, "", "v"(w2));
#pragma unroll
            for (int i = 0; i < 3; i++) acc[i] = fma(w3, (double)tb[i], fma(w2, (double)ta[i], acc[i]));
            gu = g >> 5;
            np++;
            if (chg) FLUSH(true)
            GRABBED()
            ADVANCE()
        }

        {
            const int head = blockIdx.x;
            while (lds_ld(&sm.fw_rdy) == 0) SLEEP(2)
            CBAR();
            const unsigned tt = lds_ld(&sm.tag) << 18;
            const float wh = sm.fw[wave + 8 * rsel];
            const unsigned ro = (wave + 8 * rsel) * 512 + sub * 16;
            const char* rb = (const char*)ws + WS_PART + head * (NSPLIT * 512);
            v4f* pb = (v4f*)((char*)&sm.cs[0][0] + 16384);
            __hip_bfloat16* o = out + (size_t)head * DH + 6 * sub + 3 * rsel;
            tg = __builtin_amdgcn_readfirstlane(TU(2 * NU).x) + ((head >> 4) == VENT(15) ? POLC : POLT) * 128;
            GATE("s"(tt), "v"(wh), "v"(ro), "s"(rb), "v"(o))
            v4u R;
#define PLD(A) "global_load_dwordx4 a[" #A ":" #A "+3], %6, %7 sc1\n\t"
#define PCK(A, N, L) "s_waitcnt vmcnt(" #N ")\n\tv_accvgpr_read_b32 %1, a[" #A "+1]\n\tv_accvgpr_read_b32 %3, a[" #A "+3]\n\tv_accvgpr_read_b32 %0, a[" #A "]\n\tv_accvgpr_read_b32 %2, a[" #A "+2]\n\t" \
                     "v_xor_b32 %1, %8, %1\n\tv_xor_b32 %3, %8, %3\n\tv_or_b32 %4, %1, %3\n\tv_cmp_gt_u32 vcc, %9, %4\n\ts_cmp_eq_u64 vcc, exec\n\ts_cbranch_scc1 " L "\n\t"
            { unsigned t_;
              asm volatile("s_cmp_eq_u32 %10, %12\n\ts_cbranch_scc1 1f\n\t" PLD(16) PCK(16, 0, "3f") "1:\n\t" PLD(16) "s_sleep %11\n\t" PLD(20) "s_sleep %11\n\t" PLD(24)
                           "2:\n\t" PCK(16, 2, "3f") PLD(16) PCK(20, 2, "3f") PLD(20) PCK(24, 2, "3f") PLD(24)
                           "v_add_co_u32 %5, vcc, 64, %5\n\ts_cbranch_vccz 2b\n\ts_endpgm\n3:"
                           : "=&v"(R.x), "=&v"(R.y), "=&v"(R.z), "=&v"(R.w), "=&v"(t_), "+v"(spins) : "v"(ro), "s"(rb), "s"(tt), "s"(0x40000u), "s"(head >> 4), "i"(5), "i"(VENT(15)) : "scc", "vcc", "memory"); }
            const float s0 = ldexpf(wh, (int)(R.y >> 10) - 139), s1 = ldexpf(wh, (int)(R.w >> 10) - 139);
            const float f[6] = {s0 * (float)(int)__builtin_amdgcn_sbfe(R.x, 0, 14), s0 * (float)(int)__builtin_amdgcn_sbfe(R.x, 14, 14), s0 * (float)(int)__builtin_amdgcn_sbfe(__builtin_amdgcn_alignbit(R.y, R.x, 28), 0, 14),
                                s1 * (float)(int)__builtin_amdgcn_sbfe(R.z, 0, 14), s1 * (float)(int)__builtin_amdgcn_sbfe(R.z, 14, 14), s1 * (float)(int)__builtin_amdgcn_sbfe(__builtin_amdgcn_alignbit(R.w, R.z, 28), 0, 14)};
            float sv[3];
#pragma unroll
            for (int i = 0; i < 3; i++) {
                const auto sw = __builtin_amdgcn_permlane32_swap(__float_as_uint(f[i]), __float_as_uint(f[i + 3]), false, false);
                sv[i] = __uint_as_float(sw[0]) + __uint_as_float(sw[1]);
            }
            pb[wave * 64 + lane] = (v4f){sv[0], sv[1], sv[2], 0.f};
            CBAR();
            COUNT(grab_adr + 12)
            v4f ps[NSW];
#pragma unroll
            for (int w = 0; w < NSW; w++) ps[w] = pb[w * 64 + lane];
            GRABBED()
            if (g == NSW - 1) {
                const v4f sum = ((ps[0] + ps[1]) + (ps[2] + ps[3])) + ((ps[4] + ps[5]) + (ps[6] + ps[7]));
                o[0] = __float2bfloat16(sum.x); o[1] = __float2bfloat16(sum.y); o[2] = __float2bfloat16(sum.z);
            }
        }
        return;
    }

    if (wave == NSW + 1) {
        u64 E; asm volatile("s_setprio 3\n\ts_load_dwordx2 %0, %1, 0\n\ts_waitcnt lgkmcnt(0)" : "=s"(E) : "s"(ws + WS_EPOCH) : "memory");
        START()
        unsigned t2 = ts * 128 + 64 * P0;
        int off = 0, off1 = 0, hold = 0, cum = 0, dp = 0, sp = 0;
#define NOM(U) (((int)(U) - 2) * 32 * PK + max((int)(U) - NU, 0) * 32 * (PV - PK))
#define NEWLINE(K) { if (lane >= (K) && lane <= 2 * NU) sm.tu[lane] = (v2u){t2 + NOM(lane) + (cum + (lane - (int)(K)) * dp + (lane > (K) ? sp + off : off1)) * 32, \
                                                                            (unsigned)((lane >= NU ? PV : PK) + dp + (lane == (K) ? sp + off - off1 : 0))}; }
        NEWLINE(2)
        if (lane == 0) sm.tu[1] = (v2u){t2 - 32 * P0, (unsigned)P0};
        const u64 E1 = E + 1;
        const unsigned rtag = (unsigned)E1 * 0x9e3779b1u;
        const bool boss = blockIdx.x == 0;
        u64* const slot = (u64*)(ws + WS_REF + (blockIdx.x & 63) * LSTRIDE + (blockIdx.x >> 6) * 16);
        const u64* const slots = (const u64*)(ws + WS_REF + lane * LSTRIDE);
        u64* const dec = (u64*)(ws + WS_DEC);
#define GLDS(P) __hip_atomic_load(P, __ATOMIC_RELAXED, __HIP_MEMORY_SCOPE_AGENT)
#define GSTS(P, V) __hip_atomic_store(P, V, __ATOMIC_RELAXED, __HIP_MEMORY_SCOPE_AGENT)
#define UNTIL(T) while ((unsigned)((T) - NOW() * 128) < 0x40000u) SLEEP(2)
        if (lane < 2) GSTS(slot + lane, lane ? E1 : (u64)ts | ((u64)rtag << 32));
        if (lane == 0) { sm.tag = (unsigned)E1; CBAR(); lds_st(&sm.tag_rdy, 1u); }
        unsigned dv = 0;
        bool joined = false;
#define JOIN(T0) { const int d0 = min(max((int)((T0) - ts), -1000), 1000); off -= d0 * 4; t2 += d0 * 128; joined = true; }
        unsigned pw_ = 0, tw_ = 0, now, p; int prov = 0;
#define LOOK() { u64 t_; asm volatile("ds_read_b32 %1, %2\n\ts_memrealtime %0\n\ts_waitcnt lgkmcnt(0)" : "=s"(t_), "=v"(p) : "v"(lds_addr(&sm.grab)) : "memory"); \
                 p = __builtin_amdgcn_readfirstlane(p) - 8; now = (unsigned)t_ * 128; }
#define LATENESS(P, T) ({ const v2u e = TU((P) >> 5); (int)((T) - __builtin_amdgcn_readfirstlane(e.x) - ((P) & 31) * __builtin_amdgcn_readfirstlane(e.y)); })
#define WATCH(T) for (;;) { \
            LOOK() \
            if (__builtin_expect(now - tw_ > STH * 128u && pw_ >= 64 && p < NPC - 8, 0)) { \
                const int lw = max(LATENESS(pw_, tw_), 0); \
                if (LATENESS(p, now) - lw > STH * 128) \
                    for (int d0_ = hold || ((SNOT >> (p >> 5)) & 1) ? 0 : SFIX * 4;; d0_ = 0) { \
                        const int d_ = min(max((LATENESS(p, now) - lw) >> 5, d0_), 1600); \
                        if (lane <= 2 * NU) atomicAdd((unsigned*)&sm.tu[lane], d_ * 32u); \
                        off += d_; prov += d_; hold = HOLD; \
                        if (p != pw_ || prov > 8000) break; \
                        SLEEP(1) \
                        LOOK() \
                    } \
                } \
            if (p != pw_) { pw_ = p; tw_ = now; } \
            if ((unsigned)((T) - now) >= 0x40000u) break; \
            if ((unsigned)((T) - now) > 25 * 128) SLEEP(7) else SLEEP(1) }
        if (boss) {
            TOUCH((lane & 31) * 128, dec)
            UNTIL((ts + X0) * 128)
            for (;;) {
                bool ok = true; float sum = 0.f;
#pragma unroll
                for (int i = 0; i < 4; i++) { const u64 x = GLDS(slots + 2 * i), e = GLDS(slots + 2 * i + 1); ok &= e == E1 && (unsigned)(x >> 32) == rtag; sum += (float)min(max((int)((unsigned)x - ts), -1000), 1000); }
                if (__builtin_amdgcn_ballot_w64(ok) == ~0ull) { dv = ts + (int)(wave_sum(sum) * (1.f / 256)); break; }
                SLEEP(8)
            }
            if (lane == 0) { GSTS(dec, (u64)dv | ((u64)rtag << 32)); GSTS((u64*)(ws + WS_EPOCH), E1); }
            JOIN(dv)
            dv = 0;
        }
        int sp1 = 0, tot1 = 0, tot2 = 0, lock = 0, flo = 0;
#pragma unroll 1
        for (unsigned k = 2; k < 2 * NU; k++) {
            const unsigned tk = t2 + NOM(k) + cum * 32;
            if (k >= K0 && k + 1 < 2 * NU) {
                WATCH(tk - (XM + XW) * 128)
                int sum = 0, n = 0;
                do { LOOK() sum += LATENESS(p, now) >> 7; n++; } while ((unsigned)(tk - XM * 128 - now) < 0x40000u);
                const int l = (int)((float)sum * __builtin_amdgcn_rcpf((float)n)) - BK + prov / 4;
                if (k == K0 && l - prov / 4 > RTH) {
                    const int d_ = min(l * 4 - prov, 1600);
                    if (lane <= 2 * NU) atomicAdd((unsigned*)&sm.tu[lane], d_ * 32u);
                    off += d_; prov += d_; hold = 1;
                }
                if (l > RTH && lane == 0) GSTS((unsigned*)(ws + WS_REF + (blockIdx.x & 63) * LSTRIDE + 64) + (blockIdx.x >> 6), (unsigned)min(l, 1000) | ((rtag + k) << 10));
            }
            const int cum0 = cum;
            bool got = boss;
            if (!boss && (k > K0 || !joined)) {
                WATCH(tk - XR * 128)
                const u64 x = GLDS(dec + (joined ? k * 16 : 0));
                dv = __builtin_amdgcn_readfirstlane((unsigned)x);
                got = __builtin_amdgcn_readfirstlane((unsigned)(x >> 32)) == rtag;
                if (got && !joined) { JOIN(dv) got = false; }
            }
            sp1 = sp; sp = 0;
            if (got && k > K0) { cum = dv >> 18; sp = dv >> 6 & 4095u; dp = dv & 63u; }
            const int yld = cum - cum0 + sp;
            { const int pj = min(prov, cum - cum0); off -= pj; prov -= pj; off1 = off;
              const int ps = min(prov, sp); off -= ps; prov -= ps;
              if (hold) hold--; else prov = 0;
              const int r_ = off - prov; off -= r_ > 40 || r_ < -40 ? r_ / NB : r_; }
            NEWLINE(k)
            cum = min(cum + sp + dp, 16383);
            if (boss) dv = (cum << 18) | dp;
            if (boss && k >= K0 && k + 1 < 2 * NU && (int)(NOW() * 128 - tk) < -60 * 128) {
                WATCH(tk - (XM - XD1) * 128)
                const v4u x = __builtin_amdgcn_raw_buffer_load_b128(__builtin_amdgcn_make_buffer_rsrc(ws + WS_REF + 64, 0, 64 * LSTRIDE, 0x00020000), lane * LSTRIDE, 0, SC1);
                float a[4];
#pragma unroll
                for (int i = 0; i < 4; i++) a[i] = ((x[i] ^ ((rtag + k) << 10)) >> 10) == 0 ? (float)(x[i] & 1023u) : 0.f;
                const int tot = (int)wave_sum(a[0] + a[1] + a[2] + a[3]) >> 6;
                float mn = 0.f;
                if (tot >= 40) {
                    float st = (float)tot * 0.125f;
#pragma unroll
                    for (int i = 0; i < 4; i++, st *= 0.5f) {
                        const float x_ = mn + st;
                        const int c_ = __builtin_popcountll(__builtin_amdgcn_ballot_w64(a[0] >= x_)) + __builtin_popcountll(__builtin_amdgcn_ballot_w64(a[1] >= x_)) +
                                       __builtin_popcountll(__builtin_amdgcn_ballot_w64(a[2] >= x_)) + __builtin_popcountll(__builtin_amdgcn_ballot_w64(a[3] >= x_));
                        mn = c_ >= QN ? x_ : mn;
                    }
                }
                const float c = mn + (float)CAP, fth = (float)(FTH * (2 * NU - (int)k));
                const int cut = (int)wave_sum(fminf(a[0], c) + fminf(a[1], c) + fminf(a[2], c) + fminf(a[3], c)) >> 6, m4 = (int)mn * 4;
                const bool far = __builtin_amdgcn_ballot_w64(fmaxf(fmaxf(a[0], a[1]), fmaxf(a[2], a[3])) > fth) != 0;
                const int pend = yld + sp1 * XM / 320;
                const bool stall = m4 >= SMIN * 4 || k <= K0 + 1;
                const int nj = max((stall ? tot + (k <= K0 + 1 ? tot / 8 : SADD) : m4) - pend, 0);
                const int up = k >= 6 && tot < tot1 + 32 ? max(tot - tot1, 0) : 0;
                const int ns = stall ? 0 : min(max(max(min(m4 + (tot - m4 + up) * GAIN / 16 - pend - nj, (cut - m4 + up) * GAIN / 16), 0), far ? FLOOR * 4 : 0), 4000);
                int ndp = dp;
                if (k >= KD && !((KNOT >> k) & 1) && tot >= KI && tot < 100 && tot > tot1 && tot < tot1 + 32 && tot1 < tot2 + 32 && !lock) { ndp = min(dp + DPS, 40); flo = ndp - 1; lock = LOCK; }
                if (lock) lock--;
                if (k == NU - 1) flo = 0;
                if (tot + tot1 <= 1 && dp > flo) ndp = dp - 1;
                tot2 = tot1; tot1 = tot;
                dv = (min(cum + nj, 16383) << 18) | (ns << 6) | ndp;
                if ((int)(NOW() * 128 - tk) > XL * 128) dv = (cum << 18) | dp;
                else if (lane == 0) GSTS(dec + (k + 1) * 16, (u64)dv | ((u64)rtag << 32));
            }
        }
        WATCH(p >= NPC - 8 ? now - 128 : now + 4096)
    } else if (wave == NSW + 2) {
        const int ql = lane < 48 ? lane : 47;
        unsigned c1, c2, s1, s2;
        unsigned q1, q2, lkz, lks, lvz, lvs;
#define UNIT_LOADS(HEAD) { \
        q1 = ((const unsigned*)(q + (size_t)(HEAD) * DH))[ql]; q2 = ((const unsigned*)(q + (size_t)(HEAD) * DH))[48 + ql]; \
        const size_t so = ((size_t)(HEAD) * SKV + brow0) / 2 + (lane >> 3) * 128 + (lane & 7); \
        lkz = ((const unsigned*)k_zeros)[so]; lks = ((const unsigned*)k_scales)[so]; \
        lvz = ((const unsigned*)v_zeros)[so]; lvs = ((const unsigned*)v_scales)[so]; }
#define UNIT_FILL(SL, U) { \
            v4f qq; \
            qq.x = bfr(bfr(bflo(q1) * bflo(c1)) + bfr(-bflo(q2) * bflo(s1))); \
            qq.y = bfr(bfr(bflo(q2) * bflo(c2)) + bfr(bflo(q1) * bflo(s2))); \
            qq.z = bfr(bfr(bfhi(q1) * bfhi(c1)) + bfr(-bfhi(q2) * bfhi(s1))); \
            qq.w = bfr(bfr(bfhi(q2) * bfhi(c2)) + bfr(bfhi(q1) * bfhi(s2))); \
            *(v2u*)&sm.q[SL][2 * ql] = (v2u){(__float_as_uint(qq.x) >> 16) | __float_as_uint(qq.y), (__float_as_uint(qq.z) >> 16) | __float_as_uint(qq.w)}; \
            *(v4f*)&sm.kz[SL][2 * lane] = (v4f){bflo(lkz), bflo(lks), bfhi(lkz), bfhi(lks)}; \
            *(v4f*)&sm.vz[SL][2 * lane] = (v4f){bflo(lvz), bflo(lvs), bfhi(lvz), bfhi(lvs)}; \
            CBAR(); \
            if (lane == 0) lds_st(&sm.rdy[SL], (U) + 1); \
            CBAR(); }
        asm volatile("s_setprio 3");
        asm volatile("s_waitcnt vmcnt(0)\n\tv_accvgpr_read_b32 %0, a0\n\tv_accvgpr_read_b32 %1, a1\n\tv_accvgpr_read_b32 %2, a2\n\tv_accvgpr_read_b32 %3, a3\n\tv_accvgpr_read_b32 %4, a4\n\t"
                     "v_accvgpr_read_b32 %5, a5\n\tv_accvgpr_read_b32 %6, a8\n\tv_accvgpr_read_b32 %7, a9\n\tv_accvgpr_read_b32 %8, a10\n\tv_accvgpr_read_b32 %9, a11"
                     : "=v"(c1), "=v"(c2), "=v"(s1), "=v"(s2), "=v"(q1), "=v"(q2), "=v"(lkz), "=v"(lks), "=v"(lvz), "=v"(lvs));
#pragma unroll 1
        for (unsigned u = 0; u < NU; u++) {
            if (u) UNIT_LOADS(rank + GSZ * u)
            while (u >= lds_ld(&sm.merged) + NIN) SLEEP(4);
            CBAR();
            UNIT_FILL(u % NIN, u)
            if (u == 1) asm volatile("s_setprio 0");
        }
        while (lds_ld(&sm.merged) < NU) SLEEP(16)
        CBAR();
        const unsigned T = lds_ld(&sm.tag);
        if (lane < NU) __hip_atomic_store((u64*)(ws + WS_LOG + (rank * NSPLIT + split) * LSTRIDE2) + lane, (u64)__float_as_uint(sm.ent[lane][130]) | ((u64)T << 32), __ATOMIC_RELAXED, __HIP_MEMORY_SCOPE_AGENT);
        while (lds_ld(&sm.merged) < NU + 3) SLEEP(16)
        for (;;) {
            const u64 x = __hip_atomic_load((const u64*)(ws + WS_LOG + ((blockIdx.x & 15) * NSPLIT + (lane & 15)) * LSTRIDE2) + (blockIdx.x >> 4), __ATOMIC_RELAXED, __HIP_MEMORY_SCOPE_AGENT);
            if (__builtin_amdgcn_ballot_w64((unsigned)(x >> 32) == T) == ~0ull) {
                const float L = __uint_as_float((unsigned)x);
                const float w = __builtin_amdgcn_exp2f(L - row_max(L));
                sm.fw[lane & 15] = w / row_sum(w);
                CBAR();
                if (lane == 0) lds_st(&sm.fw_rdy, 1u);
                break;
            }
            SLEEP(64)
        }
    } else {
        while (lds_ld(&sm.tag_rdy) == 0) SLEEP(1);
        CBAR();
        const unsigned T = lds_ld(&sm.tag);
        const __amdgpu_buffer_rsrc_t rsrc = __builtin_amdgcn_make_buffer_rsrc(ws + WS_PART + (rank * NSPLIT + split) * 512, 0, NH * NSPLIT * 512, 0x00020000);
        v2u* recs = (v2u*)&sm.cs[0][0];
#pragma unroll
        for (int i = 0; i < NOUT * 3; i++) (&sm.acc[0][0])[i * 64 + lane] = 0.0;
#define RECORD() \
        const float n0 = (float)(a0 - (double)h.y) * h.x, n1 = (float)(a1 - (double)h.y) * h.x, n2 = (float)(a2 - (double)h.y) * h.x; \
        const unsigned e2 = max(__float_as_uint(fmaxf(fmaxf(fabsf(n0), fabsf(n1)), fabsf(n2))) >> 23, 12u); \
        const float f2 = __uint_as_float((266u - e2) << 23); \
        const unsigned m0 = min(max((int)rintf(n0 * f2), -8191), 8191) & 0x3fff, m1 = min(max((int)rintf(n1 * f2), -8191), 8191) & 0x3fff, m2 = min(max((int)rintf(n2 * f2), -8191), 8191) & 0x3fff; \
        const v2u r = {m0 | (m1 << 14) | (m2 << 28), (m2 >> 4) | (e2 << 10) | (T << 18)};
#pragma unroll 1
        for (unsigned u = 0; u < 2 * NU - 1; u++) {
            const unsigned e = u < NU ? u : VENT(u), so = u % NOUT;
            const v4f h = *(const v4f*)&sm.ent[e][128];
            while (lds_ld(&sm.cnt[so]) != 32) SLEEP(1)
            CBAR();
            if (u < NU) {
                const float2 lg = *(const float2*)&sm.ent[e][2 * lane];
                const v4f zs = *(const v4f*)&sm.vz[u % NIN][2 * lane];
                const float m = wave_max(fmaxf(lg.x, lg.y));
                const float p0 = __expf(lg.x - m), p1 = __expf(lg.y - m);
                const float x0 = p0 * zs.y, x1 = p1 * zs.w;
                const unsigned eb = max(__float_as_uint(wave_max(fmaxf(fabsf(x0), fabsf(x1)))) >> 23, 21u);
                const float f = __uint_as_float((275u - eb) << 23);
                const float w0 = rintf(x0 * f), w1 = rintf(x1 * f);
                const float den = wave_sum(p0 + p1);
                const float bz = wave_sum(fmaf(w0, zs.x, w1 * zs.z));
                const float L = fmaf(m, 1.4426950408889634f, __builtin_amdgcn_logf(den));
                *(v2u*)&sm.ent[e][2 * lane] = (v2u){(unsigned)(int)w0, (unsigned)(int)w1};
                if (lane == 0) *(v4f*)&sm.ent[e][128] = (v4f){ldexpf(1.f / den, (int)eb - 148), bz, L, 0.f};
            } else {
                const double a0 = sm.acc[so][lane], a1 = sm.acc[so][64 + lane], a2 = sm.acc[so][128 + lane];
                RECORD()
                recs[(u - NU) * 64 + lane] = r;
                CBAR();
                sm.acc[so][lane] = 0.0; sm.acc[so][64 + lane] = 0.0; sm.acc[so][128 + lane] = 0.0;
            }
            CBAR();
            if (lane == 0) { lds_st(&sm.cnt[so], 0u); CBAR(); lds_st(&sm.merged, u + 1); }
        }
        {
            asm volatile("s_setprio 3");
            wave_sync();
            v4u rr[8]; unsigned po[8];
#pragma unroll
            for (int k = 0; k < 8; k++) { rr[k] = ((const v4u*)recs)[k * 64 + lane]; po[k] = k == 7 && lane >= 32 ? 0x7ffffff0u : (lane & 31) * 16 + (lane < 32 ? VENT(2 * k) : VENT(2 * k + 1)) * (GSZ * NSPLIT * 512); }
            const v4f h = *(const v4f*)&sm.ent[VENT(15)][128];
            asm volatile("" :: "v"(rr[0]), "v"(rr[1]), "v"(rr[2]), "v"(rr[3]), "v"(rr[4]), "v"(rr[5]), "v"(rr[6]), "v"(rr[7]), "v"(po[0]), "v"(po[1]), "v"(po[2]), "v"(po[3]), "v"(po[4]), "v"(po[5]), "v"(po[6]), "v"(po[7]), "v"(h));
            for (;;) {
                const unsigned d = __builtin_amdgcn_readfirstlane(TU(2 * NU).x) + PUBT * 128 - NOW() * 128;
                if (d >= 0x40000u) break;
                if (d > 20 * 128) SLEEP(4)
            }
#pragma unroll
            for (int k = 0; k < 8; k++) __builtin_amdgcn_raw_buffer_store_b128(rr[k], rsrc, po[k], 0, SC1);
            double a0, a1, a2;
            for (;;) {
                const unsigned c = __hip_atomic_load(&sm.cnt[NOUT - 1], __ATOMIC_RELAXED, __HIP_MEMORY_SCOPE_WORKGROUP);
                CBAR();
                a0 = sm.acc[NOUT - 1][lane]; a1 = sm.acc[NOUT - 1][64 + lane]; a2 = sm.acc[NOUT - 1][128 + lane];
                CBAR();
                if (__builtin_amdgcn_readfirstlane(c) == 32) break;
                SLEEP(0)
            }
            RECORD()
            const v4u r2 = {r.x, r.y, (unsigned)__builtin_amdgcn_update_dpp(0, (int)r.x, 0xf5, 0xf, 0xf, true), (unsigned)__builtin_amdgcn_update_dpp(0, (int)r.y, 0xf5, 0xf, 0xf, true)};
            __builtin_amdgcn_raw_buffer_store_b128(r2, rsrc, (lane & 1) ? 0x7ffffff0u : lane * 8, VENT(15) * (GSZ * NSPLIT * 512), SC1);
        }
        return;
    }

}

extern "C" void kernel_launch(void* const* d_in, const int* in_sizes, int n_in,
                              void* d_out, int out_size, void* d_ws, size_t ws_size,
                              hipStream_t stream) {
    const unsigned short* q = (const unsigned short*)d_in[0];
    const int* qk = (const int*)d_in[1];
    const unsigned short* k_scales = (const unsigned short*)d_in[2];
    const unsigned short* k_zeros = (const unsigned short*)d_in[3];
    const int* qv = (const int*)d_in[4];
    const unsigned short* v_scales = (const unsigned short*)d_in[5];
    const unsigned short* v_zeros = (const unsigned short*)d_in[6];
    const unsigned short* q_cos = (const unsigned short*)d_in[7];
    const unsigned short* q_sin = (const unsigned short*)d_in[8];
    const unsigned short* k_cos = (const unsigned short*)d_in[9];
    const unsigned short* k_sin = (const unsigned short*)d_in[10];
    unsigned char* ws = (unsigned char*)d_ws;
    attn_kernel<<<NSPLIT * GSZ, NTHREADS, 0, stream>>>(q, qk, k_scales, k_zeros, qv, v_scales, v_zeros,
                                                       q_cos, q_sin, k_cos, k_sin, (__hip_bfloat16*)d_out, ws);
}
```

```cpp
#include <hip/hip_runtime.h>
#include <hip/hip_bf16.h>
#include <stdint.h>

#define DH 192
#define HALF 96
#define SKV 2048
#define NH 256
#define NSW 8
#define NTHREADS (64 * (NSW + 4))
#define BROWS 128
#define NSPLIT (SKV / BROWS)
#define GSZ 16
#define NU (NH / GSZ)
#define NPC (2 * NU * 32)
#define NIN 4
#define NOUT 4
#define ENTW 132
#define SC1 16
#define PHI 0x0246024671357135ull
#define VORD 0x9876543210afedcbull
#define VENT(V) ((unsigned)(VORD >> (4 * ((V) & 15))) & 15u)
#define PFL 198
#define PER 1262
#define DP0 4
#define P0 1365
#define NBW 2
#define BTH 20
#define X0 250
#define K0 3
#define XM 250
#define XW 40
#define XD1 85
#define XL 90
#define XR 170
#define BK 3
#define RTH 10
#define QN 224
#define GAIN 28
#define CAP 45
#define FLOOR 3
#define FTH 15
#define KD 6
#define KI 16
#define DPS 7
#define KNOT 0x39c7000u
#define QUIET 1
#define WARN 3
#define LOCK 6
#define NB 3
#define STH 90
#define SFIX 220
#define SNOT 0x610c0cu
#define HOLD 4
#define SMIN 100
#define SADD 8
#define PUBT 40
#define POLT 92
#define POLC 120

#define WS_EPOCH 0
#define WS_DEC 8192
#define WS_REF 16384
#define LSTRIDE 4352
#define WS_LOG 524288
#define LSTRIDE2 1408
#define WS_PART 1048576

typedef unsigned int v2u __attribute__((ext_vector_type(2)));
typedef unsigned int v4u __attribute__((ext_vector_type(4)));
typedef int v4i __attribute__((ext_vector_type(4)));
typedef float v2f __attribute__((ext_vector_type(2)));
typedef float v4f __attribute__((ext_vector_type(4)));
typedef unsigned long long u64;

__device__ __forceinline__ float bflo(unsigned u) { return __uint_as_float(u << 16); }
__device__ __forceinline__ float bfhi(unsigned u) { return __uint_as_float(u & 0xffff0000u); }
__device__ __forceinline__ float bfr(float x) {
    __bf16 b = (__bf16)x;
    return __uint_as_float(((unsigned)__builtin_bit_cast(unsigned short, b)) << 16);
}

#define SLEEP(n) SLEEP_(n)
#define SLEEP_(n) asm volatile("s_sleep " #n "\n\tv_add_co_u32 %0, vcc, 1, %0\n\ts_cbranch_vccz 1f\n\ts_endpgm\n1:" : "+v"(spins) :: "vcc", "memory");
#define SPIN0 0xf0000000u
#define CBAR() asm volatile("" ::: "memory")

__device__ __forceinline__ void wave_sync() {
    __builtin_amdgcn_fence(__ATOMIC_RELEASE, "wavefront");
    __builtin_amdgcn_wave_barrier();
    __builtin_amdgcn_fence(__ATOMIC_ACQUIRE, "wavefront");
}
template <int CTRL>
__device__ __forceinline__ float dpp_add(float x) {
    int y = __builtin_amdgcn_update_dpp(0, __float_as_int(x), CTRL, 0xf, 0xf, true);
    return x + __int_as_float(y);
}
template <int CTRL>
__device__ __forceinline__ float dpp_max(float x) {
    int y = __builtin_amdgcn_update_dpp(__float_as_int(x), __float_as_int(x), CTRL, 0xf, 0xf, false);
    return fmaxf(x, __int_as_float(y));
}
__device__ __forceinline__ float row_sum(float x) { x = dpp_add<0x128>(x); x = dpp_add<0x124>(x); x = dpp_add<0x122>(x); return dpp_add<0x121>(x); }
__device__ __forceinline__ float row_max(float m) { m = dpp_max<0x128>(m); m = dpp_max<0x124>(m); m = dpp_max<0x122>(m); return dpp_max<0x121>(m); }
__device__ __forceinline__ float rdlane(float x, int l) { return __int_as_float(__builtin_amdgcn_readlane(__float_as_int(x), l)); }
__device__ __forceinline__ float wave_max(float m) { m = row_max(m); return fmaxf(fmaxf(rdlane(m, 0), rdlane(m, 16)), fmaxf(rdlane(m, 32), rdlane(m, 48))); }
__device__ __forceinline__ float wave_sum(float x) { x = row_sum(x); return (rdlane(x, 0) + rdlane(x, 16)) + (rdlane(x, 32) + rdlane(x, 48)); }
__device__ __forceinline__ unsigned lds_ld(const unsigned* p) { return __builtin_amdgcn_readfirstlane(__hip_atomic_load(p, __ATOMIC_RELAXED, __HIP_MEMORY_SCOPE_WORKGROUP)); }
__device__ __forceinline__ unsigned lds_addr(const void* p) { return (unsigned)(size_t)(const __attribute__((address_space(3))) void*)p; }
__device__ __forceinline__ void lds_st(unsigned* p, unsigned v) { __hip_atomic_store(p, v, __ATOMIC_RELAXED, __HIP_MEMORY_SCOPE_WORKGROUP); }
#define NOW() ((unsigned)__builtin_amdgcn_s_memrealtime())

struct __attribute__((aligned(16))) Smem {
    unsigned cs[32][768];
    double acc[NOUT][DH];
    float ent[NU][ENTW];
    unsigned q[NIN][HALF];
    float2 kz[NIN][BROWS];
    float2 vz[NIN][BROWS];
    v2u tu[2 * NU + 2];
    unsigned grab, merged, tabs, cdone;
    unsigned rdy[NIN], cnt[NOUT];
    unsigned tag_rdy, tag, start, fw_rdy;
    float fw[NSPLIT];
};

#define TU(I) (*(const volatile v2u __attribute__((address_space(3)))*)(size_t)lds_addr(&sm.tu[I]))
#define LDS_ADD(P, V) { u64 ex_; asm volatile("s_mov_b64 %0, exec\n\ts_mov_b64 exec, 1\n\tds_add_u32 %1, %2\n\ts_mov_b64 exec, %0" : "=&s"(ex_) : "v"(lds_addr(P)), "v"(V) : "memory"); }
#define LDS_OR(P, V) { u64 ex_; asm volatile("s_mov_b64 %0, exec\n\ts_mov_b64 exec, 1\n\tds_or_b32 %1, %2\n\ts_mov_b64 exec, %0" : "=&s"(ex_) : "v"(lds_addr(P)), "v"(V) : "memory"); }

__global__ __launch_bounds__(NTHREADS, 3) void attn_kernel(
    const unsigned short* __restrict__ q, const int* __restrict__ qk,
    const unsigned short* __restrict__ k_scales, const unsigned short* __restrict__ k_zeros,
    const int* __restrict__ qv,
    const unsigned short* __restrict__ v_scales, const unsigned short* __restrict__ v_zeros,
    const unsigned short* __restrict__ q_cos, const unsigned short* __restrict__ q_sin,
    const unsigned short* __restrict__ k_cos, const unsigned short* __restrict__ k_sin,
    __hip_bfloat16* __restrict__ out, unsigned char* __restrict__ ws)
{
    __shared__ Smem sm;
    const int tid = threadIdx.x;
    unsigned spins = SPIN0;
#define TOUCH(OFF, BASE) asm volatile("global_load_dword a6, %0, %1" :: "v"(OFF), "s"(BASE) : "a6", "memory");
    const unsigned cl = min(((blockIdx.x >> 3) & 15) * 16 + (tid & 15), PFL - 1u) * 128;
    if (tid < 64) { const char* pc0; asm volatile("s_getpc_b64 %0" : "=s"(pc0)); TOUCH(cl, pc0) }
    __builtin_amdgcn_sched_barrier(0);
    const int hw = __builtin_amdgcn_readfirstlane(tid >> 6);
    const int lane = tid & 63;
    const int split = (blockIdx.x & 3) * 4 + ((blockIdx.x >> 3) & 3);
    const int rank = (blockIdx.x >> 5) * 2 + ((blockIdx.x >> 2) & 1);
    const int brow0 = split * 16;
#define ROWOF(RG) (((RG) >> 2) * 256 + ((RG) & 3) * 4)
    if (hw == 0) {
        typedef const char* const __attribute__((address_space(4))) * kap_t;
        const char* sa = ((kap_t)__builtin_amdgcn_kernarg_segment_ptr())[(0x5623 >> (4 * (split & 3))) & 7];
        unsigned o1 = (((rank & 1 ? 0 : 2) + (blockIdx.x >> 7) * 4) * 256 + brow0) * (DH * 2) + (((blockIdx.x >> 5) & 1) * 24 + min(lane, 23)) * 128;
        unsigned o2 = ((rank + GSZ * (lane >> 3)) * SKV + (lane & 7) * 256 + (brow0 & ~63)) * 2, o3 = o2 + 8 * GSZ * SKV * 2;
        unsigned o4 = (rank + GSZ * ((lane >> 2 & 3) + 4 * (split & 3))) * (DH * 2) + min(lane & 3, 2) * 128;
        asm volatile("" : "+v"(o1), "+v"(o2), "+v"(o3), "+v"(o4));
        asm volatile("global_load_dword a6, %0, %1 nt" :: "v"(((rank + (lane & 1) * GSZ) * SKV + (lane & 2) * 896 + brow0) * (DH * 4)), "s"(qk) : "a6", "memory");
        TOUCH(o1, (blockIdx.x & 64) ? k_sin : k_cos) TOUCH(o4, q) TOUCH(o2, sa) TOUCH(o3, sa)
        asm volatile("s_barrier\n\ts_waitcnt vmcnt(2)" ::: "memory");
        if (lane == 0) lds_st(&sm.start, NOW() | 1u);
        asm volatile("s_endpgm");
    }
    const unsigned rot = 4 * ((unsigned)(PHI >> (60 - 4 * rank)) & 7u);
#define RGOF(G) ((((G) + rot) & 31u) ^ ((((G) + rot) >> 1) & 2u))
#define CS_DMA(RG) asm volatile("s_mov_b32 m0, %1\n\ts_nop 0\n\tglobal_load_lds_dwordx4 %0, %2\n\ts_mov_b32 exec_hi, 0\n\tglobal_load_lds_dwordx4 %0, %2 offset:1024\n\ts_add_u32 m0, m0, 0x600\n\ts_nop 0\n\t" \
                                "global_load_lds_dwordx4 %0, %3 offset:1024\n\ts_mov_b32 exec_hi, -1\n\tglobal_load_lds_dwordx4 %0, %3" \
                                :: "v"((brow0 + ROWOF(RG)) * (DH * 2) + lane * 16), "s"(lds_addr(&sm.cs[0][0]) + (RG) * 3072), "s"(k_cos), "s"(k_sin) : "scc", "memory");
    if (hw >= 4) CS_DMA(RGOF((unsigned)hw - 4))
    if (hw == 1) {
        const unsigned qo = min(lane, 47) * 4, so = (rank * SKV + brow0) * 2 + (lane >> 3) * 512 + (lane & 7) * 4;
        asm volatile("global_load_dword a0, %0, %3\n\tglobal_load_dword a1, %0, %3 offset:192\n\tglobal_load_dword a2, %0, %4\n\tglobal_load_dword a3, %0, %4 offset:192\n\t"
                     "global_load_dword a4, %1, %5\n\tglobal_load_dword a5, %1, %5 offset:192\n\t"
                     "global_load_dword a8, %2, %6\n\tglobal_load_dword a9, %2, %7\n\tglobal_load_dword a10, %2, %8\n\tglobal_load_dword a11, %2, %9"
                     :: "v"(qo), "v"(qo + rank * (DH * 2)), "v"(so), "s"(q_cos), "s"(q_sin), "s"(q), "s"(k_zeros), "s"(k_scales), "s"(v_zeros), "s"(v_scales)
                     : "a0", "a1", "a2", "a3", "a4", "a5", "a8", "a9", "a10", "a11", "memory");
    }
    if (hw == 3 && lane < 16) (&sm.grab)[lane] = lane ? 0 : 64;
    asm volatile("" :: "s"(q), "s"(qk), "s"(k_scales), "s"(k_zeros), "s"(qv), "s"(v_scales), "s"(v_zeros), "s"(q_cos), "s"(q_sin), "s"(k_cos), "s"(k_sin), "s"(out), "s"(ws));
    asm volatile("s_waitcnt lgkmcnt(0)\n\ts_barrier" ::: "memory");
    const int wave = (int)(0x76543210b9a8ull >> (4 * hw)) & 15;
    unsigned ts;
#define START() { while ((ts = lds_ld(&sm.start)) == 0) SLEEP(1) }

    if (__builtin_expect(wave < NSW, 1)) {
#define REPACK(RG) { \
        unsigned* cw = &sm.cs[RG][0]; \
        const unsigned* l = cw + rsel * (DH / 2) + qoff; \
        unsigned r[2][6]; \
        _Pragma("unroll") \
        for (int j = 0; j < 2; j++) { \
            unsigned e[4][2]; \
            _Pragma("unroll") \
            for (int k = 0; k < 4; k++) { \
                const unsigned* a = l + j * DH + (k & 1) * 48 + (k >> 1) * 384;       \
                unsigned x0 = a[0], x1 = a[1]; \
                e[k][0] = __builtin_amdgcn_alignbit(x1, x0, sh); \
                e[k][1] = x1 >> sh; \
            } \
            r[j][0] = (e[0][0] & 0xffffu) | (e[3][0] << 16); \
            r[j][1] = (e[0][0] >> 16) | (e[3][0] & 0xffff0000u); \
            r[j][2] = (e[0][1] & 0xffffu) | (e[3][1] << 16); \
            r[j][3] = (e[1][0] & 0xffffu) | (e[2][0] << 16); \
            r[j][4] = (e[1][0] >> 16) | (e[2][0] & 0xffff0000u); \
            r[j][5] = (e[1][1] & 0xffffu) | (e[2][1] << 16); \
        } \
        wave_sync(); \
        _Pragma("unroll") \
        for (int j = 0; j < 2; j++) { *(v4u*)&cw[j * 256 + lane * 4] = (v4u){r[j][0], r[j][1], r[j][2], r[j][3]}; *(v2u*)&cw[512 + j * 128 + lane * 2] = (v2u){r[j][4], r[j][5]}; } \
        wave_sync(); \
        LDS_OR(&sm.tabs, 1u << (RG)) }
        const int sub = lane & 31;
        const int rsel = lane >> 5;
        const unsigned sh = (sub & 1) * 16;
        const int qoff = 3 * (sub >> 1) + (sub & 1);
        const char* kbase = (const char*)qk + (size_t)(rank * SKV + brow0) * (DH * 4);
        const char* vbase = (const char*)qv + (size_t)(rank * SKV + brow0) * (DH * 4);
        const unsigned koff = (rsel * DH + 3 * sub) * 4, voff = 3 * lane * 4;
#define AR(S) (16 + (S) * 8)
        asm volatile("" ::: "a16", "a17", "a18", "a19", "a20", "a21", "a22", "a23", "a24", "a25", "a26", "a27", "a28", "a29", "a30", "a31",
                            "a32", "a33", "a34", "a35", "a36", "a37", "a38", "a39", "a40", "a41", "a42", "a43", "a44", "a45", "a46", "a47");
        asm volatile("" ::: "a48", "a49", "a50", "a51", "a52", "a53", "a54", "a55", "a56", "a57", "a58", "a59", "a60", "a61", "a62", "a63");
        int ta[3], tb[3];
#define GLD(A, OFF, BASE, IMM) asm volatile("global_load_dwordx3 a[%c0:%c1], %2, %3 offset:%4 nt" :: "i"(A), "i"((A) + 2), "v"(OFF), "s"(BASE), "i"(IMM))
#define ISSUE(S, J, BASE, OA, OB) { GLD(AR(S), OA, BASE, (J) * 1536); GLD(AR(S) + 4, OB, BASE, (J) * 1536); }
#define WAITT(S, N) asm volatile("s_waitcnt vmcnt(%6)\n\tv_accvgpr_read_b32 %0, a[%c7]\n\tv_accvgpr_read_b32 %1, a[%c8]\n\tv_accvgpr_read_b32 %2, a[%c9]\n\t" \
                              "v_accvgpr_read_b32 %3, a[%c10]\n\tv_accvgpr_read_b32 %4, a[%c11]\n\tv_accvgpr_read_b32 %5, a[%c12]" \
                              : "=v"(ta[0]), "=v"(ta[1]), "=v"(ta[2]), "=v"(tb[0]), "=v"(tb[1]), "=v"(tb[2]) \
                              : "i"(N), "i"(AR(S)), "i"(AR(S) + 1), "i"(AR(S) + 2), "i"(AR(S) + 4), "i"(AR(S) + 5), "i"(AR(S) + 6))
        const unsigned koffb = koff + 384, voffb = voff + 768;
        unsigned u = 0, rg, np = 0;
#define RG0(C) RGOF((unsigned)(wave + 8 * (C)))
        const float scale = 0.07216878364870322f;
        double acc[3] = {0.0, 0.0, 0.0};
        bool chg, none = false;
        unsigned Qp[3], Qn[3];
        float kz4[2], ksc, kres[2];
        v4u ca[2]; v2u cb[2];
        v2f qe[2][3], A[2];
        const char* nb; unsigned oa, ob;
        unsigned g, gu, mg, nrg, tg, dg, par = 0;
        int early;
        const unsigned bth = wave < NBW ? BTH * 128 : 0x3fffffff;
        const unsigned grab_adr = lds_addr(&sm.grab);
        asm volatile("v_accvgpr_write_b32 a13, 1" ::: "a13", "a15");
#define COUNT(ADR) { u64 ex_; asm volatile("s_mov_b64 %0, exec\n\ts_mov_b64 exec, 1\n\tds_add_rtn_u32 a15, %1, a13\n\ts_mov_b64 exec, %0" : "=&s"(ex_) : "v"(ADR) : "memory"); }
#define GRABBED() { unsigned t_; asm volatile("s_waitcnt lgkmcnt(0)\n\tv_accvgpr_read_b32 %1, a15\n\ts_nop 0\n\tv_readfirstlane_b32 %0, %1\n\ts_nop 1" : "=s"(g), "=v"(t_)); }
#define NEXT1() const v2u x_ = TU(g >> 5); u64 tn_; asm volatile("s_memrealtime %0" : "=s"(tn_));
#define NEXT2() { \
            chg = (g >> 5) != u; none = g >= NPC; \
            const bool nv = g >= NPC / 2; \
            nrg = RGOF(g); \
            unsigned boff; asm("s_mul_i32 %0, %1, 0x300" : "=s"(boff) : "s"((nv ? VENT(g >> 5) : g >> 5) * (GSZ * SKV) + ROWOF(nrg) + 2)); \
            nb = (nv ? vbase : kbase) + boff; \
            oa = nv ? voff : koff; ob = nv ? voffb : koffb; \
            asm volatile("s_waitcnt lgkmcnt(0)" : "+s"(tn_) : "s"(nb), "v"(oa), "v"(ob)); \
            tg = __builtin_amdgcn_readfirstlane(x_.x) + (g & 31) * __builtin_amdgcn_readfirstlane(x_.y); \
            early = none ? 0 : (int)((unsigned)tn_ * 128 - tg - bth); \
            if (early > 0) asm volatile("s_cmp_lg_u32 %3, 0\n\ts_cbranch_scc1 1f\n\t" L4(32) "\ts_branch 2f\n1:\n\t" L4(16) "2:\n\ts_mov_b64 vcc, exec\n\ts_mov_b64 exec, 1\n\tds_add_rtn_u32 a15, %4, a13\n\ts_mov_b64 exec, vcc" \
                                    :: "v"(oa), "v"(ob), "s"(nb), "s"(pr), "v"(grab_adr) : "scc", "vcc", "memory"); }
#define L4(A) "global_load_dwordx3 a[" #A ":" #A "+2], %0, %2 offset:-1536 nt\n\tglobal_load_dwordx3 a[" #A "+4:" #A "+6], %1, %2 offset:-1536 nt\n\t" \
              "global_load_dwordx3 a[" #A "+8:" #A "+10], %0, %2 nt\n\tglobal_load_dwordx3 a[" #A "+12:" #A "+14], %1, %2 nt\n"
#define GATE(...) \
            asm volatile("s_setprio 0\n1:\n\ts_memrealtime vcc\n\ts_waitcnt lgkmcnt(0)\n\ts_lshl_b32 vcc_lo, vcc_lo, 7\n\ts_sub_u32 %0, %1, vcc_lo\n\ts_cmp_lt_u32 %0, 0x40000\n\ts_cbranch_scc0 2f\n\t" \
                         "s_cmp_lt_u32 %0, %2\n\ts_cbranch_scc1 1b\n\ts_sleep %3\n\ts_branch 1b\n2:\n\ts_setprio 2" : "=&s"(dg) : "s"(tg), "i"(10 * 128), "i"(2), __VA_ARGS__ : "scc", "vcc", "memory");
#define RD6(A) "v_accvgpr_read_b32 %0, a[%c6+" #A "]\n\tv_accvgpr_read_b32 %1, a[%c6+" #A "+1]\n\tv_accvgpr_read_b32 %2, a[%c6+" #A "+2]\n\t" \
               "v_accvgpr_read_b32 %3, a[%c6+" #A "+4]\n\tv_accvgpr_read_b32 %4, a[%c6+" #A "+5]\n\tv_accvgpr_read_b32 %5, a[%c6+" #A "+6]\n"
#define LD2(A) "global_load_dwordx3 a[%c6+" #A ":%c6+" #A "+2], %7, %9 offset:%10 nt\n\tglobal_load_dwordx3 a[%c6+" #A "+4:%c6+" #A "+6], %8, %9 offset:%10 nt"
#define OUT6 "=&v"(ta[0]), "=&v"(ta[1]), "=&v"(ta[2]), "=&v"(tb[0]), "=&v"(tb[1]), "=&v"(tb[2])
#define WI(A, B, TAIL) "s_cmp_gt_i32 %13, 0\n\ts_cbranch_scc1 3f\n\ts_waitcnt vmcnt(3)\n\t" LD2(A) TAIL "\n\ts_waitcnt vmcnt(4)\n3:\n\ts_waitcnt vmcnt(%c14)\n\t" RD6(B)
#define WAITISSUE(S, TAIL, ...) asm volatile("s_cmp_lg_u32 %12, 0\n\ts_cbranch_scc1 1f\n\t" WI(16, 0, TAIL) "\ts_branch 2f\n1:\n\t" WI(0, 16, TAIL) "2:" \
                              : OUT6 : "i"(AR(S)), "v"(oa), "v"(ob), "s"(nb), "i"(((S) - 1) * 1536), "v"(grab_adr), "s"(pr), "s"(early), "i"(6 - 2 * (S)), __VA_ARGS__ : "scc", "vcc", "memory")
#define LASTT(S) asm volatile("s_waitcnt vmcnt(%c8)\n\ts_cmp_lg_u32 %7, 0\n\ts_cbranch_scc1 1f\n\t" RD6(0) "\ts_branch 2f\n1:\n\t" RD6(16) "2:" : OUT6 : "i"(AR(S)), "s"(pr), "i"(2 - 2 * (S)) : "scc")
#define DRAW "\n\ts_mov_b64 vcc, exec\n\ts_mov_b64 exec, 1\n\tds_add_rtn_u32 a15, %11, a13\n\ts_mov_b64 exec, vcc"
#define K_TAB() { _Pragma("unroll") for (int p = 0; p < 2; p++) { \
            const unsigned* cw = &sm.cs[rg][0]; \
            ca[p] = *(const v4u*)&cw[p * 256 + lane * 4]; cb[p] = *(const v2u*)&cw[512 + p * 128 + lane * 2]; } }
#define K_PRE(P) { \
        float e0, e1, e2, f0, f1, f2; \
        asm("v_dot2_f32_bf16 %0, %6, %12, 0\n\tv_dot2_f32_bf16 %3, %9, %15, 0\n\tv_dot2_f32_bf16 %1, %7, %13, 0\n\tv_dot2_f32_bf16 %4, %10, %16, 0\n\t" \
            "v_dot2_f32_bf16 %2, %8, %14, 0\n\tv_dot2_f32_bf16 %5, %11, %17, 0\n\ts_nop 2" \
            : "=&v"(e0), "=&v"(e1), "=&v"(e2), "=&v"(f0), "=&v"(f1), "=&v"(f2) \
            : "v"(Qp[0]), "v"(Qp[1]), "v"(Qp[2]), "v"(Qn[0]), "v"(Qn[1]), "v"(Qn[2]), \
              "v"(ca[P].x), "v"(ca[P].y), "v"(ca[P].z), "v"(ca[P].w), "v"(cb[P].x), "v"(cb[P].y)); \
        qe[P][0] = (v2f){e0, f0}; qe[P][1] = (v2f){e1, f1}; qe[P][2] = (v2f){e2, f2}; \
        A[P] = (qe[P][0] + qe[P][1] + qe[P][2]) * (v2f){-kz4[P], -kz4[P]}; }
#define K_SUM(P) { \
        _Pragma("unroll") \
        for (int i = 0; i < 3; i++) { v2f x = {(float)ta[i], (float)tb[i]}; A[P] = qe[P][i] * x + A[P]; } \
        kres[P] = A[P].x + A[P].y; }
#define K_RED2() { \
        const bool o1 = lane & 1; \
        float sx = (o1 ? kres[1] : kres[0]) + __int_as_float(__builtin_amdgcn_update_dpp(0, __float_as_int(o1 ? kres[0] : kres[1]), 0xb1, 0xf, 0xf, true)); \
        sx = dpp_add<0x4e>(sx); sx = dpp_add<0x124>(sx); sx = dpp_add<0x128>(sx); \
        const auto sw = __builtin_amdgcn_permlane16_swap(__float_as_uint(sx), __float_as_uint(sx), false, false); \
        sx = __uint_as_float(sw[0]) + __uint_as_float(sw[1]); \
        if ((lane & 30) == 0) sm.ent[u][rg * 4 + 2 * (lane & 1) + rsel] = sx * ksc; }
#define LDV(P) __hip_atomic_load(P, __ATOMIC_RELAXED, __HIP_MEMORY_SCOPE_WORKGROUP)
#define RDYWAIT(SL, SEQ) { while (lds_ld(&sm.rdy[SL]) != (SEQ)) SLEEP(1); CBAR(); }
#define LOADZ() { _Pragma("unroll") for (int p = 0; p < 2; p++) kz4[p] = sm.kz[u % NIN][rg * 4 + 2 * p + rsel].x; ksc = sm.kz[u % NIN][rg * 4 + 2 * (lane & 1) + rsel].y * scale; }
#define LOADQ() { _Pragma("unroll") for (int i = 0; i < 3; i++) { Qp[i] = sm.q[u % NIN][3 * sub + i]; Qn[i] = __builtin_amdgcn_alignbit(Qp[i], Qp[i], 16) ^ 0x80000000u; } }
#define FLUSH(ISV) { \
            mg = __builtin_amdgcn_readfirstlane(mg); \
            while (u >= mg + NOUT) { SLEEP(1); mg = lds_ld(&sm.merged); } \
            CBAR(); \
            if (ISV) { const unsigned aa = lds_addr(&sm.acc[u % NOUT][lane]); \
                asm volatile("ds_add_f64 %0, %1\n\tds_add_f64 %0, %2 offset:512\n\tds_add_f64 %0, %3 offset:1024" :: "v"(aa), "v"(acc[0]), "v"(acc[1]), "v"(acc[2]) : "memory"); \
                acc[0] = 0.0; acc[1] = 0.0; acc[2] = 0.0; } \
            CBAR(); \
            LDS_ADD(&sm.cnt[u % NOUT], np) \
            CBAR(); \
            np = 0; }
#define ADVANCE() { u = gu; rg = nrg; }

#define REQ0(N, S) { tg = ts * 128 + (wave + 8 * (N)) * P0; \
            const char* kb = kbase + (((N) >> 2) * (GSZ * SKV) + ROWOF(RG0((N) & 3))) * (DH * 4); GATE("s"(kb)) ISSUE(S, 0, kb, koff, koffb) ISSUE((S) + 1, 1, kb, koff, koffb) }
#define USE0(N, S, W) { u = (N) >> 2; rg = RG0((N) & 3); K_TAB() LOADQ() LOADZ() K_PRE(0) K_PRE(1) WAITT(S, W); K_SUM(0) WAITT((S) + 1, W); K_SUM(1) K_RED2() }
#define VMW(N) asm volatile("s_waitcnt vmcnt(" #N ")" ::: "memory");
        START()
        REQ0(0, 0) CS_DMA(RG0(1))
        VMW(8) REPACK(RG0(0))
        REQ0(1, 2)
        RDYWAIT(0, 1u)
        USE0(0, 0, 8)
        VMW(4) REPACK(RG0(1))
        REQ0(2, 0) CS_DMA(RG0(2))
        USE0(1, 2, 8)
        REQ0(3, 2)
        VMW(4) REPACK(RG0(2))
        USE0(2, 0, 4)
        REQ0(4, 0) CS_DMA(RG0(3))
        v2f qe2[2][2][3], A2[2][2]; float ks2[2];
        RDYWAIT(1, 2u)
        u = 1;
        LOADQ()
#pragma unroll
        for (int j = 0; j < 2; j++) {
            rg = RG0(j); K_TAB() LOADZ() K_PRE(0) K_PRE(1)
            ks2[j] = ksc;
#pragma unroll
            for (int p = 0; p < 2; p++) { A2[j][p] = A[p]; qe2[j][p][0] = qe[p][0]; qe2[j][p][1] = qe[p][1]; qe2[j][p][2] = qe[p][2]; }
        }
        asm volatile("" :: "v"(A2[0][0]), "v"(A2[0][1]), "v"(A2[1][0]), "v"(A2[1][1]), "v"(ks2[0]), "v"(ks2[1]));
        REQ0(5, 4)
        VMW(4) REPACK(RG0(3))
        USE0(3, 2, 4)
        CBAR();
        LDS_ADD(&sm.cnt[0], 4u)
        REQ0(6, 2)
        u = 1;
#define FIN0(J, S, W) { rg = RG0(J); ksc = ks2[J]; \
            _Pragma("unroll") for (int p = 0; p < 2; p++) { A[p] = A2[J][p]; qe[p][0] = qe2[J][p][0]; qe[p][1] = qe2[J][p][1]; qe[p][2] = qe2[J][p][2]; } \
            WAITT(S, W); K_SUM(0) WAITT((S) + 1, W); K_SUM(1) K_RED2() }
        FIN0(0, 0, 8)
        FIN0(1, 4, 4)
        REQ0(7, 0)
        COUNT(grab_adr)
        USE0(6, 2, 4)
        rg = RG0(3); np = 3;
        GRABBED()
#pragma unroll 1
        while (u < NU) {
            const unsigned pr = __builtin_amdgcn_readfirstlane(par); par ^= 1;
            NEXT1()
            const unsigned tb_ = LDV(&sm.tabs), fl = LDV(&sm.rdy[u % NIN]);
            mg = LDV(&sm.merged);
            CBAR();
            K_TAB() LOADQ() LOADZ()
            NEXT2()
            if (__builtin_expect(__builtin_amdgcn_readfirstlane(fl) != u + 1 || !((__builtin_amdgcn_readfirstlane(tb_) >> rg) & 1u), 0)) {
                while (!((lds_ld(&sm.tabs) >> rg) & 1u)) SLEEP(1)
                RDYWAIT(u % NIN, u + 1)
                K_TAB() LOADQ() LOADZ()
            }
            K_PRE(0) K_PRE(1)
            if (early <= 0) GATE("v"(A[0]), "v"(A[1]), "v"(oa), "v"(ob), "s"(nb), "s"(pr))
            WAITISSUE(0, DRAW, "v"(A[0]));
            K_SUM(0)
            WAITISSUE(1, "", "v"(A[1]));
            K_SUM(1)
            gu = g >> 5;
            GRABBED()
            K_RED2()
            np++;
            if (chg) FLUSH(false)
            ADVANCE()
        }
#pragma unroll 1
        while (u < 2 * NU) {
            const unsigned pr = __builtin_amdgcn_readfirstlane(par); par ^= 1;
            NEXT1()
            mg = LDV(&sm.merged);
            CBAR();
            const unsigned ve = VENT(u);
            v4i pw = *(const v4i*)&sm.ent[ve][rg * 4];
            NEXT2()
            if (__builtin_expect(__builtin_amdgcn_readfirstlane(mg) <= ve, 0)) { while (lds_ld(&sm.merged) <= ve) SLEEP(1); CBAR(); pw = *(const v4i*)&sm.ent[ve][rg * 4]; }
            const double w0 = pw.x, w1 = pw.y, w2 = pw.z, w3 = pw.w;
            if (none) { LASTT(0); } else { if (early <= 0) GATE("s"(pr), "v"(w0), "v"(w1), "v"(w2), "v"(w3), "v"(oa), "v"(ob), "s"(nb)) WAITISSUE(0, DRAW, "v"(w0)); }
#pragma unroll
            for (int i = 0; i < 3; i++) acc[i] = fma(w1, (double)tb[i], fma(w0, (double)ta[i], acc[i]));
            if (none) { LASTT(1); } else WAITISSUE(1, "", "v"(w2));
#pragma unroll
            for (int i = 0; i < 3; i++) acc[i] = fma(w3, (double)tb[i], fma(w2, (double)ta[i], acc[i]));
            gu = g >> 5;
            np++;
            if (chg) FLUSH(true)
            GRABBED()
            ADVANCE()
        }

        {
            const int head = blockIdx.x;
            while (lds_ld(&sm.fw_rdy) == 0) SLEEP(2)
            CBAR();
            const unsigned tt = lds_ld(&sm.tag) << 18;
            const float wh = sm.fw[wave + 8 * rsel];
            const unsigned ro = (wave + 8 * rsel) * 512 + sub * 16;
            const char* rb = (const char*)ws + WS_PART + head * (NSPLIT * 512);
            v4f* pb = (v4f*)((char*)&sm.cs[0][0] + 16384);
            __hip_bfloat16* o = out + (size_t)head * DH + 6 * sub + 3 * rsel;
            tg = __builtin_amdgcn_readfirstlane(TU(2 * NU).x) + ((head >> 4) == VENT(15) ? POLC : POLT) * 128;
            GATE("s"(tt), "v"(wh), "v"(ro), "s"(rb), "v"(o))
            v4u R;
#define PLD(A) "global_load_dwordx4 a[" #A ":" #A "+3], %6, %7 sc1\n\t"
#define PCK(A, N, L) "s_waitcnt vmcnt(" #N ")\n\tv_accvgpr_read_b32 %1, a[" #A "+1]\n\tv_accvgpr_read_b32 %3, a[" #A "+3]\n\tv_accvgpr_read_b32 %0, a[" #A "]\n\tv_accvgpr_read_b32 %2, a[" #A "+2]\n\t" \
                     "v_xor_b32 %1, %8, %1\n\tv_xor_b32 %3, %8, %3\n\tv_or_b32 %4, %1, %3\n\tv_cmp_gt_u32 vcc, %9, %4\n\ts_cmp_eq_u64 vcc, exec\n\ts_cbranch_scc1 " L "\n\t"
            { unsigned t_;
              asm volatile("s_cmp_eq_u32 %10, %12\n\ts_cbranch_scc1 1f\n\t" PLD(16) PCK(16, 0, "3f") "1:\n\t" PLD(16) "s_sleep %11\n\t" PLD(20) "s_sleep %11\n\t" PLD(24)
                           "2:\n\t" PCK(16, 2, "3f") PLD(16) PCK(20, 2, "3f") PLD(20) PCK(24, 2, "3f") PLD(24)
                           "v_add_co_u32 %5, vcc, 64, %5\n\ts_cbranch_vccz 2b\n\ts_endpgm\n3:"
                           : "=&v"(R.x), "=&v"(R.y), "=&v"(R.z), "=&v"(R.w), "=&v"(t_), "+v"(spins) : "v"(ro), "s"(rb), "s"(tt), "s"(0x40000u), "s"(head >> 4), "i"(5), "i"(VENT(15)) : "scc", "vcc", "memory"); }
            const float s0 = ldexpf(wh, (int)(R.y >> 10) - 139), s1 = ldexpf(wh, (int)(R.w >> 10) - 139);
            const float f[6] = {s0 * (float)(int)__builtin_amdgcn_sbfe(R.x, 0, 14), s0 * (float)(int)__builtin_amdgcn_sbfe(R.x, 14, 14), s0 * (float)(int)__builtin_amdgcn_sbfe(__builtin_amdgcn_alignbit(R.y, R.x, 28), 0, 14),
                                s1 * (float)(int)__builtin_amdgcn_sbfe(R.z, 0, 14), s1 * (float)(int)__builtin_amdgcn_sbfe(R.z, 14, 14), s1 * (float)(int)__builtin_amdgcn_sbfe(__builtin_amdgcn_alignbit(R.w, R.z, 28), 0, 14)};
            float sv[3];
#pragma unroll
            for (int i = 0; i < 3; i++) {
                const auto sw = __builtin_amdgcn_permlane32_swap(__float_as_uint(f[i]), __float_as_uint(f[i + 3]), false, false);
                sv[i] = __uint_as_float(sw[0]) + __uint_as_float(sw[1]);
            }
            pb[wave * 64 + lane] = (v4f){sv[0], sv[1], sv[2], 0.f};
            CBAR();
            COUNT(grab_adr + 12)
            v4f ps[NSW];
#pragma unroll
            for (int w = 0; w < NSW; w++) ps[w] = pb[w * 64 + lane];
            GRABBED()
            if (g == NSW - 1) {
                const v4f sum = ((ps[0] + ps[1]) + (ps[2] + ps[3])) + ((ps[4] + ps[5]) + (ps[6] + ps[7]));
                o[0] = __float2bfloat16(sum.x); o[1] = __float2bfloat16(sum.y); o[2] = __float2bfloat16(sum.z);
            }
        }
        return;
    }

    if (wave == NSW + 1) {
        u64 E; asm volatile("s_setprio 3\n\ts_load_dwordx2 %0, %1, 0\n\ts_waitcnt lgkmcnt(0)" : "=s"(E) : "s"(ws + WS_EPOCH) : "memory");
        START()
        unsigned t2 = ts * 128 + 64 * P0;
        int off = 0, off1 = 0, hold = 0, cum = 0, dp = DP0, sp = 0;
#define NOM(U) (((int)(U) - 2) * 32 * PER)
#define NEWLINE(K) { if (lane >= (K) && lane <= 2 * NU) sm.tu[lane] = (v2u){t2 + NOM(lane) + (cum + (lane - (int)(K)) * dp + (lane > (K) ? sp + off : off1)) * 32, \
                                                                            (unsigned)(PER + dp + (lane == (K) ? sp + off - off1 : 0))}; }
        NEWLINE(2)
        if (lane == 0) sm.tu[1] = (v2u){t2 - 32 * P0, (unsigned)P0};
        const u64 E1 = E + 1;
        const unsigned rtag = (unsigned)E1 * 0x9e3779b1u;
        const bool boss = blockIdx.x == 0;
        u64* const slot = (u64*)(ws + WS_REF + (blockIdx.x & 63) * LSTRIDE + (blockIdx.x >> 6) * 16);
        const u64* const slots = (const u64*)(ws + WS_REF + lane * LSTRIDE);
        u64* const dec = (u64*)(ws + WS_DEC);
#define GLDS(P) __hip_atomic_load(P, __ATOMIC_RELAXED, __HIP_MEMORY_SCOPE_AGENT)
#define GSTS(P, V) __hip_atomic_store(P, V, __ATOMIC_RELAXED, __HIP_MEMORY_SCOPE_AGENT)
#define UNTIL(T) while ((unsigned)((T) - NOW() * 128) < 0x40000u) SLEEP(2)
        if (lane < 2) GSTS(slot + lane, lane ? E1 : (u64)ts | ((u64)rtag << 32));
        if (lane == 0) { sm.tag = (unsigned)E1; CBAR(); lds_st(&sm.tag_rdy, 1u); }
        unsigned dv = 0;
        bool joined = false;
#define JOIN(T0) { const int d0 = min(max((int)((T0) - ts), -1000), 1000); off -= d0 * 4; t2 += d0 * 128; joined = true; }
        unsigned pw_ = 0, tw_ = 0, now, p; int prov = 0;
#define LOOK() { u64 t_; asm volatile("ds_read_b32 %1, %2\n\ts_memrealtime %0\n\ts_waitcnt lgkmcnt(0)" : "=s"(t_), "=v"(p) : "v"(lds_addr(&sm.grab)) : "memory"); \
                 p = __builtin_amdgcn_readfirstlane(p) - 8; now = (unsigned)t_ * 128; }
#define LATENESS(P, T) ({ const v2u e = TU((P) >> 5); (int)((T) - __builtin_amdgcn_readfirstlane(e.x) - ((P) & 31) * __builtin_amdgcn_readfirstlane(e.y)); })
#define WATCH(T) for (;;) { \
            LOOK() \
            if (__builtin_expect(now - tw_ > STH * 128u && pw_ >= 64 && p < NPC - 8, 0)) { \
                const int lw = max(LATENESS(pw_, tw_), 0); \
                if (LATENESS(p, now) - lw > STH * 128) \
                    for (int d0_ = hold || ((SNOT >> (p >> 5)) & 1) ? 0 : SFIX * 4;; d0_ = 0) { \
                        const int d_ = min(max((LATENESS(p, now) - lw) >> 5, d0_), 1600); \
                        if (lane <= 2 * NU) atomicAdd((unsigned*)&sm.tu[lane], d_ * 32u); \
                        off += d_; prov += d_; hold = HOLD; \
                        if (p != pw_ || prov > 8000) break; \
                        SLEEP(1) \
                        LOOK() \
                    } \
                } \
            if (p != pw_) { pw_ = p; tw_ = now; } \
            if ((unsigned)((T) - now) >= 0x40000u) break; \
            if ((unsigned)((T) - now) > 25 * 128) SLEEP(7) else SLEEP(1) }
        if (boss) {
            TOUCH((lane & 31) * 128, dec)
            UNTIL((ts + X0) * 128)
            for (;;) {
                bool ok = true; float sum = 0.f;
#pragma unroll
                for (int i = 0; i < 4; i++) { const u64 x = GLDS(slots + 2 * i), e = GLDS(slots + 2 * i + 1); ok &= e == E1 && (unsigned)(x >> 32) == rtag; sum += (float)min(max((int)((unsigned)x - ts), -1000), 1000); }
                if (__builtin_amdgcn_ballot_w64(ok) == ~0ull) { dv = ts + (int)(wave_sum(sum) * (1.f / 256)); break; }
                SLEEP(8)
            }
            if (lane == 0) { GSTS(dec, (u64)dv | ((u64)rtag << 32)); GSTS((u64*)(ws + WS_EPOCH), E1); }
            JOIN(dv)
            dv = 0;
        }
        int sp1 = 0, tot1 = 0, tot2 = 0, lock = 0, lk2 = 0, flo = 0;
#pragma unroll 1
        for (unsigned k = 2; k < 2 * NU; k++) {
            const unsigned tk = t2 + NOM(k) + cum * 32;
            if (k >= K0 && k + 1 < 2 * NU) {
                WATCH(tk - (XM + XW) * 128)
                int sum = 0, n = 0;
                do { LOOK() sum += LATENESS(p, now) >> 7; n++; } while ((unsigned)(tk - XM * 128 - now) < 0x40000u);
                const int l = (int)((float)sum * __builtin_amdgcn_rcpf((float)n)) - BK + prov / 4;
                if (k == K0 && l - prov / 4 > RTH) {
                    const int d_ = min(l * 4 - prov, 1600);
                    if (lane <= 2 * NU) atomicAdd((unsigned*)&sm.tu[lane], d_ * 32u);
                    off += d_; prov += d_; hold = 1;
                }
                if (l > RTH && lane == 0) GSTS((unsigned*)(ws + WS_REF + (blockIdx.x & 63) * LSTRIDE + 64) + (blockIdx.x >> 6), (unsigned)min(l, 1000) | ((rtag + k) << 10));
            }
            const int cum0 = cum;
            bool got = boss;
            if (!boss && (k > K0 || !joined)) {
                WATCH(tk - XR * 128)
                const u64 x = GLDS(dec + (joined ? k * 16 : 0));
                dv = __builtin_amdgcn_readfirstlane((unsigned)x);
                got = __builtin_amdgcn_readfirstlane((unsigned)(x >> 32)) == rtag;
                if (got && !joined) { JOIN(dv) got = false; }
            }
            sp1 = sp; sp = 0;
            if (got && k > K0) { cum = dv >> 18; sp = dv >> 6 & 4095u; dp = dv & 63u; }
            const int yld = cum - cum0 + sp;
            { const int pj = min(prov, cum - cum0); off -= pj; prov -= pj; off1 = off;
              const int ps = min(prov, sp); off -= ps; prov -= ps;
              if (hold) hold--; else prov = 0;
              const int r_ = off - prov; off -= r_ > 40 || r_ < -40 ? r_ / NB : r_; }
            NEWLINE(k)
            cum = min(cum + sp + dp, 16383);
            if (boss) dv = (cum << 18) | dp;
            if (boss && k >= K0 && k + 1 < 2 * NU && (int)(NOW() * 128 - tk) < -60 * 128) {
                WATCH(tk - (XM - XD1) * 128)
                const v4u x = __builtin_amdgcn_raw_buffer_load_b128(__builtin_amdgcn_make_buffer_rsrc(ws + WS_REF + 64, 0, 64 * LSTRIDE, 0x00020000), lane * LSTRIDE, 0, SC1);
                float a[4];
#pragma unroll
                for (int i = 0; i < 4; i++) a[i] = ((x[i] ^ ((rtag + k) << 10)) >> 10) == 0 ? (float)(x[i] & 1023u) : 0.f;
                const int tot = (int)wave_sum(a[0] + a[1] + a[2] + a[3]) >> 6;
                float mn = 0.f;
                if (tot >= 40) {
                    float st = (float)tot * 0.125f;
#pragma unroll
                    for (int i = 0; i < 4; i++, st *= 0.5f) {
                        const float x_ = mn + st;
                        const int c_ = __builtin_popcountll(__builtin_amdgcn_ballot_w64(a[0] >= x_)) + __builtin_popcountll(__builtin_amdgcn_ballot_w64(a[1] >= x_)) +
                                       __builtin_popcountll(__builtin_amdgcn_ballot_w64(a[2] >= x_)) + __builtin_popcountll(__builtin_amdgcn_ballot_w64(a[3] >= x_));
                        mn = c_ >= QN ? x_ : mn;
                    }
                }
                const float c = mn + (float)CAP, fth = (float)(FTH * (2 * NU - (int)k));
                const int cut = (int)wave_sum(fminf(a[0], c) + fminf(a[1], c) + fminf(a[2], c) + fminf(a[3], c)) >> 6, m4 = (int)mn * 4;
                const bool far = __builtin_amdgcn_ballot_w64(fmaxf(fmaxf(a[0], a[1]), fmaxf(a[2], a[3])) > fth) != 0;
                const int pend = yld + sp1 * XM / 320;
                const bool stall = m4 >= SMIN * 4 || k <= K0 + 1;
                const int nj = max((stall ? tot + (k <= K0 + 1 ? tot / 8 : SADD) : m4) - pend, 0);
                const int up = k >= 6 && tot < tot1 + 32 ? max(tot - tot1, 0) : 0;
                const int ns = stall ? 0 : min(max(max(min(m4 + (tot - m4 + up) * GAIN / 16 - pend - nj, (cut - m4 + up) * GAIN / 16), 0), far ? FLOOR * 4 : 0), 4000);
                int ndp = dp;
                if (k >= KD && !((KNOT >> k) & 1) && tot >= KI && tot < 100 && tot > tot1 && tot < tot1 + 32 && tot1 < tot2 + 32 && !lk2) { ndp = min(dp + DPS, 40); flo = ndp - 1; lk2 = LOCK; lock = LOCK; }
                else if (k >= KD && dp < DP0 && tot >= WARN && tot < 100 && tot >= tot1) { ndp = dp + 2; lock = LOCK; }
                else if (lock) lock--;
                if (lk2) lk2--;
                if (k == NU - 1) flo = 0;
                if (k >= KD && tot + tot1 <= QUIET && (!lock || dp > max(DP0, flo)) && dp > flo) ndp = dp - 1;
                tot2 = tot1; tot1 = tot;
                dv = (min(cum + nj, 16383) << 18) | (ns << 6) | ndp;
                if ((int)(NOW() * 128 - tk) > XL * 128) dv = (cum << 18) | dp;
                else if (lane == 0) GSTS(dec + (k + 1) * 16, (u64)dv | ((u64)rtag << 32));
            }
        }
        WATCH(p >= NPC - 8 ? now - 128 : now + 4096)
    } else if (wave == NSW + 2) {
        const int ql = lane < 48 ? lane : 47;
        unsigned c1, c2, s1, s2;
        unsigned q1, q2, lkz, lks, lvz, lvs;
#define UNIT_LOADS(HEAD) { \
        q1 = ((const unsigned*)(q + (size_t)(HEAD) * DH))[ql]; q2 = ((const unsigned*)(q + (size_t)(HEAD) * DH))[48 + ql]; \
        const size_t so = ((size_t)(HEAD) * SKV + brow0) / 2 + (lane >> 3) * 128 + (lane & 7); \
        lkz = ((const unsigned*)k_zeros)[so]; lks = ((const unsigned*)k_scales)[so]; \
        lvz = ((const unsigned*)v_zeros)[so]; lvs = ((const unsigned*)v_scales)[so]; }
#define UNIT_FILL(SL, U) { \
            v4f qq; \
            qq.x = bfr(bfr(bflo(q1) * bflo(c1)) + bfr(-bflo(q2) * bflo(s1))); \
            qq.y = bfr(bfr(bflo(q2) * bflo(c2)) + bfr(bflo(q1) * bflo(s2))); \
            qq.z = bfr(bfr(bfhi(q1) * bfhi(c1)) + bfr(-bfhi(q2) * bfhi(s1))); \
            qq.w = bfr(bfr(bfhi(q2) * bfhi(c2)) + bfr(bfhi(q1) * bfhi(s2))); \
            *(v2u*)&sm.q[SL][2 * ql] = (v2u){(__float_as_uint(qq.x) >> 16) | __float_as_uint(qq.y), (__float_as_uint(qq.z) >> 16) | __float_as_uint(qq.w)}; \
            *(v4f*)&sm.kz[SL][2 * lane] = (v4f){bflo(lkz), bflo(lks), bfhi(lkz), bfhi(lks)}; \
            *(v4f*)&sm.vz[SL][2 * lane] = (v4f){bflo(lvz), bflo(lvs), bfhi(lvz), bfhi(lvs)}; \
            CBAR(); \
            if (lane == 0) lds_st(&sm.rdy[SL], (U) + 1); \
            CBAR(); }
        asm volatile("s_setprio 3");
        asm volatile("s_waitcnt vmcnt(0)\n\tv_accvgpr_read_b32 %0, a0\n\tv_accvgpr_read_b32 %1, a1\n\tv_accvgpr_read_b32 %2, a2\n\tv_accvgpr_read_b32 %3, a3\n\tv_accvgpr_read_b32 %4, a4\n\t"
                     "v_accvgpr_read_b32 %5, a5\n\tv_accvgpr_read_b32 %6, a8\n\tv_accvgpr_read_b32 %7, a9\n\tv_accvgpr_read_b32 %8, a10\n\tv_accvgpr_read_b32 %9, a11"
                     : "=v"(c1), "=v"(c2), "=v"(s1), "=v"(s2), "=v"(q1), "=v"(q2), "=v"(lkz), "=v"(lks), "=v"(lvz), "=v"(lvs));
#pragma unroll 1
        for (unsigned u = 0; u < NU; u++) {
            if (u) UNIT_LOADS(rank + GSZ * u)
            while (u >= lds_ld(&sm.merged) + NIN) SLEEP(4);
            CBAR();
            UNIT_FILL(u % NIN, u)
            if (u == 1) asm volatile("s_setprio 0");
            if ((blockIdx.x >> 3) < 26 && (u >= 2 || (blockIdx.x & 8))) {
                unsigned x_; asm volatile("s_load_dword %0, %1, %2\n\ts_waitcnt lgkmcnt(0)" : "=s"(x_)
                    : "s"((blockIdx.x & 8) ? qv : qk), "s"(min((u * 12 + (blockIdx.x >> 4)) << 21, NH * SKV * DH * 4u - 128u)) : "memory");
            }
        }
        { unsigned x_; asm volatile("s_load_dword %0, %1, 0\n\ts_load_dword %0, %2, 0\n\ts_load_dword %0, %3, 0\n\ts_waitcnt lgkmcnt(0)" : "=&s"(x_)
              : "s"(out + blockIdx.x * DH), "s"(ws + WS_PART - 128 - (blockIdx.x & 31) * 128), "s"(ws + WS_PART + NH * NSPLIT * 512 + (blockIdx.x & 31) * 128) : "memory"); }
        while (lds_ld(&sm.merged) < NU) SLEEP(16)
        CBAR();
        const unsigned T = lds_ld(&sm.tag);
        if (lane < NU) __hip_atomic_store((u64*)(ws + WS_LOG + (rank * NSPLIT + split) * LSTRIDE2) + lane, (u64)__float_as_uint(sm.ent[lane][130]) | ((u64)T << 32), __ATOMIC_RELAXED, __HIP_MEMORY_SCOPE_AGENT);
        while (lds_ld(&sm.merged) < NU + 3) SLEEP(16)
        for (;;) {
            const u64 x = __hip_atomic_load((const u64*)(ws + WS_LOG + ((blockIdx.x & 15) * NSPLIT + (lane & 15)) * LSTRIDE2) + (blockIdx.x >> 4), __ATOMIC_RELAXED, __HIP_MEMORY_SCOPE_AGENT);
            if (__builtin_amdgcn_ballot_w64((unsigned)(x >> 32) == T) == ~0ull) {
                const float L = __uint_as_float((unsigned)x);
                const float w = __builtin_amdgcn_exp2f(L - row_max(L));
                sm.fw[lane & 15] = w / row_sum(w);
                CBAR();
                if (lane == 0) lds_st(&sm.fw_rdy, 1u);
                break;
            }
            SLEEP(64)
        }
    } else {
        while (lds_ld(&sm.tag_rdy) == 0) SLEEP(1);
        CBAR();
        const unsigned T = lds_ld(&sm.tag);
        const __amdgpu_buffer_rsrc_t rsrc = __builtin_amdgcn_make_buffer_rsrc(ws + WS_PART + (rank * NSPLIT + split) * 512, 0, NH * NSPLIT * 512, 0x00020000);
        v2u* recs = (v2u*)&sm.cs[0][0];
#pragma unroll
        for (int i = 0; i < NOUT * 3; i++) (&sm.acc[0][0])[i * 64 + lane] = 0.0;
#define RECORD() \
        const float n0 = (float)(a0 - (double)h.y) * h.x, n1 = (float)(a1 - (double)h.y) * h.x, n2 = (float)(a2 - (double)h.y) * h.x; \
        const unsigned e2 = max(__float_as_uint(fmaxf(fmaxf(fabsf(n0), fabsf(n1)), fabsf(n2))) >> 23, 12u); \
        const float f2 = __uint_as_float((266u - e2) << 23); \
        const unsigned m0 = min(max((int)rintf(n0 * f2), -8191), 8191) & 0x3fff, m1 = min(max((int)rintf(n1 * f2), -8191), 8191) & 0x3fff, m2 = min(max((int)rintf(n2 * f2), -8191), 8191) & 0x3fff; \
        const v2u r = {m0 | (m1 << 14) | (m2 << 28), (m2 >> 4) | (e2 << 10) | (T << 18)};
#pragma unroll 1
        for (unsigned u = 0; u < 2 * NU - 1; u++) {
            const unsigned e = u < NU ? u : VENT(u), so = u % NOUT;
            const v4f h = *(const v4f*)&sm.ent[e][128];
            while (lds_ld(&sm.cnt[so]) != 32) SLEEP(1)
            CBAR();
            if (u < NU) {
                const float2 lg = *(const float2*)&sm.ent[e][2 * lane];
                const v4f zs = *(const v4f*)&sm.vz[u % NIN][2 * lane];
                const float m = wave_max(fmaxf(lg.x, lg.y));
                const float p0 = __expf(lg.x - m), p1 = __expf(lg.y - m);
                const float x0 = p0 * zs.y, x1 = p1 * zs.w;
                const unsigned eb = max(__float_as_uint(wave_max(fmaxf(fabsf(x0), fabsf(x1)))) >> 23, 21u);
                const float f = __uint_as_float((275u - eb) << 23);
                const float w0 = rintf(x0 * f), w1 = rintf(x1 * f);
                const float den = wave_sum(p0 + p1);
                const float bz = wave_sum(fmaf(w0, zs.x, w1 * zs.z));
                const float L = fmaf(m, 1.4426950408889634f, __builtin_amdgcn_logf(den));
                *(v2u*)&sm.ent[e][2 * lane] = (v2u){(unsigned)(int)w0, (unsigned)(int)w1};
                if (lane == 0) *(v4f*)&sm.ent[e][128] = (v4f){ldexpf(1.f / den, (int)eb - 148), bz, L, 0.f};
            } else {
                const double a0 = sm.acc[so][lane], a1 = sm.acc[so][64 + lane], a2 = sm.acc[so][128 + lane];
                RECORD()
                recs[(u - NU) * 64 + lane] = r;
                CBAR();
                sm.acc[so][lane] = 0.0; sm.acc[so][64 + lane] = 0.0; sm.acc[so][128 + lane] = 0.0;
            }
            CBAR();
            if (lane == 0) { lds_st(&sm.cnt[so], 0u); CBAR(); lds_st(&sm.merged, u + 1); }
        }
        {
            asm volatile("s_setprio 3");
            wave_sync();
            v4u rr[8]; unsigned po[8];
#pragma unroll
            for (int k = 0; k < 8; k++) { rr[k] = ((const v4u*)recs)[k * 64 + lane]; po[k] = k == 7 && lane >= 32 ? 0x7ffffff0u : (lane & 31) * 16 + (lane < 32 ? VENT(2 * k) : VENT(2 * k + 1)) * (GSZ * NSPLIT * 512); }
            const v4f h = *(const v4f*)&sm.ent[VENT(15)][128];
            asm volatile("" :: "v"(rr[0]), "v"(rr[1]), "v"(rr[2]), "v"(rr[3]), "v"(rr[4]), "v"(rr[5]), "v"(rr[6]), "v"(rr[7]), "v"(po[0]), "v"(po[1]), "v"(po[2]), "v"(po[3]), "v"(po[4]), "v"(po[5]), "v"(po[6]), "v"(po[7]), "v"(h));
            for (;;) {
                const unsigned d = __builtin_amdgcn_readfirstlane(TU(2 * NU).x) + PUBT * 128 - NOW() * 128;
                if (d >= 0x40000u) break;
                if (d > 20 * 128) SLEEP(4)
            }
#pragma unroll
            for (int k = 0; k < 8; k++) __builtin_amdgcn_raw_buffer_store_b128(rr[k], rsrc, po[k], 0, SC1);
            double a0, a1, a2;
            for (;;) {
                const unsigned c = __hip_atomic_load(&sm.cnt[NOUT - 1], __ATOMIC_RELAXED, __HIP_MEMORY_SCOPE_WORKGROUP);
                CBAR();
                a0 = sm.acc[NOUT - 1][lane]; a1 = sm.acc[NOUT - 1][64 + lane]; a2 = sm.acc[NOUT - 1][128 + lane];
                CBAR();
                if (__builtin_amdgcn_readfirstlane(c) == 32) break;
                SLEEP(0)
            }
            RECORD()
            const v4u r2 = {r.x, r.y, (unsigned)__builtin_amdgcn_update_dpp(0, (int)r.x, 0xf5, 0xf, 0xf, true), (unsigned)__builtin_amdgcn_update_dpp(0, (int)r.y, 0xf5, 0xf, 0xf, true)};
            __builtin_amdgcn_raw_buffer_store_b128(r2, rsrc, (lane & 1) ? 0x7ffffff0u : lane * 8, VENT(15) * (GSZ * NSPLIT * 512), SC1);
        }
        return;
    }

}

extern "C" void kernel_launch(void* const* d_in, const int* in_sizes, int n_in,
                              void* d_out, int out_size, void* d_ws, size_t ws_size,
                              hipStream_t stream) {
    const unsigned short* q = (const unsigned short*)d_in[0];
    const int* qk = (const int*)d_in[1];
    const unsigned short* k_scales = (const unsigned short*)d_in[2];
    const unsigned short* k_zeros = (const unsigned short*)d_in[3];
    const int* qv = (const int*)d_in[4];
    const unsigned short* v_scales = (const unsigned short*)d_in[5];
    const unsigned short* v_zeros = (const unsigned short*)d_in[6];
    const unsigned short* q_cos = (const unsigned short*)d_in[7];
    const unsigned short* q_sin = (const unsigned short*)d_in[8];
    const unsigned short* k_cos = (const unsigned short*)d_in[9];
    const unsigned short* k_sin = (const unsigned short*)d_in[10];
    unsigned char* ws = (unsigned char*)d_ws;
    attn_kernel<<<NSPLIT * GSZ, NTHREADS, 0, stream>>>(q, qk, k_scales, k_zeros, qv, v_scales, v_zeros,
                                                       q_cos, q_sin, k_cos, k_sin, (__hip_bfloat16*)d_out, ws);
}
```
